```python
import torch

def _softmax(x, axis=-1):
    return torch.softmax(x, dim=axis)

def _gelu(x):
    return torch.nn.functional.gelu(x, approximate='tanh')

def _silu(x):
    return torch.nn.functional.silu(x)

def _sigmoid(x):
    return torch.sigmoid(x)

def _layernorm(x, axis=-1, eps=1e-05):
    m = x.mean(dim=axis, keepdim=True)
    v = x.var(dim=axis, keepdim=True, unbiased=False)
    return (x - m) / torch.sqrt(v + eps)

def _scatter_add(tgt, idx, src, axis=0):
    return tgt.clone().index_add_(axis, idx, src)

def _segment_sum(src, seg, n):
    out = torch.zeros((n,) + src.shape[1:], dtype=src.dtype)
    return out.index_add_(0, seg, src)

def _pad(x, pw):
    flat = tuple((v for pair in reversed(pw) for v in pair))
    return torch.nn.functional.pad(x, flat)
kernel_name = 'adaLN_cond__sliding_local_dilated__glu_gated_swiglu__prompt'

def setup_inputs():

    _w = lambda a: (lambda t: t.to(torch.bfloat16) if t.is_floating_point() else t)(torch.as_tensor(a))

    def _A_setup():
        B, S, D = (1, 2048, 1024)
        return {'x': _w((lambda *s: torch.randn(*s, dtype=torch.bfloat16) * 0.05)(B, S, D)), 'wqkv': _w((lambda *s: torch.randn(*s, dtype=torch.bfloat16) * 0.05)(D, 3 * D)), 'wo': _w((lambda *s: torch.randn(*s, dtype=torch.bfloat16) * 0.05)(D, D))}
    _A_in = _A_setup()

    def _B_setup():
        B, S, D, F = (1, 2048, 1024, 3584)
        return {'x': _w((lambda *s: torch.randn(*s, dtype=torch.bfloat16) * 0.05)(B, S, D)), 'w1': _w((lambda *s: torch.randn(*s, dtype=torch.bfloat16) * 0.8)(D, F) * (2.0 / D) ** 0.5), 'w3': _w((lambda *s: torch.randn(*s, dtype=torch.bfloat16) * 0.8)(D, F) * (2.0 / D) ** 0.5), 'w2': _w((lambda *s: torch.randn(*s, dtype=torch.bfloat16) * 0.8)(F, D) * (2.0 / F) ** 0.5)}
    _B_in = _B_setup()
    return {'A_x': _A_in['x'], 'A_wqkv': _A_in['wqkv'], 'A_wo': _A_in['wo'], 'B_x': _B_in['x'], 'B_w1': _B_in['w1'], 'B_w3': _B_in['w3'], 'B_w2': _B_in['w2'], 'c': _w((lambda *s: torch.randn(*s, dtype=torch.bfloat16) * 0.1)(1, 1024) * 8.0), 'w_mod': _w((lambda *s: torch.randn(*s, dtype=torch.bfloat16) * 0.1)(1024, 6 * 1024) * 1024 ** (-0.5) * 8.0)}

def reference(A_x, A_wqkv, A_wo, B_x, B_w1, B_w3, B_w2, c, w_mod):

    def _A_ref(x, wqkv, wo):
        B, S, D = x.shape
        H = 32
        Dh = D // H
        W, dil = (8, 2)
        qkv = (lambda x, *d: x.permute(*d))((x @ wqkv).reshape(B, S, 3, H, Dh), 2, 0, 3, 1, 4)
        q, k, v = (qkv[0], qkv[1], qkv[2])
        scale = Dh ** (-0.5)
        pos = torch.arange(S)
        off = torch.arange(W) * dil
        idx = pos[:, None] - off[None, :]
        mask = idx >= 0
        idx_c = torch.where(mask, idx, 0).reshape(-1)
        kg = k[:, :, idx_c, :].reshape(B, H, S, W, Dh)
        vg = v[:, :, idx_c, :].reshape(B, H, S, W, Dh)
        logits = torch.einsum('bhsd,bhswd->bhsw', q, kg) * scale
        logits = torch.where(mask[None, None, :, :], logits, -1000000000.0)
        attn = _softmax(logits, axis=-1)
        o = torch.einsum('bhsw,bhswd->bhsd', attn, vg)
        return (lambda x, *d: x.permute(*d))(o, 0, 2, 1, 3).reshape(B, S, D) @ wo

    def _B_ref(x, w1, w3, w2):
        gate = torch.einsum('bsd,df->bsf', x, w1)
        up = torch.einsum('bsd,df->bsf', x, w3)
        h = _silu(gate) * up
        out = torch.einsum('bsf,fd->bsd', h, w2)
        return out
    x = A_x
    mod = (_silu(c) @ w_mod).reshape(c.shape[0], 6, 1, -1)
    sa, ca, ga, sb, cb, gb = (mod[:, 0].clone(), mod[:, 1].clone(), mod[:, 2].clone(), mod[:, 3].clone(), mod[:, 4].clone(), mod[:, 5].clone())
    h = _layernorm(x, axis=-1) * (1 + ca) + sa
    x = x + ga * _A_ref(h, A_wqkv, A_wo)
    h = _layernorm(x, axis=-1) * (1 + cb) + sb
    x = x + gb * _B_ref(h, B_w1, B_w3, B_w2)
    return x
```

```cpp
#include <hip/hip_runtime.h>
#include <stdint.h>
#include <type_traits>
#pragma clang diagnostic ignored "-Winline-asm"

typedef unsigned short u16;
typedef short bf16x8 __attribute__((ext_vector_type(8)));
typedef short bf16x4 __attribute__((ext_vector_type(4)));
typedef float f32x4 __attribute__((ext_vector_type(4)));
typedef float f32x2 __attribute__((ext_vector_type(2)));
typedef unsigned int u32x4 __attribute__((ext_vector_type(4)));
typedef unsigned int u32x2 __attribute__((ext_vector_type(2)));
#define LDS_PTR(T) __attribute__((address_space(3))) T*
#define BAR() do { asm volatile("" ::: "memory"); __builtin_amdgcn_s_barrier(); asm volatile("" ::: "memory"); } while (0)
#define SCHED0() __builtin_amdgcn_sched_barrier(0)
template <int N> using IC = std::integral_constant<int, N>;
typedef std::integral_constant<bool, true> T_;
typedef std::integral_constant<bool, false> F__;

constexpr int S_ = 2048, D_ = 1024, F_ = 3584;
#define STAT_DECL
#define STAT_WRAP(x) do { unsigned spin_ = 0; x; } while (0)
#define STAT_OUT(k)
#define STAMP_AT(i)
#define STAMP_G(ia, ib)
#define STAMP_L(i)

__device__ __forceinline__ float bf2f(u16 v) { return __uint_as_float(((unsigned)v) << 16); }
__device__ __forceinline__ unsigned pk2(float lo, float hi) {
  typedef __bf16 bf2 __attribute__((ext_vector_type(2)));
  f32x2 v = {lo, hi}; bf2 r = __builtin_convertvector(v, bf2); return __builtin_bit_cast(unsigned, r);
}
__device__ __forceinline__ float lo2f(unsigned u) { return __uint_as_float(u << 16); }
__device__ __forceinline__ float hi2f(unsigned u) { return __uint_as_float(u & 0xffff0000u); }
__device__ __forceinline__ float rbf(float v) { return lo2f(pk2(v, 0.f)); }
#define glds16(sbase, voff, lds_off, imm) \
  asm volatile("s_add_u32 m0, %2, %3\n\ts_nop 0\n\tglobal_load_lds_dwordx4 %0, %1" ::"v"(voff), "s"(sbase), "s"(lds_off), "n"(imm) : "memory", "scc")
#define glds16s(sbase, voff, lds_off, imm) \
  asm volatile("s_add_u32 m0, %2, %3\n\ts_nop 0\n\tglobal_load_lds_dwordx4 %0, %1 sc1" ::"v"(voff), "s"(sbase), "s"(lds_off), "n"(imm) : "memory", "scc")
#define GLDS4(SC) "s_mov_b32 m0, %6\n\ts_nop 0\n\tglobal_load_lds_dwordx4 %0, %4" SC "\n\tglobal_load_lds_dwordx4 %1, %5 offset:1024" SC \
                  "\n\tglobal_load_lds_dwordx4 %2, %4 offset:2048" SC "\n\tglobal_load_lds_dwordx4 %3, %5 offset:3072" SC
template <bool SC1> __device__ __forceinline__ void glds16x4(const void* s0, const void* s1, const unsigned (&v)[4], unsigned lds) {
  if (SC1) asm volatile(GLDS4(" sc1") ::"v"(v[0]), "v"(v[1]), "v"(v[2]), "v"(v[3]), "s"(s0), "s"(s1), "s"(lds) : "memory");
  else asm volatile(GLDS4("") ::"v"(v[0]), "v"(v[1]), "v"(v[2]), "v"(v[3]), "s"(s0), "s"(s1), "s"(lds) : "memory");
}
__device__ __forceinline__ void glds16x2(const void* s0, unsigned v0, unsigned v1, unsigned lds) {
  asm volatile("s_mov_b32 m0, %3\n\ts_nop 0\n\tglobal_load_lds_dwordx4 %0, %2\n\tglobal_load_lds_dwordx4 %1, %2 offset:1024" ::"v"(v0), "v"(v1), "s"(s0), "s"(lds) : "memory");
}
__device__ __forceinline__ void glds4(const void* sbase, unsigned voff, unsigned lds_off) {
  asm volatile("s_mov_b32 m0, %2\n\ts_nop 0\n\tglobal_load_lds_dword %0, %1" ::"v"(voff), "s"(sbase), "s"(lds_off) : "memory");
}
__device__ __forceinline__ void stouch4(const void* p) {
  asm volatile("s_load_dwordx2 s[100:101], %0, 0x3c\n\ts_load_dwordx2 s[100:101], %0, 0xbc\n\ts_load_dwordx2 s[100:101], %0, 0x13c\n\ts_load_dwordx2 s[100:101], %0, 0x1bc"
               ::"s"(p) : "memory", "s100", "s101");
}
template <bool SC1> __device__ __forceinline__ void st16(void* sbase, unsigned voff, u32x4 v) {
  __builtin_amdgcn_raw_buffer_store_b128(v, __builtin_amdgcn_make_buffer_rsrc(sbase, 0, 0x7fffffff, 0x00020000), voff, 0, SC1 ? 16 : 0);
}
template <bool SC1> __device__ __forceinline__ u32x4 ld16(const void* sbase, unsigned voff) {
  return __builtin_amdgcn_raw_buffer_load_b128(__builtin_amdgcn_make_buffer_rsrc((void*)sbase, 0, 0x7fffffff, 0x00020000), voff, 0, SC1 ? 16 : 0);
}
template <int N> __device__ __forceinline__ void wait_vm() { asm volatile("s_waitcnt vmcnt(%0)" ::"n"(N) : "memory"); }
__device__ __forceinline__ void wait_lgkm0() { asm volatile("" ::: "memory"); __builtin_amdgcn_s_waitcnt(0xC07F); asm volatile("" ::: "memory"); }
__device__ __forceinline__ bf16x4 ld_tr4(const LDS_PTR(char) p) { return __builtin_amdgcn_ds_read_tr16_b64_v4i16((LDS_PTR(bf16x4))p); }
__device__ __forceinline__ bf16x8 cat8(bf16x4 lo, bf16x4 hi) { return __builtin_shufflevector(lo, hi, 0, 1, 2, 3, 4, 5, 6, 7); }
__device__ __forceinline__ void mfma_acc(f32x4& acc, bf16x8 a, bf16x8 b) { asm volatile("v_mfma_f32_16x16x32_bf16 %0, %1, %2, %0" : "+v"(acc) : "v"(a), "v"(b)); }
template <int STEP> __device__ __forceinline__ void mfma_sel(f32x4& acc, bf16x8 a, bf16x8 b, unsigned& mask, int& dl, int wrap) {
  asm volatile("s_add_u32 %1, %1, %1\n\tv_mfma_f32_16x16x32_bf16 %0, %3, %4, %0\n\ts_cselect_b32 %2, %5, %6" : "+v"(acc), "+s"(mask), "=s"(dl) : "v"(a), "v"(b), "s"(wrap), "n"(STEP) : "scc");
}
constexpr unsigned wrap_mask(int first, int ns) { unsigned m = 0; for (int t = 0; t < 32; ++t) if ((first + t) % ns == ns - 1) m |= 0x80000000u >> t; return m; }
template <int N> __device__ __forceinline__ void wait_lgkm() { __builtin_amdgcn_s_waitcnt(0xC07F | (N << 8)); }
template <int R, int C> __device__ __forceinline__ void mfma_done(f32x4 (&acc)[R][C]) {
  asm volatile("s_nop 9");
#pragma unroll
  for (int i = 0; i < R; ++i)
#pragma unroll
    for (int j = 0; j < C; ++j) asm volatile("" : "+v"(acc[i][j]));
}
__device__ __forceinline__ int lane_id() { int l; asm volatile("v_mbcnt_lo_u32_b32 %0, -1, 0\n\tv_mbcnt_hi_u32_b32 %0, -1, %0" : "=v"(l)); return l; }
__device__ __forceinline__ float xsum16_32(float v) {
  auto a = __builtin_amdgcn_permlane16_swap(__float_as_uint(v), __float_as_uint(v), false, false); v = __uint_as_float(a[0]) + __uint_as_float(a[1]);
  auto c = __builtin_amdgcn_permlane32_swap(__float_as_uint(v), __float_as_uint(v), false, false); return __uint_as_float(c[0]) + __uint_as_float(c[1]);
}
__device__ __forceinline__ float xmax16_32(float v) {
  auto a = __builtin_amdgcn_permlane16_swap(__float_as_uint(v), __float_as_uint(v), false, false); v = fmaxf(__uint_as_float(a[0]), __uint_as_float(a[1]));
  auto c = __builtin_amdgcn_permlane32_swap(__float_as_uint(v), __float_as_uint(v), false, false); return fmaxf(__uint_as_float(c[0]), __uint_as_float(c[1]));
}

__device__ __forceinline__ float ln_rstd(float var) { return __builtin_amdgcn_rcpf(rbf(__builtin_amdgcn_sqrtf(rbf(rbf(var) + 1e-5f)))); }

constexpr size_t WS_MOD = 0;
constexpr size_t WS_EPO = 64 << 10, WS_FL1 = 96 << 10;
constexpr size_t WS_CTL = 128 << 10;
constexpr int C2 = 0, C3 = 16, C4 = 32, C5 = 48, CM = 144;
constexpr size_t WS_FLG = WS_CTL + (152 << 10), CTL_BYTES = 216 << 10;
constexpr size_t WS_XCH = 512 << 10;
constexpr size_t WS_MOD2 = 1 << 20;
constexpr size_t WS_H = 2 << 20, WS_O = WS_H + (4 << 20), WS_X1 = WS_O + (4 << 20), WS_H2 = WS_X1 + (4 << 20), WS_HH = WS_H2 + (4 << 20),
                 WS_PART = WS_HH + (size_t)S_ * F_ * 2;

template <int NL> __device__ __forceinline__ f32x2 slab_sum(const unsigned (&mc)[NL / 2], const u32x4 (&mw)[NL]) {
  f32x2 a2[4] = {};
#pragma unroll
  for (int i = 0; i < NL; ++i) {
    const float cv = (i & 1) ? hi2f(mc[i >> 1]) : lo2f(mc[i >> 1]);
    const float sv = rbf(cv * __builtin_amdgcn_rcpf(1.f + __builtin_amdgcn_exp2f(cv * -1.4426950408889634f)));
#pragma unroll
    for (int j = 0; j < 4; ++j) a2[j] += (f32x2){lo2f(mw[i][j]), hi2f(mw[i][j])} * sv;
  }
  float acc[8] = {a2[0][0], a2[0][1], a2[1][0], a2[1][1], a2[2][0], a2[2][1], a2[3][0], a2[3][1]};
#pragma unroll
  for (int j = 0; j < 4; ++j) { auto a = __builtin_amdgcn_permlane32_swap(__float_as_uint(acc[j]), __float_as_uint(acc[j + 4]), false, false); acc[j] = __uint_as_float(a[0]) + __uint_as_float(a[1]); }
#pragma unroll
  for (int j = 0; j < 2; ++j) { auto a = __builtin_amdgcn_permlane16_swap(__float_as_uint(acc[j]), __float_as_uint(acc[j + 2]), false, false); acc[j] = __uint_as_float(a[0]) + __uint_as_float(a[1]); }
#pragma unroll
  for (int j = 0; j < 2; ++j) acc[j] += __uint_as_float(__builtin_amdgcn_mov_dpp(__float_as_uint(acc[j]), 0x128, 0xf, 0xf, false));
  return (f32x2){acc[0], acc[1]};
}

constexpr int K3_B = 18432, K3_STG = 18432 + 24576;
constexpr int K4_RED = 0;
constexpr int K5_G = 8192, K5_U = 16384 + 16, K5_STG = 24592, K5_NS = 6;
constexpr int K6_STG = 16384, K6_NS = 9;
constexpr int LDS_VEC = 158720, LDS_DUMMY = LDS_VEC + 1024, MEGA_LDS = LDS_DUMMY + 256;
constexpr unsigned SPIN_CAP = 1u << 19;
#define N0_5 3
#define HELP 0
#define HELP_HI
#define HELP_LO
#define T3_SLEEP 3
#define HOLD 24
#define NDR 2
#define EPI_PRIO 0
#define POLL5 8
#define POLL6 2

__device__ __forceinline__ void signal(unsigned* cnt, int c, int lane) {
  if (lane < 8) __hip_atomic_fetch_add(cnt + (c * 8 + lane) * 32, 1u, __ATOMIC_RELAXED, __HIP_MEMORY_SCOPE_AGENT);
}
template <int D0> __device__ __forceinline__ void poll(const unsigned* cnt, unsigned want) {
  if (D0) __builtin_amdgcn_s_sleep(D0);
#pragma unroll 1
  for (unsigned spins = 0; spins < SPIN_CAP; ++spins) {
    if (__builtin_amdgcn_ballot_w64(__hip_atomic_load(cnt, __ATOMIC_RELAXED, __HIP_MEMORY_SCOPE_AGENT) < want) == 0) break;
    __builtin_amdgcn_s_sleep(1);
  }
  __builtin_amdgcn_fence(__ATOMIC_ACQUIRE, "wavefront");
}

typedef float f32x32 __attribute__((ext_vector_type(32)));
typedef float f32x16 __attribute__((ext_vector_type(16)));
#define M5(i, j) "v_mfma_f32_16x16x32_bf16 v[28*" #i "+4*" #j ":28*" #i "+4*" #j "+3], v[128+4*" #j ":131+4*" #j "], v[112+4*" #i ":115+4*" #i "], v[28*" #i "+4*" #j ":28*" #i "+4*" #j "+3]\n\t"
#define RA5(i) "ds_read_b128 v[112+4*" #i ":115+4*" #i "], %[aA] offset:1024*" #i "\n\t"
#define RW5(j, h, a, o) "ds_read_b64_tr_b16 v[128+4*" #j "+2*" #h ":129+4*" #j "+2*" #h "], %[" #a "] offset:" #o "\n\t"
#define ST5(a) "v_add_u32 %[" #a "], %[dl], %[" #a "]\n\t"
#define SEL5A "s_add_u32 %[mask], %[mask], %[mask]\n\t"
#define SEL5B "s_cselect_b32 %[dl], %[wrap], 24592\n\t"
#define LG(n) "s_waitcnt lgkmcnt(" #n ")\n\t"
#define TRIP5A(X)                                                                                                                                       \
  M5(0, 0) RW5(6, 1, aM, 2048) M5(1, 0) RA5(2) ST5(aM) M5(1, 1) RA5(3) ST5(a0) M5(0, 1) ST5(aA) LG(6) M5(0, 2) ST5(a1) M5(1, 2) SEL5A M5(1, 3) SEL5B      \
  M5(0, 3) M5(0, 4) M5(1, 4) LG(0) M5(1, 5) M5(0, 5) "s_barrier\n\t" M5(0, 6) RA5(0) M5(1, 6) RA5(1)                                                      \
  M5(2, 0) X M5(3, 0) RW5(0, 0, a0, 0) M5(3, 1) RW5(0, 1, a0, 2048) M5(2, 1) RW5(1, 0, a0, 8208) M5(2, 2) RW5(1, 1, a0, 10256)                            \
  M5(3, 2) RW5(2, 0, a1, 0) M5(3, 3) RW5(2, 1, a1, 2048) M5(2, 3) RW5(3, 0, a1, 8208) M5(2, 4) RW5(3, 1, a1, 10256)                                       \
  M5(3, 4) RW5(4, 0, a0, 4096) M5(3, 5) RW5(4, 1, a0, 6144) M5(2, 5) RW5(5, 0, a0, 12304) M5(2, 6) RW5(5, 1, a0, 14352) LG(8) M5(3, 6) RW5(6, 0, aM, 0)
#define TRIP5B(X)                                                                                                                                       \
  M5(0, 0) RW5(5, 1, aM, 9232) M5(1, 0) RA5(2) ST5(aM) M5(1, 1) RA5(3) ST5(a0) M5(0, 1) ST5(aA) LG(6) M5(0, 2) ST5(a1) M5(1, 2) SEL5A M5(1, 3) SEL5B      \
  M5(0, 3) LG(2) M5(0, 4) M5(1, 4) LG(0) "s_barrier\n\t" M5(1, 5) RA5(1) M5(0, 5) RA5(0)                                                                  \
  M5(2, 0) X M5(3, 0) RW5(0, 0, a0, 0) M5(3, 1) RW5(0, 1, a0, 2048) M5(2, 1) RW5(1, 0, a0, 8208) M5(2, 2) RW5(1, 1, a0, 10256)                            \
  M5(3, 2) RW5(2, 0, a1, 0) M5(3, 3) RW5(2, 1, a1, 2048) M5(2, 3) RW5(3, 0, a1, 8208) M5(2, 4) RW5(3, 1, a1, 10256)                                       \
  M5(3, 4) RW5(4, 0, aM, 0) M5(3, 5) RW5(4, 1, aM, 1024) LG(6) M5(2, 5) RW5(5, 0, aM, 8208)
#define PRE5 RA5(0) RA5(1) RW5(0, 0, a0, 0) RW5(0, 1, a0, 2048) RW5(1, 0, a0, 8208) RW5(1, 1, a0, 10256) RW5(2, 0, a1, 0) RW5(2, 1, a1, 2048) RW5(3, 0, a1, 8208) RW5(3, 1, a1, 10256)
#define ACC5 "+{v[0:31]}"(c0), "+{v[32:63]}"(c1), "+{v[64:95]}"(c2), "+{v[96:111]}"(c3)
#define CLOB5 "memory", "scc", "v112", "v113", "v114", "v115", "v116", "v117", "v118", "v119", "v120", "v121", "v122", "v123", "v124", "v125", "v126", "v127", "v128", "v129", "v130", "v131", "v132", "v133", \
              "v134", "v135", "v136", "v137", "v138", "v139", "v140", "v141", "v142", "v143", "v144", "v145", "v146", "v147", "v148", "v149", "v150", "v151", "v152", "v153", "v154", "v155"
static_assert(K5_STG == 24592 && K5_U - K5_G == 8208, "TRIP5");
#define LOAD5(W, B4)                                                                                                                                    \
  "s_add_u32 m0, %[st], %[dB]\n1:\n\ts_waitcnt vmcnt(" #W ")\n\ts_barrier\n\tglobal_load_lds_dwordx4 %[v0], s[42:43]\n\tglobal_load_lds_dwordx4 %[v1], s[42:43] offset:1024\n\t" \
  "global_load_lds_dwordx4 %[v2], s[42:43] offset:2048\n\t" B4 "s_add_u32 m0, %[st], %[dA]\n\ts_add_u32 s40, s40, %[aA]\n\ts_addc_u32 s41, s41, %[hi]\n\t"     \
  "s_add_u32 s42, s42, %[aB]\n\ts_addc_u32 s43, s43, %[hi]\n\ts_getreg_b32 %[t], hwreg(HW_REG_IB_STS, 8, 4)\n\ts_cmp_gt_i32 %[t], %[lim]\n\ts_cbranch_scc1 2f\n\t" \
  "s_load_dwordx2 s[100:101], s[44:45], 0x3c\n\ts_load_dwordx2 s[100:101], s[44:45], 0xbc\n\ts_load_dwordx2 s[100:101], s[44:45], 0x13c\n\t"               \
  "s_cmp_eq_u32 %[lim], 9\n\ts_cbranch_scc1 3f\n\ts_load_dwordx2 s[100:101], s[44:45], 0x1bc\n3:\n\ts_add_u32 s44, s44, s46\n\ts_addc_u32 s45, s45, s47\n\ts_sub_u32 %[rem], %[rem], 1\n\ts_cbranch_scc0 2f\n\t"   \
  "s_mov_b64 s[44:45], s[48:49]\n\ts_mov_b64 s[46:47], s[50:51]\n\ts_mov_b32 %[rem], %[rem2]\n\ts_mov_b64 s[48:49], s[52:53]\n\ts_mov_b64 s[50:51], 0x10000\n\t" \
  "s_mov_b32 %[rem2], %[rem3]\n\ts_mov_b32 %[rem3], -1\n\ts_cmp_eq_u32 %[rem], 15\n\ts_cselect_b32 %[lim], 9, 7\n\ts_min_i32 %[lim], %[lim], %[rem]\n"                                    \
  "2:\n\ts_add_u32 %[st], %[st], 24592\n\ts_cmp_eq_u32 %[st], 147552\n\ts_cselect_b32 %[st], 0, %[st]\n\ts_add_u32 %[t], %[st], %[dB]\n\ts_sub_u32 %[n], %[n], 1\n\t" \
  "global_load_lds_dwordx4 %[vA], s[40:41] sc1\n\tglobal_load_lds_dwordx4 %[vA], s[40:41] offset:1024 sc1\n\ts_mov_b32 m0, %[t]\n\ts_cbranch_scc0 1b"

__global__ __launch_bounds__(768, 3) __attribute__((amdgpu_num_sgpr(98))) void k2_block(const u16* __restrict__ x, const u16* __restrict__ wqkv, const u16* __restrict__ wo,
                                                   const u16* __restrict__ w1, const u16* __restrict__ w3, const u16* __restrict__ w2,
                                                   const u16* __restrict__ cnd, const u16* __restrict__ wmod, u16* __restrict__ out, char* __restrict__ ws) {
  extern __shared__ __attribute__((aligned(16))) char smem_[];
  LDS_PTR(char) smem = (LDS_PTR(char))smem_;
  const int tid = threadIdx.x, lane = tid & 63;
  const int wave = __builtin_amdgcn_readfirstlane(tid >> 6);
  const unsigned lds_base = (unsigned)(uintptr_t)smem;
  const int b = blockIdx.x, xc = b & 7, ii = b >> 3;
  const int tm3 = (xc & 1) * 8 + (ii & 7), tn3 = (xc >> 1) * 4 + (ii >> 3);
  const int tm4 = (xc >> 1) * 4 + (ii & 3), tn4 = (xc & 1) * 8 + (ii >> 2);
  const int tm5 = tm3, tn5 = tn3;
  const int kh6 = xc >> 2, tm6 = tm3, tn6 = tn3 & 7;
  const int u1 = (b >> 4) * 8 + xc;
  unsigned* cnt = (unsigned*)(ws + WS_CTL);
  unsigned long long* fl1 = (unsigned long long*)(ws + WS_FL1);
  STAMP_AT(0 );

  if (wave >= 8) {
    const int lw = wave - 8, P = b * 4 + lw, lane = lane_id();
    const bool isA = lw < 2;
    asm volatile("" ::"s"(wqkv), "s"(wo), "s"(w1), "s"(w3), "s"(w2), "s"(ws));
    int kpf = 0;
    const char* tp = (const char*)wqkv + P * 512; int tstep = 524288;
    auto touch = [&]() {
      if (kpf < 43 && __builtin_amdgcn_s_getreg((3 << 11) | (8 << 6) | 7) <= 6) {
        stouch4(tp);
        ++kpf; tp += tstep;
        if (__builtin_expect((0x10001100ull >> kpf) & 1, 0)) {
          const char* wb = (const char*)((P & 1) ? w3 : w1);
          if (kpf == 8) tp = (const char*)wo + P * 512;
          else if (kpf == 12) { tp = wb + (P >> 1) * 896 - (P >> 1 ? 256 : 0); tstep = 512 * 896; }
          else { tp = wb + (8191 - (P >> 1)) * 896 + 256; tstep = -512 * 896; }
        }
      }
    };
    auto touchn = [&](int n) {
#pragma unroll 1
      for (int i = 0; i < n; ++i) { touch(); __builtin_amdgcn_s_sleep(16); }
      touch();
    };
    const char* epo = ws + WS_EPO;
    const char* wq3 = (const char*)wqkv + (32768 + b * 64) * 128;
    const unsigned swz = ((lane & 7) ^ ((lane >> 3) & 6)) * 16;
    const int cg = lw * 16 + (lw < 3 ? tn4 : tn6 * 2 + kh6);
    auto run3 = [&](auto isA_) {
      constexpr bool A_ = decltype(isA_)::value; constexpr int N = A_ ? 9 : 12;
      const char* sp = A_ ? ws + WS_H + lw * 131072 : (const char*)wqkv + tn3 * 128;
      const unsigned v0 = A_ ? lane * 16 : (lane >> 3) * 6144 + swz;
      const unsigned d0 = lds_base + (A_ ? lw * 9216 : K3_B + (lw - 2) * 12288);
      auto issue = [&](unsigned st) {
#pragma unroll
        for (int i = 0; i < N; ++i) {
          if (A_) { const int fb = tm3 * 8 - 1 + i; glds16s(sp + (fb < 0 ? 0 : fb) * 1024, v0, d0 + st, i * 1024); }
          else { const int tb = (lw - 2) * 12 + i; glds16(sp + (tb & 7) * 8 * 6144 + (tb >> 3) * 2048, v0, d0 + st, i * 1024); }
        }
        sp += A_ ? 262144 : 64 * 6144;
      };
      { unsigned long long ep;
        asm volatile("s_load_dwordx2 %0, %1, 0x0\n\ts_waitcnt lgkmcnt(0)" : "=s"(ep) : "s"(epo) : "memory");
        ep += 1;
        if (lw == 1) {
#pragma unroll
          for (int k = 0; k < 3; ++k) {
            const int i = b * 182 + (k * 64 + lane < 182 ? k * 64 + lane : 0);
            st16<true>(ws, i < 32768 ? (unsigned)WS_XCH + i * 16 : (unsigned)WS_CTL + (i - 32768) * 16, i < 32768 ? (u32x4){~0u, ~0u, ~0u, ~0u} : (u32x4){0u, 0u, 0u, 0u});
          }
        } else touchn(1);
        BAR();
        auto flag = [&](int idx) { wait_vm<0>(); if (lane < 8) __hip_atomic_store(fl1 + lane * 256 + idx, ep, __ATOMIC_RELAXED, __HIP_MEMORY_SCOPE_AGENT); };
        if (lw == 1 && (b & 8)) {
          const int kq = u1 & 3, cgp = u1 >> 2;
          float sm = 0.f;
#pragma unroll
          for (int r = 0; r < 8; ++r) sm += *(const LDS_PTR(float))(smem + 8192 + (r * 64 + lane) * 4);
          __hip_atomic_store((unsigned*)(ws + WS_MOD) + kq * 2048 + cgp * 64 + lane, __float_as_uint(sm), __ATOMIC_RELAXED, __HIP_MEMORY_SCOPE_AGENT);
          flag(((cgp >> 3) & 1) * 64 + (cgp >> 4) * 32 + (cgp & 7) * 4 + kq);
        }
        touch();
        BAR();
        if (lw == 1 && !(b & 8)) flag(128 + u1);
        if (lw < 2) {
          if (lw == 1) BAR();
#pragma unroll 1
          for (unsigned spins = 0; spins < SPIN_CAP; ++spins) {
            bool ok = true;
#pragma unroll
            for (int k = 0; k < 4; ++k) ok &= __hip_atomic_load(fl1 + xc * 256 + (lw ? k : (b >> 3) & 1) * 64 + lane, __ATOMIC_RELAXED, __HIP_MEMORY_SCOPE_AGENT) == ep;
            if (__builtin_amdgcn_ballot_w64(!ok) == 0) break;
            __builtin_amdgcn_s_sleep(4);
          }
          if (lw == 0) BAR();
          else if (b == 0 && lane == 0) __hip_atomic_store((unsigned long long*)(ws + WS_EPO), ep, __ATOMIC_RELAXED, __HIP_MEMORY_SCOPE_AGENT);
        } else { touch(); BAR(); }
        touch();
      }
      BAR();
      if (lw == 0) {
        signal(cnt, C2 + (u1 >> 3), lane); glds4(wq3, lane * 128, lds_base + LDS_DUMMY);
        poll<8>(cnt + ((C2 + ((lane & 1) && tm3 ? tm3 - 1 : tm3)) * 8 + xc) * 32, 16);
      }
      if (!A_) { issue(0); issue(K3_STG); }
      touch();
      BAR();
      if (A_) { issue(0); issue(K3_STG); }
      wait_vm<N>();
      BAR();
      issue(2 * K3_STG);
      unsigned st = 0;
#pragma unroll 1
      for (int j = 0; j < 14; ++j) {
        wait_vm<N>();
        BAR();
        if (j < 13) issue(st);
        st = st == 2 * K3_STG ? 0 : st + K3_STG;
        touch();
        if (j == 2 && lw == 2 && lane == 0 && !(b & 8)) __hip_atomic_fetch_add(cnt + (CM * 8 + (u1 >> 1)) * 32, 1u, __ATOMIC_RELAXED, __HIP_MEMORY_SCOPE_AGENT);
      }
    };
    if (isA) run3(T_()); else run3(F__());
    { const char* sp = (const char*)wo + tn4 * 128 + lw * 32 * 2048;
      const unsigned v0 = (lane >> 3) * 2048 + swz;
      auto issue = [&](int s, unsigned slot) {
#pragma unroll
        for (int i = 0; i < 4; ++i) glds16(sp + (s * 128 + i * 8) * 2048, v0, lds_base + slot + lw * 4096, i * 1024);
      };
      poll<0>(cnt + (CM * 8 + cg) * 32, 2);
      wait_vm<0>();
      issue(0, 57344);
      BAR();
      f32x4 pv[4];
#pragma unroll
      for (int i = 0; i < 4; ++i) pv[i] = __builtin_bit_cast(f32x4, ld16<true>(ws + WS_MOD2, (((lane >> 4) * 4 + i) * 4096 + cg * 64 + (lane & 15) * 4) * 4));
      SCHED0();
      BAR();
      issue(1, 57344 + 16384);
      BAR();
      issue(2, 57344 + 2 * 16384); issue(3, 57344 + 3 * 16384); issue(4, 57344 + 4 * 16384);
      touch();
      BAR();
      if (lw == 0) signal(cnt, C3 + tm3, lane);
      issue(5, 57344 + 5 * 16384); issue(6, 0); issue(7, 16384);
      if (lw == 0) poll<16>(cnt + ((C3 + tm4) * 8 + xc) * 32, 16);
      if (lw != 0) touchn(1);
      wait_vm<0>();
      BAR();
      f32x4 v = (pv[0] + pv[1]) + (pv[2] + pv[3]);
#pragma unroll
      for (int e = 0; e < 4; ++e) { v[e] = rbf(xsum16_32(v[e])); if (lw == 2) v[e] = rbf(1.f + v[e]); }
      *(LDS_PTR(f32x4))(smem + LDS_VEC + lw * 256 + (lane & 15) * 16) = v;
      touch();
      BAR();
      wait_lgkm0();
      touchn(2);
    }
    { const int odd = tn5 & 1;
      const char* sA = ws + WS_H2 + (tm5 * 8 + lw * 2) * 1024;
      const char* sW = (const char*)(lw < 2 ? w1 : w3) + tn5 * 448;
      const char* sB = sW + (odd ? 320 - (lw & 1) * 128 : (lw & 1) * 128);
      constexpr int advA = 131072, advB = 32 * F_ * 2;
      unsigned vo[4]; const unsigned vA = lane * 16;
#pragma unroll
      for (int i = 0; i < 4; ++i) vo[i] = (i * 8 + (lane >> 3)) * (F_ * 2) - i * 1024 + swz;
      const unsigned dA = lds_base + lw * 2048, dW = lds_base + (lw < 2 ? K5_G : K5_U);
      unsigned dB = dW + (lw & 1) * 4096;
      static_assert(N0_5 == 3, "early stages");
      auto issueA = [&](int s) { glds16s(sA + s * advA, vA, dA + ((s + 2) % 6) * K5_STG, 0); glds16s(sA + s * advA + 1024, vA, dA + ((s + 2) % 6) * K5_STG, 1024); };
      auto issueB = [&](int s) { glds16x4<false>(sB + s * advB, sB + s * advB, vo, dB + ((s + 2) % 6) * K5_STG); };
      BAR();
      if (lw) { issueB(0); issueB(1); issueB(2); }
      touchn(3);
      BAR();
      if (lw == 0) { signal(cnt, C4 + tm4, lane); issueB(0); issueB(1); issueB(2); poll<10>(cnt + ((C4 + tm5) * 8 + xc) * 32, 16); }
      else touchn(1);
      BAR();
      issueA(0); issueA(1); issueA(2);
      wait_vm<4>();
      BAR();
      issueB(3); issueA(3); issueB(4); issueA(4); issueB(5); issueA(5);
      if (kpf < 12) { kpf = 12; tp = (const char*)((P & 1) ? w3 : w1) + (P >> 1) * 896 - (P >> 1 ? 256 : 0); tstep = 512 * 896; }
      const char* tp3 = (const char*)w2 + (((b >> 5) * 448 - (b >> 5 ? 112 : 0) + (b & 31)) * 4 + lw) * 512;
      const char* tp2 = kpf < 28 ? (const char*)((P & 1) ? w3 : w1) + (8191 - (P >> 1)) * 896 + 256 : tp3;
      long long ts1 = tstep, ts2 = kpf < 28 ? -512 * 896 : 65536;
      int rem = (kpf < 28 ? 27 : 43) - kpf, rem2 = kpf < 28 ? 15 : 10, rem3 = kpf < 28 ? 10 : -1, lim = kpf < 28 ? 7 : 9, n = 25, t_; unsigned st = 2 * K5_STG;
      sA += 5 * advA; sB += 6 * advB;
      wait_vm<20>();
      asm volatile(LOAD5(24, "global_load_lds_dwordx4 %[v3], s[42:43] offset:3072\n\t")
                   : "+{s[40:41]}"(sA), "+{s[42:43]}"(sB), "+{s[44:45]}"(tp), "+{s[46:47]}"(ts1), "+{s[48:49]}"(tp2), "+{s[50:51]}"(ts2), "+{s[52:53]}"(tp3), [st] "+s"(st), [n] "+s"(n),
                     [rem] "+s"(rem), [rem2] "+s"(rem2), [rem3] "+s"(rem3), [lim] "+s"(lim), [t] "=&s"(t_)
                   : [v0] "v"(vo[0]), [v1] "v"(vo[1]), [v2] "v"(vo[2]), [v3] "v"(vo[3]), [vA] "v"(vA), [dA] "s"(dA), [dB] "s"(dB), [aA] "n"(advA), [aB] "n"(advB), [hi] "n"(0)
                   : "memory", "scc", "s100", "s101");
      if (lw & 1) {
        vo[0] = vo[3] + 3072;
        vo[1] = (lane >> 2) * (F_ * 2) + ((lane & 3) ^ ((lane >> 3) & 2)) * 16 + (odd ? 0 : 192); vo[2] = vo[1] + 16 * F_ * 2 - 1024;
        vo[0] += 1088;
      }
      sA += advA; sB = sW + 31 * advB + (odd ? 64 : 256) - (lw & 1) * 1088; dB = dW + (lw & 1) * 3072; n = 31;
      asm volatile(LOAD5(20, "")
                   : "+{s[40:41]}"(sA), "+{s[42:43]}"(sB), "+{s[44:45]}"(tp), "+{s[46:47]}"(ts1), "+{s[48:49]}"(tp2), "+{s[50:51]}"(ts2), "+{s[52:53]}"(tp3), [st] "+s"(st), [n] "+s"(n),
                     [rem] "+s"(rem), [rem2] "+s"(rem2), [rem3] "+s"(rem3), [lim] "+s"(lim), [t] "=&s"(t_)
                   : [v0] "v"(vo[0]), [v1] "v"(vo[1]), [v2] "v"(vo[2]), [vA] "v"(vA), [dA] "s"(dA), [dB] "s"(dB), [aA] "n"(-advA), [aB] "n"(-advB), [hi] "n"(-1)
                   : "memory", "scc", "s100", "s101");
    }
    auto run5 = [&](auto isA_) {
      constexpr bool A_ = decltype(isA_)::value; constexpr int M = 5;
      unsigned vo[4], st; const char* s0;
      constexpr int sadv = A_ ? 131072 : 32 * 2048;
      const char* sb = A_ ? ws + WS_HH + (kh6 * 56 * 128 + tm6 * 8 + lw * 4) * 1024 : (const char*)w2 + kh6 * 1792 * 2048 + tn6 * 256 + ((lw ^ kh6 ^ 1) & 1) * 128;
#pragma unroll
      for (int i = 0; i < 4; ++i) {
        const int r = i * 8 + (lane >> 3), k = 8 * ((r >> 2) & 3) + 4 * (r >> 4) + (r & 3);
        vo[i] = A_ ? lane * 16 : k * 2048 - i * 1024 + swz;
      }
      const unsigned d6 = lds_base + lw * 4096;
      st = 0;
      s0 = sb + (A_ ? 0 : 1680 * 2048);
      auto issue6 = [&](int back = 0, int more = 0) {
        glds16x4<A_>(s0, s0 - (A_ ? 0 : back), vo, d6 + st);
        s0 += sadv + (A_ ? 0 : more - back); st = st == (K6_NS - 1) * K6_STG ? 0 : st + K6_STG;
      };
      wait_vm<4 * M>(); BAR(); issue6();
      wait_vm<3 * M + 4>(); BAR(); issue6(); issue6();
      wait_vm<2 * M + 12>(); BAR(); issue6(1792 * 2048);
      wait_vm<M + 16>(); BAR(); issue6(); issue6();
      wait_vm<24>(); BAR(); issue6(0, 224 * 2048);
      wait_vm<24>(); BAR(); issue6();
#pragma unroll 1
      for (int c = 1; c < 21; ++c) { wait_vm<24>(); BAR(); issue6(0, (0x2040 >> c) & 1 ? 224 * 2048 : 0); }
      if (!A_) s0 = sb + 128 * 2048;
#pragma unroll 1
      for (int c = 0; c < 16; ++c) { wait_vm<24>(); BAR(); issue6(0, (c & 1) ? ((c & 2) ? 256 * 2048 : 64 * 2048) : 0); }
      if (!A_) s0 = sb + 192 * 2048;
#pragma unroll 1
      for (int c = 0; c < 4; ++c) {
        wait_vm<24>(); BAR(); issue6();
        wait_vm<24>(); BAR(); issue6(0, -144 * 2048);
        wait_vm<24>(); BAR(); issue6(-192 * 2048, 304 * 2048);
      }
      wait_vm<24>(); BAR(); wait_vm<20>(); BAR(); wait_vm<16>(); BAR(); wait_vm<12>(); BAR(); wait_vm<8>(); BAR(); wait_vm<4>(); BAR(); wait_vm<0>(); BAR();
    };
    if (isA) run5(T_()); else run5(F__());
    asm volatile("s_waitcnt lgkmcnt(0)" ::: "memory");
    return;
  }

  const int fr = lane & 15, g = lane >> 4, q = fr >> 2, p4 = fr & 3, row = 4 * g + q;
  const int lsw = row * 128 + (((row >> 1) & 3) * 32) + p4 * 8;
  { const int mb = u1, hf = (b >> 3) & 1, kb0 = hf * 16 + wave * 2, cofs = (g & 1) * 16 + (g >> 1) * 8;
    u32x4 v[4];
    auto ldx = [&]() {
#pragma unroll
      for (int p = 0; p < 4; ++p) v[p] = *(const u32x4*)(x + (size_t)(mb * 16 + fr) * D_ + ((kb0 + (p & 1)) ^ ((p >> 1) * 16)) * 32 + cofs);
    };
    LDS_PTR(f32x2) sred = (LDS_PTR(f32x2))(smem + 4096);
    auto up2 = [](unsigned u) { return (f32x2){lo2f(u), hi2f(u)}; };
    auto stats = [&]() {
      f32x2 s2 = {0.f, 0.f}, q2 = {0.f, 0.f};
#pragma unroll
      for (int k = 0; k < 16; ++k) s2 += up2(v[k >> 2][k & 3]);
      const float mw = xsum16_32(s2[0] + s2[1]) * (1.f / 128.f);
#pragma unroll
      for (int k = 0; k < 16; ++k) { const f32x2 d = up2(v[k >> 2][k & 3]) - mw; q2 += d * d; }
      const float qw = xsum16_32(q2[0] + q2[1]);
      if (lane < 16) sred[wave * 16 + fr] = (f32x2){mw, qw};
      wait_lgkm0();
    };
    auto rest = [&]() {
    STAMP_AT(1 );
    float mean = 0.f, m2 = 0.f; f32x2 sw[8];
#pragma unroll
    for (int w = 0; w < 8; ++w) { sw[w] = sred[w * 16 + fr]; mean += sw[w][0]; m2 += sw[w][1]; }
    mean *= 0.125f;
#pragma unroll
    for (int w = 0; w < 8; ++w) { const float d = sw[w][0] - mean; m2 += 128.f * d * d; }
    const float rstd = ln_rstd(m2 * (1.f / 1024.f));
    mean = rbf(mean);
    unsigned nv[8];
#pragma unroll
    for (int k = 0; k < 8; ++k) {
      const f32x2 d = up2(v[k >> 2][k & 3]) - mean;
      const f32x2 n = up2(pk2(d[0], d[1])) * rstd;
      nv[k] = pk2(n[0], n[1]);
      asm volatile("" : "+v"(nv[k]));
    }
    SCHED0(); BAR(); SCHED0();
    STAMP_AT(2 );
    float mp[8];
#pragma unroll
    for (int i = 0; i < 8; ++i) mp[i] = __uint_as_float(__hip_atomic_load((unsigned*)(ws + WS_MOD) + hf * 512 + tid + i * 1024, __ATOMIC_RELAXED, __HIP_MEMORY_SCOPE_AGENT));
    *(LDS_PTR(unsigned))(smem + tid * 4) = pk2((mp[0] + mp[2]) + (mp[4] + mp[6]), 1.f + rbf((mp[1] + mp[3]) + (mp[5] + mp[7])));
    wait_lgkm0();
#pragma unroll
    for (int p = 0; p < 2; ++p) {
      const LDS_PTR(u32x4) tp = (const LDS_PTR(u32x4))(smem + ((wave * 2 + p) * 32 + cofs) * 4);
      const u32x4 t0 = tp[0], t1 = tp[1];
      unsigned o[4];
#pragma unroll
      for (int j = 0; j < 4; ++j) {
        const unsigned ta = j < 2 ? t0[2 * j] : t1[2 * j - 4], tb = j < 2 ? t0[2 * j + 1] : t1[2 * j - 3];
        const unsigned u = pk2(lo2f(nv[p * 4 + j]) * hi2f(ta), hi2f(nv[p * 4 + j]) * hi2f(tb));
        o[j] = pk2(lo2f(u) + lo2f(ta), hi2f(u) + lo2f(tb));
      }
      auto s0 = __builtin_amdgcn_permlane16_swap(o[0], o[2], false, false);
      auto s1 = __builtin_amdgcn_permlane16_swap(o[1], o[3], false, false);
      st16<true>(ws + WS_H, ((kb0 + p) * 128 + mb) * 1024 + lane * 16, (u32x4){s0[0], s1[0], s0[1], s1[1]});
    }
    STAMP_AT(3 );
    wait_vm<0>();
    STAMP_AT(4 );
    BAR();
    STAMP_AT(5 );
    };
    if (hf) {
      const int mrow = (u1 & 3) * 256 + (tid >> 3) * 4;
      const u32x2 c2 = *(const u32x2*)(cnd + mrow);
      u32x4 mw[4];
#pragma unroll
      for (int i = 0; i < 4; ++i) mw[i] = __builtin_nontemporal_load((const u32x4*)(wmod + (size_t)(mrow + i) * 6144 + (u1 >> 2) * 64 + (lane & 7) * 8));
      const unsigned mc[2] = {c2[0], c2[1]};
      *(LDS_PTR(f32x2))(smem + 8192 + (wave * 64 + (lane & 7) * 8 + (lane >> 4) * 2) * 4) = slab_sum<4>(mc, mw);
      wait_lgkm0();
      BAR();
      __builtin_amdgcn_s_sleep(12);
      ldx();
      stats();
      BAR();
      rest();
    } else {
      ldx();
      const int mrow = (u1 & 1) * 512 + (tid >> 3) * 8, mcol = (u1 >> 1) * 64 + (lane & 7) * 8;
      const u32x4 c4 = *(const u32x4*)(cnd + mrow);
      SCHED0();
      u32x4 mw[8];
#pragma unroll
      for (int i = 0; i < 8; ++i) mw[i] = __builtin_nontemporal_load((const u32x4*)(wmod + (size_t)(mrow + i) * 6144 + 2048 + mcol));
      SCHED0(); BAR(); SCHED0();
      stats();
      SCHED0(); BAR(); SCHED0();
      rest();
      const unsigned mc[4] = {c4[0], c4[1], c4[2], c4[3]};
      const f32x2 ps = slab_sum<8>(mc, mw);
      if (!(lane & 8))
        __hip_atomic_store((unsigned long long*)(ws + WS_MOD2) + (((u1 & 1) * 8 + wave) * 4096 + mcol + (lane >> 4) * 2) / 2,
                           ((unsigned long long)__float_as_uint(ps[1]) << 32) | __float_as_uint(ps[0]), __ATOMIC_RELAXED, __HIP_MEMORY_SCOPE_AGENT);
    }
  }
  { const int wm = wave >> 2, wn = wave & 3;
    const int lbw = K3_B + (lsw ^ (wn * 32));
    const int o1 = wm ? 16384 : 8192, o2 = wm ? 8192 : 16384;
    f32x4 acc[4][3], acch;
#pragma unroll
    for (int i = 0; i < 4; ++i)
#pragma unroll
      for (int j = 0; j < 3; ++j) { acc[i][j] = (f32x4){0.f, 0.f, 0.f, 0.f}; asm volatile("" : "+v"(acc[i][j])); }
    acch = (f32x4){0.f, 0.f, 0.f, 0.f};
    float bias[4];
    { const int kbase = tm3 * 128 + wave * 16 - 16;
#pragma unroll
      for (int t = 0; t < 4; ++t) {
        const int kk = (t >> 1) * 16 + 4 * g + (t & 1) * 2 + (fr & 1), df = 16 + fr - kk;
        bias[t] = (df >= 0 && df <= 14 && kbase + kk >= 0) ? 0.f : -__builtin_inff();
        asm volatile("" : "+v"(bias[t]));
      } }
    struct Fr { bf16x8 a[5]; bf16x4 w[3][2]; };
    const LDS_PTR(char) pw0 = smem + lbw; const LDS_PTR(char) pw1 = smem + lbw + o1; const LDS_PTR(char) pw2 = smem + lbw + o2;
    const LDS_PTR(char) pa = smem + 1024 + wm * 4096 + lane * 16; const LDS_PTR(char) ph = smem + lane * 16;
    auto rd1 = [&](Fr& f, int u, int r) {
      if (r < 2) f.w[0][r] = ld_tr4(pw0 + u * 4096 + r * 2048);
      else if (r < 6) f.a[r - 2] = *(const LDS_PTR(bf16x8))(pa + u * 9216 + (r - 2) * 1024);
      else if (r < 8) f.w[1][r - 6] = ld_tr4(pw1 + u * 4096 + (r - 6) * 2048);
      else if (r == 8) f.a[4] = *(const LDS_PTR(bf16x8))(ph + u * 9216);
      else f.w[2][r - 9] = ld_tr4(pw2 + u * 4096 + (r - 9) * 2048);
    };
    auto rdm = [&](Fr& f, int u, int m) {
      if (m == 0) { rd1(f, u, 0); rd1(f, u, 1); }
      if (m >= 1 && m <= 4) rd1(f, u, m + 1);
      if (m == 5) { rd1(f, u, 6); rd1(f, u, 7); }
      if (m == 6) rd1(f, u, 8);
      if (m == 7) { rd1(f, u, 9); rd1(f, u, 10); }
    };
    auto mm1 = [&](const Fr& f, int m) {
      const int j = m < 4 ? 0 : m < 9 ? 1 : 2, i = m < 4 ? m : m < 9 ? m - 4 : m - 9;
      const bf16x8 w = cat8(f.w[j][0], f.w[j][1]);
      if (i == 4) acch = __builtin_amdgcn_mfma_f32_16x16x32_bf16(w, f.a[4], acch, 0, 0, 0);
      else acc[i][j] = __builtin_amdgcn_mfma_f32_16x16x32_bf16(w, f.a[i], acc[i][j], 0, 0, 0);
    };
    Fr fA, fB;
#pragma unroll
    for (int i = 0; i < 5; ++i) { fB.a[i] = (bf16x8){0, 0, 0, 0, 0, 0, 0, 0}; asm volatile("" : "+v"(fB.a[i])); }
#pragma unroll
    for (int j = 0; j < 3; ++j) { fB.w[j][0] = fB.w[j][1] = (bf16x4){0, 0, 0, 0}; asm volatile("" : "+v"(fB.w[j][0]), "+v"(fB.w[j][1])); }
    int rpos = 0, dl = K3_STG;
    wait_vm<0>();
    BAR();
    STAMP_AT(6 );
    STAMP_AT(7 );
#pragma unroll 1
    for (int t = 0; t < 16; ++t) {
      SCHED0(); BAR(); SCHED0();
#pragma unroll
      for (int m = 0; m < 13; ++m) {
        mm1(fB, m); SCHED0();
        rdm(fA, 0, m);
        if (m == 8) { asm volatile("" : "+s"(rpos)); dl = rpos == 2 ? -2 * K3_STG : K3_STG; asm volatile("" : "+s"(dl)); }
        if (m == 9) { rpos = rpos == 2 ? 0 : rpos + 1; asm volatile("" : "+s"(rpos)); }
        SCHED0();
      }
      wait_lgkm0();
      SCHED0();
#pragma unroll
      for (int m = 0; m < 13; ++m) {
        mm1(fA, m); SCHED0();
        rdm(fB, 1, m);
        if (m == 8) { pw0 += dl; pa += dl; asm volatile("" : "+v"(pw0), "+v"(pa)); }
        if (m == 9) { pw1 += dl; ph += dl; asm volatile("" : "+v"(pw1), "+v"(ph)); }
        if (m == 10) { pw2 += dl; asm volatile("" : "+v"(pw2)); }
        SCHED0();
      }
      wait_lgkm0();
    }
    SCHED0(); BAR(); SCHED0();
    STAMP_AT(8 );
    { auto pk4 = [&](const f32x4& v) { u32x2 o; o[0] = pk2(v[0], v[1]); o[1] = pk2(v[2], v[3]); return o; };
      const int lq = wn * 512 + lane * 8, lv = 36864 + wn * 512 + (lane ^ ((g >> 1) * 8)) * 8, lk = 18432 + lq;
      const int l1 = wm ? lv : lk, l2 = wm ? lk : lv;
      auto wr1 = [&](int m) {
        const int j = m < 4 ? 0 : m < 9 ? 1 : 2, i = m < 4 ? m : m < 9 ? m - 4 : m - 9;
        if (i == 4) *(LDS_PTR(u32x2))(smem + l1) = pk4(acch);
        else *(LDS_PTR(u32x2))(smem + (wm * 4 + i + 1) * 2048 + (j == 0 ? lq : j == 1 ? l1 : l2)) = pk4(acc[i][j]);
      };
#pragma unroll
      for (int m = 0; m < 16; ++m) { if (m < 13) mm1(fB, m); SCHED0(); if (m >= 3) wr1(m - 3); SCHED0(); }
    }
    wait_lgkm0();
    BAR();
    STAMP_AT(9 );
    const LDS_PTR(char) eb = smem + wave * 2048;
    auto ld2 = [&](int o) { return cat8(*(const LDS_PTR(bf16x4))(eb + o), *(const LDS_PTR(bf16x4))(eb + o + 512)); };
    const int vo = 36864 + ((p4 * 16 + row) ^ ((p4 >> 1) * 8)) * 8;
    bf16x8 qf[2], k0[2], k1[2], v0[2], v1[2];
#pragma unroll
    for (int hd = 0; hd < 2; ++hd) { qf[hd] = ld2(2048 + hd * 1024 + lane * 8); k0[hd] = ld2(18432 + hd * 1024 + lane * 8); k1[hd] = ld2(18432 + 2048 + hd * 1024 + lane * 8); }
#pragma unroll
    for (int hd = 0; hd < 2; ++hd) {
      v0[hd] = cat8(ld_tr4(eb + vo + hd * 1024), ld_tr4(eb + vo + hd * 1024 + 2048));
      v1[hd] = cat8(ld_tr4(eb + vo + hd * 1024 + 512), ld_tr4(eb + vo + hd * 1024 + 512 + 2048));
    }
    const f32x4 z = {0.f, 0.f, 0.f, 0.f};
    const bool odd = fr & 1; const int psh = (fr & 1) * 16;
#pragma unroll
    for (int hd = 0; hd < 2; ++hd) {
      const f32x4 s0 = __builtin_amdgcn_mfma_f32_16x16x32_bf16(k0[hd], qf[hd], z, 0, 0, 0), s1 = __builtin_amdgcn_mfma_f32_16x16x32_bf16(k1[hd], qf[hd], z, 0, 0, 0);
      float l[4] = {odd ? s0[1] : s0[0], odd ? s0[3] : s0[2], odd ? s1[1] : s1[0], odd ? s1[3] : s1[2]};
#pragma unroll
      for (int t = 0; t < 4; t += 2) {
        unsigned u = pk2(l[t], l[t + 1]);
        u = pk2(lo2f(u) * 0.17677669529663687f, hi2f(u) * 0.17677669529663687f);
        l[t] = lo2f(u) + bias[t]; l[t + 1] = hi2f(u) + bias[t + 1];
      }
      const float mx = xmax16_32(fmaxf(fmaxf(l[0], l[1]), fmaxf(l[2], l[3])));
#pragma unroll
      for (int t = 0; t < 4; ++t) l[t] = __builtin_amdgcn_exp2f((l[t] - mx) * 1.4426950408889634f);
      const float inv = __builtin_amdgcn_rcpf(xsum16_32((l[0] + l[1]) + (l[2] + l[3])));
      u32x4 pp;
#pragma unroll
      for (int t = 0; t < 4; ++t) pp[t] = pk2(l[t] * inv, 0.f) << psh;
      const bf16x8 pf = __builtin_bit_cast(bf16x8, pp);
      const f32x4 oa = __builtin_amdgcn_mfma_f32_16x16x32_bf16(v0[hd], pf, z, 0, 0, 0), ob = __builtin_amdgcn_mfma_f32_16x16x32_bf16(v1[hd], pf, z, 0, 0, 0);
      u32x4 ov; ov[0] = pk2(oa[0], oa[1]); ov[1] = pk2(oa[2], oa[3]); ov[2] = pk2(ob[0], ob[1]); ov[3] = pk2(ob[2], ob[3]);
      st16<true>(ws + WS_O, ((tn3 * 2 + hd) * 128 + tm3 * 8 + wave) * 1024 + lane * 16, ov);
    }
    STAMP_AT(10 );
    wait_vm<0>();
    STAMP_AT(11 );
    BAR();
    STAMP_AT(12 );
  }
  { const int wm = wave & 3, ks = wave >> 2, mb = tm4 * 8 + wm * 2 + ks;
    constexpr int R = 6;
    u32x2 rv[4];
#pragma unroll
    for (int j = 0; j < 4; ++j) rv[j] = *(const u32x2*)(x + (size_t)(mb * 16 + fr) * D_ + tn4 * 64 + j * 16 + 4 * g);
    const unsigned va0 = (ks * 128 + mb) * 1024 + lane * 16, va1 = va0 ^ 1024;
    bf16x8 fa[R][2], fw[2][4];
    constexpr int rot = 3;
    auto issueA = [&](int t) {
      const char* pa = ws + WS_O + (size_t)((((t >> 2) + rot) & 3) * 4 + (t & 3)) * 262144;
      fa[t % R][0] = __builtin_bit_cast(bf16x8, ld16<true>(pa, va0));
      fa[t % R][1] = __builtin_bit_cast(bf16x8, ld16<true>(pa, va1));
    };
    unsigned aw[4];
    auto rdw = [&](int t, int j) {
      const int o = (t & 3) * 8192;
      fw[t & 1][j] = cat8(ld_tr4(smem + aw[j] + o), ld_tr4(smem + aw[j] + o + 2048));
    };
    auto stepw = [&](int t) {
      const int gi = ((t >> 2) + rot) & 3, base = gi < 3 ? 57344 + gi * 32768 : 0;
#pragma unroll
      for (int j = 0; j < 4; ++j) { aw[j] = base + ks * 4096 + (lsw ^ (j * 32)); asm volatile("" : "+v"(aw[j])); }
    };
    f32x4 acc[2][4];
#pragma unroll
    for (int i = 0; i < 2; ++i)
#pragma unroll
      for (int j = 0; j < 4; ++j) acc[i][j] = (f32x4){0.f, 0.f, 0.f, 0.f};
    stepw(0);
    wait_lgkm0();
    SCHED0(); BAR(); SCHED0();
    STAMP_AT(13 );
#pragma unroll
    for (int t = 0; t < R; ++t) issueA(t);
#pragma unroll
    for (int j = 0; j < 4; ++j) rdw(0, j);
    SCHED0();
#pragma unroll
    for (int t = 0; t < 16; ++t) {
      if (t < 15 && ((t + 1) & 3) == 0) stepw(t + 1);
#pragma unroll
      for (int j = 0; j < 4; ++j) {
        if (t < 15) rdw(t + 1, j);
        SCHED0();
#pragma unroll
        for (int i = 0; i < 2; ++i) acc[i][j] = __builtin_amdgcn_mfma_f32_16x16x32_bf16(fw[t & 1][j], fa[t % R][i], acc[i][j], 0, 0, 0);
        SCHED0();
      }
      if (t + R < 16) issueA(t + R);
      if (t == 5) BAR();
      SCHED0();
    }
    STAMP_AT(14 );
    LDS_PTR(f32x4) red = (LDS_PTR(f32x4))(smem + K4_RED);
#pragma unroll
    for (int j = 0; j < 4; ++j) red[((wave ^ 4) * 4 + j) * 64 + lane] = acc[1][j];
    wait_lgkm0();
    BAR();
    STAMP_AT(15 );
    const LDS_PTR(float) vec = (const LDS_PTR(float))(smem + LDS_VEC);
    unsigned o[8];
#pragma unroll
    for (int j = 0; j < 4; ++j) {
      const f32x4 y = (acc[0][j] + red[(wave * 4 + j) * 64 + lane]) * *(const LDS_PTR(f32x4))(vec + j * 16 + 4 * g);
      o[2 * j] = pk2(lo2f(rv[j][0]) + y[0], hi2f(rv[j][0]) + y[1]);
      o[2 * j + 1] = pk2(lo2f(rv[j][1]) + y[2], hi2f(rv[j][1]) + y[3]);
    }
    float f[16], s = 0.f;
#pragma unroll
    for (int j = 0; j < 8; ++j) { f[2 * j] = lo2f(o[j]); f[2 * j + 1] = hi2f(o[j]); s += f[2 * j] + f[2 * j + 1]; }
    const float mt = xsum16_32(s) * (1.f / 64.f);
    float qq = 0.f;
#pragma unroll
    for (int j = 0; j < 16; ++j) { const float d = f[j] - mt; qq += d * d; }
    qq = xsum16_32(qq);
    unsigned long long* slot = (unsigned long long*)(ws + WS_XCH) + (size_t)mb * 256 + fr;
    if (lane < 32) {
      const unsigned qb = qq == qq ? __float_as_uint(qq) : 0x7fc00000u;
      __hip_atomic_store(slot + g * 32768 + tn4 * 16, ((unsigned long long)qb << 32) | __float_as_uint(mt), __ATOMIC_RELAXED, __HIP_MEMORY_SCOPE_AGENT);
    }
    st16<true>(ws + WS_X1, (mb * 16 + tn4) * 2048 + lane * 16, (u32x4){o[0], o[1], o[2], o[3]});
    st16<true>(ws + WS_X1, (mb * 16 + tn4) * 2048 + 1024 + lane * 16, (u32x4){o[4], o[5], o[6], o[7]});
    STAMP_AT(16 );
    f32x4 ss[4], gg[4];
#pragma unroll
    for (int j = 0; j < 4; ++j) {
      ss[j] = *(const LDS_PTR(f32x4))(vec + 64 + j * 16 + 4 * g); gg[j] = *(const LDS_PTR(f32x4))(vec + 128 + j * 16 + 4 * g);
      asm volatile("" : "+v"(ss[j]), "+v"(gg[j]));
    }
    unsigned long long pv[4];
    __builtin_amdgcn_s_sleep(22);
    for (unsigned spins = 0;; ++spins) {
      bool ok = true;
#pragma unroll
      for (int i = 0; i < 4; ++i) { pv[i] = __hip_atomic_load(slot + (xc & 1) * 32768 + (4 * g + i) * 16, __ATOMIC_RELAXED, __HIP_MEMORY_SCOPE_AGENT); ok &= (unsigned)(pv[i] >> 32) != 0xffffffffu; }
      if (__builtin_amdgcn_ballot_w64(!ok) == 0 || spins > SPIN_CAP) break;
      __builtin_amdgcn_s_sleep(4);
    }
    STAMP_AT(17 );
    float ms = 0.f, m2 = 0.f;
#pragma unroll
    for (int i = 0; i < 4; ++i) { ms += __uint_as_float((unsigned)pv[i]); m2 += __uint_as_float((unsigned)(pv[i] >> 32)); }
    float mean = xsum16_32(ms) * (1.f / 16.f);
#pragma unroll
    for (int i = 0; i < 4; ++i) { const float d = __uint_as_float((unsigned)pv[i]) - mean; m2 += 64.f * d * d; }
    const float rstd = ln_rstd(xsum16_32(m2) * (1.f / 1024.f));
    mean = rbf(mean);
#pragma unroll
    for (int p = 0; p < 2; ++p) {
      u32x4 v;
#pragma unroll
      for (int j = 0; j < 4; ++j) {
        const int jb = 2 * p + (j >> 1), e = (j & 1) * 2;
        v[j] = pk2((f[8 * p + 2 * j] - mean) * rstd * gg[jb][e] + ss[jb][e], (f[8 * p + 2 * j + 1] - mean) * rstd * gg[jb][e + 1] + ss[jb][e + 1]);
      }
      st16<true>(ws + WS_H2, ((tn4 * 2 + p) * 128 + mb) * 1024 + lane * 16, v);
    }
    STAMP_AT(18 );
    wait_vm<0>();
    BAR();
    STAMP_AT(19 );

  }
  const int grp = wave >> 2;
  int kpf = grp ? 99 : 0;
  const char* tp = (const char*)w2 + ((b >> 5 ? (b >> 5) * 448 + 240 : 3456) + (b & 31)) * 2048 + (wave & 3) * 512;
  auto idle = [&]() {
    if (kpf < (b >> 5 ? 3 : 4) && __builtin_amdgcn_s_getreg((3 << 11) | (8 << 6) | 7) <= 6) { stouch4(tp); tp += 65536; ++kpf; }
    SCHED0(); BAR(); SCHED0();
  };
  { const int wm = wave & 1, wn = (wave >> 1) & 1, odd = tn5 & 1, x3 = odd && !grp ? 3 : 0;
    const LDS_PTR(char) sb = smem + 2 * K5_STG;
    const LDS_PTR(char) a0 = sb + K5_G + (lsw ^ (((2 * wn) ^ x3) * 32));
    const LDS_PTR(char) a1 = sb + K5_G + (lsw ^ (((2 * wn + 1) ^ x3) * 32));
    const LDS_PTR(char) aM = sb + 4096 + (p4 < 2 ? K5_G : K5_U) + ((lsw & ~16) ^ (((grp ? 3 : 1) ^ (odd * 3)) * 32 + wn * 16));
    const LDS_PTR(char) aA = sb + wm * 4096 + lane * 16;
    f32x32 c0, c1, c2; f32x16 c3;
    asm volatile(".irp r,0,8,16,24,32,40,48,56,64,72,80,88,96,104\n\tv_mov_b64 v[\\r:\\r+1], 0\n\tv_mov_b64 v[\\r+2:\\r+3], 0\n\tv_mov_b64 v[\\r+4:\\r+5], 0\n\tv_mov_b64 v[\\r+6:\\r+7], 0\n\t.endr"
                 : "={v[0:31]}"(c0), "={v[32:63]}"(c1), "={v[64:95]}"(c2), "={v[96:111]}"(c3)::"memory");
    int wrap = -(K5_NS - 1) * K5_STG, dl = K5_STG, n = grp ? 31 : 7;
    unsigned mask = wrap_mask(3, K5_NS);
    const LDS_PTR(char) aH = smem + 4 * K5_STG + K5_G + 4096 + row * 64 + ((wn ^ (g & 1)) * 32) + p4 * 8;
    if (grp) { a0 += 2 * K5_STG; a1 += 2 * K5_STG; }
    asm volatile("" : "+s"(wrap), "+v"(a0), "+v"(a1), "+v"(aM), "+v"(aA), "+v"(aH));
    if (!grp) __builtin_amdgcn_s_setprio(2);
    BAR();
    STAMP_AT(20 );
    wait_lgkm0();
    BAR();
    STAMP_G(21, 26);
    asm volatile("s_cmp_eq_u32 %[n], 7\n\ts_cbranch_scc0 2f\n\t"
                 PRE5 RW5(4, 0, a0, 4096) RW5(4, 1, a0, 6144) RW5(5, 0, a0, 12304) RW5(5, 1, a0, 14352) RW5(6, 0, aM, 0) LG(9)
                 ".p2align 5\n1:\n\t" TRIP5A("") TRIP5A("") TRIP5A("") TRIP5A("s_sub_u32 %[n], %[n], 1\n\t") "s_cbranch_scc0 1b\n\ts_branch 4f\n"
                 "2:\n\t" RW5(6, 0, aM, 0) RW5(6, 1, aM, 2048) RA5(0) RA5(1) RA5(2) RA5(3) ST5(aM) ST5(aA) SEL5A SEL5B "s_sub_u32 %[n], %[n], 1\n\t"
                 LG(0) HELP_HI M5(0, 6) M5(1, 6) M5(2, 6) M5(3, 6) HELP_LO "s_barrier\n\ts_cbranch_scc0 2b\n\t"
                 "s_setprio 1\n\tv_mov_b32 %[aM], %[aH]\n\ts_mov_b32 %[n], 7\n\ts_mov_b32 %[mask], %[mb]\n\t"
                 PRE5 RW5(4, 0, aM, 0) RW5(4, 1, aM, 1024) RW5(5, 0, aM, 8208) LG(7)
                 ".p2align 5\n3:\n\t" TRIP5B("") TRIP5B("s_setprio 2\n\t") TRIP5B("") TRIP5B("s_sub_u32 %[n], %[n], 1\n\t") "s_cbranch_scc0 3b\n"
                 "4:\n\t" LG(0) "s_nop 7"
                 : ACC5, [a0] "+v"(a0), [a1] "+v"(a1), [aM] "+v"(aM), [aA] "+v"(aA), [dl] "+s"(dl), [mask] "+s"(mask), [n] "+s"(n)
                 : [wrap] "s"(wrap), [aH] "v"(aH), [mb] "n"(wrap_mask(35, K5_NS)) : CLOB5);
    f32x4 acc[4][7];
#pragma unroll
    for (int e = 0; e < 112; ++e) acc[e / 28][(e % 28) / 4][e & 3] = e < 32 ? c0[e & 31] : e < 64 ? c1[e & 31] : e < 96 ? c2[e & 31] : c3[e & 15];
    __builtin_amdgcn_s_setprio(EPI_PRIO);
    STAMP_G(22, 27);
    BAR();
    auto swi4 = [](const f32x4& gv, const f32x4& uv, unsigned& o0, unsigned& o1) {
      float t0, t1, t2, t3, p0, p1, p2, p3;
      asm volatile("v_mul_f32 %2, 0xbfb8aa3b, %10\n\tv_mul_f32 %3, 0xbfb8aa3b, %11\n\tv_mul_f32 %4, 0xbfb8aa3b, %12\n\tv_mul_f32 %5, 0xbfb8aa3b, %13\n\t"
                   "v_exp_f32 %2, %2\n\tv_exp_f32 %3, %3\n\tv_exp_f32 %4, %4\n\tv_exp_f32 %5, %5\n\t"
                   "v_mul_f32 %6, %10, %14\n\tv_mul_f32 %7, %11, %15\n\tv_mul_f32 %8, %12, %16\n\tv_mul_f32 %9, %13, %17\n\t"
                   "v_add_f32 %2, 1.0, %2\n\tv_add_f32 %3, 1.0, %3\n\tv_add_f32 %4, 1.0, %4\n\tv_add_f32 %5, 1.0, %5\n\t"
                   "v_rcp_f32 %2, %2\n\tv_rcp_f32 %3, %3\n\tv_rcp_f32 %4, %4\n\tv_rcp_f32 %5, %5\n\t"
                   "v_mul_f32 %6, %6, %2\n\tv_mul_f32 %7, %7, %3\n\tv_mul_f32 %8, %8, %4\n\tv_mul_f32 %9, %9, %5\n\t"
                   "v_cvt_pk_bf16_f32 %0, %6, %7\n\tv_cvt_pk_bf16_f32 %1, %8, %9\n\ts_nop 1"
                   : "=&v"(o0), "=&v"(o1), "=&v"(t0), "=&v"(t1), "=&v"(t2), "=&v"(t3), "=&v"(p0), "=&v"(p1), "=&v"(p2), "=&v"(p3)
                   : "v"(gv[0]), "v"(gv[1]), "v"(gv[2]), "v"(gv[3]), "v"(uv[0]), "v"(uv[1]), "v"(uv[2]), "v"(uv[3]));
    };
    const int lz = lane_id();
    const int gz = lz >> 4, fz = lz & 15;
    const int qb = kh6 * 224 + ((tn5 + 1) & 7) * 14, ql = kh6 * 224 + 176 + ((tn5 >> 1) & 3) * 12;
    const int q0 = grp ? kh6 * 224 + 112 + (tn5 & 7) * 8 + 4 * wn : qb + (odd ? 12 - 4 * wn : 4 * wn), q1 = q0 + (x3 ? -2 : 2);
    const int q2 = grp ? ql + 4 * odd + 2 * wn : qb + (odd ? 4 - 4 * wn : 8 + 4 * wn), qm = wn + (grp ? ql + 8 + 2 * odd : qb + (odd ? 2 : 10));
    const int e0 = ((gz & 1) ? q1 : q0) + (gz >> 1), e1 = (gz & 1) ? qm : q2 + (gz >> 1);
    const unsigned ho0 = (e0 >> 2) * 131072 + (e0 & 3) * 256 + fz * 16, ho1 = (e1 >> 2) * 131072 + (e1 & 3) * 256 + fz * 16;
    char* hb = ws + WS_HH + (tm5 * 8 + wm * 4) * 1024;
    const int c5 = C5 + (tm5 * 2 + kh6) * 3;
    auto tr16 = [](const unsigned (&o)[4]) {
      auto s0 = __builtin_amdgcn_permlane16_swap(o[0], o[2], false, false);
      auto s1 = __builtin_amdgcn_permlane16_swap(o[1], o[3], false, false);
      u32x4 v = {s0[0], s1[0], s0[1], s1[1]}; asm volatile("" : "+v"(v)); return v;
    };
#define SEG() do { SCHED0(); BAR(); SCHED0(); } while (0)
#pragma unroll
    for (int i = 0; i < 4; i += 2) {
      unsigned o[4], r[4];
      swi4(acc[i][0], acc[i][1], o[0], o[1]); SEG();
      swi4(acc[i][2], acc[i][3], o[2], o[3]); SEG();
      const u32x4 v = tr16(o);
      swi4(acc[i + 1][0], acc[i + 1][1], r[0], r[1]); SEG();
      swi4(acc[i + 1][2], acc[i + 1][3], r[2], r[3]);
      st16<true>(hb, ho0 + i * 1024, v); st16<true>(hb, ho0 + i * 1024 + 1024, tr16(r)); SEG();
    }
#pragma unroll
    for (int i = 0; i < 4; i += 2) {
      unsigned o[4], r[4];
      swi4(acc[i][4], acc[i][5], o[0], o[1]); SEG();
      swi4(acc[i + 1][4], acc[i + 1][5], r[0], r[1]);
      if (i == 0) wait_vm<0>();
      SEG();
      if (i == 0 && wave == 4) signal(cnt, c5 + 1, lz);
      f32x4 gv, uv;
#pragma unroll
      for (int e = 0; e < 4; ++e) {
        auto w = __builtin_amdgcn_permlane32_swap(__float_as_uint(acc[i + (e >> 1)][6][e & 1]), __float_as_uint(acc[i + (e >> 1)][6][2 + (e & 1)]), false, false);
        gv[e] = __uint_as_float(w[0]); uv[e] = __uint_as_float(w[1]);
      }
      unsigned m0, m1;
      swi4(gv, uv, m0, m1);
      auto x0 = __builtin_amdgcn_permlane32_swap(m0, m0, false, false); o[2] = x0[0]; o[3] = x0[1];
      auto x1 = __builtin_amdgcn_permlane32_swap(m1, m1, false, false); r[2] = x1[0]; r[3] = x1[1];
      const u32x4 v = tr16(o), w = tr16(r);
      if (gz != 3) { st16<true>(hb, ho1 + i * 1024, v); st16<true>(hb, ho1 + i * 1024 + 1024, w); }
      SEG();
    }
    STAMP_G(23, 28);
    idle(); idle();
    wait_vm<0>();
    BAR();
    STAMP_G(24, 29);
    if (wave > 4) return;
    if ((wave & 3) == 0) signal(cnt, c5 + 2 * grp, lz);
    auto hold = [&](int part) {
      unsigned seen = 8;
      const unsigned* pc = cnt + ((c5 + part) * 8 + tn6) * 32;
      if ((wave & 3) == 0) asm volatile("global_load_dword %0, %1, %2 sc1" : "=v"(seen) : "v"(0), "s"(pc) : "memory");
      BAR(); BAR();
      asm volatile("s_waitcnt vmcnt(0)" : "+v"(seen)::"memory");
      if (__builtin_amdgcn_readfirstlane(seen) < 8) poll<0>(pc, 8);
    };
    if (grp) {
      hold(1);
      STAMP_G(25, 30);
#pragma unroll 1
      for (int t = 21; t < 35; ++t) BAR();
      hold(2);
      return;
    }
#pragma unroll 1
    for (int t = 51; t < 57; ++t) idle();
    hold(0);
    STAMP_G(25, 30);
  }
  { const int lane = lane_id(), fr = lane & 15, g = lane >> 4, p4 = fr & 3, row = 4 * g + (fr >> 2);
    const int wm = wave & 1, wn = wave >> 1;
    const int lw0 = 8192 + row * 128 + (((row >> 1) & 3) * 32) + p4 * 8;
    const int mb0 = tm6 * 8 + wm * 4, t64 = tn6 * 2 + kh6;
    idle();
    u32x4 rv[4];
#pragma unroll
    for (int i = 0; i < 4; ++i) rv[i] = ld16<true>(ws + WS_X1, ((mb0 + i) * 16 + t64) * 2048 + wn * 1024 + lane * 16);
    f32x4 acc[4][4];
#pragma unroll
    for (int i = 0; i < 4; ++i)
#pragma unroll
      for (int j = 0; j < 4; ++j) { acc[i][j] = (f32x4){0.f, 0.f, 0.f, 0.f}; asm volatile("" : "+v"(acc[i][j])); }
    BAR(); BAR(); BAR();
#pragma unroll
    for (int i = 0; i < 4; ++i) asm volatile("" : "+v"(rv[i]));
    wait_lgkm0();
    BAR();
    struct Fr { bf16x8 a[4]; bf16x4 w[4][2]; };
    const LDS_PTR(char) aw0 = smem + (lw0 ^ (wn * 64)); const LDS_PTR(char) aw1 = smem + (lw0 ^ (wn * 64 + 32));
    const LDS_PTR(char) aa = smem + wm * 4096 + lane * 16;
    auto rd1 = [&](Fr& f, int r) {
      if (r < 2) f.w[0][r] = ld_tr4(aw0 + r * 2048);
      else if (r < 4) f.a[r - 2] = *(const LDS_PTR(bf16x8))(aa + (r - 2) * 1024);
      else if (r < 10) { const int j = (r - 2) >> 1; f.w[j][r & 1] = ld_tr4(((j & 1) ? aw1 : aw0) + (j >> 1) * 4096 + (r & 1) * 2048); }
      else f.a[r - 8] = *(const LDS_PTR(bf16x8))(aa + (r - 8) * 1024);
    };
    int dl, wrap = -(K6_NS - 1) * K6_STG;
    unsigned mask = wrap_mask(1, K6_NS);
    Fr fA, fB;
    asm volatile("" : "+s"(wrap), "+v"(aw0), "+v"(aw1), "+v"(aa));
    __builtin_amdgcn_s_setprio(1);
    BAR();
    STAMP_AT(31 );
#pragma unroll
    for (int r = 0; r < 12; ++r) { SCHED0(); rd1(fA, r); }
    SCHED0();
    aw0 += K6_STG; aw1 += K6_STG; aa += K6_STG;
    wait_lgkm<6>();
    BAR();
    auto trip = [&](const Fr& cur, Fr& nxt) {
      SCHED0();
#pragma unroll
      for (int m = 0; m < 16; ++m) {
        const int j = (m & 7) >> 1, i = (m >> 3) * 2 + ((m ^ j) & 1);
        if (m == 2) mfma_sel<K6_STG>(acc[i][j], cat8(cur.w[j][0], cur.w[j][1]), cur.a[i], mask, dl, wrap);
        else mfma_acc(acc[i][j], cat8(cur.w[j][0], cur.w[j][1]), cur.a[i]);
        SCHED0();
        if (m < 12) { rd1(nxt, m); SCHED0(); }
        if (m == 3) wait_lgkm<4>();
        if (m == 12) { aw0 += dl; aw1 += dl; asm volatile("" : "+v"(aw0), "+v"(aw1)); }
        if (m == 13) { aa += dl; asm volatile("" : "+v"(aa)); }
        if (m == 14) wait_lgkm<6>();
        if (m == 15) BAR();
        SCHED0();
      }
    };
#pragma unroll 1
    for (int h = 0; h < 2; ++h) {
#pragma unroll 1
      for (int t = 0; t < 7; ++t) { trip(fA, fB); trip(fB, fA); trip(fA, fB); trip(fB, fA); }
      mask = wrap_mask(29, K6_NS);
    }
    mfma_done(acc);
    STAMP_AT(35 );
    const int slot = wm * 2 + wn, pb = (b ^ 4) * 4 + slot;
    unsigned* flags = (unsigned*)(ws + WS_FLG);
    { char* pp = ws + WS_PART + (size_t)(b * 4 + slot) * 4096;
#pragma unroll
      for (int i = 0; i < 4; ++i)
        st16<true>(pp, i * 1024 + lane * 16, (u32x4){pk2(acc[i][0][0], acc[i][0][1]), pk2(acc[i][0][2], acc[i][0][3]), pk2(acc[i][1][0], acc[i][1][1]), pk2(acc[i][1][2], acc[i][1][3])});
      wait_vm<0>();
      STAMP_AT(36 );
      if (lane == 0) __hip_atomic_store(flags + (b * 4 + slot) * 16, 1u, __ATOMIC_RELAXED, __HIP_MEMORY_SCOPE_AGENT);
    }
    { const LDS_PTR(float) vec = (const LDS_PTR(float))(smem + LDS_VEC) + 192 + wn * 32 + 4 * g;
      const f32x4 gv0 = *(const LDS_PTR(f32x4))vec, gv1 = *(const LDS_PTR(f32x4))(vec + 16);
      poll<POLL6>(flags + pb * 16, 1);
      STAMP_AT(37 );
      const char* pp = ws + WS_PART + (size_t)pb * 4096;
      u32x4 z[4];
#pragma unroll
      for (int i = 0; i < 4; ++i) z[i] = ld16<true>(pp, i * 1024 + lane * 16);
#pragma unroll
      for (int i = 0; i < 4; ++i) {
        const f32x4 v0 = (acc[i][2] + (f32x4){lo2f(z[i][0]), hi2f(z[i][0]), lo2f(z[i][1]), hi2f(z[i][1])}) * gv0;
        const f32x4 v1 = (acc[i][3] + (f32x4){lo2f(z[i][2]), hi2f(z[i][2]), lo2f(z[i][3]), hi2f(z[i][3])}) * gv1;
        const unsigned o0 = pk2(lo2f(rv[i][0]) + v0[0], hi2f(rv[i][0]) + v0[1]), o1 = pk2(lo2f(rv[i][1]) + v0[2], hi2f(rv[i][1]) + v0[3]);
        const unsigned o2 = pk2(lo2f(rv[i][2]) + v1[0], hi2f(rv[i][2]) + v1[1]), o3 = pk2(lo2f(rv[i][3]) + v1[2], hi2f(rv[i][3]) + v1[3]);
        auto s0 = __builtin_amdgcn_permlane16_swap(o0, o2, false, false);
        auto s1 = __builtin_amdgcn_permlane16_swap(o1, o3, false, false);
        st16<false>(out, (((mb0 + i) * 16 + fr) * D_ + t64 * 64 + wn * 32 + (g & 1) * 16 + (g >> 1) * 8) * 2, (u32x4){s0[0], s1[0], s0[1], s1[1]});
      }
    }
    STAMP_AT(38 );
  }
}

extern "C" void kernel_launch(void* const* d_in, const int* in_sizes, int n_in, void* d_out, int out_size, void* d_ws,
                              size_t ws_size, hipStream_t stream) {
  const u16* x = (const u16*)d_in[0];
  const u16* wqkv = (const u16*)d_in[1];
  const u16* wo = (const u16*)d_in[2];
  const u16* w1 = (const u16*)d_in[4];
  const u16* w3 = (const u16*)d_in[5];
  const u16* w2 = (const u16*)d_in[6];
  const u16* c = (const u16*)d_in[7];
  const u16* wmod = (const u16*)d_in[8];
  (void)hipFuncSetAttribute((const void*)k2_block, hipFuncAttributeMaxDynamicSharedMemorySize, MEGA_LDS);
  char* ws = (char*)d_ws;
  k2_block<<<256, 768, MEGA_LDS, stream>>>(x, wqkv, wo, w1, w3, w2, c, wmod, (u16*)d_out, ws);
}
```

```cpp
#include <hip/hip_runtime.h>
#include <stdint.h>
#include <type_traits>
#pragma clang diagnostic ignored "-Winline-asm"

typedef unsigned short u16;
typedef short bf16x8 __attribute__((ext_vector_type(8)));
typedef short bf16x4 __attribute__((ext_vector_type(4)));
typedef float f32x4 __attribute__((ext_vector_type(4)));
typedef float f32x2 __attribute__((ext_vector_type(2)));
typedef unsigned int u32x4 __attribute__((ext_vector_type(4)));
typedef unsigned int u32x2 __attribute__((ext_vector_type(2)));
#define LDS_PTR(T) __attribute__((address_space(3))) T*
#define BAR() do { asm volatile("" ::: "memory"); __builtin_amdgcn_s_barrier(); asm volatile("" ::: "memory"); } while (0)
#define SCHED0() __builtin_amdgcn_sched_barrier(0)
template <int N> using IC = std::integral_constant<int, N>;
typedef std::integral_constant<bool, true> T_;
typedef std::integral_constant<bool, false> F__;

constexpr int S_ = 2048, D_ = 1024, F_ = 3584;
#define STAT_DECL
#define STAT_WRAP(x) do { unsigned spin_ = 0; x; } while (0)
#define STAT_OUT(k)
#define STAMP_AT(i)
#define STAMP_G(ia, ib)
#define STAMP_L(i)

__device__ __forceinline__ float bf2f(u16 v) { return __uint_as_float(((unsigned)v) << 16); }
__device__ __forceinline__ unsigned pk2(float lo, float hi) {
  typedef __bf16 bf2 __attribute__((ext_vector_type(2)));
  f32x2 v = {lo, hi}; bf2 r = __builtin_convertvector(v, bf2); return __builtin_bit_cast(unsigned, r);
}
__device__ __forceinline__ float lo2f(unsigned u) { return __uint_as_float(u << 16); }
__device__ __forceinline__ float hi2f(unsigned u) { return __uint_as_float(u & 0xffff0000u); }
__device__ __forceinline__ float rbf(float v) { return lo2f(pk2(v, 0.f)); }
#define glds16(sbase, voff, lds_off, imm) \
  asm volatile("s_add_u32 m0, %2, %3\n\ts_nop 0\n\tglobal_load_lds_dwordx4 %0, %1" ::"v"(voff), "s"(sbase), "s"(lds_off), "n"(imm) : "memory", "scc")
#define glds16s(sbase, voff, lds_off, imm) \
  asm volatile("s_add_u32 m0, %2, %3\n\ts_nop 0\n\tglobal_load_lds_dwordx4 %0, %1 sc1" ::"v"(voff), "s"(sbase), "s"(lds_off), "n"(imm) : "memory", "scc")
#define GLDS4(SC) "s_mov_b32 m0, %6\n\ts_nop 0\n\tglobal_load_lds_dwordx4 %0, %4" SC "\n\tglobal_load_lds_dwordx4 %1, %5 offset:1024" SC \
                  "\n\tglobal_load_lds_dwordx4 %2, %4 offset:2048" SC "\n\tglobal_load_lds_dwordx4 %3, %5 offset:3072" SC
template <bool SC1> __device__ __forceinline__ void glds16x4(const void* s0, const void* s1, const unsigned (&v)[4], unsigned lds) {
  if (SC1) asm volatile(GLDS4(" sc1") ::"v"(v[0]), "v"(v[1]), "v"(v[2]), "v"(v[3]), "s"(s0), "s"(s1), "s"(lds) : "memory");
  else asm volatile(GLDS4("") ::"v"(v[0]), "v"(v[1]), "v"(v[2]), "v"(v[3]), "s"(s0), "s"(s1), "s"(lds) : "memory");
}
__device__ __forceinline__ void glds16x2(const void* s0, unsigned v0, unsigned v1, unsigned lds) {
  asm volatile("s_mov_b32 m0, %3\n\ts_nop 0\n\tglobal_load_lds_dwordx4 %0, %2\n\tglobal_load_lds_dwordx4 %1, %2 offset:1024" ::"v"(v0), "v"(v1), "s"(s0), "s"(lds) : "memory");
}
__device__ __forceinline__ void glds4(const void* sbase, unsigned voff, unsigned lds_off) {
  asm volatile("s_mov_b32 m0, %2\n\ts_nop 0\n\tglobal_load_lds_dword %0, %1" ::"v"(voff), "s"(sbase), "s"(lds_off) : "memory");
}
__device__ __forceinline__ void stouch4(const void* p) {
  asm volatile("s_load_dwordx2 s[100:101], %0, 0x3c\n\ts_load_dwordx2 s[100:101], %0, 0xbc\n\ts_load_dwordx2 s[100:101], %0, 0x13c\n\ts_load_dwordx2 s[100:101], %0, 0x1bc"
               ::"s"(p) : "memory", "s100", "s101");
}
template <bool SC1> __device__ __forceinline__ void st16(void* sbase, unsigned voff, u32x4 v) {
  __builtin_amdgcn_raw_buffer_store_b128(v, __builtin_amdgcn_make_buffer_rsrc(sbase, 0, 0x7fffffff, 0x00020000), voff, 0, SC1 ? 16 : 0);
}
template <bool SC1> __device__ __forceinline__ u32x4 ld16(const void* sbase, unsigned voff) {
  return __builtin_amdgcn_raw_buffer_load_b128(__builtin_amdgcn_make_buffer_rsrc((void*)sbase, 0, 0x7fffffff, 0x00020000), voff, 0, SC1 ? 16 : 0);
}
template <int N> __device__ __forceinline__ void wait_vm() { asm volatile("s_waitcnt vmcnt(%0)" ::"n"(N) : "memory"); }
__device__ __forceinline__ void wait_lgkm0() { asm volatile("" ::: "memory"); __builtin_amdgcn_s_waitcnt(0xC07F); asm volatile("" ::: "memory"); }
__device__ __forceinline__ bf16x4 ld_tr4(const LDS_PTR(char) p) { return __builtin_amdgcn_ds_read_tr16_b64_v4i16((LDS_PTR(bf16x4))p); }
__device__ __forceinline__ bf16x8 cat8(bf16x4 lo, bf16x4 hi) { return __builtin_shufflevector(lo, hi, 0, 1, 2, 3, 4, 5, 6, 7); }
__device__ __forceinline__ void mfma_acc(f32x4& acc, bf16x8 a, bf16x8 b) { asm volatile("v_mfma_f32_16x16x32_bf16 %0, %1, %2, %0" : "+v"(acc) : "v"(a), "v"(b)); }
template <int STEP> __device__ __forceinline__ void mfma_sel(f32x4& acc, bf16x8 a, bf16x8 b, unsigned& mask, int& dl, int wrap) {
  asm volatile("s_add_u32 %1, %1, %1\n\tv_mfma_f32_16x16x32_bf16 %0, %3, %4, %0\n\ts_cselect_b32 %2, %5, %6" : "+v"(acc), "+s"(mask), "=s"(dl) : "v"(a), "v"(b), "s"(wrap), "n"(STEP) : "scc");
}
constexpr unsigned wrap_mask(int first, int ns) { unsigned m = 0; for (int t = 0; t < 32; ++t) if ((first + t) % ns == ns - 1) m |= 0x80000000u >> t; return m; }
template <int N> __device__ __forceinline__ void wait_lgkm() { __builtin_amdgcn_s_waitcnt(0xC07F | (N << 8)); }
template <int R, int C> __device__ __forceinline__ void mfma_done(f32x4 (&acc)[R][C]) {
  asm volatile("s_nop 9");
#pragma unroll
  for (int i = 0; i < R; ++i)
#pragma unroll
    for (int j = 0; j < C; ++j) asm volatile("" : "+v"(acc[i][j]));
}
__device__ __forceinline__ int lane_id() { int l; asm volatile("v_mbcnt_lo_u32_b32 %0, -1, 0\n\tv_mbcnt_hi_u32_b32 %0, -1, %0" : "=v"(l)); return l; }
__device__ __forceinline__ float xsum16_32(float v) {
  auto a = __builtin_amdgcn_permlane16_swap(__float_as_uint(v), __float_as_uint(v), false, false); v = __uint_as_float(a[0]) + __uint_as_float(a[1]);
  auto c = __builtin_amdgcn_permlane32_swap(__float_as_uint(v), __float_as_uint(v), false, false); return __uint_as_float(c[0]) + __uint_as_float(c[1]);
}
__device__ __forceinline__ float xmax16_32(float v) {
  auto a = __builtin_amdgcn_permlane16_swap(__float_as_uint(v), __float_as_uint(v), false, false); v = fmaxf(__uint_as_float(a[0]), __uint_as_float(a[1]));
  auto c = __builtin_amdgcn_permlane32_swap(__float_as_uint(v), __float_as_uint(v), false, false); return fmaxf(__uint_as_float(c[0]), __uint_as_float(c[1]));
}

__device__ __forceinline__ float ln_rstd(float var) { return __builtin_amdgcn_rcpf(rbf(__builtin_amdgcn_sqrtf(rbf(rbf(var) + 1e-5f)))); }

constexpr size_t WS_MOD = 0;
constexpr size_t WS_EPO = 64 << 10, WS_FL1 = 96 << 10;
constexpr size_t WS_CTL = 128 << 10;
constexpr int C2 = 0, C3 = 16, C4 = 32, C5 = 48, CM = 80;
constexpr size_t WS_FLG = WS_CTL + (88 << 10), CTL_BYTES = 152 << 10;
constexpr size_t WS_XCH = 512 << 10;
constexpr size_t WS_MOD2 = 1 << 20;
constexpr size_t WS_H = 2 << 20, WS_O = WS_H + (4 << 20), WS_X1 = WS_O + (4 << 20), WS_H2 = WS_X1 + (4 << 20), WS_HH = WS_H2 + (4 << 20),
                 WS_PART = WS_HH + (size_t)S_ * F_ * 2;

template <int NL> __device__ __forceinline__ f32x2 slab_sum(const unsigned (&mc)[NL / 2], const u32x4 (&mw)[NL]) {
  f32x2 a2[4] = {};
#pragma unroll
  for (int i = 0; i < NL; ++i) {
    const float cv = (i & 1) ? hi2f(mc[i >> 1]) : lo2f(mc[i >> 1]);
    const float sv = rbf(cv * __builtin_amdgcn_rcpf(1.f + __builtin_amdgcn_exp2f(cv * -1.4426950408889634f)));
#pragma unroll
    for (int j = 0; j < 4; ++j) a2[j] += (f32x2){lo2f(mw[i][j]), hi2f(mw[i][j])} * sv;
  }
  float acc[8] = {a2[0][0], a2[0][1], a2[1][0], a2[1][1], a2[2][0], a2[2][1], a2[3][0], a2[3][1]};
#pragma unroll
  for (int j = 0; j < 4; ++j) { auto a = __builtin_amdgcn_permlane32_swap(__float_as_uint(acc[j]), __float_as_uint(acc[j + 4]), false, false); acc[j] = __uint_as_float(a[0]) + __uint_as_float(a[1]); }
#pragma unroll
  for (int j = 0; j < 2; ++j) { auto a = __builtin_amdgcn_permlane16_swap(__float_as_uint(acc[j]), __float_as_uint(acc[j + 2]), false, false); acc[j] = __uint_as_float(a[0]) + __uint_as_float(a[1]); }
#pragma unroll
  for (int j = 0; j < 2; ++j) acc[j] += __uint_as_float(__builtin_amdgcn_mov_dpp(__float_as_uint(acc[j]), 0x128, 0xf, 0xf, false));
  return (f32x2){acc[0], acc[1]};
}

constexpr int K3_B = 18432, K3_STG = 18432 + 24576;
constexpr int K4_RED = 0;
constexpr int K5_G = 16384, K5_U = 16384 + 7168 + 16, K5_STG = 30736, K5_NS = 5;
constexpr int K6_NW = 12, K6_NA = 7, K6_A = K6_NW * 8192;
constexpr int LDS_VEC = 158720, LDS_DUMMY = LDS_VEC + 1024, LDS_ARR = LDS_DUMMY + 256, MEGA_LDS = LDS_ARR + 64;
constexpr unsigned SPIN_CAP = 1u << 19;
#define NB5 4
#define PD4 12
#define HELP 0
#define HELP_HI
#define HELP_LO
#define T3_SLEEP 3
#define PD5 44
#define PRIO_A 2
#define PRIO_B 1
#define W2PF 4
#define W2OFF 2
#define DR5 1
#define BHOLD 0
#define PS5 4
#define KL5 40
#define TS5 3
#define PRE6 4
#define SKIP6 0x7cull
static_assert(__builtin_popcountll(SKIP6) == 5, "44 stages of W are issued behind 49 barriers");
#define HOLD 11
#define NDR 2
#define EPI_PRIO 0
#define POLL5 8
#define POLL6 2

__device__ __forceinline__ void signal(unsigned* cnt, int c, int lane) {
  if (lane < 8) __hip_atomic_fetch_add(cnt + (c * 8 + lane) * 32, 1u, __ATOMIC_RELAXED, __HIP_MEMORY_SCOPE_AGENT);
}
template <int D0, int D1 = 1> __device__ __forceinline__ void poll(const unsigned* cnt, unsigned want) {
  if (D0) __builtin_amdgcn_s_sleep(D0);
#pragma unroll 1
  for (unsigned spins = 0; spins < SPIN_CAP; ++spins) {
    if (__builtin_amdgcn_ballot_w64(__hip_atomic_load(cnt, __ATOMIC_RELAXED, __HIP_MEMORY_SCOPE_AGENT) < want) == 0) break;
    __builtin_amdgcn_s_sleep(D1);
  }
  __builtin_amdgcn_fence(__ATOMIC_ACQUIRE, "wavefront");
}

typedef float f32x32 __attribute__((ext_vector_type(32)));
typedef float f32x16 __attribute__((ext_vector_type(16)));
#define M5(i, j) "v_mfma_f32_16x16x32_bf16 v[28*" #i "+4*" #j ":28*" #i "+4*" #j "+3], v[128+4*" #j ":131+4*" #j "], v[112+4*" #i ":115+4*" #i "], v[28*" #i "+4*" #j ":28*" #i "+4*" #j "+3]\n\t"
#define RA5(i) "ds_read_b128 v[112+4*" #i ":115+4*" #i "], %[aA] offset:1024*" #i "\n\t"
#define RW5(j, h, a, o) "ds_read_b64_tr_b16 v[128+4*" #j "+2*" #h ":129+4*" #j "+2*" #h "], %[" #a "] offset:" #o "\n\t"
#define ST5(a) "v_add_u32 %[" #a "], %[dl], %[" #a "]\n\t"
#define SEL5A "s_add_u32 %[mask], %[mask], %[mask]\n\t"
#define SEL5B "s_cselect_b32 %[dl], %[wrap], 30736\n\t"
#define LG(n) "s_waitcnt lgkmcnt(" #n ")\n\t"
#define RP5(j, h) RW5(j, h, aP, 32*(j/2)+7184*(j&1)+3584*h)
#define TRIP5(X)                                                                                                                                        \
  M5(0, 0) RW5(6, 1, aM, 3584) M5(1, 0) RA5(2) ST5(aM) M5(1, 1) RA5(3) ST5(aP) M5(0, 1) ST5(aA) LG(6) M5(0, 2) M5(1, 2) SEL5A M5(1, 3) SEL5B              \
  M5(0, 3) M5(0, 4) M5(1, 4) LG(0) M5(1, 5) M5(0, 5) "s_barrier\n\t" M5(0, 6) RA5(0) M5(1, 6) RA5(1)                                                      \
  M5(2, 0) X M5(3, 0) RP5(0, 0) M5(3, 1) RP5(0, 1) M5(2, 1) RP5(1, 0) M5(2, 2) RP5(1, 1) M5(3, 2) RP5(2, 0) M5(3, 3) RP5(2, 1) M5(2, 3) RP5(3, 0)          \
  M5(2, 4) RP5(3, 1) M5(3, 4) RP5(4, 0) M5(3, 5) RP5(4, 1) M5(2, 5) RP5(5, 0) M5(2, 6) RP5(5, 1) LG(8) M5(3, 6) RW5(6, 0, aM, 0)
#define ACC5 "+{v[0:31]}"(c0), "+{v[32:63]}"(c1), "+{v[64:95]}"(c2), "+{v[96:111]}"(c3)
#define CLOB5 "memory", "scc", "v112", "v113", "v114", "v115", "v116", "v117", "v118", "v119", "v120", "v121", "v122", "v123", "v124", "v125", "v126", "v127", "v128", "v129", "v130", "v131", "v132", "v133", \
              "v134", "v135", "v136", "v137", "v138", "v139", "v140", "v141", "v142", "v143", "v144", "v145", "v146", "v147", "v148", "v149", "v150", "v151", "v152", "v153", "v154", "v155"
static_assert(K5_STG == 30736 && K5_U - K5_G == 7184, "TRIP5");

__device__ __forceinline__ int w2_unit(int k, int P) {
  const int n = (k < 4 ? k : k - 4) * 512 + (P >> 1), t = k < 4 ? n >> 7 : (n * 52429) >> 24, r = k < 4 ? n & 127 : n - t * 320 + 128;
  return (P & 1) * 1792 * 2048 + t * 112 * 2048 + r * 512;
}
__global__ __launch_bounds__(768, 3) __attribute__((amdgpu_num_sgpr(98))) void k2_block(const u16* __restrict__ x, const u16* __restrict__ wqkv, const u16* __restrict__ wo,
                                                   const u16* __restrict__ w1, const u16* __restrict__ w3, const u16* __restrict__ w2,
                                                   const u16* __restrict__ cnd, const u16* __restrict__ wmod, u16* __restrict__ out, char* __restrict__ ws) {
  extern __shared__ __attribute__((aligned(16))) char smem_[];
  LDS_PTR(char) smem = (LDS_PTR(char))smem_;
  const int tid = threadIdx.x, lane = tid & 63;
  const int wave = __builtin_amdgcn_readfirstlane(tid >> 6);
  const unsigned lds_base = (unsigned)(uintptr_t)smem;
  const int b = blockIdx.x, xc = b & 7, ii = b >> 3;
  const int tm3 = (xc & 1) * 8 + (ii & 7), tn3 = (xc >> 1) * 4 + (ii >> 3);
  const int tm4 = (xc >> 1) * 4 + (ii & 3), tn4 = (xc & 1) * 8 + (ii >> 2);
  const int tm5 = (xc & 1) * 4 + (ii & 3), tn5 = (xc >> 1) * 8 + (ii >> 2);
  const int kh6 = xc >> 2, tm6 = tm5 * 2 + ((ii >> 2) & 1), tn6 = tn3 & 7;
  const int u1 = (b >> 4) * 8 + xc;
  unsigned* cnt = (unsigned*)(ws + WS_CTL);
  unsigned long long* fl1 = (unsigned long long*)(ws + WS_FL1);
  STAMP_AT(0 );

  if (wave >= 8) {
    const int lw = wave - 8, P = b * 4 + lw, lane = lane_id();
    const bool isA = lw < 2;
    asm volatile("" ::"s"(wqkv), "s"(wo), "s"(w1), "s"(w3), "s"(w2), "s"(ws));
    int kpf = 0;
    auto touch = [&]() {
      if (kpf < 40 && __builtin_amdgcn_s_getreg((3 << 11) | (8 << 6) | 7) <= 6) {
        stouch4(kpf < 8 ? (const char*)wqkv + (kpf * 4096 + P * 4) * 128
                : kpf < 12 ? (const char*)wo + ((kpf - 8) * 4096 + P * 4) * 128
                           : (const char*)((P & 1) ? w3 : w1) + ((kpf - 12) * 2048 + (P >> 1) * 4) * 128);
        ++kpf;
      }
    };
    auto touch5 = [&]() {
      if (kpf < KL5 && __builtin_amdgcn_s_getreg((3 << 11) | (8 << 6) | 7) <= 6) {
        stouch4(kpf < 40 ? (const char*)((P & 1) ? w3 : w1) + ((kpf - 12) * 2048 + (P >> 1) * 4) * 128 : (const char*)w2 + w2_unit(kpf - 40, P));
        ++kpf;
      }
    };
    auto touchn = [&](int n) {
#pragma unroll 1
      for (int i = 0; i < n; ++i) { touch(); __builtin_amdgcn_s_sleep(16); }
      touch();
    };
    const char* epo = ws + WS_EPO;
    const char* wq3 = (const char*)wqkv + (32768 + b * 64) * 128;
    const unsigned swz = ((lane & 7) ^ ((lane >> 3) & 6)) * 16;
    const int cg = lw * 16 + (lw < 3 ? tn4 : tn6 * 2 + kh6);
    auto run3 = [&](auto isA_) {
      constexpr bool A_ = decltype(isA_)::value; constexpr int N = A_ ? 9 : 12;
      const char* sp = A_ ? ws + WS_H + lw * 131072 : (const char*)wqkv + tn3 * 128;
      const unsigned v0 = A_ ? lane * 16 : (lane >> 3) * 6144 + swz;
      const unsigned d0 = lds_base + (A_ ? lw * 9216 : K3_B + (lw - 2) * 12288);
      auto issue = [&](unsigned st) {
#pragma unroll
        for (int i = 0; i < N; ++i) {
          if (A_) { const int fb = tm3 * 8 - 1 + i; glds16s(sp + (fb < 0 ? 0 : fb) * 1024, v0, d0 + st, i * 1024); }
          else { const int tb = (lw - 2) * 12 + i; glds16(sp + (tb & 7) * 8 * 6144 + (tb >> 3) * 2048, v0, d0 + st, i * 1024); }
        }
        sp += A_ ? 262144 : 64 * 6144;
      };
      { unsigned long long ep;
        asm volatile("s_load_dwordx2 %0, %1, 0x0\n\ts_waitcnt lgkmcnt(0)" : "=s"(ep) : "s"(epo) : "memory");
        ep += 1;
        if (lw == 1) {
#pragma unroll
          for (int k = 0; k < 3; ++k) {
            const int i = b * 166 + (k * 64 + lane < 166 ? k * 64 + lane : 0);
            st16<true>(ws, i < 32768 ? (unsigned)WS_XCH + i * 16 : (unsigned)WS_CTL + (i - 32768) * 16, i < 32768 ? (u32x4){~0u, ~0u, ~0u, ~0u} : (u32x4){0u, 0u, 0u, 0u});
          }
        } else touchn(1);
        BAR();
        auto flag = [&](int idx) { wait_vm<0>(); if (lane < 8) __hip_atomic_store(fl1 + lane * 256 + idx, ep, __ATOMIC_RELAXED, __HIP_MEMORY_SCOPE_AGENT); };
        if (lw == 1 && (b & 8)) {
          const int kq = u1 & 3, cgp = u1 >> 2;
          float sm = 0.f;
#pragma unroll
          for (int r = 0; r < 8; ++r) sm += *(const LDS_PTR(float))(smem + 8192 + (r * 64 + lane) * 4);
          __hip_atomic_store((unsigned*)(ws + WS_MOD) + kq * 2048 + cgp * 64 + lane, __float_as_uint(sm), __ATOMIC_RELAXED, __HIP_MEMORY_SCOPE_AGENT);
          flag(((cgp >> 3) & 1) * 64 + (cgp >> 4) * 32 + (cgp & 7) * 4 + kq);
        }
        touch();
        BAR();
        if (lw == 1 && !(b & 8)) flag(128 + u1);
        if (lw < 2) {
          if (lw == 1) BAR();
#pragma unroll 1
          for (unsigned spins = 0; spins < SPIN_CAP; ++spins) {
            bool ok = true;
#pragma unroll
            for (int k = 0; k < 4; ++k) ok &= __hip_atomic_load(fl1 + xc * 256 + (lw ? k : (b >> 3) & 1) * 64 + lane, __ATOMIC_RELAXED, __HIP_MEMORY_SCOPE_AGENT) == ep;
            if (__builtin_amdgcn_ballot_w64(!ok) == 0) break;
            __builtin_amdgcn_s_sleep(4);
          }
          if (lw == 0) BAR();
          else if (b == 0 && lane == 0) __hip_atomic_store((unsigned long long*)(ws + WS_EPO), ep, __ATOMIC_RELAXED, __HIP_MEMORY_SCOPE_AGENT);
        } else { touch(); BAR(); }
        touch();
      }
      BAR();
      if (lw == 0) {
        signal(cnt, C2 + (u1 >> 3), lane); glds4(wq3, lane * 128, lds_base + LDS_DUMMY);
        poll<8>(cnt + ((C2 + ((lane & 1) && tm3 ? tm3 - 1 : tm3)) * 8 + xc) * 32, 16);
      }
      if (!A_) { issue(0); issue(K3_STG); }
      touch();
      BAR();
      if (A_) { issue(0); issue(K3_STG); }
      wait_vm<N>();
      BAR();
      issue(2 * K3_STG);
      unsigned st = 0;
#pragma unroll 1
      for (int j = 0; j < 14; ++j) {
        wait_vm<N>();
        BAR();
        if (j < 13) issue(st);
        st = st == 2 * K3_STG ? 0 : st + K3_STG;
        touch();
        if (j == 2 && lw == 2 && lane == 0 && !(b & 8)) __hip_atomic_fetch_add(cnt + (CM * 8 + (u1 >> 1)) * 32, 1u, __ATOMIC_RELAXED, __HIP_MEMORY_SCOPE_AGENT);
      }
    };
    if (isA) run3(T_()); else run3(F__());
    { const char* sp = (const char*)wo + tn4 * 128 + lw * 32 * 2048;
      const unsigned v0 = (lane >> 3) * 2048 + swz;
      auto issue = [&](int s, unsigned slot) {
#pragma unroll
        for (int i = 0; i < 4; ++i) glds16(sp + (s * 128 + i * 8) * 2048, v0, lds_base + slot + lw * 4096, i * 1024);
      };
      poll<0>(cnt + (CM * 8 + cg) * 32, 2);
      wait_vm<0>();
      issue(0, 57344);
      BAR();
      f32x4 pv[4];
#pragma unroll
      for (int i = 0; i < 4; ++i) pv[i] = __builtin_bit_cast(f32x4, ld16<true>(ws + WS_MOD2, (((lane >> 4) * 4 + i) * 4096 + cg * 64 + (lane & 15) * 4) * 4));
      SCHED0();
      BAR();
      issue(1, 57344 + 16384);
      BAR();
      issue(2, 57344 + 2 * 16384); issue(3, 57344 + 3 * 16384); issue(4, 57344 + 4 * 16384);
      touch();
      BAR();
      if (lw == 0) signal(cnt, C3 + tm3, lane);
      issue(5, 57344 + 5 * 16384); issue(6, 0); issue(7, 16384);
      if (lw == 0) poll<16>(cnt + ((C3 + tm4) * 8 + xc) * 32, 16);
      if (lw != 0) touchn(1);
      wait_vm<0>();
      BAR();
      f32x4 v = (pv[0] + pv[1]) + (pv[2] + pv[3]);
#pragma unroll
      for (int e = 0; e < 4; ++e) { v[e] = rbf(xsum16_32(v[e])); if (lw == 2) v[e] = rbf(1.f + v[e]); }
      *(LDS_PTR(f32x4))(smem + LDS_VEC + lw * 256 + (lane & 15) * 16) = v;
      *(LDS_PTR(u32x2))(smem + LDS_ARR) = (u32x2){0u, 0u};
      touch();
      BAR();
      wait_lgkm0();
      touchn(2);
    }
    auto run5 = [&](auto isA_) {
      constexpr bool A_ = decltype(isA_)::value; constexpr int N = A_ ? 8 : 7;
      const char* sp = A_ ? ws + WS_H2 + (tm5 * 16 + lw * 8) * 1024 : (const char*)(lw == 2 ? w1 : w3) + tn5 * 224;
      unsigned vp[N];
#pragma unroll
      for (int i = 0; i < N; ++i) { const int L = i * 64 + lane, r = L / 14, c = L - r * 14; vp[i] = A_ ? L * 16 : r * (F_ * 2) + c * 16; }
      const unsigned d0 = lds_base + (A_ ? lw * 8192 : lw == 2 ? K5_G : K5_U);
      unsigned st = 3 * K5_STG;
      auto issue = [&]() {
#pragma unroll
        for (int i = 0; i < N; ++i) { if (A_) glds16s(sp, vp[i], d0 + st, i * 1024); else glds16(sp, vp[i], d0 + st, i * 1024); }
        sp += A_ ? 131072 : 32 * F_ * 2; st = st == (K5_NS - 1) * K5_STG ? 0 : st + K5_STG;
      };
      BAR();
      if (!A_) { issue(); issue(); }
      touchn(3);
      BAR();
      if (A_ && lw == 0) { signal(cnt, C4 + tm4, lane); poll<PD4>(cnt + ((C4 + tm5 * 2 + (lane & 1)) * 8 + xc) * 32, 16); }
      else { if (!A_) { __builtin_amdgcn_s_sleep(2); for (int s = 2; s < NB5; ++s) issue(); } touchn(1); }
      BAR();
      if (A_) { issue(); issue(); }
      wait_vm<A_ ? N : (NB5 - 1) * N>();
      BAR();
#pragma unroll 1
      for (int s = A_ ? 2 : NB5; s < K5_NS; ++s) issue();
      if (kpf < 12) kpf = 12;
#pragma unroll 1
      for (int j = 0; j < 27; ++j) {
        wait_vm<3 * N>();
        BAR();
        issue();
        touch5(); __builtin_amdgcn_s_sleep(TS5); touch5();
      }
      const char* sb = A_ ? ws + WS_HH + (kh6 * 56 * 128 + tm6 * 8 + lw * 4) * 1024 : (const char*)w2 + kh6 * 1792 * 2048 + tn6 * 256 + ((lw ^ kh6 ^ 1) & 1) * 128;
      unsigned vo[4];
#pragma unroll
      for (int i = 0; i < 4; ++i) {
        const int r = i * 8 + (lane >> 3), k = 8 * ((r >> 2) & 3) + 4 * (r >> 4) + (r & 3);
        vo[i] = A_ ? lane * 16 : k * 2048 - i * 1024 + swz;
      }
      const unsigned d6 = lds_base + (A_ ? K6_A : 0) + (lw & 1) * 4096;
      st = 0;
      const int j2 = lane - 16, p5 = (j2 * 52) >> 8, r5 = j2 - p5 * 5;
      const int tab = lane < 16 ? lane * 112 * 2048 : (p5 * 224 + 32 + r5 * 32 + (r5 > 2 ? 32 : 0)) * 2048 + (r5 == 2);
      int jn = 0;
      auto issue6 = [&]() {
        if (A_) { glds16x4<true>(sb, sb, vo, d6 + st); sb += 131072; }
        else { const int o = __builtin_amdgcn_readlane(tab, jn); const char* s0 = sb + (o & ~1); glds16x4<false>(s0, s0 + (o & 1) * 65536, vo, d6 + st); ++jn; }
        st = st == ((A_ ? K6_NA : K6_NW) - 1) * 8192 ? 0 : st + 8192;
      };
      auto pre6 = [&](int n) {
        if (!A_) {
#pragma unroll 1
          for (int i = 0; i < n; ++i) issue6();
        }
      };
      constexpr int E = A_ ? 0 : 4;
      glds4(w2, w2_unit((lane >> 2) < 14 ? lane >> 2 : 0, P) + (lane & 3) * 128, lds_base + LDS_DUMMY);
      wait_vm<3 * N + 1>(); BAR();
      wait_vm<2 * N + 1>(); BAR(); pre6(PRE6);
      wait_vm<N + PRE6 * E + 1>(); BAR(); pre6(4);
      wait_vm<(PRE6 + 4) * E + 1>(); BAR(); pre6(8 - PRE6);
      BAR();
      if (A_) {
        volatile LDS_PTR(unsigned) go = (volatile LDS_PTR(unsigned))(smem + LDS_ARR + 4);
        if (lw == 0) { poll<PD5, PS5>(cnt + ((C5 + (tm6 >> 1) * 4 + kh6 * 2) * 8 + tn6) * 32, 16); *go = 1u; }
        else {
#pragma unroll 1
          for (unsigned spins = 0; spins < SPIN_CAP && __builtin_amdgcn_readfirstlane(*go) == 0; ++spins) __builtin_amdgcn_s_sleep(1);
          asm volatile("" ::: "memory");
        }
        STAMP_L(31);
        issue6(); issue6(); issue6();
        wait_vm<8>(); BAR(); issue6(); issue6();
        wait_vm<12>(); BAR(); issue6(); issue6();
      } else { wait_vm<16>(); BAR(); BAR(); }
#pragma unroll 1
      for (int c = 2; c < 51; ++c) { wait_vm<16>(); BAR(); if (A_ || !((SKIP6 >> c) & 1)) issue6(); }
      wait_vm<16>(); BAR(); wait_vm<12>(); BAR(); wait_vm<8>(); BAR(); wait_vm<4>(); BAR(); wait_vm<0>(); BAR();
    };
    if (isA) run5(T_()); else run5(F__());
    asm volatile("s_waitcnt lgkmcnt(0)" ::: "memory");
    return;
  }

  const int fr = lane & 15, g = lane >> 4, q = fr >> 2, p4 = fr & 3, row = 4 * g + q;
  const int lsw = row * 128 + (((row >> 1) & 3) * 32) + p4 * 8;
  { const int mb = u1, hf = (b >> 3) & 1, kb0 = hf * 16 + wave * 2, cofs = (g & 1) * 16 + (g >> 1) * 8;
    u32x4 v[4];
    auto ldx = [&]() {
#pragma unroll
      for (int p = 0; p < 4; ++p) v[p] = *(const u32x4*)(x + (size_t)(mb * 16 + fr) * D_ + ((kb0 + (p & 1)) ^ ((p >> 1) * 16)) * 32 + cofs);
    };
    LDS_PTR(f32x2) sred = (LDS_PTR(f32x2))(smem + 4096);
    auto up2 = [](unsigned u) { return (f32x2){lo2f(u), hi2f(u)}; };
    auto stats = [&]() {
      f32x2 s2 = {0.f, 0.f}, q2 = {0.f, 0.f};
#pragma unroll
      for (int k = 0; k < 16; ++k) s2 += up2(v[k >> 2][k & 3]);
      const float mw = xsum16_32(s2[0] + s2[1]) * (1.f / 128.f);
#pragma unroll
      for (int k = 0; k < 16; ++k) { const f32x2 d = up2(v[k >> 2][k & 3]) - mw; q2 += d * d; }
      const float qw = xsum16_32(q2[0] + q2[1]);
      if (lane < 16) sred[wave * 16 + fr] = (f32x2){mw, qw};
      wait_lgkm0();
    };
    auto rest = [&]() {
    STAMP_AT(1 );
    float mean = 0.f, m2 = 0.f; f32x2 sw[8];
#pragma unroll
    for (int w = 0; w < 8; ++w) { sw[w] = sred[w * 16 + fr]; mean += sw[w][0]; m2 += sw[w][1]; }
    mean *= 0.125f;
#pragma unroll
    for (int w = 0; w < 8; ++w) { const float d = sw[w][0] - mean; m2 += 128.f * d * d; }
    const float rstd = ln_rstd(m2 * (1.f / 1024.f));
    mean = rbf(mean);
    unsigned nv[8];
#pragma unroll
    for (int k = 0; k < 8; ++k) {
      const f32x2 d = up2(v[k >> 2][k & 3]) - mean;
      const f32x2 n = up2(pk2(d[0], d[1])) * rstd;
      nv[k] = pk2(n[0], n[1]);
      asm volatile("" : "+v"(nv[k]));
    }
    SCHED0(); BAR(); SCHED0();
    STAMP_AT(2 );
    float mp[8];
#pragma unroll
    for (int i = 0; i < 8; ++i) mp[i] = __uint_as_float(__hip_atomic_load((unsigned*)(ws + WS_MOD) + hf * 512 + tid + i * 1024, __ATOMIC_RELAXED, __HIP_MEMORY_SCOPE_AGENT));
    *(LDS_PTR(unsigned))(smem + tid * 4) = pk2((mp[0] + mp[2]) + (mp[4] + mp[6]), 1.f + rbf((mp[1] + mp[3]) + (mp[5] + mp[7])));
    wait_lgkm0();
#pragma unroll
    for (int p = 0; p < 2; ++p) {
      const LDS_PTR(u32x4) tp = (const LDS_PTR(u32x4))(smem + ((wave * 2 + p) * 32 + cofs) * 4);
      const u32x4 t0 = tp[0], t1 = tp[1];
      unsigned o[4];
#pragma unroll
      for (int j = 0; j < 4; ++j) {
        const unsigned ta = j < 2 ? t0[2 * j] : t1[2 * j - 4], tb = j < 2 ? t0[2 * j + 1] : t1[2 * j - 3];
        const unsigned u = pk2(lo2f(nv[p * 4 + j]) * hi2f(ta), hi2f(nv[p * 4 + j]) * hi2f(tb));
        o[j] = pk2(lo2f(u) + lo2f(ta), hi2f(u) + lo2f(tb));
      }
      auto s0 = __builtin_amdgcn_permlane16_swap(o[0], o[2], false, false);
      auto s1 = __builtin_amdgcn_permlane16_swap(o[1], o[3], false, false);
      st16<true>(ws + WS_H, ((kb0 + p) * 128 + mb) * 1024 + lane * 16, (u32x4){s0[0], s1[0], s0[1], s1[1]});
    }
    STAMP_AT(3 );
    wait_vm<0>();
    STAMP_AT(4 );
    BAR();
    STAMP_AT(5 );
    };
    if (hf) {
      const int mrow = (u1 & 3) * 256 + (tid >> 3) * 4;
      const u32x2 c2 = *(const u32x2*)(cnd + mrow);
      u32x4 mw[4];
#pragma unroll
      for (int i = 0; i < 4; ++i) mw[i] = __builtin_nontemporal_load((const u32x4*)(wmod + (size_t)(mrow + i) * 6144 + (u1 >> 2) * 64 + (lane & 7) * 8));
      const unsigned mc[2] = {c2[0], c2[1]};
      *(LDS_PTR(f32x2))(smem + 8192 + (wave * 64 + (lane & 7) * 8 + (lane >> 4) * 2) * 4) = slab_sum<4>(mc, mw);
      wait_lgkm0();
      BAR();
      __builtin_amdgcn_s_sleep(12);
      ldx();
      stats();
      BAR();
      rest();
    } else {
      ldx();
      const int mrow = (u1 & 1) * 512 + (tid >> 3) * 8, mcol = (u1 >> 1) * 64 + (lane & 7) * 8;
      const u32x4 c4 = *(const u32x4*)(cnd + mrow);
      SCHED0();
      u32x4 mw[8];
#pragma unroll
      for (int i = 0; i < 8; ++i) mw[i] = __builtin_nontemporal_load((const u32x4*)(wmod + (size_t)(mrow + i) * 6144 + 2048 + mcol));
      SCHED0(); BAR(); SCHED0();
      stats();
      SCHED0(); BAR(); SCHED0();
      rest();
      const unsigned mc[4] = {c4[0], c4[1], c4[2], c4[3]};
      const f32x2 ps = slab_sum<8>(mc, mw);
      if (!(lane & 8))
        __hip_atomic_store((unsigned long long*)(ws + WS_MOD2) + (((u1 & 1) * 8 + wave) * 4096 + mcol + (lane >> 4) * 2) / 2,
                           ((unsigned long long)__float_as_uint(ps[1]) << 32) | __float_as_uint(ps[0]), __ATOMIC_RELAXED, __HIP_MEMORY_SCOPE_AGENT);
    }
  }
  { const int wm = wave >> 2, wn = wave & 3;
    const int lbw = K3_B + (lsw ^ (wn * 32));
    const int o1 = wm ? 16384 : 8192, o2 = wm ? 8192 : 16384;
    f32x4 acc[4][3], acch;
#pragma unroll
    for (int i = 0; i < 4; ++i)
#pragma unroll
      for (int j = 0; j < 3; ++j) { acc[i][j] = (f32x4){0.f, 0.f, 0.f, 0.f}; asm volatile("" : "+v"(acc[i][j])); }
    acch = (f32x4){0.f, 0.f, 0.f, 0.f};
    float bias[4];
    { const int kbase = tm3 * 128 + wave * 16 - 16;
#pragma unroll
      for (int t = 0; t < 4; ++t) {
        const int kk = (t >> 1) * 16 + 4 * g + (t & 1) * 2 + (fr & 1), df = 16 + fr - kk;
        bias[t] = (df >= 0 && df <= 14 && kbase + kk >= 0) ? 0.f : -__builtin_inff();
        asm volatile("" : "+v"(bias[t]));
      } }
    struct Fr { bf16x8 a[5]; bf16x4 w[3][2]; };
    const LDS_PTR(char) pw0 = smem + lbw; const LDS_PTR(char) pw1 = smem + lbw + o1; const LDS_PTR(char) pw2 = smem + lbw + o2;
    const LDS_PTR(char) pa = smem + 1024 + wm * 4096 + lane * 16; const LDS_PTR(char) ph = smem + lane * 16;
    auto rd1 = [&](Fr& f, int u, int r) {
      if (r < 2) f.w[0][r] = ld_tr4(pw0 + u * 4096 + r * 2048);
      else if (r < 6) f.a[r - 2] = *(const LDS_PTR(bf16x8))(pa + u * 9216 + (r - 2) * 1024);
      else if (r < 8) f.w[1][r - 6] = ld_tr4(pw1 + u * 4096 + (r - 6) * 2048);
      else if (r == 8) f.a[4] = *(const LDS_PTR(bf16x8))(ph + u * 9216);
      else f.w[2][r - 9] = ld_tr4(pw2 + u * 4096 + (r - 9) * 2048);
    };
    auto rdm = [&](Fr& f, int u, int m) {
      if (m == 0) { rd1(f, u, 0); rd1(f, u, 1); }
      if (m >= 1 && m <= 4) rd1(f, u, m + 1);
      if (m == 5) { rd1(f, u, 6); rd1(f, u, 7); }
      if (m == 6) rd1(f, u, 8);
      if (m == 7) { rd1(f, u, 9); rd1(f, u, 10); }
    };
    auto mm1 = [&](const Fr& f, int m) {
      const int j = m < 4 ? 0 : m < 9 ? 1 : 2, i = m < 4 ? m : m < 9 ? m - 4 : m - 9;
      const bf16x8 w = cat8(f.w[j][0], f.w[j][1]);
      if (i == 4) acch = __builtin_amdgcn_mfma_f32_16x16x32_bf16(w, f.a[4], acch, 0, 0, 0);
      else acc[i][j] = __builtin_amdgcn_mfma_f32_16x16x32_bf16(w, f.a[i], acc[i][j], 0, 0, 0);
    };
    Fr fA, fB;
#pragma unroll
    for (int i = 0; i < 5; ++i) { fB.a[i] = (bf16x8){0, 0, 0, 0, 0, 0, 0, 0}; asm volatile("" : "+v"(fB.a[i])); }
#pragma unroll
    for (int j = 0; j < 3; ++j) { fB.w[j][0] = fB.w[j][1] = (bf16x4){0, 0, 0, 0}; asm volatile("" : "+v"(fB.w[j][0]), "+v"(fB.w[j][1])); }
    int rpos = 0, dl = K3_STG;
    wait_vm<0>();
    BAR();
    STAMP_AT(6 );
    STAMP_AT(7 );
#pragma unroll 1
    for (int t = 0; t < 16; ++t) {
      SCHED0(); BAR(); SCHED0();
#pragma unroll
      for (int m = 0; m < 13; ++m) {
        mm1(fB, m); SCHED0();
        rdm(fA, 0, m);
        if (m == 8) { asm volatile("" : "+s"(rpos)); dl = rpos == 2 ? -2 * K3_STG : K3_STG; asm volatile("" : "+s"(dl)); }
        if (m == 9) { rpos = rpos == 2 ? 0 : rpos + 1; asm volatile("" : "+s"(rpos)); }
        SCHED0();
      }
      wait_lgkm0();
      SCHED0();
#pragma unroll
      for (int m = 0; m < 13; ++m) {
        mm1(fA, m); SCHED0();
        rdm(fB, 1, m);
        if (m == 8) { pw0 += dl; pa += dl; asm volatile("" : "+v"(pw0), "+v"(pa)); }
        if (m == 9) { pw1 += dl; ph += dl; asm volatile("" : "+v"(pw1), "+v"(ph)); }
        if (m == 10) { pw2 += dl; asm volatile("" : "+v"(pw2)); }
        SCHED0();
      }
      wait_lgkm0();
    }
    SCHED0(); BAR(); SCHED0();
    STAMP_AT(8 );
    { auto pk4 = [&](const f32x4& v) { u32x2 o; o[0] = pk2(v[0], v[1]); o[1] = pk2(v[2], v[3]); return o; };
      const int lq = wn * 512 + lane * 8, lv = 36864 + wn * 512 + (lane ^ ((g >> 1) * 8)) * 8, lk = 18432 + lq;
      const int l1 = wm ? lv : lk, l2 = wm ? lk : lv;
      auto wr1 = [&](int m) {
        const int j = m < 4 ? 0 : m < 9 ? 1 : 2, i = m < 4 ? m : m < 9 ? m - 4 : m - 9;
        if (i == 4) *(LDS_PTR(u32x2))(smem + l1) = pk4(acch);
        else *(LDS_PTR(u32x2))(smem + (wm * 4 + i + 1) * 2048 + (j == 0 ? lq : j == 1 ? l1 : l2)) = pk4(acc[i][j]);
      };
#pragma unroll
      for (int m = 0; m < 16; ++m) { if (m < 13) mm1(fB, m); SCHED0(); if (m >= 3) wr1(m - 3); SCHED0(); }
    }
    wait_lgkm0();
    BAR();
    STAMP_AT(9 );
    const LDS_PTR(char) eb = smem + wave * 2048;
    auto ld2 = [&](int o) { return cat8(*(const LDS_PTR(bf16x4))(eb + o), *(const LDS_PTR(bf16x4))(eb + o + 512)); };
    const int vo = 36864 + ((p4 * 16 + row) ^ ((p4 >> 1) * 8)) * 8;
    bf16x8 qf[2], k0[2], k1[2], v0[2], v1[2];
#pragma unroll
    for (int hd = 0; hd < 2; ++hd) { qf[hd] = ld2(2048 + hd * 1024 + lane * 8); k0[hd] = ld2(18432 + hd * 1024 + lane * 8); k1[hd] = ld2(18432 + 2048 + hd * 1024 + lane * 8); }
#pragma unroll
    for (int hd = 0; hd < 2; ++hd) {
      v0[hd] = cat8(ld_tr4(eb + vo + hd * 1024), ld_tr4(eb + vo + hd * 1024 + 2048));
      v1[hd] = cat8(ld_tr4(eb + vo + hd * 1024 + 512), ld_tr4(eb + vo + hd * 1024 + 512 + 2048));
    }
    const f32x4 z = {0.f, 0.f, 0.f, 0.f};
    const bool odd = fr & 1; const int psh = (fr & 1) * 16;
#pragma unroll
    for (int hd = 0; hd < 2; ++hd) {
      const f32x4 s0 = __builtin_amdgcn_mfma_f32_16x16x32_bf16(k0[hd], qf[hd], z, 0, 0, 0), s1 = __builtin_amdgcn_mfma_f32_16x16x32_bf16(k1[hd], qf[hd], z, 0, 0, 0);
      float l[4] = {odd ? s0[1] : s0[0], odd ? s0[3] : s0[2], odd ? s1[1] : s1[0], odd ? s1[3] : s1[2]};
#pragma unroll
      for (int t = 0; t < 4; t += 2) {
        unsigned u = pk2(l[t], l[t + 1]);
        u = pk2(lo2f(u) * 0.17677669529663687f, hi2f(u) * 0.17677669529663687f);
        l[t] = lo2f(u) + bias[t]; l[t + 1] = hi2f(u) + bias[t + 1];
      }
      const float mx = xmax16_32(fmaxf(fmaxf(l[0], l[1]), fmaxf(l[2], l[3])));
#pragma unroll
      for (int t = 0; t < 4; ++t) l[t] = __builtin_amdgcn_exp2f((l[t] - mx) * 1.4426950408889634f);
      const float inv = __builtin_amdgcn_rcpf(xsum16_32((l[0] + l[1]) + (l[2] + l[3])));
      u32x4 pp;
#pragma unroll
      for (int t = 0; t < 4; ++t) pp[t] = pk2(l[t] * inv, 0.f) << psh;
      const bf16x8 pf = __builtin_bit_cast(bf16x8, pp);
      const f32x4 oa = __builtin_amdgcn_mfma_f32_16x16x32_bf16(v0[hd], pf, z, 0, 0, 0), ob = __builtin_amdgcn_mfma_f32_16x16x32_bf16(v1[hd], pf, z, 0, 0, 0);
      u32x4 ov; ov[0] = pk2(oa[0], oa[1]); ov[1] = pk2(oa[2], oa[3]); ov[2] = pk2(ob[0], ob[1]); ov[3] = pk2(ob[2], ob[3]);
      st16<true>(ws + WS_O, ((tn3 * 2 + hd) * 128 + tm3 * 8 + wave) * 1024 + lane * 16, ov);
    }
    STAMP_AT(10 );
    wait_vm<0>();
    STAMP_AT(11 );
    BAR();
    STAMP_AT(12 );
  }
  { const int wm = wave & 3, ks = wave >> 2, mb = tm4 * 8 + wm * 2 + ks;
    constexpr int R = 6;
    u32x2 rv[4];
#pragma unroll
    for (int j = 0; j < 4; ++j) rv[j] = *(const u32x2*)(x + (size_t)(mb * 16 + fr) * D_ + tn4 * 64 + j * 16 + 4 * g);
    const unsigned va0 = (ks * 128 + mb) * 1024 + lane * 16, va1 = va0 ^ 1024;
    bf16x8 fa[R][2], fw[2][4];
    constexpr int rot = 3;
    auto issueA = [&](int t) {
      const char* pa = ws + WS_O + (size_t)((((t >> 2) + rot) & 3) * 4 + (t & 3)) * 262144;
      fa[t % R][0] = __builtin_bit_cast(bf16x8, ld16<true>(pa, va0));
      fa[t % R][1] = __builtin_bit_cast(bf16x8, ld16<true>(pa, va1));
    };
    unsigned aw[4];
    auto rdw = [&](int t, int j) {
      const int o = (t & 3) * 8192;
      fw[t & 1][j] = cat8(ld_tr4(smem + aw[j] + o), ld_tr4(smem + aw[j] + o + 2048));
    };
    auto stepw = [&](int t) {
      const int gi = ((t >> 2) + rot) & 3, base = gi < 3 ? 57344 + gi * 32768 : 0;
#pragma unroll
      for (int j = 0; j < 4; ++j) { aw[j] = base + ks * 4096 + (lsw ^ (j * 32)); asm volatile("" : "+v"(aw[j])); }
    };
    f32x4 acc[2][4];
#pragma unroll
    for (int i = 0; i < 2; ++i)
#pragma unroll
      for (int j = 0; j < 4; ++j) acc[i][j] = (f32x4){0.f, 0.f, 0.f, 0.f};
    stepw(0);
    wait_lgkm0();
    SCHED0(); BAR(); SCHED0();
    STAMP_AT(13 );
#pragma unroll
    for (int t = 0; t < R; ++t) issueA(t);
#pragma unroll
    for (int j = 0; j < 4; ++j) rdw(0, j);
    SCHED0();
#pragma unroll
    for (int t = 0; t < 16; ++t) {
      if (t < 15 && ((t + 1) & 3) == 0) stepw(t + 1);
#pragma unroll
      for (int j = 0; j < 4; ++j) {
        if (t < 15) rdw(t + 1, j);
        SCHED0();
#pragma unroll
        for (int i = 0; i < 2; ++i) acc[i][j] = __builtin_amdgcn_mfma_f32_16x16x32_bf16(fw[t & 1][j], fa[t % R][i], acc[i][j], 0, 0, 0);
        SCHED0();
      }
      if (t + R < 16) issueA(t + R);
      if (t == 5) BAR();
      SCHED0();
    }
    STAMP_AT(14 );
    LDS_PTR(f32x4) red = (LDS_PTR(f32x4))(smem + K4_RED);
#pragma unroll
    for (int j = 0; j < 4; ++j) red[((wave ^ 4) * 4 + j) * 64 + lane] = acc[1][j];
    wait_lgkm0();
    BAR();
    STAMP_AT(15 );
    const LDS_PTR(float) vec = (const LDS_PTR(float))(smem + LDS_VEC);
    unsigned o[8];
#pragma unroll
    for (int j = 0; j < 4; ++j) {
      const f32x4 y = (acc[0][j] + red[(wave * 4 + j) * 64 + lane]) * *(const LDS_PTR(f32x4))(vec + j * 16 + 4 * g);
      o[2 * j] = pk2(lo2f(rv[j][0]) + y[0], hi2f(rv[j][0]) + y[1]);
      o[2 * j + 1] = pk2(lo2f(rv[j][1]) + y[2], hi2f(rv[j][1]) + y[3]);
    }
    float f[16], s = 0.f;
#pragma unroll
    for (int j = 0; j < 8; ++j) { f[2 * j] = lo2f(o[j]); f[2 * j + 1] = hi2f(o[j]); s += f[2 * j] + f[2 * j + 1]; }
    const float mt = xsum16_32(s) * (1.f / 64.f);
    float qq = 0.f;
#pragma unroll
    for (int j = 0; j < 16; ++j) { const float d = f[j] - mt; qq += d * d; }
    qq = xsum16_32(qq);
    unsigned long long* slot = (unsigned long long*)(ws + WS_XCH) + (size_t)mb * 256 + fr;
    if (lane < 32) {
      const unsigned qb = qq == qq ? __float_as_uint(qq) : 0x7fc00000u;
      __hip_atomic_store(slot + g * 32768 + tn4 * 16, ((unsigned long long)qb << 32) | __float_as_uint(mt), __ATOMIC_RELAXED, __HIP_MEMORY_SCOPE_AGENT);
    }
    st16<true>(ws + WS_X1, (mb * 16 + tn4) * 2048 + lane * 16, (u32x4){o[0], o[1], o[2], o[3]});
    st16<true>(ws + WS_X1, (mb * 16 + tn4) * 2048 + 1024 + lane * 16, (u32x4){o[4], o[5], o[6], o[7]});
    STAMP_AT(16 );
    f32x4 ss[4], gg[4];
#pragma unroll
    for (int j = 0; j < 4; ++j) {
      ss[j] = *(const LDS_PTR(f32x4))(vec + 64 + j * 16 + 4 * g); gg[j] = *(const LDS_PTR(f32x4))(vec + 128 + j * 16 + 4 * g);
      asm volatile("" : "+v"(ss[j]), "+v"(gg[j]));
    }
    unsigned long long pv[4];
    __builtin_amdgcn_s_sleep(22);
    for (unsigned spins = 0;; ++spins) {
      bool ok = true;
#pragma unroll
      for (int i = 0; i < 4; ++i) { pv[i] = __hip_atomic_load(slot + (xc & 1) * 32768 + (4 * g + i) * 16, __ATOMIC_RELAXED, __HIP_MEMORY_SCOPE_AGENT); ok &= (unsigned)(pv[i] >> 32) != 0xffffffffu; }
      if (__builtin_amdgcn_ballot_w64(!ok) == 0 || spins > SPIN_CAP) break;
      __builtin_amdgcn_s_sleep(4);
    }
    STAMP_AT(17 );
    float ms = 0.f, m2 = 0.f;
#pragma unroll
    for (int i = 0; i < 4; ++i) { ms += __uint_as_float((unsigned)pv[i]); m2 += __uint_as_float((unsigned)(pv[i] >> 32)); }
    float mean = xsum16_32(ms) * (1.f / 16.f);
#pragma unroll
    for (int i = 0; i < 4; ++i) { const float d = __uint_as_float((unsigned)pv[i]) - mean; m2 += 64.f * d * d; }
    const float rstd = ln_rstd(xsum16_32(m2) * (1.f / 1024.f));
    mean = rbf(mean);
#pragma unroll
    for (int p = 0; p < 2; ++p) {
      u32x4 v;
#pragma unroll
      for (int j = 0; j < 4; ++j) {
        const int jb = 2 * p + (j >> 1), e = (j & 1) * 2;
        v[j] = pk2((f[8 * p + 2 * j] - mean) * rstd * gg[jb][e] + ss[jb][e], (f[8 * p + 2 * j + 1] - mean) * rstd * gg[jb][e + 1] + ss[jb][e + 1]);
      }
      st16<true>(ws + WS_H2, ((tn4 * 2 + p) * 128 + mb) * 1024 + lane * 16, v);
    }
    STAMP_AT(18 );
    wait_vm<0>();
    BAR();
    STAMP_AT(19 );

  }
  const int grp = wave >> 2;
  { const int wm = wave & 3;
    const LDS_PTR(char) sb = smem + 3 * K5_STG;
    const LDS_PTR(char) aP = sb + K5_G + row * 224 + grp * 96 + p4 * 8;
    const LDS_PTR(char) aM = sb + (p4 < 2 ? K5_G : K5_U) + row * 224 + (12 + grp) * 16 + (p4 & 1) * 8;
    const LDS_PTR(char) aA = sb + wm * 4096 + lane * 16;
    f32x32 c0, c1, c2; f32x16 c3;
    asm volatile(".irp r,0,8,16,24,32,40,48,56,64,72,80,88,96,104\n\tv_mov_b64 v[\\r:\\r+1], 0\n\tv_mov_b64 v[\\r+2:\\r+3], 0\n\tv_mov_b64 v[\\r+4:\\r+5], 0\n\tv_mov_b64 v[\\r+6:\\r+7], 0\n\t.endr"
                 : "={v[0:31]}"(c0), "={v[32:63]}"(c1), "={v[64:95]}"(c2), "={v[96:111]}"(c3)::"memory");
    int wrap = -(K5_NS - 1) * K5_STG, dl = K5_STG, n = 7;
    unsigned mask = wrap_mask(4, K5_NS);
    asm volatile("" : "+s"(wrap), "+v"(aP), "+v"(aM), "+v"(aA));
    if (grp) __builtin_amdgcn_s_setprio(PRIO_B); else __builtin_amdgcn_s_setprio(PRIO_A);
    BAR();
    STAMP_AT(20 );
    BAR();
    STAMP_G(21, 26);
    asm volatile(RA5(0) RA5(1) RP5(0, 0) RP5(0, 1) RP5(1, 0) RP5(1, 1) RP5(2, 0) RP5(2, 1) RP5(3, 0) RP5(3, 1) RP5(4, 0) RP5(4, 1) RP5(5, 0) RP5(5, 1) RW5(6, 0, aM, 0) LG(9)
                 ".p2align 5\n1:\n\t" TRIP5("") TRIP5("") TRIP5("") TRIP5("s_sub_u32 %[n], %[n], 1\n\t") "s_cbranch_scc0 1b\n\t" LG(0) "s_nop 7"
                 : ACC5, [aP] "+v"(aP), [aM] "+v"(aM), [aA] "+v"(aA), [dl] "+s"(dl), [mask] "+s"(mask), [n] "+s"(n) : [wrap] "s"(wrap) : CLOB5);
    f32x4 acc[4][7];
#pragma unroll
    for (int e = 0; e < 112; ++e) acc[e / 28][(e % 28) / 4][e & 3] = e < 32 ? c0[e & 31] : e < 64 ? c1[e & 31] : e < 96 ? c2[e & 31] : c3[e & 15];
    STAMP_G(22, 27);
    auto swi4 = [](const f32x4& gv, const f32x4& uv, unsigned& o0, unsigned& o1) {
      f32x2 ta = gv.xy * -1.4426950408889634f, tb = gv.zw * -1.4426950408889634f, pa = gv.xy * uv.xy, pb = gv.zw * uv.zw;
      ta = (f32x2){__builtin_amdgcn_exp2f(ta[0]), __builtin_amdgcn_exp2f(ta[1])} + 1.f; tb = (f32x2){__builtin_amdgcn_exp2f(tb[0]), __builtin_amdgcn_exp2f(tb[1])} + 1.f;
      pa *= (f32x2){__builtin_amdgcn_rcpf(ta[0]), __builtin_amdgcn_rcpf(ta[1])}; pb *= (f32x2){__builtin_amdgcn_rcpf(tb[0]), __builtin_amdgcn_rcpf(tb[1])};
      o0 = pk2(pa[0], pa[1]); o1 = pk2(pb[0], pb[1]);
    };
    const int lz = lane_id();
    const int gz = lz >> 4, fz = lz & 15;
    const int qb = (tn5 >> 4) * 224, ql = qb + 64 + (tn5 & 15) * 10;
    auto hof = [&](int e, int i) { return (unsigned)((e >> 2) * 131072 + (e & 3) * 256 + fz * 16 + i * 1024); };
    const unsigned ho0 = hof((grp ? ql + 2 : qb + (tn5 & 15) * 4) + 2 * (gz & 1) + (gz >> 1), 0), ho1 = hof(ql + 6 * grp + (gz >> 1), gz & 1), hom = hof(ql + 8 + grp, gz);
    char* hb = ws + WS_HH + (tm5 * 16 + wm * 4) * 1024;
    const int c5 = C5 + tm5 * 4 + (tn5 >> 4) * 2;
    auto tr16 = [](const unsigned (&o)[4]) {
      auto s0 = __builtin_amdgcn_permlane16_swap(o[0], o[2], false, false);
      auto s1 = __builtin_amdgcn_permlane16_swap(o[1], o[3], false, false);
      u32x4 v = {s0[0], s1[0], s0[1], s1[1]}; asm volatile("" : "+v"(v)); return v;
    };
    auto pair = [&](int p, int i) {
      unsigned o[4];
      swi4(acc[i][2 * p], acc[i][2 * p + 1], o[0], o[1]);
      swi4(acc[i + 1][2 * p], acc[i + 1][2 * p + 1], o[2], o[3]);
      return tr16(o);
    };
    u32x4 v[4];
#pragma unroll
    for (int i = 0; i < 4; ++i) {
      unsigned o[4];
      swi4(acc[i][0], acc[i][1], o[0], o[1]);
      swi4(acc[i][2], acc[i][3], o[2], o[3]);
      v[i] = tr16(o);
      if (!grp || i >= BHOLD) st16<true>(hb, ho0 + i * 1024, v[i]);
      if (grp && i == BHOLD - 1) {
#pragma unroll
        for (int k = 0; k < BHOLD; ++k) st16<true>(hb, ho0 + k * 1024, v[k]);
      }
      SCHED0();
    }
    STAMP_G(23, 28);
    v[0] = pair(2, 0);
    if (!grp) {
      wait_vm<0>();
      unsigned arr = 0;
      if (lz == 0) arr = __hip_atomic_fetch_add((LDS_PTR(unsigned))(smem + LDS_ARR), 1u, __ATOMIC_RELAXED, __HIP_MEMORY_SCOPE_WORKGROUP);
      if (__builtin_amdgcn_readfirstlane(arr) == 3) signal(cnt, c5, lz);
    }
    STAMP_G(24, 29);
    v[1] = pair(2, 2);
    { unsigned m[4];
#pragma unroll
      for (int i = 0; i < 4; i += 2) {
        f32x4 gv, uv;
#pragma unroll
        for (int e = 0; e < 4; ++e) {
          auto w = __builtin_amdgcn_permlane32_swap(__float_as_uint(acc[i + (e >> 1)][6][e & 1]), __float_as_uint(acc[i + (e >> 1)][6][2 + (e & 1)]), false, false);
          gv[e] = __uint_as_float(w[0]); uv[e] = __uint_as_float(w[1]);
        }
        swi4(gv, uv, m[i], m[i + 1]);
      }
      auto x0 = __builtin_amdgcn_permlane32_swap(m[0], m[2], false, false);
      auto x1 = __builtin_amdgcn_permlane32_swap(m[1], m[3], false, false);
      const unsigned o[4] = {x0[0], x0[1], x1[0], x1[1]};
      v[2] = tr16(o);
      st16<true>(hb, ho1, v[0]); st16<true>(hb, ho1 + 2048, v[1]); st16<true>(hb, hom, v[2]);
    }
    STAMP_G(25, 30);
    if (grp) {
#pragma unroll
      for (int t = 0; t < NDR; ++t) BAR();
      wait_vm<0>();
      BAR();
      if (wave == 4) signal(cnt, c5 + 1, lz);
      if (wave > 4) return;
      int kpf = 14;
      auto idle = [&]() {
        if (kpf < 14 && __builtin_amdgcn_s_getreg((3 << 11) | (8 << 6) | 7) <= 6) { stouch4((const char*)w2 + w2_unit(kpf, b * 4 + wm)); ++kpf; }
        BAR();
      };
#pragma unroll 1
      for (int t = NDR + 1; t < HOLD - 2; ++t) idle();
      unsigned seen = 16;
      const unsigned* pc = cnt + ((C5 + (tm6 >> 1) * 4 + kh6 * 2 + 1) * 8 + tn6) * 32;
      if (wave == 4) asm volatile("global_load_dword %0, %1, %2 sc1" : "=v"(seen) : "v"(0), "s"(pc) : "memory");
      idle(); idle();
      asm volatile("s_waitcnt vmcnt(0)" : "+v"(seen)::"memory");
      if (__builtin_amdgcn_readfirstlane(seen) < 16) poll<0>(pc, 16);
#pragma unroll 1
      for (int t = HOLD; t < 48 && (kpf < 14 || __builtin_amdgcn_s_getreg((3 << 11) | (8 << 6) | 7)); ++t) idle();
      wait_lgkm0();
      return;
    }
  }
  { const int lane = lane_id(), fr = lane & 15, g = lane >> 4, p4 = fr & 3, row = 4 * g + (fr >> 2);
    const int wm = wave & 1, wn = wave >> 1;
    const int lw0 = row * 128 + (((row >> 1) & 3) * 32) + p4 * 8;
    const int mb0 = tm6 * 8 + wm * 4, t64 = tn6 * 2 + kh6;
    u32x4 rv[4];
#pragma unroll
    for (int i = 0; i < 4; ++i) rv[i] = ld16<true>(ws + WS_X1, ((mb0 + i) * 16 + t64) * 2048 + wn * 1024 + lane * 16);
    f32x4 acc[4][4];
#pragma unroll
    for (int i = 0; i < 4; ++i)
#pragma unroll
      for (int j = 0; j < 4; ++j) { acc[i][j] = (f32x4){0.f, 0.f, 0.f, 0.f}; asm volatile("" : "+v"(acc[i][j])); }
    struct Fr { bf16x8 a[4]; bf16x4 w[4][2]; };
    const LDS_PTR(char) aw0 = smem + (lw0 ^ (wn * 64)); const LDS_PTR(char) aw1 = smem + (lw0 ^ (wn * 64 + 32));
    const LDS_PTR(char) aa = smem + K6_A + wm * 4096 + lane * 16;
    auto rd1 = [&](Fr& f, int r) {
      if (r < 2) f.w[0][r] = ld_tr4(aw0 + r * 2048);
      else if (r < 4) f.a[r - 2] = *(const LDS_PTR(bf16x8))(aa + (r - 2) * 1024);
      else if (r < 10) { const int j = (r - 2) >> 1; f.w[j][r & 1] = ld_tr4(((j & 1) ? aw1 : aw0) + (j >> 1) * 4096 + (r & 1) * 2048); }
      else f.a[r - 8] = *(const LDS_PTR(bf16x8))(aa + (r - 8) * 1024);
    };
    int dl, da, wrap = -(K6_NW - 1) * 8192, wrapa = -(K6_NA - 1) * 8192;
    unsigned mask = wrap_mask(1, K6_NW), maska = wrap_mask(1, K6_NA);
    Fr fA, fB;
    asm volatile("" : "+s"(wrap), "+s"(wrapa), "+v"(aw0), "+v"(aw1), "+v"(aa));
    wait_vm<0>();
    BAR();
    STAMP_AT(32 );
#pragma unroll
    for (int r = 0; r < 12; ++r) { SCHED0(); rd1(fA, r); }
    SCHED0();
    aw0 += 8192; aw1 += 8192; aa += 8192;
    wait_lgkm<6>();
    BAR();
    auto trip = [&](const Fr& cur, Fr& nxt) {
      SCHED0();
#pragma unroll
      for (int m = 0; m < 16; ++m) {
        const int j = (m & 7) >> 1, i = (m >> 3) * 2 + ((m ^ j) & 1);
        if (m == 2) mfma_sel<8192>(acc[i][j], cat8(cur.w[j][0], cur.w[j][1]), cur.a[i], mask, dl, wrap);
        else if (m == 6) mfma_sel<8192>(acc[i][j], cat8(cur.w[j][0], cur.w[j][1]), cur.a[i], maska, da, wrapa);
        else mfma_acc(acc[i][j], cat8(cur.w[j][0], cur.w[j][1]), cur.a[i]);
        SCHED0();
        if (m < 12) { rd1(nxt, m); SCHED0(); }
        if (m == 3) wait_lgkm<4>();
        if (m == 12) { aw0 += dl; aw1 += dl; asm volatile("" : "+v"(aw0), "+v"(aw1)); }
        if (m == 13) { aa += da; asm volatile("" : "+v"(aa)); }
        if (m == 14) wait_lgkm<6>();
        if (m == 15) BAR();
        SCHED0();
      }
    };
#pragma unroll 1
    for (int h = 0; h < 2; ++h) {
#pragma unroll 1
      for (int t = 0; t < 7; ++t) { trip(fA, fB); trip(fB, fA); trip(fA, fB); trip(fB, fA); }
      mask = wrap_mask(29, K6_NW); maska = wrap_mask(29, K6_NA);
    }
    mfma_done(acc);
    STAMP_AT(34 );
    const int slot = wm * 2 + wn, pb = (b ^ 4) * 4 + slot;
    unsigned* flags = (unsigned*)(ws + WS_FLG);
    { char* pp = ws + WS_PART + (size_t)(b * 4 + slot) * 4096;
#pragma unroll
      for (int i = 0; i < 4; ++i)
        st16<true>(pp, i * 1024 + lane * 16, (u32x4){pk2(acc[i][0][0], acc[i][0][1]), pk2(acc[i][0][2], acc[i][0][3]), pk2(acc[i][1][0], acc[i][1][1]), pk2(acc[i][1][2], acc[i][1][3])});
      wait_vm<0>();
      STAMP_AT(35 );
      if (lane == 0) __hip_atomic_store(flags + (b * 4 + slot) * 16, 1u, __ATOMIC_RELAXED, __HIP_MEMORY_SCOPE_AGENT);
    }
    { const LDS_PTR(float) vec = (const LDS_PTR(float))(smem + LDS_VEC) + 192 + wn * 32 + 4 * g;
      const f32x4 gv0 = *(const LDS_PTR(f32x4))vec, gv1 = *(const LDS_PTR(f32x4))(vec + 16);
      poll<POLL6>(flags + pb * 16, 1);
      STAMP_AT(36 );
      const char* pp = ws + WS_PART + (size_t)pb * 4096;
      u32x4 z[4];
#pragma unroll
      for (int i = 0; i < 4; ++i) z[i] = ld16<true>(pp, i * 1024 + lane * 16);
#pragma unroll
      for (int i = 0; i < 4; ++i) {
        const f32x4 v0 = (acc[i][2] + (f32x4){lo2f(z[i][0]), hi2f(z[i][0]), lo2f(z[i][1]), hi2f(z[i][1])}) * gv0;
        const f32x4 v1 = (acc[i][3] + (f32x4){lo2f(z[i][2]), hi2f(z[i][2]), lo2f(z[i][3]), hi2f(z[i][3])}) * gv1;
        const unsigned o0 = pk2(lo2f(rv[i][0]) + v0[0], hi2f(rv[i][0]) + v0[1]), o1 = pk2(lo2f(rv[i][1]) + v0[2], hi2f(rv[i][1]) + v0[3]);
        const unsigned o2 = pk2(lo2f(rv[i][2]) + v1[0], hi2f(rv[i][2]) + v1[1]), o3 = pk2(lo2f(rv[i][3]) + v1[2], hi2f(rv[i][3]) + v1[3]);
        auto s0 = __builtin_amdgcn_permlane16_swap(o0, o2, false, false);
        auto s1 = __builtin_amdgcn_permlane16_swap(o1, o3, false, false);
        st16<false>(out, (((mb0 + i) * 16 + fr) * D_ + t64 * 64 + wn * 32 + (g & 1) * 16 + (g >> 1) * 8) * 2, (u32x4){s0[0], s1[0], s0[1], s1[1]});
      }
    }
    STAMP_AT(37 );
  }
}

extern "C" void kernel_launch(void* const* d_in, const int* in_sizes, int n_in, void* d_out, int out_size, void* d_ws,
                              size_t ws_size, hipStream_t stream) {
  const u16* x = (const u16*)d_in[0];
  const u16* wqkv = (const u16*)d_in[1];
  const u16* wo = (const u16*)d_in[2];
  const u16* w1 = (const u16*)d_in[4];
  const u16* w3 = (const u16*)d_in[5];
  const u16* w2 = (const u16*)d_in[6];
  const u16* c = (const u16*)d_in[7];
  const u16* wmod = (const u16*)d_in[8];
  (void)hipFuncSetAttribute((const void*)k2_block, hipFuncAttributeMaxDynamicSharedMemorySize, MEGA_LDS);
  char* ws = (char*)d_ws;
  k2_block<<<256, 768, MEGA_LDS, stream>>>(x, wqkv, wo, w1, w3, w2, c, wmod, (u16*)d_out, ws);
}
```

```cpp
#include <hip/hip_runtime.h>
#include <stdint.h>
#include <type_traits>
#pragma clang diagnostic ignored "-Winline-asm"

typedef unsigned short u16;
typedef short bf16x8 __attribute__((ext_vector_type(8)));
typedef short bf16x4 __attribute__((ext_vector_type(4)));
typedef float f32x4 __attribute__((ext_vector_type(4)));
typedef float f32x2 __attribute__((ext_vector_type(2)));
typedef unsigned int u32x4 __attribute__((ext_vector_type(4)));
typedef unsigned int u32x2 __attribute__((ext_vector_type(2)));
#define LDS_PTR(T) __attribute__((address_space(3))) T*
#define BAR() do { asm volatile("" ::: "memory"); __builtin_amdgcn_s_barrier(); asm volatile("" ::: "memory"); } while (0)
#define SCHED0() __builtin_amdgcn_sched_barrier(0)
template <int N> using IC = std::integral_constant<int, N>;
typedef std::integral_constant<bool, true> T_;
typedef std::integral_constant<bool, false> F__;

constexpr int S_ = 2048, D_ = 1024, F_ = 3584;
#define STAT_DECL
#define STAT_WRAP(x) do { unsigned spin_ = 0; x; } while (0)
#define STAT_OUT(k)
#define STAMP_AT(i)
#define STAMP_G(ia, ib)
#define STAMP_L(i)

__device__ __forceinline__ float bf2f(u16 v) { return __uint_as_float(((unsigned)v) << 16); }
__device__ __forceinline__ unsigned pk2(float lo, float hi) {
  typedef __bf16 bf2 __attribute__((ext_vector_type(2)));
  f32x2 v = {lo, hi}; bf2 r = __builtin_convertvector(v, bf2); return __builtin_bit_cast(unsigned, r);
}
__device__ __forceinline__ float lo2f(unsigned u) { return __uint_as_float(u << 16); }
__device__ __forceinline__ float hi2f(unsigned u) { return __uint_as_float(u & 0xffff0000u); }
__device__ __forceinline__ float rbf(float v) { return lo2f(pk2(v, 0.f)); }
#define glds16(sbase, voff, lds_off, imm) \
  asm volatile("s_add_u32 m0, %2, %3\n\ts_nop 0\n\tglobal_load_lds_dwordx4 %0, %1" ::"v"(voff), "s"(sbase), "s"(lds_off), "n"(imm) : "memory", "scc")
#define glds16s(sbase, voff, lds_off, imm) \
  asm volatile("s_add_u32 m0, %2, %3\n\ts_nop 0\n\tglobal_load_lds_dwordx4 %0, %1 sc1" ::"v"(voff), "s"(sbase), "s"(lds_off), "n"(imm) : "memory", "scc")
#define GLDS4(SC) "s_mov_b32 m0, %6\n\ts_nop 0\n\tglobal_load_lds_dwordx4 %0, %4" SC "\n\tglobal_load_lds_dwordx4 %1, %5 offset:1024" SC \
                  "\n\tglobal_load_lds_dwordx4 %2, %4 offset:2048" SC "\n\tglobal_load_lds_dwordx4 %3, %5 offset:3072" SC
template <bool SC1> __device__ __forceinline__ void glds16x4(const void* s0, const void* s1, const unsigned (&v)[4], unsigned lds) {
  if (SC1) asm volatile(GLDS4(" sc1") ::"v"(v[0]), "v"(v[1]), "v"(v[2]), "v"(v[3]), "s"(s0), "s"(s1), "s"(lds) : "memory");
  else asm volatile(GLDS4("") ::"v"(v[0]), "v"(v[1]), "v"(v[2]), "v"(v[3]), "s"(s0), "s"(s1), "s"(lds) : "memory");
}
__device__ __forceinline__ void glds16x2(const void* s0, unsigned v0, unsigned v1, unsigned lds) {
  asm volatile("s_mov_b32 m0, %3\n\ts_nop 0\n\tglobal_load_lds_dwordx4 %0, %2\n\tglobal_load_lds_dwordx4 %1, %2 offset:1024" ::"v"(v0), "v"(v1), "s"(s0), "s"(lds) : "memory");
}
__device__ __forceinline__ void glds4(const void* sbase, unsigned voff, unsigned lds_off) {
  asm volatile("s_mov_b32 m0, %2\n\ts_nop 0\n\tglobal_load_lds_dword %0, %1" ::"v"(voff), "s"(sbase), "s"(lds_off) : "memory");
}
__device__ __forceinline__ void stouch4(const void* p) {
  asm volatile("s_load_dwordx2 s[100:101], %0, 0x3c\n\ts_load_dwordx2 s[100:101], %0, 0xbc\n\ts_load_dwordx2 s[100:101], %0, 0x13c\n\ts_load_dwordx2 s[100:101], %0, 0x1bc"
               ::"s"(p) : "memory", "s100", "s101");
}
template <bool SC1> __device__ __forceinline__ void st16(void* sbase, unsigned voff, u32x4 v) {
  __builtin_amdgcn_raw_buffer_store_b128(v, __builtin_amdgcn_make_buffer_rsrc(sbase, 0, 0x7fffffff, 0x00020000), voff, 0, SC1 ? 16 : 0);
}
template <bool SC1> __device__ __forceinline__ u32x4 ld16(const void* sbase, unsigned voff) {
  return __builtin_amdgcn_raw_buffer_load_b128(__builtin_amdgcn_make_buffer_rsrc((void*)sbase, 0, 0x7fffffff, 0x00020000), voff, 0, SC1 ? 16 : 0);
}
template <int N> __device__ __forceinline__ void wait_vm() { asm volatile("s_waitcnt vmcnt(%0)" ::"n"(N) : "memory"); }
__device__ __forceinline__ void wait_lgkm0() { asm volatile("" ::: "memory"); __builtin_amdgcn_s_waitcnt(0xC07F); asm volatile("" ::: "memory"); }
__device__ __forceinline__ bf16x4 ld_tr4(const LDS_PTR(char) p) { return __builtin_amdgcn_ds_read_tr16_b64_v4i16((LDS_PTR(bf16x4))p); }
__device__ __forceinline__ bf16x8 cat8(bf16x4 lo, bf16x4 hi) { return __builtin_shufflevector(lo, hi, 0, 1, 2, 3, 4, 5, 6, 7); }
__device__ __forceinline__ void mfma_acc(f32x4& acc, bf16x8 a, bf16x8 b) { asm volatile("v_mfma_f32_16x16x32_bf16 %0, %1, %2, %0" : "+v"(acc) : "v"(a), "v"(b)); }
template <int STEP> __device__ __forceinline__ void mfma_sel(f32x4& acc, bf16x8 a, bf16x8 b, unsigned& mask, int& dl, int wrap) {
  asm volatile("s_add_u32 %1, %1, %1\n\tv_mfma_f32_16x16x32_bf16 %0, %3, %4, %0\n\ts_cselect_b32 %2, %5, %6" : "+v"(acc), "+s"(mask), "=s"(dl) : "v"(a), "v"(b), "s"(wrap), "n"(STEP) : "scc");
}
constexpr unsigned wrap_mask(int first, int ns) { unsigned m = 0; for (int t = 0; t < 32; ++t) if ((first + t) % ns == ns - 1) m |= 0x80000000u >> t; return m; }
template <int N> __device__ __forceinline__ void wait_lgkm() { __builtin_amdgcn_s_waitcnt(0xC07F | (N << 8)); }
template <int R, int C> __device__ __forceinline__ void mfma_done(f32x4 (&acc)[R][C]) {
  asm volatile("s_nop 9");
#pragma unroll
  for (int i = 0; i < R; ++i)
#pragma unroll
    for (int j = 0; j < C; ++j) asm volatile("" : "+v"(acc[i][j]));
}
__device__ __forceinline__ int lane_id() { int l; asm volatile("v_mbcnt_lo_u32_b32 %0, -1, 0\n\tv_mbcnt_hi_u32_b32 %0, -1, %0" : "=v"(l)); return l; }
__device__ __forceinline__ float xsum16_32(float v) {
  auto a = __builtin_amdgcn_permlane16_swap(__float_as_uint(v), __float_as_uint(v), false, false); v = __uint_as_float(a[0]) + __uint_as_float(a[1]);
  auto c = __builtin_amdgcn_permlane32_swap(__float_as_uint(v), __float_as_uint(v), false, false); return __uint_as_float(c[0]) + __uint_as_float(c[1]);
}
__device__ __forceinline__ float xmax16_32(float v) {
  auto a = __builtin_amdgcn_permlane16_swap(__float_as_uint(v), __float_as_uint(v), false, false); v = fmaxf(__uint_as_float(a[0]), __uint_as_float(a[1]));
  auto c = __builtin_amdgcn_permlane32_swap(__float_as_uint(v), __float_as_uint(v), false, false); return fmaxf(__uint_as_float(c[0]), __uint_as_float(c[1]));
}

__device__ __forceinline__ float ln_rstd(float var) { return __builtin_amdgcn_rcpf(rbf(__builtin_amdgcn_sqrtf(rbf(rbf(var) + 1e-5f)))); }

constexpr size_t WS_MOD = 0;
constexpr size_t WS_EPO = 64 << 10, WS_FL1 = 96 << 10;
constexpr size_t WS_CTL = 128 << 10;
constexpr int C2 = 0, C3 = 16, C4 = 32, C5 = 48, CM = 80;
constexpr size_t WS_FLG = WS_CTL + (88 << 10), CTL_BYTES = 152 << 10;
constexpr size_t WS_XCH = 512 << 10;
constexpr size_t WS_MOD2 = 1 << 20;
constexpr size_t WS_H = 2 << 20, WS_O = WS_H + (4 << 20), WS_X1 = WS_O + (4 << 20), WS_H2 = WS_X1 + (4 << 20), WS_HH = WS_H2 + (4 << 20),
                 WS_PART = WS_HH + (size_t)S_ * F_ * 2;

template <int NL> __device__ __forceinline__ f32x2 slab_sum(const unsigned (&mc)[NL / 2], const u32x4 (&mw)[NL]) {
  f32x2 a2[4] = {};
#pragma unroll
  for (int i = 0; i < NL; ++i) {
    const float cv = (i & 1) ? hi2f(mc[i >> 1]) : lo2f(mc[i >> 1]);
    const float sv = rbf(cv * __builtin_amdgcn_rcpf(1.f + __builtin_amdgcn_exp2f(cv * -1.4426950408889634f)));
#pragma unroll
    for (int j = 0; j < 4; ++j) a2[j] += (f32x2){lo2f(mw[i][j]), hi2f(mw[i][j])} * sv;
  }
  float acc[8] = {a2[0][0], a2[0][1], a2[1][0], a2[1][1], a2[2][0], a2[2][1], a2[3][0], a2[3][1]};
#pragma unroll
  for (int j = 0; j < 4; ++j) { auto a = __builtin_amdgcn_permlane32_swap(__float_as_uint(acc[j]), __float_as_uint(acc[j + 4]), false, false); acc[j] = __uint_as_float(a[0]) + __uint_as_float(a[1]); }
#pragma unroll
  for (int j = 0; j < 2; ++j) { auto a = __builtin_amdgcn_permlane16_swap(__float_as_uint(acc[j]), __float_as_uint(acc[j + 2]), false, false); acc[j] = __uint_as_float(a[0]) + __uint_as_float(a[1]); }
#pragma unroll
  for (int j = 0; j < 2; ++j) acc[j] += __uint_as_float(__builtin_amdgcn_mov_dpp(__float_as_uint(acc[j]), 0x128, 0xf, 0xf, false));
  return (f32x2){acc[0], acc[1]};
}

constexpr int K3_B = 18432, K3_STG = 18432 + 24576;
constexpr int K4_RED = 0;
constexpr int K5_G = 16384, K5_U = 16384 + 7168 + 16, K5_STG = 30736, K5_NS = 5;
constexpr int K6_NW = 12, K6_NA = 7, K6_A = K6_NW * 8192;
constexpr int LDS_VEC = 158720, LDS_DUMMY = LDS_VEC + 1024, LDS_ARR = LDS_DUMMY + 256, MEGA_LDS = LDS_ARR + 64;
constexpr unsigned SPIN_CAP = 1u << 19;
#define NB5 4
#define PD4 12
#define HELP 0
#define HELP_HI
#define HELP_LO
#define T3_SLEEP 3
#define PD5 44
#define PRIO_A 2
#define PRIO_B 1
#define W2PF 4
#define W2OFF 2
#define DR5 1
#define BHOLD 0
#define PS5 4
#define KL5 40
#define TS5 3
#define PRE6 4
#define SKIP6 0x7cull
static_assert(__builtin_popcountll(SKIP6) == 5, "44 stages of W are issued behind 49 barriers");
#define VB3 0
#define KB3 0
#define V2 1
#define LATE41 0
#define PF0 6
#define PFD 0
#define T6 0
#define SLB 15
#define HOLD 11
#define NDR 2
#define EPI_PRIO 0
#define POLL5 8
#define POLL6 2

__device__ __forceinline__ void signal(unsigned* cnt, int c, int lane) {
  if (lane < 8) __hip_atomic_fetch_add(cnt + (c * 8 + lane) * 32, 1u, __ATOMIC_RELAXED, __HIP_MEMORY_SCOPE_AGENT);
}
template <int D0, int D1 = 1> __device__ __forceinline__ void poll(const unsigned* cnt, unsigned want) {
  if (D0) __builtin_amdgcn_s_sleep(D0);
#pragma unroll 1
  for (unsigned spins = 0; spins < SPIN_CAP; ++spins) {
    if (__builtin_amdgcn_ballot_w64(__hip_atomic_load(cnt, __ATOMIC_RELAXED, __HIP_MEMORY_SCOPE_AGENT) < want) == 0) break;
    __builtin_amdgcn_s_sleep(D1);
  }
  __builtin_amdgcn_fence(__ATOMIC_ACQUIRE, "wavefront");
}

typedef float f32x32 __attribute__((ext_vector_type(32)));
typedef float f32x16 __attribute__((ext_vector_type(16)));
#define M5(i, j) "v_mfma_f32_16x16x32_bf16 v[28*" #i "+4*" #j ":28*" #i "+4*" #j "+3], v[128+4*" #j ":131+4*" #j "], v[112+4*" #i ":115+4*" #i "], v[28*" #i "+4*" #j ":28*" #i "+4*" #j "+3]\n\t"
#define RA5(i) "ds_read_b128 v[112+4*" #i ":115+4*" #i "], %[aA] offset:1024*" #i "\n\t"
#define RW5(j, h, a, o) "ds_read_b64_tr_b16 v[128+4*" #j "+2*" #h ":129+4*" #j "+2*" #h "], %[" #a "] offset:" #o "\n\t"
#define ST5(a) "v_add_u32 %[" #a "], %[dl], %[" #a "]\n\t"
#define SEL5A "s_add_u32 %[mask], %[mask], %[mask]\n\t"
#define SEL5B "s_cselect_b32 %[dl], %[wrap], 30736\n\t"
#define LG(n) "s_waitcnt lgkmcnt(" #n ")\n\t"
#define RP5(j, h) RW5(j, h, aP, 32*(j/2)+7184*(j&1)+3584*h)
#define TRIP5(X)                                                                                                                                        \
  M5(0, 0) RW5(6, 1, aM, 3584) M5(1, 0) RA5(2) ST5(aM) M5(1, 1) RA5(3) ST5(aP) M5(0, 1) ST5(aA) LG(6) M5(0, 2) M5(1, 2) SEL5A M5(1, 3) SEL5B              \
  M5(0, 3) M5(0, 4) M5(1, 4) LG(0) M5(1, 5) M5(0, 5) "s_barrier\n\t" M5(0, 6) RA5(0) M5(1, 6) RA5(1)                                                      \
  M5(2, 0) X M5(3, 0) RP5(0, 0) M5(3, 1) RP5(0, 1) M5(2, 1) RP5(1, 0) M5(2, 2) RP5(1, 1) M5(3, 2) RP5(2, 0) M5(3, 3) RP5(2, 1) M5(2, 3) RP5(3, 0)          \
  M5(2, 4) RP5(3, 1) M5(3, 4) RP5(4, 0) M5(3, 5) RP5(4, 1) M5(2, 5) RP5(5, 0) M5(2, 6) RP5(5, 1) LG(8) M5(3, 6) RW5(6, 0, aM, 0)
#define ACC5 "+{v[0:31]}"(c0), "+{v[32:63]}"(c1), "+{v[64:95]}"(c2), "+{v[96:111]}"(c3)
#define CLOB5 "memory", "scc", "v112", "v113", "v114", "v115", "v116", "v117", "v118", "v119", "v120", "v121", "v122", "v123", "v124", "v125", "v126", "v127", "v128", "v129", "v130", "v131", "v132", "v133", \
              "v134", "v135", "v136", "v137", "v138", "v139", "v140", "v141", "v142", "v143", "v144", "v145", "v146", "v147", "v148", "v149", "v150", "v151", "v152", "v153", "v154", "v155"
static_assert(K5_STG == 30736 && K5_U - K5_G == 7184, "TRIP5");

#define C3(i, j) "v[24*" #i "+4*" #j "-12*(" #i "/4):24*" #i "+4*" #j "-12*(" #i "/4)+3]"
#define M3(i, j) "v_mfma_f32_16x16x32_bf16 " C3(i, j) ", v[124+4*" #j ":127+4*" #j "], v[104+4*" #i ":107+4*" #i "], " C3(i, j) "\n\t"
#define RA3(i, u) "ds_read_b128 v[104+4*" #i ":107+4*" #i "], %[aA] offset:1024*" #i "+9216*" #u "\n\t"
#define RH3(u) "ds_read_b128 v[120:123], %[aH] offset:9216*" #u "\n\t"
#define RW3(j, h, a, u) "ds_read_b64_tr_b16 v[124+4*" #j "+2*" #h ":125+4*" #j "+2*" #h "], %[" #a "] offset:8192*(" #j "/2)+2048*" #h "+4096*" #u "\n\t"
#define ST3(a, d) "v_add_u32 %[" #a "], " #d ", %[" #a "]\n\t"
#define H2_3(u, S0, S1, S2, B) M3(0, 0) RW3(5, 1, a1, u) M3(1, 0) RA3(2, u) M3(1, 1) RA3(3, u) S0 M3(0, 1) LG(7) S1 M3(0, 2) S2 M3(1, 2) M3(1, 3) M3(0, 3) LG(2) M3(0, 4) M3(1, 4) LG(0) B
#define H1_3(n, X)                                                                                                                                      \
  M3(1, 5) RA3(1, n) M3(0, 5) RA3(0, n) M3(2, 0) X M3(3, 0) RW3(0, 0, a0, n) M3(3, 1) RW3(0, 1, a0, n) M3(2, 1) RW3(1, 0, a1, n) M3(2, 2) RW3(1, 1, a1, n) \
  M3(3, 2) RW3(2, 0, a1, n) M3(3, 3) RW3(2, 1, a1, n) M3(2, 3) M3(4, 3) RW3(3, 0, a0, n) M3(4, 4) RW3(3, 1, a0, n) M3(2, 4) RH3(n)                         \
  M3(3, 4) RW3(4, 0, a0, n) M3(3, 5) RW3(4, 1, a0, n) LG(7) M3(2, 5) RW3(5, 0, a1, n)
#define STAGE3(d, X) H2_3(0, , , , ) H1_3(1, ) H2_3(1, ST3(a0, d), ST3(aA, d), ST3(a1, d) ST3(aH, d), "s_barrier\n\t") H1_3(0, X)
#define CV3(i, j, t) "v_cvt_pk_bf16_f32 v[104+2*" #t "], v[24*" #i "+4*" #j "-12*(" #i "/4)], v[24*" #i "+4*" #j "-12*(" #i "/4)+1]\n\tv_cvt_pk_bf16_f32 v[105+2*" #t "], v[24*" #i "+4*" #j "-12*(" #i "/4)+2], v[24*" #i "+4*" #j "-12*(" #i "/4)+3]\n\t"
#define WR3(i, t, a, o) "ds_write_b64 %[" #a "], v[104+2*" #t ":105+2*" #t "] offset:2048*" #i "+" #o "\n\t"
#define CW3(i, j, t, a, o) CV3(i, j, t) WR3(i, t, a, o)
#define V8(a) "v" #a "0", "v" #a "1", "v" #a "2", "v" #a "3", "v" #a "4", "v" #a "5", "v" #a "6", "v" #a "7"
#define V10(a) V8(a), "v" #a "8", "v" #a "9"
static_assert(K3_STG == 43008, "STAGE3");
__device__ __forceinline__ int w2_unit(int k, int P) {
  const int n = (k < 4 ? k : k - 4) * 512 + (P >> 1), t = k < 4 ? n >> 7 : (n * 52429) >> 24, r = k < 4 ? n & 127 : n - t * 320 + 128;
  return (P & 1) * 1792 * 2048 + t * 112 * 2048 + r * 512;
}
__global__ __launch_bounds__(768, 3) __attribute__((amdgpu_num_sgpr(98))) void k2_block(const u16* __restrict__ x, const u16* __restrict__ wqkv, const u16* __restrict__ wo,
                                                   const u16* __restrict__ w1, const u16* __restrict__ w3, const u16* __restrict__ w2,
                                                   const u16* __restrict__ cnd, const u16* __restrict__ wmod, u16* __restrict__ out, char* __restrict__ ws) {
  extern __shared__ __attribute__((aligned(16))) char smem_[];
  LDS_PTR(char) smem = (LDS_PTR(char))smem_;
  const int tid = threadIdx.x;
  int lane = tid & 63;
  const int wave = __builtin_amdgcn_readfirstlane(tid >> 6);
  const unsigned lds_base = (unsigned)(uintptr_t)smem;
  const int b = blockIdx.x, xc = b & 7, ii = b >> 3;
  const int tm3 = (xc & 1) * 8 + (ii & 7), tn3 = (xc >> 1) * 4 + (ii >> 3);
  const int tm4 = (xc >> 1) * 4 + (ii & 3), tn4 = (xc & 1) * 8 + (ii >> 2);
  const int tm5 = (xc & 1) * 4 + (ii & 3), tn5 = (xc >> 1) * 8 + (ii >> 2);
  const int kh6 = xc >> 2, tm6 = tm5 * 2 + ((ii >> 2) & 1), tn6 = tn3 & 7;
  const int u1 = (b >> 4) * 8 + xc;
  unsigned* cnt = (unsigned*)(ws + WS_CTL);
  unsigned long long* fl1 = (unsigned long long*)(ws + WS_FL1);
  STAMP_AT(0 );

  if (wave >= 8) {
    const int lw = wave - 8, P = b * 4 + lw, lane = lane_id();
    const bool isA = lw < 2;
    asm volatile("" ::"s"(wqkv), "s"(wo), "s"(w1), "s"(w3), "s"(w2), "s"(ws));
    int kpf = 99;
    auto touch = [&]() {
      if (kpf < 28 && __builtin_amdgcn_s_getreg((3 << 11) | (8 << 6) | 7) <= 6) {
        stouch4(kpf < -4 ? (const char*)wqkv + ((kpf + 16) * 4096 + P * 4) * 128 : kpf < 0 ? (const char*)wo + ((kpf + 4) * 4096 + P * 4) * 128 : (const char*)((P & 1) ? w3 : w1) + (kpf * 2048 + (P >> 1) * 4) * 128);
        ++kpf;
      }
    };
    const char* epo = ws + WS_EPO;
    const unsigned swz = ((lane & 7) ^ ((lane >> 3) & 6)) * 16;
    const int cg = lw * 16 + (lw < 3 ? tn4 : tn6 * 2 + kh6);
    auto issue4 = [&](int s, unsigned slot) {
      const char* p = (const char*)wo + tn4 * 128 + (lw * 32 + s * 128) * 2048;
      unsigned v4[4];
#pragma unroll
      for (int i = 0; i < 4; ++i) v4[i] = (lane >> 3) * 2048 + swz + i * 15360;
      glds16x4<false>(p, p, v4, lds_base + slot + lw * 4096);
    };
    auto run3 = [&](auto isA_) {
      constexpr bool A_ = decltype(isA_)::value; constexpr int N = A_ ? 9 : 12;
      const char* sp = A_ ? ws + WS_H + lw * 131072 + (tm3 * 8 - 1) * 1024 : (const char*)wqkv + tn3 * 128;
      unsigned vo[3][4];
#pragma unroll
      for (int i = 0; i < 12; ++i) {
        const int tb = (lw - 2) * 12 + i;
        vo[i >> 2][i & 3] = A_ ? lane * 16 + (i < 8 ? 1024 + (i >> 2) * 4096 : tm3 ? 0 : 1024) : ((tb & 7) * 8 + (lane >> 3)) * 6144 + (tb >> 3) * 2048 - (i & 3) * 1024 + swz;
      }
      const unsigned d0 = lds_base + (A_ ? lw * 9216 : K3_B + (lw - 2) * 12288);
      auto issue = [&](unsigned st) {
        if (A_) { glds16x4<true>(sp, sp, vo[0], d0 + st + 1024); glds16s(sp, vo[2][0], d0 + st, 0); glds16x4<true>(sp, sp, vo[1], d0 + st + 5120); }
        else {
#pragma unroll
          for (int i = 0; i < 3; ++i) glds16x4<false>(sp, sp, vo[i], d0 + st + i * 4096);
        }
        sp += A_ ? 262144 : 64 * 6144;
      };
      { unsigned long long ep;
        asm volatile("s_load_dwordx2 %0, %1, 0x0\n\ts_waitcnt lgkmcnt(0)" : "=s"(ep) : "s"(epo) : "memory");
        ep += 1;
        if (lw == 3) { *(LDS_PTR(u32x4))(smem + LDS_ARR) = (u32x4){0u, 0u, 0u, 0u}; wait_lgkm0(); }
        if (lw == 1) {
#pragma unroll
          for (int k = 0; k < 3; ++k) {
            const int i = b * 166 + (k * 64 + lane < 166 ? k * 64 + lane : 0);
            st16<true>(ws, i < 32768 ? (unsigned)WS_XCH + i * 16 : (unsigned)WS_CTL + (i - 32768) * 16, i < 32768 ? (u32x4){~0u, ~0u, ~0u, ~0u} : (u32x4){0u, 0u, 0u, 0u});
          }
        }
        BAR();
        auto flag = [&](int idx) { wait_vm<0>(); if (lane < 8) __hip_atomic_store(fl1 + lane * 256 + idx, ep, __ATOMIC_RELAXED, __HIP_MEMORY_SCOPE_AGENT); };
        if (lw == 1 && (b & 8)) {
          const int kq = u1 & 3, cgp = u1 >> 2;
          float sm = 0.f;
#pragma unroll
          for (int r = 0; r < 8; ++r) sm += *(const LDS_PTR(float))(smem + 8192 + (r * 64 + lane) * 4);
          __hip_atomic_store((unsigned*)(ws + WS_MOD) + kq * 2048 + cgp * 64 + lane, __float_as_uint(sm), __ATOMIC_RELAXED, __HIP_MEMORY_SCOPE_AGENT);
          flag(((cgp >> 3) & 1) * 64 + (cgp >> 4) * 32 + (cgp & 7) * 4 + kq);
        }
        BAR();
        if (lw == 1 && !(b & 8)) flag(128 + u1);
        if (lw < 2) {
          if (lw == 1) BAR();
#pragma unroll 1
          for (unsigned spins = 0; spins < SPIN_CAP; ++spins) {
            bool ok = true;
#pragma unroll
            for (int k = 0; k < 4; ++k) ok &= __hip_atomic_load(fl1 + xc * 256 + (lw ? k : (b >> 3) & 1) * 64 + lane, __ATOMIC_RELAXED, __HIP_MEMORY_SCOPE_AGENT) == ep;
            if (__builtin_amdgcn_ballot_w64(!ok) == 0) break;
            __builtin_amdgcn_s_sleep(4);
          }
          if (lw == 0) BAR();
          else if (b == 0 && lane == 0) __hip_atomic_store((unsigned long long*)(ws + WS_EPO), ep, __ATOMIC_RELAXED, __HIP_MEMORY_SCOPE_AGENT);
        } else BAR();
      }
      BAR();
      kpf = V2 == 2 ? 2 * PF0 - 16 : -4;
      if (lw == 0) {
        signal(cnt, C2 + (u1 >> 3), lane);
        if (V2 == 0) glds4(wqkv, (32768 + b * 64 + lane) * 128, lds_base + LDS_DUMMY);
        poll<8>(cnt + ((C2 + ((lane & 1) && tm3 ? tm3 - 1 : tm3)) * 8 + xc) * 32, 16);
      }
      if (!A_) { issue(0); issue(K3_STG); }
      touch();
      BAR();
      if (A_) { issue(0); issue(K3_STG); }
      wait_vm<N>();
      BAR();
      issue(2 * K3_STG);
      unsigned st = 0;
#pragma unroll 1
      for (int j = 0; j < 13; ++j) {
        wait_vm<N>();
        BAR();
        issue(st);
        st = st == 2 * K3_STG ? 0 : st + K3_STG;
        touch();
      }
      wait_vm<N>();
      BAR();
      issue4(0, 57344);
      if (kpf < 0) kpf = 0;
      if (VB3) { glds4((P & 1) ? w3 : w1, ((kpf + ((lane >> 2) < VB3 ? lane >> 2 : 0)) * 2048 + (P >> 1) * 4 + (lane & 3)) * 128, lds_base + LDS_DUMMY); kpf += VB3; }
      wait_vm<VB3 ? 5 : 4>();
    };
    if (isA) run3(T_()); else run3(F__());
    { auto issue = issue4;
      BAR();
      BAR();
      if (!LATE41) issue(1, 57344 + 16384);
      BAR();
      if (LATE41) issue(1, 57344 + 16384);
      issue(2, 57344 + 2 * 16384); issue(3, 57344 + 3 * 16384); issue(4, 57344 + 4 * 16384);
      BAR();
      if (lw == 0) signal(cnt, C3 + tm3, lane);
      issue(5, 57344 + 5 * 16384); issue(6, 0); issue(7, 16384);
      if (lw == 0) poll<16>(cnt + ((C3 + tm4) * 8 + xc) * 32, 16);
      wait_vm<0>();
      BAR();
      BAR();
      poll<0, 4>(cnt + (CM * 8 + cg) * 32, 4);
      f32x4 pv[4];
#pragma unroll
      for (int i = 0; i < 4; ++i) pv[i] = __builtin_bit_cast(f32x4, ld16<true>(ws + WS_MOD2, (((lane >> 4) * 4 + i) * 4096 + cg * 64 + (lane & 15) * 4) * 4));
      f32x4 v = (pv[0] + pv[1]) + (pv[2] + pv[3]);
#pragma unroll
      for (int e = 0; e < 4; ++e) { v[e] = rbf(xsum16_32(v[e])); if (lw == 2) v[e] = rbf(1.f + v[e]); }
      *(LDS_PTR(f32x4))(smem + LDS_VEC + lw * 256 + (lane & 15) * 16) = v;
      wait_lgkm0();
    }
    auto run5 = [&](auto isA_) {
      constexpr bool A_ = decltype(isA_)::value; constexpr int N = A_ ? 8 : 7;
      const char* sp = A_ ? ws + WS_H2 + (tm5 * 16 + lw * 8) * 1024 : (const char*)(lw == 2 ? w1 : w3) + tn5 * 224;
      unsigned vp[N];
#pragma unroll
      for (int i = 0; i < N; ++i) { const int L = i * 64 + lane, r = L / 14, c = L - r * 14; vp[i] = A_ ? L * 16 : r * (F_ * 2) + c * 16; }
      const unsigned d0 = lds_base + (A_ ? lw * 8192 : lw == 2 ? K5_G : K5_U);
      unsigned st = 3 * K5_STG;
      auto issue = [&]() {
#pragma unroll
        for (int i = 0; i < N; ++i) { if (A_) glds16s(sp, vp[i], d0 + st, i * 1024); else glds16(sp, vp[i], d0 + st, i * 1024); }
        sp += A_ ? 131072 : 32 * F_ * 2; st = st == (K5_NS - 1) * K5_STG ? 0 : st + K5_STG;
      };
      BAR();
      if (!A_) { issue(); issue(); }
      BAR();
      if (A_ && lw == 0) { signal(cnt, C4 + tm4, lane); poll<PD4>(cnt + ((C4 + tm5 * 2 + (lane & 1)) * 8 + xc) * 32, 16); }
      else { if (!A_) { __builtin_amdgcn_s_sleep(2); for (int s = 2; s < NB5; ++s) issue(); } if (T6) touch(); }
      BAR();
      if (A_) { issue(); issue(); }
      wait_vm<A_ ? N : (NB5 - 1) * N>();
      BAR();
#pragma unroll 1
      for (int s = A_ ? 2 : NB5; s < K5_NS; ++s) issue();
#pragma unroll 1
      for (int j = 0; j < 27; ++j) {
        wait_vm<3 * N>();
        BAR();
        issue();
        touch(); __builtin_amdgcn_s_sleep(TS5); touch();
      }
      const char* sb = A_ ? ws + WS_HH + (kh6 * 56 * 128 + tm6 * 8 + lw * 4) * 1024 : (const char*)w2 + kh6 * 1792 * 2048 + tn6 * 256 + ((lw ^ kh6 ^ 1) & 1) * 128;
      unsigned vo[4];
#pragma unroll
      for (int i = 0; i < 4; ++i) {
        const int r = i * 8 + (lane >> 3), k = 8 * ((r >> 2) & 3) + 4 * (r >> 4) + (r & 3);
        vo[i] = A_ ? lane * 16 : k * 2048 - i * 1024 + swz;
      }
      const unsigned d6 = lds_base + (A_ ? K6_A : 0) + (lw & 1) * 4096;
      st = 0;
      const int j2 = lane - 16, p5 = (j2 * 52) >> 8, r5 = j2 - p5 * 5;
      const int tab = lane < 16 ? lane * 112 * 2048 : (p5 * 224 + 32 + r5 * 32 + (r5 > 2 ? 32 : 0)) * 2048 + (r5 == 2);
      int jn = 0;
      auto issue6 = [&]() {
        if (A_) { glds16x4<true>(sb, sb, vo, d6 + st); sb += 131072; }
        else { const int o = __builtin_amdgcn_readlane(tab, jn); const char* s0 = sb + (o & ~1); glds16x4<false>(s0, s0 + (o & 1) * 65536, vo, d6 + st); ++jn; }
        st = st == ((A_ ? K6_NA : K6_NW) - 1) * 8192 ? 0 : st + 8192;
      };
      auto pre6 = [&](int n) {
        if (!A_) {
#pragma unroll 1
          for (int i = 0; i < n; ++i) issue6();
        }
      };
      constexpr int E = A_ ? 0 : 4;
      glds4(w2, w2_unit((lane >> 2) < 14 ? lane >> 2 : 0, P) + (lane & 3) * 128, lds_base + LDS_DUMMY);
      wait_vm<3 * N + 1>(); BAR();
      wait_vm<2 * N + 1>(); BAR(); pre6(PRE6);
      wait_vm<N + PRE6 * E + 1>(); BAR(); pre6(4);
      wait_vm<(PRE6 + 4) * E + 1>(); BAR(); pre6(8 - PRE6);
      BAR();
      if (A_) {
        volatile LDS_PTR(unsigned) go = (volatile LDS_PTR(unsigned))(smem + LDS_ARR + 4);
        if (lw == 0) { poll<PD5, PS5>(cnt + ((C5 + (tm6 >> 1) * 4 + kh6 * 2) * 8 + tn6) * 32, 16); *go = 1u; }
        else {
#pragma unroll 1
          for (unsigned spins = 0; spins < SPIN_CAP && __builtin_amdgcn_readfirstlane(*go) == 0; ++spins) __builtin_amdgcn_s_sleep(1);
          asm volatile("" ::: "memory");
        }
        STAMP_L(31);
        issue6(); issue6(); issue6();
        wait_vm<8>(); BAR(); issue6(); issue6();
        wait_vm<12>(); BAR(); issue6(); issue6();
      } else { wait_vm<16>(); BAR(); BAR(); }
#pragma unroll 1
      for (int c = 2; c < 51; ++c) { wait_vm<16>(); BAR(); if (A_ || !((SKIP6 >> c) & 1)) issue6(); }
      wait_vm<16>(); BAR(); wait_vm<12>(); BAR(); wait_vm<8>(); BAR(); wait_vm<4>(); BAR(); wait_vm<0>(); BAR();
    };
    if (isA) run5(T_()); else run5(F__());
    asm volatile("s_waitcnt lgkmcnt(0)" ::: "memory");
    return;
  }

  int fr = lane & 15, g = lane >> 4, p4 = fr & 3, row = 4 * g + (fr >> 2);
  int lsw = row * 128 + (((row >> 1) & 3) * 32) + p4 * 8;
  { const int mb = u1, hf = (b >> 3) & 1, kb0 = hf * 16 + wave * 2, cofs = (g & 1) * 16 + (g >> 1) * 8;
    u32x4 v[4];
    auto ldx = [&]() {
#pragma unroll
      for (int p = 0; p < 4; ++p) v[p] = *(const u32x4*)(x + (size_t)(mb * 16 + fr) * D_ + ((kb0 + (p & 1)) ^ ((p >> 1) * 16)) * 32 + cofs);
    };
    LDS_PTR(f32x2) sred = (LDS_PTR(f32x2))(smem + 4096);
    auto up2 = [](unsigned u) { return (f32x2){lo2f(u), hi2f(u)}; };
    auto stats = [&]() {
      f32x2 s2 = {0.f, 0.f}, q2 = {0.f, 0.f};
#pragma unroll
      for (int k = 0; k < 16; ++k) s2 += up2(v[k >> 2][k & 3]);
      const float mw = xsum16_32(s2[0] + s2[1]) * (1.f / 128.f);
#pragma unroll
      for (int k = 0; k < 16; ++k) { const f32x2 d = up2(v[k >> 2][k & 3]) - mw; q2 += d * d; }
      const float qw = xsum16_32(q2[0] + q2[1]);
      if (lane < 16) sred[wave * 16 + fr] = (f32x2){mw, qw};
      wait_lgkm0();
    };
    auto rest = [&]() {
    STAMP_AT(1 );
    float mean = 0.f, m2 = 0.f; f32x2 sw[8];
#pragma unroll
    for (int w = 0; w < 8; ++w) { sw[w] = sred[w * 16 + fr]; mean += sw[w][0]; m2 += sw[w][1]; }
    mean *= 0.125f;
#pragma unroll
    for (int w = 0; w < 8; ++w) { const float d = sw[w][0] - mean; m2 += 128.f * d * d; }
    const float rstd = ln_rstd(m2 * (1.f / 1024.f));
    mean = rbf(mean);
    unsigned nv[8];
#pragma unroll
    for (int k = 0; k < 8; ++k) {
      const f32x2 d = up2(v[k >> 2][k & 3]) - mean;
      const f32x2 n = up2(pk2(d[0], d[1])) * rstd;
      nv[k] = pk2(n[0], n[1]);
      asm volatile("" : "+v"(nv[k]));
    }
    SCHED0(); BAR(); SCHED0();
    STAMP_AT(2 );
    float mp[8];
#pragma unroll
    for (int i = 0; i < 8; ++i) mp[i] = __uint_as_float(__hip_atomic_load((unsigned*)(ws + WS_MOD) + hf * 512 + tid + i * 1024, __ATOMIC_RELAXED, __HIP_MEMORY_SCOPE_AGENT));
    *(LDS_PTR(unsigned))(smem + tid * 4) = pk2((mp[0] + mp[2]) + (mp[4] + mp[6]), 1.f + rbf((mp[1] + mp[3]) + (mp[5] + mp[7])));
    wait_lgkm0();
#pragma unroll
    for (int p = 0; p < 2; ++p) {
      const LDS_PTR(u32x4) tp = (const LDS_PTR(u32x4))(smem + ((wave * 2 + p) * 32 + cofs) * 4);
      const u32x4 t0 = tp[0], t1 = tp[1];
      unsigned o[4];
#pragma unroll
      for (int j = 0; j < 4; ++j) {
        const unsigned ta = j < 2 ? t0[2 * j] : t1[2 * j - 4], tb = j < 2 ? t0[2 * j + 1] : t1[2 * j - 3];
        const unsigned u = pk2(lo2f(nv[p * 4 + j]) * hi2f(ta), hi2f(nv[p * 4 + j]) * hi2f(tb));
        o[j] = pk2(lo2f(u) + lo2f(ta), hi2f(u) + lo2f(tb));
      }
      auto s0 = __builtin_amdgcn_permlane16_swap(o[0], o[2], false, false);
      auto s1 = __builtin_amdgcn_permlane16_swap(o[1], o[3], false, false);
      st16<true>(ws + WS_H, ((kb0 + p) * 128 + mb) * 1024 + lane * 16, (u32x4){s0[0], s1[0], s0[1], s1[1]});
    }
    STAMP_AT(3 );
    wait_vm<0>();
    STAMP_AT(4 );
    BAR();
    STAMP_AT(5 );
    };
    if (hf) {
      const int mrow = (u1 & 3) * 256 + (tid >> 3) * 4;
      const u32x2 c2 = *(const u32x2*)(cnd + mrow);
      u32x4 mw[4];
#pragma unroll
      for (int i = 0; i < 4; ++i) mw[i] = __builtin_nontemporal_load((const u32x4*)(wmod + (size_t)(mrow + i) * 6144 + (u1 >> 2) * 64 + (lane & 7) * 8));
      const unsigned mc[2] = {c2[0], c2[1]};
      *(LDS_PTR(f32x2))(smem + 8192 + (wave * 64 + (lane & 7) * 8 + (lane >> 4) * 2) * 4) = slab_sum<4>(mc, mw);
      wait_lgkm0();
      BAR();
      __builtin_amdgcn_s_sleep(12);
      ldx();
      stats();
      BAR();
      rest();
    } else {
      ldx();
      SCHED0();
      const int un = wave * 128 + u1;
      unsigned pf = 0;
      if (PFD) __builtin_amdgcn_s_sleep(PFD);
      if (wave < PF0) pf = *(const unsigned*)((un < 768 ? (const char*)wqkv + un * 8192 : (const char*)wo + (un - 768) * 8192) + lane * 128);
      SCHED0(); BAR(); SCHED0();
      stats();
      SCHED0(); BAR(); SCHED0();
      rest();
      asm volatile("" ::"v"(pf));
    }
  }
  {
    float bias[4];
    { const int kbase = tm3 * 128 + wave * 16 - 16;
#pragma unroll
      for (int t = 0; t < 4; ++t) {
        const int kk = (t >> 1) * 16 + 4 * g + (t & 1) * 2 + (fr & 1), df = 16 + fr - kk;
        bias[t] = (df >= 0 && df <= 14 && kbase + kk >= 0) ? 0.f : -__builtin_inff();
        asm volatile("" : "+v"(bias[t]));
      } }
    u32x4 mc, mw[8];
    if (wave < 4) {
      const int wm = wave & 1, bA = wave;
      unsigned a0 = lds_base + K3_B + (lsw ^ (bA * 32)), a1 = lds_base + K3_B + (lsw ^ ((bA ^ 1) * 32)), aA = lds_base + 1024 + wm * 4096 + lane * 16, aH = lds_base + lane * 16;
      unsigned lq = lds_base + bA * 512 + lane * 8, lv = lds_base + 36864 + bA * 512 + (lane ^ ((g >> 1) * 8)) * 8;
      int n = 4;
      asm volatile(".irp r,0,8,16,24,32,40,48,56,64,72,80,88,96\n\tv_mov_b64 v[\\r:\\r+1], 0\n\tv_mov_b64 v[\\r+2:\\r+3], 0\n\tv_mov_b64 v[\\r+4:\\r+5], 0\n\tv_mov_b64 v[\\r+6:\\r+7], 0\n\t.endr\n\t"
                   "s_setprio 2\n\ts_waitcnt vmcnt(0)\n\ts_barrier\n\t"
                   "s_barrier\n\t"
                   RA3(1, 0) RA3(0, 0) RW3(0, 0, a0, 0) RW3(0, 1, a0, 0) RW3(1, 0, a1, 0) RW3(1, 1, a1, 0) RW3(2, 0, a1, 0) RW3(2, 1, a1, 0) RW3(3, 0, a0, 0) RW3(3, 1, a0, 0) RH3(0)
                   RW3(4, 0, a0, 0) RW3(4, 1, a0, 0) RW3(5, 0, a1, 0) LG(8)
                   ".p2align 5\n1:\n\t" STAGE3(43008, ) STAGE3(43008, ) STAGE3(-86016, "s_sub_u32 %[n], %[n], 1\n\t") "s_cbranch_scc0 1b\n\t"
                   H2_3(0, , , , ) H1_3(1, )
                   H2_3(1, "v_add_u32 %[a0], %[mo], %[lq]\n\t", "v_add_u32 %[aA], %[mo], %[lv]\n\t", "v_xor_b32 %[a1], 512, %[a0]\n\tv_xor_b32 %[aH], 512, %[aA]\n\t", "s_barrier\n\t")
                   M3(1, 5) M3(0, 5) M3(2, 0) CV3(0, 0, 0) M3(3, 0) WR3(0, 0, a0, 0) CV3(1, 0, 1) M3(3, 1) WR3(1, 1, a0, 0) CV3(0, 1, 2) M3(2, 1) WR3(0, 2, a1, 0) CV3(1, 1, 3)
                   M3(2, 2) WR3(1, 3, a1, 0) CV3(0, 2, 0) M3(3, 2) WR3(0, 0, a1, 18432) CV3(1, 2, 1) M3(3, 3) WR3(1, 1, a1, 18432) CV3(0, 3, 2) M3(2, 3) WR3(0, 2, a0, 18432) CV3(1, 3, 3)
                   M3(4, 3) WR3(1, 3, a0, 18432) CV3(0, 4, 0) M3(4, 4) WR3(0, 0, aA, 0) CV3(1, 4, 1) M3(2, 4) WR3(1, 1, aA, 0) CV3(0, 5, 2) M3(3, 4) WR3(0, 2, aH, 0) CV3(1, 5, 3)
                   M3(3, 5) WR3(1, 3, aH, 0) CV3(2, 0, 0) M3(2, 5) WR3(2, 0, a0, 0)
                   CW3(3, 0, 1, a0, 0) CW3(3, 1, 2, a1, 0) CW3(2, 1, 3, a1, 0) CW3(2, 2, 0, a1, 18432) CW3(3, 2, 1, a1, 18432) CW3(3, 3, 2, a0, 18432) CW3(2, 3, 3, a0, 18432)
                   CV3(4, 3, 0) WR3(0, 0, lq, 18432) CV3(4, 4, 1) WR3(0, 1, lv, 0) CW3(2, 4, 2, aA, 0) CW3(3, 4, 3, aA, 0) CW3(3, 5, 0, aH, 0) CW3(2, 5, 1, aH, 0)
                   "s_setprio 0\n\t" LG(0)
                   : [a0] "+v"(a0), [a1] "+v"(a1), [aA] "+v"(aA), [aH] "+v"(aH), [n] "+s"(n)
                   : [lq] "v"(lq), [lv] "v"(lv), [mo] "s"((wm * 4 + 1) * 2048)
                   : "memory", "scc", V10(), V10(1), V10(2), V10(3), V10(4), V10(5), V10(6), V10(7), V10(8), V10(9), V10(10), V10(11), V10(12), V10(13), V8(14));
    } else {
      wait_vm<0>();
      BAR(); BAR();
#pragma unroll 1
      for (int t = 0; t < SLB; ++t) BAR();
      const int mrow = ((b & 3) * 4 + (wave & 3)) * 64 + (lane >> 3) * 8;
      mc = *(const u32x4*)(cnd + mrow);
#pragma unroll
      for (int i = 0; i < 8; ++i) mw[i] = __builtin_nontemporal_load((const u32x4*)(wmod + (size_t)(mrow + i) * 6144 + 2048 + (b >> 2) * 64 + (lane & 7) * 8));
      SCHED0();
#pragma unroll
      for (int t = SLB; t < 16; ++t) BAR();
    }
    lane = lane_id(); fr = lane & 15; g = lane >> 4; p4 = fr & 3; row = 4 * g + (fr >> 2); lsw = row * 128 + (((row >> 1) & 3) * 32) + p4 * 8;
    BAR();
    STAMP_AT(9 );
    const LDS_PTR(char) eb = smem + wave * 2048;
    auto ld2 = [&](int o) { return cat8(*(const LDS_PTR(bf16x4))(eb + o), *(const LDS_PTR(bf16x4))(eb + o + 512)); };
    const int vo = 36864 + ((p4 * 16 + row) ^ ((p4 >> 1) * 8)) * 8;
    bf16x8 qf[2], k0[2], k1[2], v0[2], v1[2];
#pragma unroll
    for (int hd = 0; hd < 2; ++hd) { qf[hd] = ld2(2048 + hd * 1024 + lane * 8); k0[hd] = ld2(18432 + hd * 1024 + lane * 8); k1[hd] = ld2(18432 + 2048 + hd * 1024 + lane * 8); }
#pragma unroll
    for (int hd = 0; hd < 2; ++hd) {
      v0[hd] = cat8(ld_tr4(eb + vo + hd * 1024), ld_tr4(eb + vo + hd * 1024 + 2048));
      v1[hd] = cat8(ld_tr4(eb + vo + hd * 1024 + 512), ld_tr4(eb + vo + hd * 1024 + 512 + 2048));
    }
    const f32x4 z = {0.f, 0.f, 0.f, 0.f};
    const bool odd = fr & 1; const int psh = (fr & 1) * 16;
#pragma unroll
    for (int hd = 0; hd < 2; ++hd) {
      const f32x4 s0 = __builtin_amdgcn_mfma_f32_16x16x32_bf16(k0[hd], qf[hd], z, 0, 0, 0), s1 = __builtin_amdgcn_mfma_f32_16x16x32_bf16(k1[hd], qf[hd], z, 0, 0, 0);
      float l[4] = {odd ? s0[1] : s0[0], odd ? s0[3] : s0[2], odd ? s1[1] : s1[0], odd ? s1[3] : s1[2]};
#pragma unroll
      for (int t = 0; t < 4; t += 2) {
        unsigned u = pk2(l[t], l[t + 1]);
        u = pk2(lo2f(u) * 0.17677669529663687f, hi2f(u) * 0.17677669529663687f);
        l[t] = lo2f(u) + bias[t]; l[t + 1] = hi2f(u) + bias[t + 1];
      }
      const float mx = xmax16_32(fmaxf(fmaxf(l[0], l[1]), fmaxf(l[2], l[3])));
#pragma unroll
      for (int t = 0; t < 4; ++t) l[t] = __builtin_amdgcn_exp2f((l[t] - mx) * 1.4426950408889634f);
      const float inv = __builtin_amdgcn_rcpf(xsum16_32((l[0] + l[1]) + (l[2] + l[3])));
      u32x4 pp;
#pragma unroll
      for (int t = 0; t < 4; ++t) pp[t] = pk2(l[t] * inv, 0.f) << psh;
      const bf16x8 pf = __builtin_bit_cast(bf16x8, pp);
      const f32x4 oa = __builtin_amdgcn_mfma_f32_16x16x32_bf16(v0[hd], pf, z, 0, 0, 0), ob = __builtin_amdgcn_mfma_f32_16x16x32_bf16(v1[hd], pf, z, 0, 0, 0);
      u32x4 ov; ov[0] = pk2(oa[0], oa[1]); ov[1] = pk2(oa[2], oa[3]); ov[2] = pk2(ob[0], ob[1]); ov[3] = pk2(ob[2], ob[3]);
      st16<true>(ws + WS_O, ((tn3 * 2 + hd) * 128 + tm3 * 8 + wave) * 1024 + lane * 16, ov);
    }
    STAMP_AT(10 );
    wait_vm<0>();
    STAMP_AT(11 );
    BAR();
    STAMP_AT(12 );
    if (wave >= 4) {
      const unsigned m4[4] = {mc[0], mc[1], mc[2], mc[3]};
      const f32x2 ps = slab_sum<8>(m4, mw);
      if (!(lane & 8))
        __hip_atomic_store((unsigned long long*)(ws + WS_MOD2) + (((b & 3) * 4 + (wave & 3)) * 4096 + (b >> 2) * 64 + (lane & 7) * 8 + (lane >> 4) * 2) / 2,
                           ((unsigned long long)__float_as_uint(ps[1]) << 32) | __float_as_uint(ps[0]), __ATOMIC_RELAXED, __HIP_MEMORY_SCOPE_AGENT);
      wait_vm<0>();
      if (lane == 0 && __hip_atomic_fetch_add((LDS_PTR(unsigned))(smem + LDS_ARR + 8), 1u, __ATOMIC_RELAXED, __HIP_MEMORY_SCOPE_WORKGROUP) == 3)
        __hip_atomic_fetch_add(cnt + (CM * 8 + (b >> 2)) * 32, 1u, __ATOMIC_RELAXED, __HIP_MEMORY_SCOPE_AGENT);
    }
  }
  { const int wm = wave & 3, ks = wave >> 2, mb = tm4 * 8 + wm * 2 + ks;
    constexpr int R = 6;
    u32x2 rv[4];
#pragma unroll
    for (int j = 0; j < 4; ++j) rv[j] = *(const u32x2*)(x + (size_t)(mb * 16 + fr) * D_ + tn4 * 64 + j * 16 + 4 * g);
    const unsigned va0 = (ks * 128 + mb) * 1024 + lane * 16, va1 = va0 ^ 1024;
    bf16x8 fa[R][2], fw[2][4];
    constexpr int rot = 3;
    auto issueA = [&](int t) {
      const char* pa = ws + WS_O + (size_t)((((t >> 2) + rot) & 3) * 4 + (t & 3)) * 262144;
      fa[t % R][0] = __builtin_bit_cast(bf16x8, ld16<true>(pa, va0));
      fa[t % R][1] = __builtin_bit_cast(bf16x8, ld16<true>(pa, va1));
    };
    unsigned aw[4];
    auto rdw = [&](int t, int j) {
      const int o = (t & 3) * 8192;
      fw[t & 1][j] = cat8(ld_tr4(smem + aw[j] + o), ld_tr4(smem + aw[j] + o + 2048));
    };
    auto stepw = [&](int t) {
      const int gi = ((t >> 2) + rot) & 3, base = gi < 3 ? 57344 + gi * 32768 : 0;
#pragma unroll
      for (int j = 0; j < 4; ++j) { aw[j] = base + ks * 4096 + (lsw ^ (j * 32)); asm volatile("" : "+v"(aw[j])); }
    };
    f32x4 acc[2][4];
#pragma unroll
    for (int i = 0; i < 2; ++i)
#pragma unroll
      for (int j = 0; j < 4; ++j) acc[i][j] = (f32x4){0.f, 0.f, 0.f, 0.f};
    stepw(0);
    wait_lgkm0();
    SCHED0(); BAR(); SCHED0();
    STAMP_AT(13 );
#pragma unroll
    for (int t = 0; t < R; ++t) issueA(t);
#pragma unroll
    for (int j = 0; j < 4; ++j) rdw(0, j);
    SCHED0();
#pragma unroll
    for (int t = 0; t < 16; ++t) {
      if (t < 15 && ((t + 1) & 3) == 0) stepw(t + 1);
#pragma unroll
      for (int j = 0; j < 4; ++j) {
        if (t < 15) rdw(t + 1, j);
        SCHED0();
#pragma unroll
        for (int i = 0; i < 2; ++i) acc[i][j] = __builtin_amdgcn_mfma_f32_16x16x32_bf16(fw[t & 1][j], fa[t % R][i], acc[i][j], 0, 0, 0);
        SCHED0();
      }
      if (t + R < 16) issueA(t + R);
      if (t == 5) BAR();
      SCHED0();
    }
    STAMP_AT(14 );
    LDS_PTR(f32x4) red = (LDS_PTR(f32x4))(smem + K4_RED);
#pragma unroll
    for (int j = 0; j < 4; ++j) red[((wave ^ 4) * 4 + j) * 64 + lane] = acc[1][j];
    wait_lgkm0();
    BAR();
    STAMP_AT(15 );
    const LDS_PTR(float) vec = (const LDS_PTR(float))(smem + LDS_VEC);
    unsigned o[8];
#pragma unroll
    for (int j = 0; j < 4; ++j) {
      const f32x4 y = (acc[0][j] + red[(wave * 4 + j) * 64 + lane]) * *(const LDS_PTR(f32x4))(vec + j * 16 + 4 * g);
      o[2 * j] = pk2(lo2f(rv[j][0]) + y[0], hi2f(rv[j][0]) + y[1]);
      o[2 * j + 1] = pk2(lo2f(rv[j][1]) + y[2], hi2f(rv[j][1]) + y[3]);
    }
    float f[16], s = 0.f;
#pragma unroll
    for (int j = 0; j < 8; ++j) { f[2 * j] = lo2f(o[j]); f[2 * j + 1] = hi2f(o[j]); s += f[2 * j] + f[2 * j + 1]; }
    const float mt = xsum16_32(s) * (1.f / 64.f);
    float qq = 0.f;
#pragma unroll
    for (int j = 0; j < 16; ++j) { const float d = f[j] - mt; qq += d * d; }
    qq = xsum16_32(qq);
    unsigned long long* slot = (unsigned long long*)(ws + WS_XCH) + (size_t)mb * 256 + fr;
    if (lane < 32) {
      const unsigned qb = qq == qq ? __float_as_uint(qq) : 0x7fc00000u;
      __hip_atomic_store(slot + g * 32768 + tn4 * 16, ((unsigned long long)qb << 32) | __float_as_uint(mt), __ATOMIC_RELAXED, __HIP_MEMORY_SCOPE_AGENT);
    }
    st16<true>(ws + WS_X1, (mb * 16 + tn4) * 2048 + lane * 16, (u32x4){o[0], o[1], o[2], o[3]});
    st16<true>(ws + WS_X1, (mb * 16 + tn4) * 2048 + 1024 + lane * 16, (u32x4){o[4], o[5], o[6], o[7]});
    STAMP_AT(16 );
    f32x4 ss[4], gg[4];
#pragma unroll
    for (int j = 0; j < 4; ++j) {
      ss[j] = *(const LDS_PTR(f32x4))(vec + 64 + j * 16 + 4 * g); gg[j] = *(const LDS_PTR(f32x4))(vec + 128 + j * 16 + 4 * g);
      asm volatile("" : "+v"(ss[j]), "+v"(gg[j]));
    }
    unsigned long long pv[4];
    __builtin_amdgcn_s_sleep(22);
    for (unsigned spins = 0;; ++spins) {
      bool ok = true;
#pragma unroll
      for (int i = 0; i < 4; ++i) { pv[i] = __hip_atomic_load(slot + (xc & 1) * 32768 + (4 * g + i) * 16, __ATOMIC_RELAXED, __HIP_MEMORY_SCOPE_AGENT); ok &= (unsigned)(pv[i] >> 32) != 0xffffffffu; }
      if (__builtin_amdgcn_ballot_w64(!ok) == 0 || spins > SPIN_CAP) break;
      __builtin_amdgcn_s_sleep(4);
    }
    STAMP_AT(17 );
    float ms = 0.f, m2 = 0.f;
#pragma unroll
    for (int i = 0; i < 4; ++i) { ms += __uint_as_float((unsigned)pv[i]); m2 += __uint_as_float((unsigned)(pv[i] >> 32)); }
    float mean = xsum16_32(ms) * (1.f / 16.f);
#pragma unroll
    for (int i = 0; i < 4; ++i) { const float d = __uint_as_float((unsigned)pv[i]) - mean; m2 += 64.f * d * d; }
    const float rstd = ln_rstd(xsum16_32(m2) * (1.f / 1024.f));
    mean = rbf(mean);
#pragma unroll
    for (int p = 0; p < 2; ++p) {
      u32x4 v;
#pragma unroll
      for (int j = 0; j < 4; ++j) {
        const int jb = 2 * p + (j >> 1), e = (j & 1) * 2;
        v[j] = pk2((f[8 * p + 2 * j] - mean) * rstd * gg[jb][e] + ss[jb][e], (f[8 * p + 2 * j + 1] - mean) * rstd * gg[jb][e + 1] + ss[jb][e + 1]);
      }
      st16<true>(ws + WS_H2, ((tn4 * 2 + p) * 128 + mb) * 1024 + lane * 16, v);
    }
    STAMP_AT(18 );
    wait_vm<0>();
    BAR();
    STAMP_AT(19 );

  }
  const int grp = wave >> 2;
  { const int wm = wave & 3;
    const LDS_PTR(char) sb = smem + 3 * K5_STG;
    const LDS_PTR(char) aP = sb + K5_G + row * 224 + grp * 96 + p4 * 8;
    const LDS_PTR(char) aM = sb + (p4 < 2 ? K5_G : K5_U) + row * 224 + (12 + grp) * 16 + (p4 & 1) * 8;
    const LDS_PTR(char) aA = sb + wm * 4096 + lane * 16;
    f32x32 c0, c1, c2; f32x16 c3;
    asm volatile(".irp r,0,8,16,24,32,40,48,56,64,72,80,88,96,104\n\tv_mov_b64 v[\\r:\\r+1], 0\n\tv_mov_b64 v[\\r+2:\\r+3], 0\n\tv_mov_b64 v[\\r+4:\\r+5], 0\n\tv_mov_b64 v[\\r+6:\\r+7], 0\n\t.endr"
                 : "={v[0:31]}"(c0), "={v[32:63]}"(c1), "={v[64:95]}"(c2), "={v[96:111]}"(c3)::"memory");
    int wrap = -(K5_NS - 1) * K5_STG, dl = K5_STG, n = 7;
    unsigned mask = wrap_mask(4, K5_NS);
    asm volatile("" : "+s"(wrap), "+v"(aP), "+v"(aM), "+v"(aA));
    if (grp) __builtin_amdgcn_s_setprio(PRIO_B); else __builtin_amdgcn_s_setprio(PRIO_A);
    BAR();
    STAMP_AT(20 );
    BAR();
    STAMP_G(21, 26);
    asm volatile(RA5(0) RA5(1) RP5(0, 0) RP5(0, 1) RP5(1, 0) RP5(1, 1) RP5(2, 0) RP5(2, 1) RP5(3, 0) RP5(3, 1) RP5(4, 0) RP5(4, 1) RP5(5, 0) RP5(5, 1) RW5(6, 0, aM, 0) LG(9)
                 ".p2align 5\n1:\n\t" TRIP5("") TRIP5("") TRIP5("") TRIP5("s_sub_u32 %[n], %[n], 1\n\t") "s_cbranch_scc0 1b\n\t" LG(0) "s_nop 7"
                 : ACC5, [aP] "+v"(aP), [aM] "+v"(aM), [aA] "+v"(aA), [dl] "+s"(dl), [mask] "+s"(mask), [n] "+s"(n) : [wrap] "s"(wrap) : CLOB5);
    f32x4 acc[4][7];
#pragma unroll
    for (int e = 0; e < 112; ++e) acc[e / 28][(e % 28) / 4][e & 3] = e < 32 ? c0[e & 31] : e < 64 ? c1[e & 31] : e < 96 ? c2[e & 31] : c3[e & 15];
    STAMP_G(22, 27);
    auto swi4 = [](const f32x4& gv, const f32x4& uv, unsigned& o0, unsigned& o1) {
      f32x2 ta = gv.xy * -1.4426950408889634f, tb = gv.zw * -1.4426950408889634f, pa = gv.xy * uv.xy, pb = gv.zw * uv.zw;
      ta = (f32x2){__builtin_amdgcn_exp2f(ta[0]), __builtin_amdgcn_exp2f(ta[1])} + 1.f; tb = (f32x2){__builtin_amdgcn_exp2f(tb[0]), __builtin_amdgcn_exp2f(tb[1])} + 1.f;
      pa *= (f32x2){__builtin_amdgcn_rcpf(ta[0]), __builtin_amdgcn_rcpf(ta[1])}; pb *= (f32x2){__builtin_amdgcn_rcpf(tb[0]), __builtin_amdgcn_rcpf(tb[1])};
      o0 = pk2(pa[0], pa[1]); o1 = pk2(pb[0], pb[1]);
    };
    const int lz = lane_id();
    const int gz = lz >> 4, fz = lz & 15;
    const int qb = (tn5 >> 4) * 224, ql = qb + 64 + (tn5 & 15) * 10;
    auto hof = [&](int e, int i) { return (unsigned)((e >> 2) * 131072 + (e & 3) * 256 + fz * 16 + i * 1024); };
    const unsigned ho0 = hof((grp ? ql + 2 : qb + (tn5 & 15) * 4) + 2 * (gz & 1) + (gz >> 1), 0), ho1 = hof(ql + 6 * grp + (gz >> 1), gz & 1), hom = hof(ql + 8 + grp, gz);
    char* hb = ws + WS_HH + (tm5 * 16 + wm * 4) * 1024;
    const int c5 = C5 + tm5 * 4 + (tn5 >> 4) * 2;
    auto tr16 = [](const unsigned (&o)[4]) {
      auto s0 = __builtin_amdgcn_permlane16_swap(o[0], o[2], false, false);
      auto s1 = __builtin_amdgcn_permlane16_swap(o[1], o[3], false, false);
      u32x4 v = {s0[0], s1[0], s0[1], s1[1]}; asm volatile("" : "+v"(v)); return v;
    };
    auto pair = [&](int p, int i) {
      unsigned o[4];
      swi4(acc[i][2 * p], acc[i][2 * p + 1], o[0], o[1]);
      swi4(acc[i + 1][2 * p], acc[i + 1][2 * p + 1], o[2], o[3]);
      return tr16(o);
    };
    u32x4 v[4];
#pragma unroll
    for (int i = 0; i < 4; ++i) {
      unsigned o[4];
      swi4(acc[i][0], acc[i][1], o[0], o[1]);
      swi4(acc[i][2], acc[i][3], o[2], o[3]);
      v[i] = tr16(o);
      if (!grp || i >= BHOLD) st16<true>(hb, ho0 + i * 1024, v[i]);
      if (grp && i == BHOLD - 1) {
#pragma unroll
        for (int k = 0; k < BHOLD; ++k) st16<true>(hb, ho0 + k * 1024, v[k]);
      }
      SCHED0();
    }
    STAMP_G(23, 28);
    v[0] = pair(2, 0);
    if (!grp) {
      wait_vm<0>();
      unsigned arr = 0;
      if (lz == 0) arr = __hip_atomic_fetch_add((LDS_PTR(unsigned))(smem + LDS_ARR), 1u, __ATOMIC_RELAXED, __HIP_MEMORY_SCOPE_WORKGROUP);
      if (__builtin_amdgcn_readfirstlane(arr) == 3) signal(cnt, c5, lz);
    }
    STAMP_G(24, 29);
    v[1] = pair(2, 2);
    { unsigned m[4];
#pragma unroll
      for (int i = 0; i < 4; i += 2) {
        f32x4 gv, uv;
#pragma unroll
        for (int e = 0; e < 4; ++e) {
          auto w = __builtin_amdgcn_permlane32_swap(__float_as_uint(acc[i + (e >> 1)][6][e & 1]), __float_as_uint(acc[i + (e >> 1)][6][2 + (e & 1)]), false, false);
          gv[e] = __uint_as_float(w[0]); uv[e] = __uint_as_float(w[1]);
        }
        swi4(gv, uv, m[i], m[i + 1]);
      }
      auto x0 = __builtin_amdgcn_permlane32_swap(m[0], m[2], false, false);
      auto x1 = __builtin_amdgcn_permlane32_swap(m[1], m[3], false, false);
      const unsigned o[4] = {x0[0], x0[1], x1[0], x1[1]};
      v[2] = tr16(o);
      st16<true>(hb, ho1, v[0]); st16<true>(hb, ho1 + 2048, v[1]); st16<true>(hb, hom, v[2]);
    }
    STAMP_G(25, 30);
    if (grp) {
#pragma unroll
      for (int t = 0; t < NDR; ++t) BAR();
      wait_vm<0>();
      BAR();
      if (wave == 4) signal(cnt, c5 + 1, lz);
      if (wave > 4) return;
      int kpf = 14;
      auto idle = [&]() {
        if (kpf < 14 && __builtin_amdgcn_s_getreg((3 << 11) | (8 << 6) | 7) <= 6) { stouch4((const char*)w2 + w2_unit(kpf, b * 4 + wm)); ++kpf; }
        BAR();
      };
#pragma unroll 1
      for (int t = NDR + 1; t < HOLD - 2; ++t) idle();
      unsigned seen = 16;
      const unsigned* pc = cnt + ((C5 + (tm6 >> 1) * 4 + kh6 * 2 + 1) * 8 + tn6) * 32;
      if (wave == 4) asm volatile("global_load_dword %0, %1, %2 sc1" : "=v"(seen) : "v"(0), "s"(pc) : "memory");
      idle(); idle();
      asm volatile("s_waitcnt vmcnt(0)" : "+v"(seen)::"memory");
      if (__builtin_amdgcn_readfirstlane(seen) < 16) poll<0>(pc, 16);
#pragma unroll 1
      for (int t = HOLD; t < 48 && (kpf < 14 || __builtin_amdgcn_s_getreg((3 << 11) | (8 << 6) | 7)); ++t) idle();
      wait_lgkm0();
      return;
    }
  }
  { const int lane = lane_id(), fr = lane & 15, g = lane >> 4, p4 = fr & 3, row = 4 * g + (fr >> 2);
    const int wm = wave & 1, wn = wave >> 1;
    const int lw0 = row * 128 + (((row >> 1) & 3) * 32) + p4 * 8;
    const int mb0 = tm6 * 8 + wm * 4, t64 = tn6 * 2 + kh6;
    u32x4 rv[4];
#pragma unroll
    for (int i = 0; i < 4; ++i) rv[i] = ld16<true>(ws + WS_X1, ((mb0 + i) * 16 + t64) * 2048 + wn * 1024 + lane * 16);
    f32x4 acc[4][4];
#pragma unroll
    for (int i = 0; i < 4; ++i)
#pragma unroll
      for (int j = 0; j < 4; ++j) { acc[i][j] = (f32x4){0.f, 0.f, 0.f, 0.f}; asm volatile("" : "+v"(acc[i][j])); }
    struct Fr { bf16x8 a[4]; bf16x4 w[4][2]; };
    const LDS_PTR(char) aw0 = smem + (lw0 ^ (wn * 64)); const LDS_PTR(char) aw1 = smem + (lw0 ^ (wn * 64 + 32));
    const LDS_PTR(char) aa = smem + K6_A + wm * 4096 + lane * 16;
    auto rd1 = [&](Fr& f, int r) {
      if (r < 2) f.w[0][r] = ld_tr4(aw0 + r * 2048);
      else if (r < 4) f.a[r - 2] = *(const LDS_PTR(bf16x8))(aa + (r - 2) * 1024);
      else if (r < 10) { const int j = (r - 2) >> 1; f.w[j][r & 1] = ld_tr4(((j & 1) ? aw1 : aw0) + (j >> 1) * 4096 + (r & 1) * 2048); }
      else f.a[r - 8] = *(const LDS_PTR(bf16x8))(aa + (r - 8) * 1024);
    };
    int dl, da, wrap = -(K6_NW - 1) * 8192, wrapa = -(K6_NA - 1) * 8192;
    unsigned mask = wrap_mask(1, K6_NW), maska = wrap_mask(1, K6_NA);
    Fr fA, fB;
    asm volatile("" : "+s"(wrap), "+s"(wrapa), "+v"(aw0), "+v"(aw1), "+v"(aa));
    wait_vm<0>();
    BAR();
    STAMP_AT(32 );
#pragma unroll
    for (int r = 0; r < 12; ++r) { SCHED0(); rd1(fA, r); }
    SCHED0();
    aw0 += 8192; aw1 += 8192; aa += 8192;
    wait_lgkm<6>();
    BAR();
    auto trip = [&](const Fr& cur, Fr& nxt) {
      SCHED0();
#pragma unroll
      for (int m = 0; m < 16; ++m) {
        const int j = (m & 7) >> 1, i = (m >> 3) * 2 + ((m ^ j) & 1);
        if (m == 2) mfma_sel<8192>(acc[i][j], cat8(cur.w[j][0], cur.w[j][1]), cur.a[i], mask, dl, wrap);
        else if (m == 6) mfma_sel<8192>(acc[i][j], cat8(cur.w[j][0], cur.w[j][1]), cur.a[i], maska, da, wrapa);
        else mfma_acc(acc[i][j], cat8(cur.w[j][0], cur.w[j][1]), cur.a[i]);
        SCHED0();
        if (m < 12) { rd1(nxt, m); SCHED0(); }
        if (m == 3) wait_lgkm<4>();
        if (m == 12) { aw0 += dl; aw1 += dl; asm volatile("" : "+v"(aw0), "+v"(aw1)); }
        if (m == 13) { aa += da; asm volatile("" : "+v"(aa)); }
        if (m == 14) wait_lgkm<6>();
        if (m == 15) BAR();
        SCHED0();
      }
    };
#pragma unroll 1
    for (int h = 0; h < 2; ++h) {
#pragma unroll 1
      for (int t = 0; t < 7; ++t) { trip(fA, fB); trip(fB, fA); trip(fA, fB); trip(fB, fA); }
      mask = wrap_mask(29, K6_NW); maska = wrap_mask(29, K6_NA);
    }
    mfma_done(acc);
    STAMP_AT(34 );
    const int slot = wm * 2 + wn, pb = (b ^ 4) * 4 + slot;
    unsigned* flags = (unsigned*)(ws + WS_FLG);
    { char* pp = ws + WS_PART + (size_t)(b * 4 + slot) * 4096;
#pragma unroll
      for (int i = 0; i < 4; ++i)
        st16<true>(pp, i * 1024 + lane * 16, (u32x4){pk2(acc[i][0][0], acc[i][0][1]), pk2(acc[i][0][2], acc[i][0][3]), pk2(acc[i][1][0], acc[i][1][1]), pk2(acc[i][1][2], acc[i][1][3])});
      wait_vm<0>();
      STAMP_AT(35 );
      if (lane == 0) __hip_atomic_store(flags + (b * 4 + slot) * 16, 1u, __ATOMIC_RELAXED, __HIP_MEMORY_SCOPE_AGENT);
    }
    { const LDS_PTR(float) vec = (const LDS_PTR(float))(smem + LDS_VEC) + 192 + wn * 32 + 4 * g;
      const f32x4 gv0 = *(const LDS_PTR(f32x4))vec, gv1 = *(const LDS_PTR(f32x4))(vec + 16);
      poll<POLL6>(flags + pb * 16, 1);
      STAMP_AT(36 );
      const char* pp = ws + WS_PART + (size_t)pb * 4096;
      u32x4 z[4];
#pragma unroll
      for (int i = 0; i < 4; ++i) z[i] = ld16<true>(pp, i * 1024 + lane * 16);
#pragma unroll
      for (int i = 0; i < 4; ++i) {
        const f32x4 v0 = (acc[i][2] + (f32x4){lo2f(z[i][0]), hi2f(z[i][0]), lo2f(z[i][1]), hi2f(z[i][1])}) * gv0;
        const f32x4 v1 = (acc[i][3] + (f32x4){lo2f(z[i][2]), hi2f(z[i][2]), lo2f(z[i][3]), hi2f(z[i][3])}) * gv1;
        const unsigned o0 = pk2(lo2f(rv[i][0]) + v0[0], hi2f(rv[i][0]) + v0[1]), o1 = pk2(lo2f(rv[i][1]) + v0[2], hi2f(rv[i][1]) + v0[3]);
        const unsigned o2 = pk2(lo2f(rv[i][2]) + v1[0], hi2f(rv[i][2]) + v1[1]), o3 = pk2(lo2f(rv[i][3]) + v1[2], hi2f(rv[i][3]) + v1[3]);
        auto s0 = __builtin_amdgcn_permlane16_swap(o0, o2, false, false);
        auto s1 = __builtin_amdgcn_permlane16_swap(o1, o3, false, false);
        st16<false>(out, (((mb0 + i) * 16 + fr) * D_ + t64 * 64 + wn * 32 + (g & 1) * 16 + (g >> 1) * 8) * 2, (u32x4){s0[0], s1[0], s0[1], s1[1]});
      }
    }
    STAMP_AT(37 );
  }
}

extern "C" void kernel_launch(void* const* d_in, const int* in_sizes, int n_in, void* d_out, int out_size, void* d_ws,
                              size_t ws_size, hipStream_t stream) {
  const u16* x = (const u16*)d_in[0];
  const u16* wqkv = (const u16*)d_in[1];
  const u16* wo = (const u16*)d_in[2];
  const u16* w1 = (const u16*)d_in[4];
  const u16* w3 = (const u16*)d_in[5];
  const u16* w2 = (const u16*)d_in[6];
  const u16* c = (const u16*)d_in[7];
  const u16* wmod = (const u16*)d_in[8];
  (void)hipFuncSetAttribute((const void*)k2_block, hipFuncAttributeMaxDynamicSharedMemorySize, MEGA_LDS);
  char* ws = (char*)d_ws;
  k2_block<<<256, 768, MEGA_LDS, stream>>>(x, wqkv, wo, w1, w3, w2, c, wmod, (u16*)d_out, ws);
}
```

```cpp
#include <hip/hip_runtime.h>
#include <stdint.h>
#include <type_traits>
#pragma clang diagnostic ignored "-Winline-asm"

typedef unsigned short u16;
typedef short bf16x8 __attribute__((ext_vector_type(8)));
typedef short bf16x4 __attribute__((ext_vector_type(4)));
typedef float f32x4 __attribute__((ext_vector_type(4)));
typedef float f32x2 __attribute__((ext_vector_type(2)));
typedef unsigned int u32x4 __attribute__((ext_vector_type(4)));
typedef unsigned int u32x2 __attribute__((ext_vector_type(2)));
#define LDS_PTR(T) __attribute__((address_space(3))) T*
#define BAR() do { asm volatile("" ::: "memory"); __builtin_amdgcn_s_barrier(); asm volatile("" ::: "memory"); } while (0)
#define SCHED0() __builtin_amdgcn_sched_barrier(0)
template <int N> using IC = std::integral_constant<int, N>;
typedef std::integral_constant<bool, true> T_;
typedef std::integral_constant<bool, false> F__;

constexpr int S_ = 2048, D_ = 1024, F_ = 3584;
#define STAMP_AT(i)
#define STAMP_G(ia, ib)
#define STAMP_L(i)

__device__ __forceinline__ float bf2f(u16 v) { return __uint_as_float(((unsigned)v) << 16); }
__device__ __forceinline__ unsigned pk2(float lo, float hi) {
  typedef __bf16 bf2 __attribute__((ext_vector_type(2)));
  f32x2 v = {lo, hi}; bf2 r = __builtin_convertvector(v, bf2); return __builtin_bit_cast(unsigned, r);
}
__device__ __forceinline__ float lo2f(unsigned u) { return __uint_as_float(u << 16); }
__device__ __forceinline__ float hi2f(unsigned u) { return __uint_as_float(u & 0xffff0000u); }
__device__ __forceinline__ float rbf(float v) { return lo2f(pk2(v, 0.f)); }
#define glds16(sbase, voff, lds_off, imm) \
  asm volatile("s_add_u32 m0, %2, %3\n\ts_nop 0\n\tglobal_load_lds_dwordx4 %0, %1" ::"v"(voff), "s"(sbase), "s"(lds_off), "n"(imm) : "memory", "scc")
#define glds16s(sbase, voff, lds_off, imm) \
  asm volatile("s_add_u32 m0, %2, %3\n\ts_nop 0\n\tglobal_load_lds_dwordx4 %0, %1 sc1" ::"v"(voff), "s"(sbase), "s"(lds_off), "n"(imm) : "memory", "scc")
#define GLDS4(SC) "s_mov_b32 m0, %6\n\ts_nop 0\n\tglobal_load_lds_dwordx4 %0, %4" SC "\n\tglobal_load_lds_dwordx4 %1, %5 offset:1024" SC \
                  "\n\tglobal_load_lds_dwordx4 %2, %4 offset:2048" SC "\n\tglobal_load_lds_dwordx4 %3, %5 offset:3072" SC
template <bool SC1> __device__ __forceinline__ void glds16x4(const void* s0, const void* s1, const unsigned (&v)[4], unsigned lds) {
  if (SC1) asm volatile(GLDS4(" sc1") ::"v"(v[0]), "v"(v[1]), "v"(v[2]), "v"(v[3]), "s"(s0), "s"(s1), "s"(lds) : "memory");
  else asm volatile(GLDS4("") ::"v"(v[0]), "v"(v[1]), "v"(v[2]), "v"(v[3]), "s"(s0), "s"(s1), "s"(lds) : "memory");
}
__device__ __forceinline__ void glds4(const void* sbase, unsigned voff, unsigned lds_off) {
  asm volatile("s_mov_b32 m0, %2\n\ts_nop 0\n\tglobal_load_lds_dword %0, %1" ::"v"(voff), "s"(sbase), "s"(lds_off) : "memory");
}
__device__ __forceinline__ void stouch4(const void* p) {
  asm volatile("s_load_dwordx2 s[100:101], %0, 0x3c\n\ts_load_dwordx2 s[100:101], %0, 0xbc\n\ts_load_dwordx2 s[100:101], %0, 0x13c\n\ts_load_dwordx2 s[100:101], %0, 0x1bc"
               ::"s"(p) : "memory", "s100", "s101");
}
template <bool SC1> __device__ __forceinline__ void st16(void* sbase, unsigned voff, u32x4 v) {
  __builtin_amdgcn_raw_buffer_store_b128(v, __builtin_amdgcn_make_buffer_rsrc(sbase, 0, 0x7fffffff, 0x00020000), voff, 0, SC1 ? 16 : 0);
}
template <bool SC1> __device__ __forceinline__ u32x4 ld16(const void* sbase, unsigned voff) {
  return __builtin_amdgcn_raw_buffer_load_b128(__builtin_amdgcn_make_buffer_rsrc((void*)sbase, 0, 0x7fffffff, 0x00020000), voff, 0, SC1 ? 16 : 0);
}
template <int N> __device__ __forceinline__ void wait_vm() { asm volatile("s_waitcnt vmcnt(%0)" ::"n"(N) : "memory"); }
__device__ __forceinline__ void wait_lgkm0() { asm volatile("" ::: "memory"); __builtin_amdgcn_s_waitcnt(0xC07F); asm volatile("" ::: "memory"); }
__device__ __forceinline__ bf16x4 ld_tr4(const LDS_PTR(char) p) { return __builtin_amdgcn_ds_read_tr16_b64_v4i16((LDS_PTR(bf16x4))p); }
__device__ __forceinline__ bf16x8 cat8(bf16x4 lo, bf16x4 hi) { return __builtin_shufflevector(lo, hi, 0, 1, 2, 3, 4, 5, 6, 7); }
__device__ __forceinline__ void mfma_acc(f32x4& acc, bf16x8 a, bf16x8 b) { asm volatile("v_mfma_f32_16x16x32_bf16 %0, %1, %2, %0" : "+v"(acc) : "v"(a), "v"(b)); }
template <int STEP> __device__ __forceinline__ void mfma_sel(f32x4& acc, bf16x8 a, bf16x8 b, unsigned& mask, int& dl, int wrap) {
  asm volatile("s_add_u32 %1, %1, %1\n\tv_mfma_f32_16x16x32_bf16 %0, %3, %4, %0\n\ts_cselect_b32 %2, %5, %6" : "+v"(acc), "+s"(mask), "=s"(dl) : "v"(a), "v"(b), "s"(wrap), "n"(STEP) : "scc");
}
constexpr unsigned wrap_mask(int first, int ns) { unsigned m = 0; for (int t = 0; t < 32; ++t) if ((first + t) % ns == ns - 1) m |= 0x80000000u >> t; return m; }
template <int N> __device__ __forceinline__ void wait_lgkm() { __builtin_amdgcn_s_waitcnt(0xC07F | (N << 8)); }
template <int R, int C> __device__ __forceinline__ void mfma_done(f32x4 (&acc)[R][C]) {
  asm volatile("s_nop 9");
#pragma unroll
  for (int i = 0; i < R; ++i)
#pragma unroll
    for (int j = 0; j < C; ++j) asm volatile("" : "+v"(acc[i][j]));
}
__device__ __forceinline__ int lane_id() { int l; asm volatile("v_mbcnt_lo_u32_b32 %0, -1, 0\n\tv_mbcnt_hi_u32_b32 %0, -1, %0" : "=v"(l)); return l; }
__device__ __forceinline__ float xsum16_32(float v) {
  auto a = __builtin_amdgcn_permlane16_swap(__float_as_uint(v), __float_as_uint(v), false, false); v = __uint_as_float(a[0]) + __uint_as_float(a[1]);
  auto c = __builtin_amdgcn_permlane32_swap(__float_as_uint(v), __float_as_uint(v), false, false); return __uint_as_float(c[0]) + __uint_as_float(c[1]);
}

__device__ __forceinline__ float ln_rstd(float var) { return __builtin_amdgcn_rcpf(rbf(__builtin_amdgcn_sqrtf(rbf(rbf(var) + 1e-5f)))); }

constexpr size_t WS_MOD = 0;
constexpr size_t WS_EPO = 64 << 10, WS_FL1 = 96 << 10;
constexpr size_t WS_CTL = 128 << 10;
constexpr int C2 = 0, C3 = 16, C4 = 32, C5 = 48, CM = 80;
constexpr size_t WS_FLG = WS_CTL + (88 << 10), CTL_BYTES = 152 << 10;
constexpr size_t WS_XCH = 512 << 10;
constexpr size_t WS_MOD2 = 1 << 20;
constexpr size_t WS_H = 2 << 20, WS_O = WS_H + (4 << 20), WS_X1 = WS_O + (4 << 20), WS_H2 = WS_X1 + (4 << 20), WS_HH = WS_H2 + (4 << 20),
                 WS_PART = WS_HH + (size_t)S_ * F_ * 2;

template <int NL> __device__ __forceinline__ f32x2 slab_sum(const unsigned (&mc)[NL / 2], const u32x4 (&mw)[NL]) {
  f32x2 a2[4] = {};
#pragma unroll
  for (int i = 0; i < NL; ++i) {
    const float cv = (i & 1) ? hi2f(mc[i >> 1]) : lo2f(mc[i >> 1]);
    const float sv = rbf(cv * __builtin_amdgcn_rcpf(1.f + __builtin_amdgcn_exp2f(cv * -1.4426950408889634f)));
#pragma unroll
    for (int j = 0; j < 4; ++j) a2[j] += (f32x2){lo2f(mw[i][j]), hi2f(mw[i][j])} * sv;
  }
  float acc[8] = {a2[0][0], a2[0][1], a2[1][0], a2[1][1], a2[2][0], a2[2][1], a2[3][0], a2[3][1]};
#pragma unroll
  for (int j = 0; j < 4; ++j) { auto a = __builtin_amdgcn_permlane32_swap(__float_as_uint(acc[j]), __float_as_uint(acc[j + 4]), false, false); acc[j] = __uint_as_float(a[0]) + __uint_as_float(a[1]); }
#pragma unroll
  for (int j = 0; j < 2; ++j) { auto a = __builtin_amdgcn_permlane16_swap(__float_as_uint(acc[j]), __float_as_uint(acc[j + 2]), false, false); acc[j] = __uint_as_float(a[0]) + __uint_as_float(a[1]); }
#pragma unroll
  for (int j = 0; j < 2; ++j) acc[j] += __uint_as_float(__builtin_amdgcn_mov_dpp(__float_as_uint(acc[j]), 0x128, 0xf, 0xf, false));
  return (f32x2){acc[0], acc[1]};
}

constexpr int K3_B = 18432, K3_STG = 18432 + 24576;
constexpr int K4_RED = 57344;
constexpr int K5_G = 16384, K5_U = 16384 + 7168 + 16, K5_STG = 30736, K5_NS = 5;
constexpr int K6_NW = 12, K6_NA = 7, K6_A = K6_NW * 8192;
constexpr int LDS_VEC = 158720, LDS_DUMMY = LDS_VEC + 1024, LDS_ARR = LDS_DUMMY + 256, MEGA_LDS = LDS_ARR + 64;
constexpr unsigned SPIN_CAP = 1u << 19;
#define KNOBS PD2 = 8, PS2 = 3, PD3 = 12, PS3 = 3, XS0 = 22, XS1 = 4, PD4 = 12, PD5 = 44, PS5 = 4, PD6 = 2, XD1 = 12, TS5 = 3, NB5 = 4, VB3 = 4, SLB = 15, PRE6 = 4, NDR = 2
enum { KNOBS };
constexpr unsigned long long SKIP6 = 0x7c;
static_assert(__builtin_popcountll(SKIP6) == 5, "44 stages of W are issued behind 49 barriers");

__device__ __forceinline__ void signal(unsigned* cnt, int c, int lane, unsigned v = 1) {
  if (lane < 8) __hip_atomic_fetch_add(cnt + (c * 8 + lane) * 32, v, __ATOMIC_RELAXED, __HIP_MEMORY_SCOPE_AGENT);
}
template <int D0, int D1 = 1> __device__ __forceinline__ void poll(const unsigned* cnt, unsigned want) {
  if (D0) __builtin_amdgcn_s_sleep(D0);
#pragma unroll 1
  for (unsigned spins = 0; spins < SPIN_CAP; ++spins) {
    if (__builtin_amdgcn_ballot_w64(__hip_atomic_load(cnt, __ATOMIC_RELAXED, __HIP_MEMORY_SCOPE_AGENT) < want) == 0) break;
    __builtin_amdgcn_s_sleep(D1);
  }
  __builtin_amdgcn_fence(__ATOMIC_ACQUIRE, "wavefront");
}

typedef float f32x32 __attribute__((ext_vector_type(32)));
typedef float f32x16 __attribute__((ext_vector_type(16)));
#define M5(i, j) "v_mfma_f32_16x16x32_bf16 v[28*" #i "+4*" #j ":28*" #i "+4*" #j "+3], v[128+4*" #j ":131+4*" #j "], v[112+4*" #i ":115+4*" #i "], v[28*" #i "+4*" #j ":28*" #i "+4*" #j "+3]\n\t"
#define RA5(i) "ds_read_b128 v[112+4*" #i ":115+4*" #i "], %[aA] offset:1024*" #i "\n\t"
#define RW5(j, h, a, o) "ds_read_b64_tr_b16 v[128+4*" #j "+2*" #h ":129+4*" #j "+2*" #h "], %[" #a "] offset:" #o "\n\t"
#define ST5(a) "v_add_u32 %[" #a "], %[dl], %[" #a "]\n\t"
#define SEL5A "s_add_u32 %[mask], %[mask], %[mask]\n\t"
#define SEL5B "s_cselect_b32 %[dl], %[wrap], 30736\n\t"
#define LG(n) "s_waitcnt lgkmcnt(" #n ")\n\t"
#define RP5(j, h) RW5(j, h, aP, 32*(j/2)+7184*(j&1)+3584*h)
#define TRIP5(X)                                                                                                                                        \
  M5(0, 0) RW5(6, 1, aM, 3584) M5(1, 0) RA5(2) ST5(aM) M5(1, 1) RA5(3) ST5(aP) M5(0, 1) ST5(aA) LG(6) M5(0, 2) M5(1, 2) SEL5A M5(1, 3) SEL5B              \
  M5(0, 3) M5(0, 4) M5(1, 4) LG(0) M5(1, 5) M5(0, 5) "s_barrier\n\t" M5(0, 6) RA5(0) M5(1, 6) RA5(1)                                                      \
  M5(2, 0) X M5(3, 0) RP5(0, 0) M5(3, 1) RP5(0, 1) M5(2, 1) RP5(1, 0) M5(2, 2) RP5(1, 1) M5(3, 2) RP5(2, 0) M5(3, 3) RP5(2, 1) M5(2, 3) RP5(3, 0)          \
  M5(2, 4) RP5(3, 1) M5(3, 4) RP5(4, 0) M5(3, 5) RP5(4, 1) M5(2, 5) RP5(5, 0) M5(2, 6) RP5(5, 1) LG(8) M5(3, 6) RW5(6, 0, aM, 0)
#define ACC5 "+{v[0:31]}"(c0), "+{v[32:63]}"(c1), "+{v[64:95]}"(c2), "+{v[96:111]}"(c3)
#define CLOB5 "memory", "scc", "v112", "v113", "v114", "v115", "v116", "v117", "v118", "v119", "v120", "v121", "v122", "v123", "v124", "v125", "v126", "v127", "v128", "v129", "v130", "v131", "v132", "v133", \
              "v134", "v135", "v136", "v137", "v138", "v139", "v140", "v141", "v142", "v143", "v144", "v145", "v146", "v147", "v148", "v149", "v150", "v151", "v152", "v153", "v154", "v155"
static_assert(K5_STG == 30736 && K5_U - K5_G == 7184, "TRIP5");

#define C3(i, j) "v[24*" #i "+4*" #j "-12*(" #i "/4):24*" #i "+4*" #j "-12*(" #i "/4)+3]"
#define M3(i, j) "v_mfma_f32_16x16x32_bf16 " C3(i, j) ", v[124+4*" #j ":127+4*" #j "], v[104+4*" #i ":107+4*" #i "], " C3(i, j) "\n\t"
#define RA3(i, u) "ds_read_b128 v[104+4*" #i ":107+4*" #i "], %[aA] offset:1024*" #i "+9216*" #u "\n\t"
#define RH3(u) "ds_read_b128 v[120:123], %[aH] offset:9216*" #u "\n\t"
#define RW3(j, h, a, u) "ds_read_b64_tr_b16 v[124+4*" #j "+2*" #h ":125+4*" #j "+2*" #h "], %[" #a "] offset:8192*(" #j "/2)+2048*" #h "+4096*" #u "\n\t"
#define ST3(a, d) "v_add_u32 %[" #a "], " #d ", %[" #a "]\n\t"
#define H2_3(u, S0, S1, S2, B) M3(0, 0) RW3(5, 1, a1, u) M3(1, 0) RA3(2, u) M3(1, 1) RA3(3, u) S0 M3(0, 1) LG(7) S1 M3(0, 2) S2 M3(1, 2) M3(1, 3) M3(0, 3) LG(2) M3(0, 4) M3(1, 4) LG(0) B
#define H1_3(n, X)                                                                                                                                      \
  M3(1, 5) RA3(1, n) M3(0, 5) RA3(0, n) M3(2, 0) X M3(3, 0) RW3(0, 0, a0, n) M3(3, 1) RW3(0, 1, a0, n) M3(2, 1) RW3(1, 0, a1, n) M3(2, 2) RW3(1, 1, a1, n) \
  M3(3, 2) RW3(2, 0, a1, n) M3(3, 3) RW3(2, 1, a1, n) M3(2, 3) M3(4, 3) RW3(3, 0, a0, n) M3(4, 4) RW3(3, 1, a0, n) M3(2, 4) RH3(n)                         \
  M3(3, 4) RW3(4, 0, a0, n) M3(3, 5) RW3(4, 1, a0, n) LG(7) M3(2, 5) RW3(5, 0, a1, n)
#define STAGE3(d, X) H2_3(0, , , , ) H1_3(1, ) H2_3(1, ST3(a0, d), ST3(aA, d), ST3(a1, d) ST3(aH, d), "s_barrier\n\t") H1_3(0, X)
#define CV3(i, j, t) "v_cvt_pk_bf16_f32 v[104+2*" #t "], v[24*" #i "+4*" #j "-12*(" #i "/4)], v[24*" #i "+4*" #j "-12*(" #i "/4)+1]\n\tv_cvt_pk_bf16_f32 v[105+2*" #t "], v[24*" #i "+4*" #j "-12*(" #i "/4)+2], v[24*" #i "+4*" #j "-12*(" #i "/4)+3]\n\t"
#define WR3(i, t, a, o) "ds_write_b64 %[" #a "], v[104+2*" #t ":105+2*" #t "] offset:2048*" #i "+" #o "\n\t"
#define CW3(i, j, t, a, o) CV3(i, j, t) WR3(i, t, a, o)
#define V8(a) "v" #a "0", "v" #a "1", "v" #a "2", "v" #a "3", "v" #a "4", "v" #a "5", "v" #a "6", "v" #a "7"
#define V10(a) V8(a), "v" #a "8", "v" #a "9"
static_assert(K3_STG == 43008, "STAGE3");
__device__ __forceinline__ int w2_stage(int j) {
  const int j2 = j - 16, p = (j2 * 52) >> 8, r = j2 - p * 5;
  return j < 16 ? j * 112 * 2048 : (p * 224 + 32 + r * 32 + (r > 2 ? 32 : 0)) * 2048 + (r == 2);
}
__global__ __launch_bounds__(768, 3) __attribute__((amdgpu_num_sgpr(98))) void k2_block(const u16* __restrict__ x, const u16* __restrict__ wqkv, const u16* __restrict__ wo,
                                                   const u16* __restrict__ w1, const u16* __restrict__ w3, const u16* __restrict__ w2,
                                                   const u16* __restrict__ cnd, const u16* __restrict__ wmod, u16* __restrict__ out, char* __restrict__ ws) {
  extern __shared__ __attribute__((aligned(16))) char smem_[];
  LDS_PTR(char) smem = (LDS_PTR(char))smem_;
  const int tid = threadIdx.x;
  int lane = tid & 63;
  const int wave = __builtin_amdgcn_readfirstlane(tid >> 6);
  const unsigned lds_base = (unsigned)(uintptr_t)smem;
  const int b = blockIdx.x, xc = b & 7, ii = b >> 3;
  const int tm3 = (xc & 1) * 8 + (ii & 7), tn3 = (xc >> 1) * 4 + (ii >> 3);
  const int tm4 = (xc >> 1) * 4 + (ii & 3), tn4 = (xc & 1) * 8 + (ii >> 2);
  const int tm5 = (xc & 1) * 4 + (ii & 3), tn5 = (xc >> 1) * 8 + (ii >> 2);
  const int kh6 = xc >> 2, tm6 = tm5 * 2 + ((ii >> 2) & 1), tn6 = tn3 & 7;
  const int u1 = (b >> 4) * 8 + xc;
  unsigned* cnt = (unsigned*)(ws + WS_CTL);
  unsigned long long* fl1 = (unsigned long long*)(ws + WS_FL1);
  STAMP_AT(0 );

  if (wave >= 8) {
    const int lw = wave - 8, P = b * 4 + lw, lane = lane_id();
    const bool isA = lw < 2;
    asm volatile("" ::"s"(wqkv), "s"(wo), "s"(w1), "s"(w3), "s"(w2), "s"(ws));
    int kpf = -4;
    auto touch = [&]() {
      if (kpf < 28 && __builtin_amdgcn_s_getreg((3 << 11) | (8 << 6) | 7) <= 7) {
        stouch4(kpf < 0 ? (const char*)wo + ((kpf + 4) * 4096 + P * 4) * 128 : (const char*)((P & 1) ? w3 : w1) + (kpf * 2048 + (P >> 1) * 4) * 128);
        ++kpf;
      }
    };
    const char* epo = ws + WS_EPO;
    const unsigned swz = ((lane & 7) ^ ((lane >> 3) & 6)) * 16;
    const int cg = lw * 16 + (lw < 3 ? tn4 : tn6 * 2 + kh6);
    auto issue4 = [&](int s, unsigned slot) {
      const char* p = (const char*)wo + tn4 * 128 + (lw * 32 + s * 128) * 2048;
      unsigned v4[4];
#pragma unroll
      for (int i = 0; i < 4; ++i) v4[i] = (lane >> 3) * 2048 + swz + i * 15360;
      glds16x4<false>(p, p, v4, lds_base + slot + lw * 4096);
    };
    auto run3 = [&](auto isA_) {
      constexpr bool A_ = decltype(isA_)::value; constexpr int N = A_ ? 9 : 12;
      const char* sp = A_ ? ws + WS_H + lw * 131072 + (tm3 * 8 - 1) * 1024 : (const char*)wqkv + tn3 * 128;
      unsigned vo[3][4];
#pragma unroll
      for (int i = 0; i < 12; ++i) {
        const int tb = (lw - 2) * 12 + i;
        vo[i >> 2][i & 3] = A_ ? lane * 16 + (i < 8 ? 1024 + (i >> 2) * 4096 : tm3 ? 0 : 1024) : ((tb & 7) * 8 + (lane >> 3)) * 6144 + (tb >> 3) * 2048 - (i & 3) * 1024 + swz;
      }
      const unsigned d0 = lds_base + (A_ ? lw * 9216 : K3_B + (lw - 2) * 12288);
      auto issue = [&](unsigned st) {
        if (A_) { glds16x4<true>(sp, sp, vo[0], d0 + st + 1024); glds16s(sp, vo[2][0], d0 + st, 0); glds16x4<true>(sp, sp, vo[1], d0 + st + 5120); }
        else {
#pragma unroll
          for (int i = 0; i < 3; ++i) glds16x4<false>(sp, sp, vo[i], d0 + st + i * 4096);
        }
        sp += A_ ? 262144 : 64 * 6144;
      };
      { unsigned long long ep;
        asm volatile("s_load_dwordx2 %0, %1, 0x0\n\ts_waitcnt lgkmcnt(0)" : "=s"(ep) : "s"(epo) : "memory");
        ep += 1;
        if (lw == 3) { *(LDS_PTR(u32x4))(smem + LDS_ARR) = (u32x4){0u, 0u, 0u, 0u}; wait_lgkm0(); }
        if (lw == 1) {
#pragma unroll
          for (int k = 0; k < 3; ++k) {
            const int i = b * 166 + (k * 64 + lane < 166 ? k * 64 + lane : 0);
            st16<true>(ws, i < 32768 ? (unsigned)WS_XCH + i * 16 : (unsigned)WS_CTL + (i - 32768) * 16, i < 32768 ? (u32x4){~0u, ~0u, ~0u, ~0u} : (u32x4){0u, 0u, 0u, 0u});
          }
        }
        BAR();
        auto flag = [&](int idx) { wait_vm<0>(); if (lane < 8) __hip_atomic_store(fl1 + lane * 256 + idx, ep, __ATOMIC_RELAXED, __HIP_MEMORY_SCOPE_AGENT); };
        if (lw == 1 && (b & 8)) {
          const int kq = u1 & 3, cgp = u1 >> 2;
          float sm = 0.f;
#pragma unroll
          for (int r = 0; r < 8; ++r) sm += *(const LDS_PTR(float))(smem + 8192 + (r * 64 + lane) * 4);
          __hip_atomic_store((unsigned*)(ws + WS_MOD) + kq * 2048 + cgp * 64 + lane, __float_as_uint(sm), __ATOMIC_RELAXED, __HIP_MEMORY_SCOPE_AGENT);
          flag(((cgp >> 3) & 1) * 64 + (cgp >> 4) * 32 + (cgp & 7) * 4 + kq);
        }
        BAR();
        if (lw == 1 && !(b & 8)) flag(128 + u1);
        if (lw < 2) {
          if (lw == 1) BAR();
#pragma unroll 1
          for (unsigned spins = 0; spins < SPIN_CAP; ++spins) {
            bool ok = true;
#pragma unroll
            for (int k = 0; k < 4; ++k) ok &= __hip_atomic_load(fl1 + xc * 256 + (lw ? k : (b >> 3) & 1) * 64 + lane, __ATOMIC_RELAXED, __HIP_MEMORY_SCOPE_AGENT) == ep;
            if (__builtin_amdgcn_ballot_w64(!ok) == 0) break;
            __builtin_amdgcn_s_sleep(4);
          }
          if (lw == 0) BAR();
          else if (b == 0 && lane == 0) __hip_atomic_store((unsigned long long*)(ws + WS_EPO), ep, __ATOMIC_RELAXED, __HIP_MEMORY_SCOPE_AGENT);
        } else BAR();
      }
      BAR();
      if (lw == 0) {
        signal(cnt, C2 + (u1 >> 3), lane, (b & 8) ? 1u : 0x10000u);
        poll<PD2, PS2>(cnt + ((C2 + ((lane & 1) && tm3 ? tm3 - 1 : tm3)) * 8 + xc) * 32, 0x80000u);
      }
      if (!A_) { issue(0); issue(K3_STG); }
      touch();
      BAR();
      if (A_) { issue(0); issue(K3_STG); }
      wait_vm<N>();
      BAR();
      issue(2 * K3_STG);
      unsigned st = 0;
#pragma unroll 1
      for (int j = 0; j < 13; ++j) {
        wait_vm<N>();
        BAR();
        issue(st);
        st = st == 2 * K3_STG ? 0 : st + K3_STG;
        touch();
      }
      wait_vm<N>();
      BAR();
      issue4(0, 57344); issue4(5, 57344 + 5 * 16384);
      if (kpf < 0) kpf = 0;
      {
        glds4((P & 1) ? w3 : w1, ((kpf + ((lane >> 2) < VB3 ? lane >> 2 : 0)) * 2048 + (P >> 1) * 4 + (lane & 3)) * 128, lds_base + LDS_DUMMY); kpf += VB3; }
      wait_vm<9>();
    };
    if (isA) run3(T_()); else run3(F__());
    { auto issue = issue4;
      BAR();
      BAR();
      issue(1, 57344 + 16384);
      BAR();
      issue(2, 57344 + 2 * 16384); issue(3, 57344 + 3 * 16384); issue(4, 57344 + 4 * 16384);
      BAR();
      if (lw == 0) signal(cnt, C3 + tm3, lane);
      issue(6, 0); issue(7, 16384);
      if (lw == 0) poll<PD3, PS3>(cnt + ((C3 + tm4) * 8 + xc) * 32, 16);
      wait_vm<8>();
      BAR();
      wait_vm<0>();
      BAR();
      poll<0, 4>(cnt + (CM * 8 + cg) * 32, 4);
      f32x4 pv[4];
#pragma unroll
      for (int i = 0; i < 4; ++i) pv[i] = __builtin_bit_cast(f32x4, ld16<true>(ws + WS_MOD2, (((lane >> 4) * 4 + i) * 4096 + cg * 64 + (lane & 15) * 4) * 4));
      f32x4 v = (pv[0] + pv[1]) + (pv[2] + pv[3]);
#pragma unroll
      for (int e = 0; e < 4; ++e) { v[e] = rbf(xsum16_32(v[e])); if (lw == 2) v[e] = rbf(1.f + v[e]); if (lw == 1 || lw == 2) v[e] *= -1.4426950408889634f; }
      *(LDS_PTR(f32x4))(smem + LDS_VEC + lw * 256 + (lane & 15) * 16) = v;
      wait_lgkm0();
    }
    auto run5 = [&](auto isA_) {
      constexpr bool A_ = decltype(isA_)::value; constexpr int N = A_ ? 8 : 7;
      const char* sp = A_ ? ws + WS_H2 + (tm5 * 16 + lw * 8) * 1024 : (const char*)(lw == 2 ? w1 : w3) + tn5 * 224;
      unsigned vp[N];
#pragma unroll
      for (int i = 0; i < N; ++i) { const int L = i * 64 + lane, r = L / 14, c = L - r * 14; vp[i] = A_ ? L * 16 : r * (F_ * 2) + c * 16; }
      const unsigned d0 = lds_base + (A_ ? lw * 8192 : lw == 2 ? K5_G : K5_U);
      unsigned st = 3 * K5_STG;
      auto issue = [&]() {
#pragma unroll
        for (int i = 0; i < N; ++i) { if (A_) glds16s(sp, vp[i], d0 + st, i * 1024); else glds16(sp, vp[i], d0 + st, i * 1024); }
        sp += A_ ? 131072 : 32 * F_ * 2; st = st == (K5_NS - 1) * K5_STG ? 0 : st + K5_STG;
      };
      BAR();
      if (!A_) { issue(); issue(); }
      BAR();
      if (A_ && lw == 0) { signal(cnt, C4 + tm4, lane); poll<PD4>(cnt + ((C4 + tm5 * 2 + (lane & 1)) * 8 + xc) * 32, 16); }
      else { if (!A_) { __builtin_amdgcn_s_sleep(2); for (int s = 2; s < NB5; ++s) issue(); } }
      BAR();
      if (A_) { issue(); issue(); }
      wait_vm<A_ ? N : (NB5 - 1) * N>();
      BAR();
#pragma unroll 1
      for (int s = A_ ? 2 : NB5; s < K5_NS; ++s) issue();
#pragma unroll 1
      for (int j = 0; j < 27; ++j) {
        wait_vm<3 * N>();
        BAR();
        issue();
        touch(); __builtin_amdgcn_s_sleep(TS5); touch();
      }
      const char* sb = A_ ? ws + WS_HH + (kh6 * 56 * 128 + tm6 * 8 + lw * 4) * 1024 : (const char*)w2 + kh6 * 1792 * 2048 + tn6 * 256 + ((lw ^ kh6 ^ 1) & 1) * 128;
      unsigned vo[4];
#pragma unroll
      for (int i = 0; i < 4; ++i) {
        const int r = i * 8 + (lane >> 3), k = 8 * ((r >> 2) & 3) + 4 * (r >> 4) + (r & 3);
        vo[i] = A_ ? lane * 16 : k * 2048 - i * 1024 + swz;
      }
      const unsigned d6 = lds_base + (A_ ? K6_A : 0) + (lw & 1) * 4096;
      st = 0;
      const int tab = w2_stage(lane);
      int jn = 0;
      auto issue6 = [&]() {
        if (A_) { glds16x4<true>(sb, sb, vo, d6 + st); sb += 131072; }
        else { const int o = __builtin_amdgcn_readlane(tab, jn); const char* s0 = sb + (o & ~1); glds16x4<false>(s0, s0 + (o & 1) * 65536, vo, d6 + st); ++jn; }
        st = st == ((A_ ? K6_NA : K6_NW) - 1) * 8192 ? 0 : st + 8192;
      };
      auto pre6 = [&](int n) {
        if (!A_) {
#pragma unroll 1
          for (int i = 0; i < n; ++i) issue6();
        }
      };
      constexpr int E = A_ ? 0 : 4;
      { const int o = w2_stage(((xc & 1) ^ (lane >= 56)) * 28 + 7 * lw + (lane < 56 ? lane >> 3 : lane < 63 ? lane - 56 : 0));
        glds4((const char*)w2 + (kh6 * 1792 + ii) * 2048 + (tn6 >> 2) * 1024, (o & ~1) + (o & (ii >> 4) & 1) * 65536 + (lane < 56 ? lane & 7 : (3 - ((unsigned)(uintptr_t)w2 >> 7)) & 7) * 128,
              lds_base + LDS_DUMMY); }
      wait_vm<3 * N + 1>(); BAR();
      wait_vm<2 * N + 1>(); BAR(); pre6(PRE6);
      wait_vm<N + PRE6 * E + 1>(); BAR(); pre6(4);
      wait_vm<(PRE6 + 4) * E + 1>(); BAR(); pre6(8 - PRE6);
      BAR();
      if (A_) {
        volatile LDS_PTR(unsigned) go = (volatile LDS_PTR(unsigned))(smem + LDS_ARR + 4);
        if (lw == 0) { poll<PD5, PS5>(cnt + ((C5 + (tm6 >> 1) * 4 + kh6 * 2) * 8 + tn6) * 32, 16); *go = 1u; }
        else {
#pragma unroll 1
          for (unsigned spins = 0; spins < SPIN_CAP && __builtin_amdgcn_readfirstlane(*go) == 0; ++spins) __builtin_amdgcn_s_sleep(1);
          asm volatile("" ::: "memory");
        }
        STAMP_L(31);
        issue6(); issue6(); issue6();
        wait_vm<8>(); BAR(); issue6(); issue6();
        wait_vm<12>(); BAR(); issue6(); issue6();
      } else { wait_vm<16>(); BAR(); BAR(); }
#pragma unroll 1
      for (int c = 2; c < 51; ++c) { wait_vm<16>(); BAR(); if (A_ || !((SKIP6 >> c) & 1)) issue6(); }
      wait_vm<16>(); BAR(); wait_vm<12>(); BAR(); wait_vm<8>(); BAR(); wait_vm<4>(); BAR(); wait_vm<0>(); BAR();
    };
    if (isA) run5(T_()); else run5(F__());
    asm volatile("s_waitcnt lgkmcnt(0)" ::: "memory");
    return;
  }

  int fr = lane & 15, g = lane >> 4, p4 = fr & 3, row = 4 * g + (fr >> 2);
  int lsw = row * 128 + (((row >> 1) & 3) * 32) + p4 * 8;
  { const int mb = u1, hf = (b >> 3) & 1, kb0 = hf * 16 + wave * 2, cofs = (g & 1) * 16 + (g >> 1) * 8;
    u32x4 v[4];
    auto ldx = [&]() {
#pragma unroll
      for (int p = 0; p < 4; ++p) v[p] = *(const u32x4*)(x + (size_t)(mb * 16 + fr) * D_ + ((kb0 + (p & 1)) ^ ((p >> 1) * 16)) * 32 + cofs);
    };
    LDS_PTR(f32x2) sred = (LDS_PTR(f32x2))(smem + 4096);
    auto up2 = [](unsigned u) { return (f32x2){lo2f(u), hi2f(u)}; };
    auto stats = [&]() {
      f32x2 s2 = {0.f, 0.f}, q2 = {0.f, 0.f};
#pragma unroll
      for (int k = 0; k < 16; ++k) s2 += up2(v[k >> 2][k & 3]);
      const float mw = xsum16_32(s2[0] + s2[1]) * (1.f / 128.f);
#pragma unroll
      for (int k = 0; k < 16; ++k) { const f32x2 d = up2(v[k >> 2][k & 3]) - mw; q2 += d * d; }
      const float qw = xsum16_32(q2[0] + q2[1]);
      if (lane < 16) sred[wave * 16 + fr] = (f32x2){mw, qw};
      wait_lgkm0();
    };
    auto rest = [&]() {
    STAMP_AT(1 );
    float mean = 0.f, m2 = 0.f; f32x2 sw[8];
#pragma unroll
    for (int w = 0; w < 8; ++w) { sw[w] = sred[w * 16 + fr]; mean += sw[w][0]; m2 += sw[w][1]; }
    mean *= 0.125f;
#pragma unroll
    for (int w = 0; w < 8; ++w) { const float d = sw[w][0] - mean; m2 += 128.f * d * d; }
    const float rstd = ln_rstd(m2 * (1.f / 1024.f));
    mean = rbf(mean);
    unsigned nv[8];
#pragma unroll
    for (int k = 0; k < 8; ++k) {
      const f32x2 d = up2(v[k >> 2][k & 3]) - mean;
      const f32x2 n = up2(pk2(d[0], d[1])) * rstd;
      nv[k] = pk2(n[0], n[1]);
      asm volatile("" : "+v"(nv[k]));
    }
    SCHED0(); BAR(); SCHED0();
    STAMP_AT(2 );
    float mp[8];
#pragma unroll
    for (int i = 0; i < 8; ++i) mp[i] = __uint_as_float(__hip_atomic_load((unsigned*)(ws + WS_MOD) + hf * 512 + tid + i * 1024, __ATOMIC_RELAXED, __HIP_MEMORY_SCOPE_AGENT));
    *(LDS_PTR(unsigned))(smem + tid * 4) = pk2((mp[0] + mp[2]) + (mp[4] + mp[6]), 1.f + rbf((mp[1] + mp[3]) + (mp[5] + mp[7])));
    wait_lgkm0();
#pragma unroll
    for (int p = 0; p < 2; ++p) {
      const LDS_PTR(u32x4) tp = (const LDS_PTR(u32x4))(smem + ((wave * 2 + p) * 32 + cofs) * 4);
      const u32x4 t0 = tp[0], t1 = tp[1];
      unsigned o[4];
#pragma unroll
      for (int j = 0; j < 4; ++j) {
        const unsigned ta = j < 2 ? t0[2 * j] : t1[2 * j - 4], tb = j < 2 ? t0[2 * j + 1] : t1[2 * j - 3];
        const unsigned u = pk2(lo2f(nv[p * 4 + j]) * hi2f(ta), hi2f(nv[p * 4 + j]) * hi2f(tb));
        o[j] = pk2(lo2f(u) + lo2f(ta), hi2f(u) + lo2f(tb));
      }
      auto s0 = __builtin_amdgcn_permlane16_swap(o[0], o[2], false, false);
      auto s1 = __builtin_amdgcn_permlane16_swap(o[1], o[3], false, false);
      st16<true>(ws + WS_H, ((kb0 + p) * 128 + mb) * 1024 + lane * 16, (u32x4){s0[0], s1[0], s0[1], s1[1]});
    }
    STAMP_AT(3 );
    wait_vm<0>();
    STAMP_AT(4 );
    BAR();
    STAMP_AT(5 );
    };
    if (hf) {
      const int mrow = (u1 & 3) * 256 + (tid >> 3) * 4;
      const u32x2 c2 = *(const u32x2*)(cnd + mrow);
      u32x4 mw[4];
#pragma unroll
      for (int i = 0; i < 4; ++i) mw[i] = __builtin_nontemporal_load((const u32x4*)(wmod + (size_t)(mrow + i) * 6144 + (u1 >> 2) * 64 + (lane & 7) * 8));
      const unsigned mc[2] = {c2[0], c2[1]};
      *(LDS_PTR(f32x2))(smem + 8192 + (wave * 64 + (lane & 7) * 8 + (lane >> 4) * 2) * 4) = slab_sum<4>(mc, mw);
      wait_lgkm0();
      BAR();
      __builtin_amdgcn_s_sleep(XD1);
      ldx();
      stats();
      BAR();
      rest();
    } else {
      ldx();
      SCHED0();
      const int sl = (3 - ((unsigned)(uintptr_t)wqkv >> 7) - (xc >> 1) * 4) & 7;
      const int i2 = tid - 384, r0 = i2 < 0 ? tid / 12 : i2 / 3, c0 = i2 < 0 ? tid - r0 * 12 : (i2 - r0 * 3) * 4 + sl;
      const bool on = i2 < 0 || (sl < 4 && i2 < 96);
      const unsigned pf = *(const unsigned*)((const char*)wqkv + (xc >> 1) * 512 + (on ? ((((xc & 1) ^ (i2 >= 0)) * 512 + (ii >> 1) * 32 + r0) * 48 + (c0 >> 2) * 16 + (c0 & 3)) * 128 : 0));
      SCHED0(); BAR(); SCHED0();
      stats();
      SCHED0(); BAR(); SCHED0();
      rest();
      asm volatile("" ::"v"(pf));
    }
  }
  {
    f32x4 bias[2];
    { const int kbase = tm3 * 128 + wave * 16 - 16;
#pragma unroll
      for (int t = 0; t < 4; ++t) {
        const int kk = (t >> 1) * 16 + 4 * g + (t & 1) * 2 + (fr & 1), df = 16 + fr - kk;
        bias[t >> 1][(t & 1) * 2] = bias[t >> 1][(t & 1) * 2 + 1] = (df >= 0 && df <= 14 && kbase + kk >= 0) ? 0.f : -__builtin_inff();
      }
      asm volatile("" : "+v"(bias[0]), "+v"(bias[1])); }
    u32x4 mc, mw[8];
    if (wave < 4) {
      const int wm = wave & 1, bA = wave;
      unsigned a0 = lds_base + K3_B + (lsw ^ (bA * 32)), a1 = lds_base + K3_B + (lsw ^ ((bA ^ 1) * 32)), aA = lds_base + 1024 + wm * 4096 + lane * 16, aH = lds_base + lane * 16;
      unsigned lq = lds_base + bA * 512 + lane * 8, lv = lds_base + 36864 + bA * 512 + (lane ^ ((g >> 1) * 8)) * 8;
      int n = 4;
      asm volatile(".irp r,0,8,16,24,32,40,48,56,64,72,80,88,96\n\tv_mov_b64 v[\\r:\\r+1], 0\n\tv_mov_b64 v[\\r+2:\\r+3], 0\n\tv_mov_b64 v[\\r+4:\\r+5], 0\n\tv_mov_b64 v[\\r+6:\\r+7], 0\n\t.endr\n\t"
                   "s_setprio 2\n\ts_waitcnt vmcnt(0)\n\ts_barrier\n\t"
                   "s_barrier\n\t"
                   RA3(1, 0) RA3(0, 0) RW3(0, 0, a0, 0) RW3(0, 1, a0, 0) RW3(1, 0, a1, 0) RW3(1, 1, a1, 0) RW3(2, 0, a1, 0) RW3(2, 1, a1, 0) RW3(3, 0, a0, 0) RW3(3, 1, a0, 0) RH3(0)
                   RW3(4, 0, a0, 0) RW3(4, 1, a0, 0) RW3(5, 0, a1, 0) LG(8)
                   ".p2align 5\n1:\n\t" STAGE3(43008, ) STAGE3(43008, ) STAGE3(-86016, "s_sub_u32 %[n], %[n], 1\n\t") "s_cbranch_scc0 1b\n\t"
                   H2_3(0, , , , ) H1_3(1, )
                   H2_3(1, "v_add_u32 %[a0], %[mo], %[lq]\n\t", "v_add_u32 %[aA], %[mo], %[lv]\n\t", "v_xor_b32 %[a1], 512, %[a0]\n\tv_xor_b32 %[aH], 512, %[aA]\n\t", "s_barrier\n\t")
                   M3(1, 5) M3(0, 5) M3(2, 0) CV3(0, 0, 0) M3(3, 0) WR3(0, 0, a0, 0) CV3(1, 0, 1) M3(3, 1) WR3(1, 1, a0, 0) CV3(0, 1, 2) M3(2, 1) WR3(0, 2, a1, 0) CV3(1, 1, 3)
                   M3(2, 2) WR3(1, 3, a1, 0) CV3(0, 2, 0) M3(3, 2) WR3(0, 0, a1, 18432) CV3(1, 2, 1) M3(3, 3) WR3(1, 1, a1, 18432) CV3(0, 3, 2) M3(2, 3) WR3(0, 2, a0, 18432) CV3(1, 3, 3)
                   M3(4, 3) WR3(1, 3, a0, 18432) CV3(0, 4, 0) M3(4, 4) WR3(0, 0, aA, 0) CV3(1, 4, 1) M3(2, 4) WR3(1, 1, aA, 0) CV3(0, 5, 2) M3(3, 4) WR3(0, 2, aH, 0) CV3(1, 5, 3)
                   M3(3, 5) WR3(1, 3, aH, 0) CV3(2, 0, 0) M3(2, 5) WR3(2, 0, a0, 0)
                   CW3(3, 0, 1, a0, 0) CW3(3, 1, 2, a1, 0) CW3(2, 1, 3, a1, 0) CW3(2, 2, 0, a1, 18432) CW3(3, 2, 1, a1, 18432) CW3(3, 3, 2, a0, 18432) CW3(2, 3, 3, a0, 18432)
                   CV3(4, 3, 0) WR3(0, 0, lq, 18432) CV3(4, 4, 1) WR3(0, 1, lv, 0) CW3(2, 4, 2, aA, 0) CW3(3, 4, 3, aA, 0) CW3(3, 5, 0, aH, 0) CW3(2, 5, 1, aH, 0)
                   "s_setprio 0\n\t" LG(0)
                   : [a0] "+v"(a0), [a1] "+v"(a1), [aA] "+v"(aA), [aH] "+v"(aH), [n] "+s"(n)
                   : [lq] "v"(lq), [lv] "v"(lv), [mo] "s"((wm * 4 + 1) * 2048)
                   : "memory", "scc", V10(), V10(1), V10(2), V10(3), V10(4), V10(5), V10(6), V10(7), V10(8), V10(9), V10(10), V10(11), V10(12), V10(13), V8(14));
    } else {
      wait_vm<0>();
      BAR(); BAR();
      {
        const unsigned* pc = cnt + ((C2 + ((lane & 1) && tm3 ? tm3 - 1 : tm3)) * 8 + xc) * 32;
        unsigned seen = 0x80008u;
        BAR(); BAR(); BAR();
        if (wave == 4) asm volatile("global_load_dword %0, %1, off sc1" : "=v"(seen) : "v"(pc) : "memory");
        BAR(); BAR();
        asm volatile("s_waitcnt vmcnt(0)" : "+v"(seen)::"memory");
        if (__builtin_amdgcn_ballot_w64(seen < 0x80008u) != 0) poll<0>(pc, 0x80008u);
      }
#pragma unroll 1
      for (int t = 5; t < SLB; ++t) BAR();
      const int mrow = ((b & 3) * 4 + (wave & 3)) * 64 + (lane >> 3) * 8;
      mc = *(const u32x4*)(cnd + mrow);
#pragma unroll
      for (int i = 0; i < 8; ++i) mw[i] = __builtin_nontemporal_load((const u32x4*)(wmod + (size_t)(mrow + i) * 6144 + 2048 + (b >> 2) * 64 + (lane & 7) * 8));
      SCHED0();
#pragma unroll
      for (int t = SLB; t < 16; ++t) BAR();
    }
    lane = lane_id(); fr = lane & 15; g = lane >> 4; p4 = fr & 3; row = 4 * g + (fr >> 2); lsw = row * 128 + (((row >> 1) & 3) * 32) + p4 * 8;
    wait_lgkm0();
    BAR();
    STAMP_AT(9 );
    const LDS_PTR(char) eb = smem + wave * 2048;
    auto ld2 = [&](int o) { return cat8(*(const LDS_PTR(bf16x4))(eb + o), *(const LDS_PTR(bf16x4))(eb + o + 512)); };
    const int vo = 36864 + ((p4 * 16 + row) ^ ((p4 >> 1) * 8)) * 8;
    bf16x8 qf[2], k0[2], k1[2], v0[2], v1[2];
#pragma unroll
    for (int hd = 0; hd < 2; ++hd) { qf[hd] = ld2(2048 + hd * 1024 + lane * 8); k0[hd] = ld2(18432 + hd * 1024 + lane * 8); k1[hd] = ld2(18432 + 2048 + hd * 1024 + lane * 8); }
#pragma unroll
    for (int hd = 0; hd < 2; ++hd) {
      v0[hd] = cat8(ld_tr4(eb + vo + hd * 1024), ld_tr4(eb + vo + hd * 1024 + 2048));
      v1[hd] = cat8(ld_tr4(eb + vo + hd * 1024 + 512), ld_tr4(eb + vo + hd * 1024 + 512 + 2048));
    }
    SCHED0();
    const f32x4 z = {0.f, 0.f, 0.f, 0.f};
    const bool odd = fr & 1;
    const unsigned sel0 = odd ? 0x01000c0cu : 0x0c0c0100u, sel1 = odd ? 0x03020c0cu : 0x0c0c0302u;
    float inf = __builtin_inff(); asm("" : "+v"(inf));
    auto mx2 = [&](float a, float c) { return __builtin_amdgcn_fmed3f(a, c, inf); };
    auto both = [](float a, float c, auto op, auto fin) {
      auto u = __builtin_amdgcn_permlane16_swap(__float_as_uint(a), __float_as_uint(c), false, false);
      float r = op(__uint_as_float(u[0]), __uint_as_float(u[1]));
      auto w = __builtin_amdgcn_permlane32_swap(__float_as_uint(r), __float_as_uint(r), false, false);
      r = fin(op(__uint_as_float(w[0]), __uint_as_float(w[1])));
      auto y = __builtin_amdgcn_permlane16_swap(__float_as_uint(r), __float_as_uint(r), false, false);
      return (f32x2){__uint_as_float(y[0]), __uint_as_float(y[1])};
    };
    f32x2 l[2][2];
#pragma unroll
    for (int hd = 0; hd < 2; ++hd) {
      const f32x4 s0 = __builtin_amdgcn_mfma_f32_16x16x32_bf16(k0[hd], qf[hd], bias[0], 0, 0, 0), s1 = __builtin_amdgcn_mfma_f32_16x16x32_bf16(k1[hd], qf[hd], bias[1], 0, 0, 0);
#pragma unroll
      for (int t = 0; t < 2; ++t) {
        const f32x4 sv = t ? s1 : s0;
        unsigned u = pk2(odd ? sv[1] : sv[0], odd ? sv[3] : sv[2]);
        u = pk2(lo2f(u) * 0.17677669529663687f, hi2f(u) * 0.17677669529663687f);
        l[hd][t] = (f32x2){lo2f(u), hi2f(u)};
      }
    }
    const f32x2 mx = both(mx2(mx2(l[0][0][0], l[0][0][1]), mx2(l[0][1][0], l[0][1][1])), mx2(mx2(l[1][0][0], l[1][0][1]), mx2(l[1][1][0], l[1][1][1])), mx2,
                          [](float r) { return r * -1.4426950408889634f; });
    float sm[2];
#pragma unroll
    for (int hd = 0; hd < 2; ++hd) {
#pragma unroll
      for (int t = 0; t < 2; ++t) { const f32x2 e = l[hd][t] * 1.4426950408889634f + mx[hd]; l[hd][t] = (f32x2){__builtin_amdgcn_exp2f(e[0]), __builtin_amdgcn_exp2f(e[1])}; }
      const f32x2 a = l[hd][0] + l[hd][1]; sm[hd] = a[0] + a[1];
    }
    const f32x2 inv = both(sm[0], sm[1], [](float a, float c) { return a + c; }, [](float r) { return __builtin_amdgcn_rcpf(r); });
#pragma unroll
    for (int hd = 0; hd < 2; ++hd) {
      u32x4 pp;
#pragma unroll
      for (int t = 0; t < 2; ++t) {
        const f32x2 p = l[hd][t] * inv[hd]; const unsigned u = pk2(p[0], p[1]);
        pp[2 * t] = __builtin_amdgcn_perm(u, u, sel0); pp[2 * t + 1] = __builtin_amdgcn_perm(u, u, sel1);
      }
      const bf16x8 pf = __builtin_bit_cast(bf16x8, pp);
      const f32x4 oa = __builtin_amdgcn_mfma_f32_16x16x32_bf16(v0[hd], pf, z, 0, 0, 0), ob = __builtin_amdgcn_mfma_f32_16x16x32_bf16(v1[hd], pf, z, 0, 0, 0);
      u32x4 ov; ov[0] = pk2(oa[0], oa[1]); ov[1] = pk2(oa[2], oa[3]); ov[2] = pk2(ob[0], ob[1]); ov[3] = pk2(ob[2], ob[3]);
      st16<true>(ws + WS_O, ((tn3 * 2 + hd) * 128 + tm3 * 8 + wave) * 1024 + lane * 16, ov);
    }
    STAMP_AT(10 );
    wait_vm<0>();
    STAMP_AT(11 );
    BAR();
    STAMP_AT(12 );
    if (wave >= 4) {
      const unsigned m4[4] = {mc[0], mc[1], mc[2], mc[3]};
      const f32x2 ps = slab_sum<8>(m4, mw);
      if (!(lane & 8))
        __hip_atomic_store((unsigned long long*)(ws + WS_MOD2) + (((b & 3) * 4 + (wave & 3)) * 4096 + (b >> 2) * 64 + (lane & 7) * 8 + (lane >> 4) * 2) / 2,
                           ((unsigned long long)__float_as_uint(ps[1]) << 32) | __float_as_uint(ps[0]), __ATOMIC_RELAXED, __HIP_MEMORY_SCOPE_AGENT);
      wait_vm<0>();
      if (lane == 0 && __hip_atomic_fetch_add((LDS_PTR(unsigned))(smem + LDS_ARR + 8), 1u, __ATOMIC_RELAXED, __HIP_MEMORY_SCOPE_WORKGROUP) == 3)
        __hip_atomic_fetch_add(cnt + (CM * 8 + (b >> 2)) * 32, 1u, __ATOMIC_RELAXED, __HIP_MEMORY_SCOPE_AGENT);
    }
  }
  { const int wm = wave & 3, ks = wave >> 2, mb = tm4 * 8 + wm * 2 + ks;
    constexpr int R = 6;
    u32x4 rx[2];
#pragma unroll
    for (int p = 0; p < 2; ++p) rx[p] = *(const u32x4*)(x + (size_t)(mb * 16 + fr) * D_ + tn4 * 64 + p * 32 + (g & 1) * 16 + (g >> 1) * 8);
    const unsigned va0 = (ks * 128 + mb) * 1024 + lane * 16, va1 = va0 ^ 1024;
    bf16x8 fa[R][2], fw[2][4];
    auto issueA = [&](int t) {
      const char* pa = ws + WS_O + t * 262144;
      fa[t % R][0] = __builtin_bit_cast(bf16x8, ld16<true>(pa, va0));
      fa[t % R][1] = __builtin_bit_cast(bf16x8, ld16<true>(pa, va1));
    };
    unsigned aw[4];
    auto rdw = [&](int t, int j) {
      const int o = (t & 3) * 8192;
      fw[t & 1][j] = cat8(ld_tr4(smem + aw[j] + o), ld_tr4(smem + aw[j] + o + 2048));
    };
    auto stepw = [&](int t) {
      const int gi = t >> 2, base = gi < 3 ? 57344 + gi * 32768 : 0;
#pragma unroll
      for (int j = 0; j < 4; ++j) { aw[j] = base + ks * 4096 + (lsw ^ (j * 32)); asm volatile("" : "+v"(aw[j])); }
    };
    f32x4 acc[2][4];
#pragma unroll
    for (int i = 0; i < 2; ++i)
#pragma unroll
      for (int j = 0; j < 4; ++j) acc[i][j] = (f32x4){0.f, 0.f, 0.f, 0.f};
    stepw(0);
    wait_lgkm0();
    SCHED0(); BAR(); SCHED0();
    STAMP_AT(13 );
#pragma unroll
    for (int t = 0; t < R; ++t) issueA(t);
#pragma unroll
    for (int j = 0; j < 4; ++j) rdw(0, j);
    SCHED0();
#pragma unroll
    for (int t = 0; t < 16; ++t) {
      if (t < 15 && ((t + 1) & 3) == 0) stepw(t + 1);
#pragma unroll
      for (int j = 0; j < 4; ++j) {
        if (t < 15) rdw(t + 1, j);
        SCHED0();
#pragma unroll
        for (int i = 0; i < 2; ++i) acc[i][j] = __builtin_amdgcn_mfma_f32_16x16x32_bf16(fw[t & 1][j], fa[t % R][i], acc[i][j], 0, 0, 0);
        SCHED0();
      }
      if (t + R < 16) issueA(t + R);
      if (t == 2) {
#pragma unroll
        for (int p = 0; p < 2; ++p) {
          auto s0 = __builtin_amdgcn_permlane16_swap(rx[p][0], rx[p][2], false, false);
          auto s1 = __builtin_amdgcn_permlane16_swap(rx[p][1], rx[p][3], false, false);
          rx[p] = (u32x4){s0[0], s1[0], s0[1], s1[1]}; asm volatile("" : "+v"(rx[p]));
        }
      }
      if (t == 5) BAR();
      SCHED0();
    }
    STAMP_AT(14 );
    LDS_PTR(f32x4) red = (LDS_PTR(f32x4))(smem + K4_RED);
#pragma unroll
    for (int j = 0; j < 4; ++j) red[((wave ^ 4) * 4 + j) * 64 + lane] = acc[1][j];
    wait_lgkm0();
    BAR();
    STAMP_AT(15 );
    const LDS_PTR(float) vec = (const LDS_PTR(float))(smem + LDS_VEC);
    unsigned o[8];
#pragma unroll
    for (int j = 0; j < 4; ++j) {
      const f32x4 y = (acc[0][j] + red[(wave * 4 + j) * 64 + lane]) * *(const LDS_PTR(f32x4))(vec + j * 16 + 4 * g);
      const unsigned r0 = rx[j >> 1][(j & 1) * 2], r1 = rx[j >> 1][(j & 1) * 2 + 1];
      o[2 * j] = pk2(lo2f(r0) + y[0], hi2f(r0) + y[1]);
      o[2 * j + 1] = pk2(lo2f(r1) + y[2], hi2f(r1) + y[3]);
    }
    float f[16], s = 0.f;
#pragma unroll
    for (int j = 0; j < 8; ++j) { f[2 * j] = lo2f(o[j]); f[2 * j + 1] = hi2f(o[j]); s += f[2 * j] + f[2 * j + 1]; }
    const float mt = xsum16_32(s) * (1.f / 64.f);
    float qq = 0.f;
#pragma unroll
    for (int j = 0; j < 16; ++j) { const float d = f[j] - mt; qq += d * d; }
    qq = xsum16_32(qq);
    unsigned long long* slot = (unsigned long long*)(ws + WS_XCH) + (size_t)mb * 256 + fr;
    if (lane < 32) {
      const unsigned qb = qq == qq ? __float_as_uint(qq) : 0x7fc00000u;
      __hip_atomic_store(slot + g * 32768 + tn4 * 16, ((unsigned long long)qb << 32) | __float_as_uint(mt), __ATOMIC_RELAXED, __HIP_MEMORY_SCOPE_AGENT);
    }
    st16<true>(ws + WS_X1, (mb * 16 + tn4) * 2048 + lane * 16, (u32x4){o[0], o[1], o[2], o[3]});
    st16<true>(ws + WS_X1, (mb * 16 + tn4) * 2048 + 1024 + lane * 16, (u32x4){o[4], o[5], o[6], o[7]});
    STAMP_AT(16 );
    f32x4 ss[4], gg[4];
#pragma unroll
    for (int j = 0; j < 4; ++j) {
      ss[j] = *(const LDS_PTR(f32x4))(vec + 64 + j * 16 + 4 * g); gg[j] = *(const LDS_PTR(f32x4))(vec + 128 + j * 16 + 4 * g);
      asm volatile("" : "+v"(ss[j]), "+v"(gg[j]));
    }
    unsigned long long pv[4];
    __builtin_amdgcn_s_sleep(XS0);
    for (unsigned spins = 0;; ++spins) {
      bool ok = true;
#pragma unroll
      for (int i = 0; i < 4; ++i) { pv[i] = __hip_atomic_load(slot + (xc & 1) * 32768 + (4 * g + i) * 16, __ATOMIC_RELAXED, __HIP_MEMORY_SCOPE_AGENT); ok &= (unsigned)(pv[i] >> 32) != 0xffffffffu; }
      if (__builtin_amdgcn_ballot_w64(!ok) == 0 || spins > SPIN_CAP) break;
      __builtin_amdgcn_s_sleep(XS1);
    }
    STAMP_AT(17 );
    float ms = 0.f, m2 = 0.f;
#pragma unroll
    for (int i = 0; i < 4; ++i) { ms += __uint_as_float((unsigned)pv[i]); m2 += __uint_as_float((unsigned)(pv[i] >> 32)); }
    float mean = xsum16_32(ms) * (1.f / 16.f);
#pragma unroll
    for (int i = 0; i < 4; ++i) { const float d = __uint_as_float((unsigned)pv[i]) - mean; m2 += 64.f * d * d; }
    const float rstd = ln_rstd(xsum16_32(m2) * (1.f / 1024.f));
    mean = rbf(mean);
#pragma unroll
    for (int p = 0; p < 2; ++p) {
      u32x4 v;
#pragma unroll
      for (int j = 0; j < 4; ++j) {
        const int jb = 2 * p + (j >> 1), e = (j & 1) * 2;
        v[j] = pk2((f[8 * p + 2 * j] - mean) * rstd * gg[jb][e] + ss[jb][e], (f[8 * p + 2 * j + 1] - mean) * rstd * gg[jb][e + 1] + ss[jb][e + 1]);
      }
      st16<true>(ws + WS_H2, ((tn4 * 2 + p) * 128 + mb) * 1024 + lane * 16, v);
    }
    STAMP_AT(18 );
    wait_vm<0>();
    BAR();
    STAMP_AT(19 );

  }
  const int grp = wave >> 2;
  { const int wm = wave & 3;
    const LDS_PTR(char) sb = smem + 3 * K5_STG;
    const LDS_PTR(char) aP = sb + K5_G + row * 224 + grp * 96 + p4 * 8;
    const LDS_PTR(char) aM = sb + (p4 < 2 ? K5_G : K5_U) + row * 224 + (12 + grp) * 16 + (p4 & 1) * 8;
    const LDS_PTR(char) aA = sb + wm * 4096 + lane * 16;
    f32x32 c0, c1, c2; f32x16 c3;
    asm volatile(".irp r,0,8,16,24,32,40,48,56,64,72,80,88,96,104\n\tv_mov_b64 v[\\r:\\r+1], 0\n\tv_mov_b64 v[\\r+2:\\r+3], 0\n\tv_mov_b64 v[\\r+4:\\r+5], 0\n\tv_mov_b64 v[\\r+6:\\r+7], 0\n\t.endr"
                 : "={v[0:31]}"(c0), "={v[32:63]}"(c1), "={v[64:95]}"(c2), "={v[96:111]}"(c3)::"memory");
    int wrap = -(K5_NS - 1) * K5_STG, dl = K5_STG, n = 7;
    unsigned mask = wrap_mask(4, K5_NS);
    asm volatile("" : "+s"(wrap), "+v"(aP), "+v"(aM), "+v"(aA));
    if (grp) __builtin_amdgcn_s_setprio(1); else __builtin_amdgcn_s_setprio(2);
    BAR();
    STAMP_AT(20 );
    BAR();
    STAMP_G(21, 26);
    asm volatile(RA5(0) RA5(1) RP5(0, 0) RP5(0, 1) RP5(1, 0) RP5(1, 1) RP5(2, 0) RP5(2, 1) RP5(3, 0) RP5(3, 1) RP5(4, 0) RP5(4, 1) RP5(5, 0) RP5(5, 1) RW5(6, 0, aM, 0) LG(9)
                 ".p2align 5\n1:\n\t" TRIP5("") TRIP5("") TRIP5("") TRIP5("s_sub_u32 %[n], %[n], 1\n\t") "s_cbranch_scc0 1b\n\t" LG(0) "s_nop 7"
                 : ACC5, [aP] "+v"(aP), [aM] "+v"(aM), [aA] "+v"(aA), [dl] "+s"(dl), [mask] "+s"(mask), [n] "+s"(n) : [wrap] "s"(wrap) : CLOB5);
    f32x4 acc[4][7];
#pragma unroll
    for (int e = 0; e < 112; ++e) acc[e / 28][(e % 28) / 4][e & 3] = e < 32 ? c0[e & 31] : e < 64 ? c1[e & 31] : e < 96 ? c2[e & 31] : c3[e & 15];
    STAMP_G(22, 27);
    auto swi4 = [](const f32x4& gv, const f32x4& uv, unsigned& o0, unsigned& o1) {
      constexpr float c2 = 2.0813689810056077f;
      f32x2 pa = gv.xy * uv.xy, pb = gv.zw * uv.zw;
      const f32x2 ta = (f32x2){__builtin_amdgcn_exp2f(gv[0]), __builtin_amdgcn_exp2f(gv[1])} * c2 + c2, tb = (f32x2){__builtin_amdgcn_exp2f(gv[2]), __builtin_amdgcn_exp2f(gv[3])} * c2 + c2;
      pa *= (f32x2){__builtin_amdgcn_rcpf(ta[0]), __builtin_amdgcn_rcpf(ta[1])}; pb *= (f32x2){__builtin_amdgcn_rcpf(tb[0]), __builtin_amdgcn_rcpf(tb[1])};
      o0 = pk2(pa[0], pa[1]); o1 = pk2(pb[0], pb[1]);
    };
    const int lz = lane_id();
    const int gz = lz >> 4, fz = lz & 15;
    const int qb = (tn5 >> 4) * 224, ql = qb + 64 + (tn5 & 15) * 10;
    auto hof = [&](int e, int i) { return (unsigned)((e >> 2) * 131072 + (e & 3) * 256 + fz * 16 + i * 1024); };
    const unsigned ho0 = hof((grp ? ql + 2 : qb + (tn5 & 15) * 4) + 2 * (gz & 1) + (gz >> 1), 0), ho1 = hof(ql + 6 * grp + (gz >> 1), gz & 1), hom = hof(ql + 8 + grp, gz);
    char* hb = ws + WS_HH + (tm5 * 16 + wm * 4) * 1024;
    const int c5 = C5 + tm5 * 4 + (tn5 >> 4) * 2;
    auto tr16 = [](const unsigned (&o)[4]) {
      auto s0 = __builtin_amdgcn_permlane16_swap(o[0], o[2], false, false);
      auto s1 = __builtin_amdgcn_permlane16_swap(o[1], o[3], false, false);
      u32x4 v = {s0[0], s1[0], s0[1], s1[1]}; asm volatile("" : "+v"(v)); return v;
    };
    auto pair = [&](int p, int i) {
      unsigned o[4];
      swi4(acc[i][2 * p], acc[i][2 * p + 1], o[0], o[1]);
      swi4(acc[i + 1][2 * p], acc[i + 1][2 * p + 1], o[2], o[3]);
      return tr16(o);
    };
    u32x4 v[3];
#pragma unroll
    for (int i = 0; i < 4; ++i) {
      unsigned o[4];
      swi4(acc[i][0], acc[i][1], o[0], o[1]);
      swi4(acc[i][2], acc[i][3], o[2], o[3]);
      st16<true>(hb, ho0 + i * 1024, tr16(o));
      SCHED0();
    }
    STAMP_G(23, 28);
    v[0] = pair(2, 0);
    if (!grp) {
      wait_vm<0>();
      unsigned arr = 0;
      if (lz == 0) arr = __hip_atomic_fetch_add((LDS_PTR(unsigned))(smem + LDS_ARR), 1u, __ATOMIC_RELAXED, __HIP_MEMORY_SCOPE_WORKGROUP);
      if (__builtin_amdgcn_readfirstlane(arr) == 3) signal(cnt, c5, lz);
    }
    STAMP_G(24, 29);
    v[1] = pair(2, 2);
    { unsigned m[4];
#pragma unroll
      for (int i = 0; i < 4; i += 2) {
        f32x4 gv, uv;
#pragma unroll
        for (int e = 0; e < 4; ++e) {
          auto w = __builtin_amdgcn_permlane32_swap(__float_as_uint(acc[i + (e >> 1)][6][e & 1]), __float_as_uint(acc[i + (e >> 1)][6][2 + (e & 1)]), false, false);
          gv[e] = __uint_as_float(w[0]); uv[e] = __uint_as_float(w[1]);
        }
        swi4(gv, uv, m[i], m[i + 1]);
      }
      auto x0 = __builtin_amdgcn_permlane32_swap(m[0], m[2], false, false);
      auto x1 = __builtin_amdgcn_permlane32_swap(m[1], m[3], false, false);
      const unsigned o[4] = {x0[0], x0[1], x1[0], x1[1]};
      v[2] = tr16(o);
      st16<true>(hb, ho1, v[0]); st16<true>(hb, ho1 + 2048, v[1]); st16<true>(hb, hom, v[2]);
    }
    STAMP_G(25, 30);
    if (grp) {
#pragma unroll
      for (int t = 0; t < NDR; ++t) BAR();
      wait_vm<0>();
      BAR();
      if (wave == 4) signal(cnt, c5 + 1, lz);
      if (wave > 4) return;
#pragma unroll 1
      for (int t = NDR + 1; t < 9; ++t) BAR();
      unsigned seen = 16;
      const unsigned* pc = cnt + ((C5 + (tm6 >> 1) * 4 + kh6 * 2 + 1) * 8 + tn6) * 32;
      if (wave == 4) asm volatile("global_load_dword %0, %1, %2 sc1" : "=v"(seen) : "v"(0), "s"(pc) : "memory");
      BAR(); BAR();
      asm volatile("s_waitcnt vmcnt(0)" : "+v"(seen)::"memory");
      if (__builtin_amdgcn_readfirstlane(seen) < 16) poll<0>(pc, 16);
      return;
    }
  }
  { const int lane = lane_id(), fr = lane & 15, g = lane >> 4, p4 = fr & 3, row = 4 * g + (fr >> 2);
    const int wm = wave & 1, wn = wave >> 1;
    const int lw0 = row * 128 + (((row >> 1) & 3) * 32) + p4 * 8;
    const int mb0 = tm6 * 8 + wm * 4, t64 = tn6 * 2 + kh6;
    u32x4 rv[4];
#pragma unroll
    for (int i = 0; i < 4; ++i) rv[i] = ld16<true>(ws + WS_X1, ((mb0 + i) * 16 + t64) * 2048 + wn * 1024 + lane * 16);
    f32x4 acc[4][4];
#pragma unroll
    for (int i = 0; i < 4; ++i)
#pragma unroll
      for (int j = 0; j < 4; ++j) { acc[i][j] = (f32x4){0.f, 0.f, 0.f, 0.f}; asm volatile("" : "+v"(acc[i][j])); }
    struct Fr { bf16x8 a[4]; bf16x4 w[4][2]; };
    const LDS_PTR(char) aw0 = smem + (lw0 ^ (wn * 64)); const LDS_PTR(char) aw1 = smem + (lw0 ^ (wn * 64 + 32));
    const LDS_PTR(char) aa = smem + K6_A + wm * 4096 + lane * 16;
    auto rd1 = [&](Fr& f, int r) {
      if (r < 2) f.w[0][r] = ld_tr4(aw0 + r * 2048);
      else if (r < 4) f.a[r - 2] = *(const LDS_PTR(bf16x8))(aa + (r - 2) * 1024);
      else if (r < 10) { const int j = (r - 2) >> 1; f.w[j][r & 1] = ld_tr4(((j & 1) ? aw1 : aw0) + (j >> 1) * 4096 + (r & 1) * 2048); }
      else f.a[r - 8] = *(const LDS_PTR(bf16x8))(aa + (r - 8) * 1024);
    };
    int dl, da, wrap = -(K6_NW - 1) * 8192, wrapa = -(K6_NA - 1) * 8192;
    unsigned mask = wrap_mask(1, K6_NW), maska = wrap_mask(1, K6_NA);
    Fr fA, fB;
    asm volatile("" : "+s"(wrap), "+s"(wrapa), "+v"(aw0), "+v"(aw1), "+v"(aa));
    wait_vm<0>();
    BAR();
    STAMP_AT(32 );
#pragma unroll
    for (int r = 0; r < 12; ++r) { SCHED0(); rd1(fA, r); }
    SCHED0();
    aw0 += 8192; aw1 += 8192; aa += 8192;
    wait_lgkm<6>();
    BAR();
    auto trip = [&](const Fr& cur, Fr& nxt) {
      SCHED0();
#pragma unroll
      for (int m = 0; m < 16; ++m) {
        const int j = (m & 7) >> 1, i = (m >> 3) * 2 + ((m ^ j) & 1);
        if (m == 2) mfma_sel<8192>(acc[i][j], cat8(cur.w[j][0], cur.w[j][1]), cur.a[i], mask, dl, wrap);
        else if (m == 6) mfma_sel<8192>(acc[i][j], cat8(cur.w[j][0], cur.w[j][1]), cur.a[i], maska, da, wrapa);
        else mfma_acc(acc[i][j], cat8(cur.w[j][0], cur.w[j][1]), cur.a[i]);
        SCHED0();
        if (m < 12) { rd1(nxt, m); SCHED0(); }
        if (m == 3) wait_lgkm<4>();
        if (m == 12) { aw0 += dl; aw1 += dl; asm volatile("" : "+v"(aw0), "+v"(aw1)); }
        if (m == 13) { aa += da; asm volatile("" : "+v"(aa)); }
        if (m == 14) wait_lgkm<6>();
        if (m == 15) BAR();
        SCHED0();
      }
    };
#pragma unroll 1
    for (int h = 0; h < 2; ++h) {
#pragma unroll 1
      for (int t = 0; t < 7; ++t) { trip(fA, fB); trip(fB, fA); trip(fA, fB); trip(fB, fA); }
      mask = wrap_mask(29, K6_NW); maska = wrap_mask(29, K6_NA);
    }
    mfma_done(acc);
    STAMP_AT(34 );
    const int slot = wm * 2 + wn, pb = (b ^ 4) * 4 + slot;
    unsigned* flags = (unsigned*)(ws + WS_FLG);
    { char* pp = ws + WS_PART + (size_t)(b * 4 + slot) * 4096;
#pragma unroll
      for (int i = 0; i < 4; ++i)
        st16<true>(pp, i * 1024 + lane * 16, (u32x4){pk2(acc[i][0][0], acc[i][0][1]), pk2(acc[i][0][2], acc[i][0][3]), pk2(acc[i][1][0], acc[i][1][1]), pk2(acc[i][1][2], acc[i][1][3])});
      wait_vm<0>();
      STAMP_AT(35 );
      if (lane == 0) __hip_atomic_store(flags + (b * 4 + slot) * 16, 1u, __ATOMIC_RELAXED, __HIP_MEMORY_SCOPE_AGENT);
    }
    { const LDS_PTR(float) vec = (const LDS_PTR(float))(smem + LDS_VEC) + 192 + wn * 32 + 4 * g;
      const f32x4 gv0 = *(const LDS_PTR(f32x4))vec, gv1 = *(const LDS_PTR(f32x4))(vec + 16);
      poll<PD6>(flags + pb * 16, 1);
      STAMP_AT(36 );
      const char* pp = ws + WS_PART + (size_t)pb * 4096;
      u32x4 z[4];
#pragma unroll
      for (int i = 0; i < 4; ++i) z[i] = ld16<true>(pp, i * 1024 + lane * 16);
#pragma unroll
      for (int i = 0; i < 4; ++i) {
        const f32x4 v0 = (acc[i][2] + (f32x4){lo2f(z[i][0]), hi2f(z[i][0]), lo2f(z[i][1]), hi2f(z[i][1])}) * gv0;
        const f32x4 v1 = (acc[i][3] + (f32x4){lo2f(z[i][2]), hi2f(z[i][2]), lo2f(z[i][3]), hi2f(z[i][3])}) * gv1;
        const unsigned o0 = pk2(lo2f(rv[i][0]) + v0[0], hi2f(rv[i][0]) + v0[1]), o1 = pk2(lo2f(rv[i][1]) + v0[2], hi2f(rv[i][1]) + v0[3]);
        const unsigned o2 = pk2(lo2f(rv[i][2]) + v1[0], hi2f(rv[i][2]) + v1[1]), o3 = pk2(lo2f(rv[i][3]) + v1[2], hi2f(rv[i][3]) + v1[3]);
        auto s0 = __builtin_amdgcn_permlane16_swap(o0, o2, false, false);
        auto s1 = __builtin_amdgcn_permlane16_swap(o1, o3, false, false);
        st16<false>(out, (((mb0 + i) * 16 + fr) * D_ + t64 * 64 + wn * 32 + (g & 1) * 16 + (g >> 1) * 8) * 2, (u32x4){s0[0], s1[0], s0[1], s1[1]});
      }
    }
    STAMP_AT(37 );
  }
}

extern "C" void kernel_launch(void* const* d_in, const int* in_sizes, int n_in, void* d_out, int out_size, void* d_ws,
                              size_t ws_size, hipStream_t stream) {
  const u16* x = (const u16*)d_in[0];
  const u16* wqkv = (const u16*)d_in[1];
  const u16* wo = (const u16*)d_in[2];
  const u16* w1 = (const u16*)d_in[4];
  const u16* w3 = (const u16*)d_in[5];
  const u16* w2 = (const u16*)d_in[6];
  const u16* c = (const u16*)d_in[7];
  const u16* wmod = (const u16*)d_in[8];
  (void)hipFuncSetAttribute((const void*)k2_block, hipFuncAttributeMaxDynamicSharedMemorySize, MEGA_LDS);
  char* ws = (char*)d_ws;
  k2_block<<<256, 768, MEGA_LDS, stream>>>(x, wqkv, wo, w1, w3, w2, c, wmod, (u16*)d_out, ws);
}
```

```cpp
#include <hip/hip_runtime.h>
#include <stdint.h>
#include <type_traits>
#pragma clang diagnostic ignored "-Winline-asm"

typedef unsigned short u16;
typedef short bf16x8 __attribute__((ext_vector_type(8)));
typedef short bf16x4 __attribute__((ext_vector_type(4)));
typedef float f32x4 __attribute__((ext_vector_type(4)));
typedef float f32x2 __attribute__((ext_vector_type(2)));
typedef unsigned int u32x4 __attribute__((ext_vector_type(4)));
typedef unsigned int u32x2 __attribute__((ext_vector_type(2)));
#define LDS_PTR(T) __attribute__((address_space(3))) T*
#define BAR() do { asm volatile("" ::: "memory"); __builtin_amdgcn_s_barrier(); asm volatile("" ::: "memory"); } while (0)
#define SCHED0() __builtin_amdgcn_sched_barrier(0)
template <int N> using IC = std::integral_constant<int, N>;
typedef std::integral_constant<bool, true> T_;
typedef std::integral_constant<bool, false> F__;

#define PROBE 0
constexpr int S_ = 2048, D_ = 1024, F_ = 3584;
#define STAMP_AT(i) do { if ((i) == 0) pr0 = __builtin_amdgcn_s_memrealtime(); else if ((i) == PROBE) prk = __builtin_amdgcn_s_memrealtime(); \
  if ((i) == 37 && wave == 0) { __builtin_amdgcn_s_waitcnt(0); if (PROBE == 37) prk = __builtin_amdgcn_s_memrealtime(); while ((long long)__builtin_amdgcn_s_memrealtime() < prk + 10000) __builtin_amdgcn_s_sleep(1); } } while (0)
#define STAMP_G(ia, ib) STAMP_AT(ia)
#define STAMP_L(i)

__device__ __forceinline__ float bf2f(u16 v) { return __uint_as_float(((unsigned)v) << 16); }
__device__ __forceinline__ unsigned pk2(float lo, float hi) {
  typedef __bf16 bf2 __attribute__((ext_vector_type(2)));
  f32x2 v = {lo, hi}; bf2 r = __builtin_convertvector(v, bf2); return __builtin_bit_cast(unsigned, r);
}
__device__ __forceinline__ float lo2f(unsigned u) { return __uint_as_float(u << 16); }
__device__ __forceinline__ float hi2f(unsigned u) { return __uint_as_float(u & 0xffff0000u); }
__device__ __forceinline__ float rbf(float v) { return lo2f(pk2(v, 0.f)); }
#define glds16(sbase, voff, lds_off, imm) \
  asm volatile("s_add_u32 m0, %2, %3\n\ts_nop 0\n\tglobal_load_lds_dwordx4 %0, %1" ::"v"(voff), "s"(sbase), "s"(lds_off), "n"(imm) : "memory", "scc")
#define glds16s(sbase, voff, lds_off, imm) \
  asm volatile("s_add_u32 m0, %2, %3\n\ts_nop 0\n\tglobal_load_lds_dwordx4 %0, %1 sc1" ::"v"(voff), "s"(sbase), "s"(lds_off), "n"(imm) : "memory", "scc")
#define GLDS4(SC) "s_mov_b32 m0, %6\n\ts_nop 0\n\tglobal_load_lds_dwordx4 %0, %4" SC "\n\tglobal_load_lds_dwordx4 %1, %5 offset:1024" SC \
                  "\n\tglobal_load_lds_dwordx4 %2, %4 offset:2048" SC "\n\tglobal_load_lds_dwordx4 %3, %5 offset:3072" SC
template <bool SC1> __device__ __forceinline__ void glds16x4(const void* s0, const void* s1, const unsigned (&v)[4], unsigned lds) {
  if (SC1) asm volatile(GLDS4(" sc1") ::"v"(v[0]), "v"(v[1]), "v"(v[2]), "v"(v[3]), "s"(s0), "s"(s1), "s"(lds) : "memory");
  else asm volatile(GLDS4("") ::"v"(v[0]), "v"(v[1]), "v"(v[2]), "v"(v[3]), "s"(s0), "s"(s1), "s"(lds) : "memory");
}
__device__ __forceinline__ void glds4(const void* sbase, unsigned voff, unsigned lds_off) {
  asm volatile("s_mov_b32 m0, %2\n\ts_nop 0\n\tglobal_load_lds_dword %0, %1" ::"v"(voff), "s"(sbase), "s"(lds_off) : "memory");
}
__device__ __forceinline__ void stouch4(const void* p) {
  asm volatile("s_load_dwordx2 s[100:101], %0, 0x3c\n\ts_load_dwordx2 s[100:101], %0, 0xbc\n\ts_load_dwordx2 s[100:101], %0, 0x13c\n\ts_load_dwordx2 s[100:101], %0, 0x1bc"
               ::"s"(p) : "memory", "s100", "s101");
}
template <bool SC1> __device__ __forceinline__ void st16(void* sbase, unsigned voff, u32x4 v) {
  __builtin_amdgcn_raw_buffer_store_b128(v, __builtin_amdgcn_make_buffer_rsrc(sbase, 0, 0x7fffffff, 0x00020000), voff, 0, SC1 ? 16 : 0);
}
template <bool SC1> __device__ __forceinline__ u32x4 ld16(const void* sbase, unsigned voff) {
  return __builtin_amdgcn_raw_buffer_load_b128(__builtin_amdgcn_make_buffer_rsrc((void*)sbase, 0, 0x7fffffff, 0x00020000), voff, 0, SC1 ? 16 : 0);
}
template <int N> __device__ __forceinline__ void wait_vm() { asm volatile("s_waitcnt vmcnt(%0)" ::"n"(N) : "memory"); }
__device__ __forceinline__ void wait_lgkm0() { asm volatile("" ::: "memory"); __builtin_amdgcn_s_waitcnt(0xC07F); asm volatile("" ::: "memory"); }
__device__ __forceinline__ bf16x4 ld_tr4(const LDS_PTR(char) p) { return __builtin_amdgcn_ds_read_tr16_b64_v4i16((LDS_PTR(bf16x4))p); }
__device__ __forceinline__ bf16x8 cat8(bf16x4 lo, bf16x4 hi) { return __builtin_shufflevector(lo, hi, 0, 1, 2, 3, 4, 5, 6, 7); }
__device__ __forceinline__ void mfma_acc(f32x4& acc, bf16x8 a, bf16x8 b) { asm volatile("v_mfma_f32_16x16x32_bf16 %0, %1, %2, %0" : "+v"(acc) : "v"(a), "v"(b)); }
template <int STEP> __device__ __forceinline__ void mfma_sel(f32x4& acc, bf16x8 a, bf16x8 b, unsigned& mask, int& dl, int wrap) {
  asm volatile("s_add_u32 %1, %1, %1\n\tv_mfma_f32_16x16x32_bf16 %0, %3, %4, %0\n\ts_cselect_b32 %2, %5, %6" : "+v"(acc), "+s"(mask), "=s"(dl) : "v"(a), "v"(b), "s"(wrap), "n"(STEP) : "scc");
}
constexpr unsigned wrap_mask(int first, int ns) { unsigned m = 0; for (int t = 0; t < 32; ++t) if ((first + t) % ns == ns - 1) m |= 0x80000000u >> t; return m; }
template <int N> __device__ __forceinline__ void wait_lgkm() { __builtin_amdgcn_s_waitcnt(0xC07F | (N << 8)); }
template <int R, int C> __device__ __forceinline__ void mfma_done(f32x4 (&acc)[R][C]) {
  asm volatile("s_nop 9");
#pragma unroll
  for (int i = 0; i < R; ++i)
#pragma unroll
    for (int j = 0; j < C; ++j) asm volatile("" : "+v"(acc[i][j]));
}
__device__ __forceinline__ int lane_id() { int l; asm volatile("v_mbcnt_lo_u32_b32 %0, -1, 0\n\tv_mbcnt_hi_u32_b32 %0, -1, %0" : "=v"(l)); return l; }
__device__ __forceinline__ float xsum16_32(float v) {
  auto a = __builtin_amdgcn_permlane16_swap(__float_as_uint(v), __float_as_uint(v), false, false); v = __uint_as_float(a[0]) + __uint_as_float(a[1]);
  auto c = __builtin_amdgcn_permlane32_swap(__float_as_uint(v), __float_as_uint(v), false, false); return __uint_as_float(c[0]) + __uint_as_float(c[1]);
}

__device__ __forceinline__ float ln_rstd(float var) { return __builtin_amdgcn_rcpf(rbf(__builtin_amdgcn_sqrtf(rbf(rbf(var) + 1e-5f)))); }

constexpr size_t WS_MOD = 0;
constexpr size_t WS_EPO = 64 << 10, WS_FL1 = 96 << 10;
constexpr size_t WS_CTL = 128 << 10;
constexpr int C2 = 0, C3 = 16, C4 = 32, C5 = 48, CM = 80;
constexpr size_t WS_FLG = WS_CTL + (88 << 10), CTL_BYTES = 152 << 10;
constexpr size_t WS_XCH = 512 << 10;
constexpr size_t WS_MOD2 = 1 << 20;
constexpr size_t WS_H = 2 << 20, WS_O = WS_H + (4 << 20), WS_X1 = WS_O + (4 << 20), WS_H2 = WS_X1 + (4 << 20), WS_HH = WS_H2 + (4 << 20),
                 WS_PART = WS_HH + (size_t)S_ * F_ * 2;

template <int NL> __device__ __forceinline__ f32x2 slab_sum(const unsigned (&mc)[NL / 2], const u32x4 (&mw)[NL]) {
  f32x2 a2[4] = {};
#pragma unroll
  for (int i = 0; i < NL; ++i) {
    const float cv = (i & 1) ? hi2f(mc[i >> 1]) : lo2f(mc[i >> 1]);
    const float sv = rbf(cv * __builtin_amdgcn_rcpf(1.f + __builtin_amdgcn_exp2f(cv * -1.4426950408889634f)));
#pragma unroll
    for (int j = 0; j < 4; ++j) a2[j] += (f32x2){lo2f(mw[i][j]), hi2f(mw[i][j])} * sv;
  }
  float acc[8] = {a2[0][0], a2[0][1], a2[1][0], a2[1][1], a2[2][0], a2[2][1], a2[3][0], a2[3][1]};
#pragma unroll
  for (int j = 0; j < 4; ++j) { auto a = __builtin_amdgcn_permlane32_swap(__float_as_uint(acc[j]), __float_as_uint(acc[j + 4]), false, false); acc[j] = __uint_as_float(a[0]) + __uint_as_float(a[1]); }
#pragma unroll
  for (int j = 0; j < 2; ++j) { auto a = __builtin_amdgcn_permlane16_swap(__float_as_uint(acc[j]), __float_as_uint(acc[j + 2]), false, false); acc[j] = __uint_as_float(a[0]) + __uint_as_float(a[1]); }
#pragma unroll
  for (int j = 0; j < 2; ++j) acc[j] += __uint_as_float(__builtin_amdgcn_mov_dpp(__float_as_uint(acc[j]), 0x128, 0xf, 0xf, false));
  return (f32x2){acc[0], acc[1]};
}

constexpr int K3_B = 18432, K3_STG = 18432 + 24576;
constexpr int K4_RED = 57344;
constexpr int K5_G = 16384, K5_U = 16384 + 7168 + 16, K5_STG = 30736, K5_NS = 5;
constexpr int K6_NW = 12, K6_NA = 7, K6_A = K6_NW * 8192;
constexpr int LDS_VEC = 158720, LDS_DUMMY = LDS_VEC + 1024, LDS_ARR = LDS_DUMMY + 256, MEGA_LDS = LDS_ARR + 64;
constexpr unsigned SPIN_CAP = 1u << 19;
#define KNOBS PD2 = 8, PS2 = 3, PD3 = 12, PS3 = 3, XS0 = 22, XS1 = 4, PD4 = 12, PD5 = 44, PS5 = 4, PD6 = 2, XD1 = 12, TS5 = 3, NB5 = 4, VB3 = 4, SLB = 15, PRE6 = 4, NDR = 2
enum { KNOBS };
constexpr unsigned long long SKIP6 = 0x7c;
static_assert(__builtin_popcountll(SKIP6) == 5, "44 stages of W are issued behind 49 barriers");

__device__ __forceinline__ void signal(unsigned* cnt, int c, int lane, unsigned v = 1) {
  if (lane < 8) __hip_atomic_fetch_add(cnt + (c * 8 + lane) * 32, v, __ATOMIC_RELAXED, __HIP_MEMORY_SCOPE_AGENT);
}
template <int D0, int D1 = 1> __device__ __forceinline__ void poll(const unsigned* cnt, unsigned want) {
  if (D0) __builtin_amdgcn_s_sleep(D0);
#pragma unroll 1
  for (unsigned spins = 0; spins < SPIN_CAP; ++spins) {
    if (__builtin_amdgcn_ballot_w64(__hip_atomic_load(cnt, __ATOMIC_RELAXED, __HIP_MEMORY_SCOPE_AGENT) < want) == 0) break;
    __builtin_amdgcn_s_sleep(D1);
  }
  __builtin_amdgcn_fence(__ATOMIC_ACQUIRE, "wavefront");
}

typedef float f32x32 __attribute__((ext_vector_type(32)));
typedef float f32x16 __attribute__((ext_vector_type(16)));
#define M5(i, j) "v_mfma_f32_16x16x32_bf16 v[28*" #i "+4*" #j ":28*" #i "+4*" #j "+3], v[128+4*" #j ":131+4*" #j "], v[112+4*" #i ":115+4*" #i "], v[28*" #i "+4*" #j ":28*" #i "+4*" #j "+3]\n\t"
#define RA5(i) "ds_read_b128 v[112+4*" #i ":115+4*" #i "], %[aA] offset:1024*" #i "\n\t"
#define RW5(j, h, a, o) "ds_read_b64_tr_b16 v[128+4*" #j "+2*" #h ":129+4*" #j "+2*" #h "], %[" #a "] offset:" #o "\n\t"
#define ST5(a) "v_add_u32 %[" #a "], %[dl], %[" #a "]\n\t"
#define SEL5A "s_add_u32 %[mask], %[mask], %[mask]\n\t"
#define SEL5B "s_cselect_b32 %[dl], %[wrap], 30736\n\t"
#define LG(n) "s_waitcnt lgkmcnt(" #n ")\n\t"
#define RP5(j, h) RW5(j, h, aP, 32*(j/2)+7184*(j&1)+3584*h)
#define TRIP5(X)                                                                                                                                        \
  M5(0, 0) RW5(6, 1, aM, 3584) M5(1, 0) RA5(2) ST5(aM) M5(1, 1) RA5(3) ST5(aP) M5(0, 1) ST5(aA) LG(6) M5(0, 2) M5(1, 2) SEL5A M5(1, 3) SEL5B              \
  M5(0, 3) M5(0, 4) M5(1, 4) LG(0) M5(1, 5) M5(0, 5) "s_barrier\n\t" M5(0, 6) RA5(0) M5(1, 6) RA5(1)                                                      \
  M5(2, 0) X M5(3, 0) RP5(0, 0) M5(3, 1) RP5(0, 1) M5(2, 1) RP5(1, 0) M5(2, 2) RP5(1, 1) M5(3, 2) RP5(2, 0) M5(3, 3) RP5(2, 1) M5(2, 3) RP5(3, 0)          \
  M5(2, 4) RP5(3, 1) M5(3, 4) RP5(4, 0) M5(3, 5) RP5(4, 1) M5(2, 5) RP5(5, 0) M5(2, 6) RP5(5, 1) LG(8) M5(3, 6) RW5(6, 0, aM, 0)
#define ACC5 "+{v[0:31]}"(c0), "+{v[32:63]}"(c1), "+{v[64:95]}"(c2), "+{v[96:111]}"(c3)
#define CLOB5 "memory", "scc", "v112", "v113", "v114", "v115", "v116", "v117", "v118", "v119", "v120", "v121", "v122", "v123", "v124", "v125", "v126", "v127", "v128", "v129", "v130", "v131", "v132", "v133", \
              "v134", "v135", "v136", "v137", "v138", "v139", "v140", "v141", "v142", "v143", "v144", "v145", "v146", "v147", "v148", "v149", "v150", "v151", "v152", "v153", "v154", "v155"
static_assert(K5_STG == 30736 && K5_U - K5_G == 7184, "TRIP5");

#define C3(i, j) "v[24*" #i "+4*" #j "-12*(" #i "/4):24*" #i "+4*" #j "-12*(" #i "/4)+3]"
#define M3(i, j) "v_mfma_f32_16x16x32_bf16 " C3(i, j) ", v[124+4*" #j ":127+4*" #j "], v[104+4*" #i ":107+4*" #i "], " C3(i, j) "\n\t"
#define RA3(i, u) "ds_read_b128 v[104+4*" #i ":107+4*" #i "], %[aA] offset:1024*" #i "+9216*" #u "\n\t"
#define RH3(u) "ds_read_b128 v[120:123], %[aH] offset:9216*" #u "\n\t"
#define RW3(j, h, a, u) "ds_read_b64_tr_b16 v[124+4*" #j "+2*" #h ":125+4*" #j "+2*" #h "], %[" #a "] offset:8192*(" #j "/2)+2048*" #h "+4096*" #u "\n\t"
#define ST3(a, d) "v_add_u32 %[" #a "], " #d ", %[" #a "]\n\t"
#define H2_3(u, S0, S1, S2, B) M3(0, 0) RW3(5, 1, a1, u) M3(1, 0) RA3(2, u) M3(1, 1) RA3(3, u) S0 M3(0, 1) LG(7) S1 M3(0, 2) S2 M3(1, 2) M3(1, 3) M3(0, 3) LG(2) M3(0, 4) M3(1, 4) LG(0) B
#define H1_3(n, X)                                                                                                                                      \
  M3(1, 5) RA3(1, n) M3(0, 5) RA3(0, n) M3(2, 0) X M3(3, 0) RW3(0, 0, a0, n) M3(3, 1) RW3(0, 1, a0, n) M3(2, 1) RW3(1, 0, a1, n) M3(2, 2) RW3(1, 1, a1, n) \
  M3(3, 2) RW3(2, 0, a1, n) M3(3, 3) RW3(2, 1, a1, n) M3(2, 3) M3(4, 3) RW3(3, 0, a0, n) M3(4, 4) RW3(3, 1, a0, n) M3(2, 4) RH3(n)                         \
  M3(3, 4) RW3(4, 0, a0, n) M3(3, 5) RW3(4, 1, a0, n) LG(7) M3(2, 5) RW3(5, 0, a1, n)
#define STAGE3(d, X) H2_3(0, , , , ) H1_3(1, ) H2_3(1, ST3(a0, d), ST3(aA, d), ST3(a1, d) ST3(aH, d), "s_barrier\n\t") H1_3(0, X)
#define CV3(i, j, t) "v_cvt_pk_bf16_f32 v[104+2*" #t "], v[24*" #i "+4*" #j "-12*(" #i "/4)], v[24*" #i "+4*" #j "-12*(" #i "/4)+1]\n\tv_cvt_pk_bf16_f32 v[105+2*" #t "], v[24*" #i "+4*" #j "-12*(" #i "/4)+2], v[24*" #i "+4*" #j "-12*(" #i "/4)+3]\n\t"
#define WR3(i, t, a, o) "ds_write_b64 %[" #a "], v[104+2*" #t ":105+2*" #t "] offset:2048*" #i "+" #o "\n\t"
#define CW3(i, j, t, a, o) CV3(i, j, t) WR3(i, t, a, o)
#define V8(a) "v" #a "0", "v" #a "1", "v" #a "2", "v" #a "3", "v" #a "4", "v" #a "5", "v" #a "6", "v" #a "7"
#define V10(a) V8(a), "v" #a "8", "v" #a "9"
static_assert(K3_STG == 43008, "STAGE3");
__device__ __forceinline__ int w2_stage(int j) {
  const int j2 = j - 16, p = (j2 * 52) >> 8, r = j2 - p * 5;
  return j < 16 ? j * 112 * 2048 : (p * 224 + 32 + r * 32 + (r > 2 ? 32 : 0)) * 2048 + (r == 2);
}
__global__ __launch_bounds__(768, 3) __attribute__((amdgpu_num_sgpr(98))) void k2_block(const u16* __restrict__ x, const u16* __restrict__ wqkv, const u16* __restrict__ wo,
                                                   const u16* __restrict__ w1, const u16* __restrict__ w3, const u16* __restrict__ w2,
                                                   const u16* __restrict__ cnd, const u16* __restrict__ wmod, u16* __restrict__ out, char* __restrict__ ws) {
  extern __shared__ __attribute__((aligned(16))) char smem_[];
  LDS_PTR(char) smem = (LDS_PTR(char))smem_;
  const int tid = threadIdx.x;
  int lane = tid & 63;
  const int wave = __builtin_amdgcn_readfirstlane(tid >> 6);
  const unsigned lds_base = (unsigned)(uintptr_t)smem;
  const int b = blockIdx.x, xc = b & 7, ii = b >> 3;
  const int tm3 = (xc & 1) * 8 + (ii & 7), tn3 = (xc >> 1) * 4 + (ii >> 3);
  const int tm4 = (xc >> 1) * 4 + (ii & 3), tn4 = (xc & 1) * 8 + (ii >> 2);
  const int tm5 = (xc & 1) * 4 + (ii & 3), tn5 = (xc >> 1) * 8 + (ii >> 2);
  const int kh6 = xc >> 2, tm6 = tm5 * 2 + ((ii >> 2) & 1), tn6 = tn3 & 7;
  const int u1 = (b >> 4) * 8 + xc;
  unsigned* cnt = (unsigned*)(ws + WS_CTL);
  unsigned long long* fl1 = (unsigned long long*)(ws + WS_FL1);
  long long pr0 = 0, prk = 0;
  STAMP_AT(0 );
  if (PROBE == 0) prk = pr0;

  if (wave >= 8) {
    const int lw = wave - 8, P = b * 4 + lw, lane = lane_id();
    const bool isA = lw < 2;
    asm volatile("" ::"s"(wqkv), "s"(wo), "s"(w1), "s"(w3), "s"(w2), "s"(ws));
    int kpf = -4;
    auto touch = [&]() {
      if (kpf < 28 && __builtin_amdgcn_s_getreg((3 << 11) | (8 << 6) | 7) <= 7) {
        stouch4(kpf < 0 ? (const char*)wo + ((kpf + 4) * 4096 + P * 4) * 128 : (const char*)((P & 1) ? w3 : w1) + (kpf * 2048 + (P >> 1) * 4) * 128);
        ++kpf;
      }
    };
    const char* epo = ws + WS_EPO;
    const unsigned swz = ((lane & 7) ^ ((lane >> 3) & 6)) * 16;
    const int cg = lw * 16 + (lw < 3 ? tn4 : tn6 * 2 + kh6);
    auto issue4 = [&](int s, unsigned slot) {
      const char* p = (const char*)wo + tn4 * 128 + (lw * 32 + s * 128) * 2048;
      unsigned v4[4];
#pragma unroll
      for (int i = 0; i < 4; ++i) v4[i] = (lane >> 3) * 2048 + swz + i * 15360;
      glds16x4<false>(p, p, v4, lds_base + slot + lw * 4096);
    };
    auto run3 = [&](auto isA_) {
      constexpr bool A_ = decltype(isA_)::value; constexpr int N = A_ ? 9 : 12;
      const char* sp = A_ ? ws + WS_H + lw * 131072 + (tm3 * 8 - 1) * 1024 : (const char*)wqkv + tn3 * 128;
      unsigned vo[3][4];
#pragma unroll
      for (int i = 0; i < 12; ++i) {
        const int tb = (lw - 2) * 12 + i;
        vo[i >> 2][i & 3] = A_ ? lane * 16 + (i < 8 ? 1024 + (i >> 2) * 4096 : tm3 ? 0 : 1024) : ((tb & 7) * 8 + (lane >> 3)) * 6144 + (tb >> 3) * 2048 - (i & 3) * 1024 + swz;
      }
      const unsigned d0 = lds_base + (A_ ? lw * 9216 : K3_B + (lw - 2) * 12288);
      auto issue = [&](unsigned st) {
        if (A_) { glds16x4<true>(sp, sp, vo[0], d0 + st + 1024); glds16s(sp, vo[2][0], d0 + st, 0); glds16x4<true>(sp, sp, vo[1], d0 + st + 5120); }
        else {
#pragma unroll
          for (int i = 0; i < 3; ++i) glds16x4<false>(sp, sp, vo[i], d0 + st + i * 4096);
        }
        sp += A_ ? 262144 : 64 * 6144;
      };
      { unsigned long long ep;
        asm volatile("s_load_dwordx2 %0, %1, 0x0\n\ts_waitcnt lgkmcnt(0)" : "=s"(ep) : "s"(epo) : "memory");
        ep += 1;
        if (lw == 3) { *(LDS_PTR(u32x4))(smem + LDS_ARR) = (u32x4){0u, 0u, 0u, 0u}; wait_lgkm0(); }
        if (lw == 1) {
#pragma unroll
          for (int k = 0; k < 3; ++k) {
            const int i = b * 166 + (k * 64 + lane < 166 ? k * 64 + lane : 0);
            st16<true>(ws, i < 32768 ? (unsigned)WS_XCH + i * 16 : (unsigned)WS_CTL + (i - 32768) * 16, i < 32768 ? (u32x4){~0u, ~0u, ~0u, ~0u} : (u32x4){0u, 0u, 0u, 0u});
          }
        }
        BAR();
        auto flag = [&](int idx) { wait_vm<0>(); if (lane < 8) __hip_atomic_store(fl1 + lane * 256 + idx, ep, __ATOMIC_RELAXED, __HIP_MEMORY_SCOPE_AGENT); };
        if (lw == 1 && (b & 8)) {
          const int kq = u1 & 3, cgp = u1 >> 2;
          float sm = 0.f;
#pragma unroll
          for (int r = 0; r < 8; ++r) sm += *(const LDS_PTR(float))(smem + 8192 + (r * 64 + lane) * 4);
          __hip_atomic_store((unsigned*)(ws + WS_MOD) + kq * 2048 + cgp * 64 + lane, __float_as_uint(sm), __ATOMIC_RELAXED, __HIP_MEMORY_SCOPE_AGENT);
          flag(((cgp >> 3) & 1) * 64 + (cgp >> 4) * 32 + (cgp & 7) * 4 + kq);
        }
        BAR();
        if (lw == 1 && !(b & 8)) flag(128 + u1);
        if (lw < 2) {
          if (lw == 1) BAR();
#pragma unroll 1
          for (unsigned spins = 0; spins < SPIN_CAP; ++spins) {
            bool ok = true;
#pragma unroll
            for (int k = 0; k < 4; ++k) ok &= __hip_atomic_load(fl1 + xc * 256 + (lw ? k : (b >> 3) & 1) * 64 + lane, __ATOMIC_RELAXED, __HIP_MEMORY_SCOPE_AGENT) == ep;
            if (__builtin_amdgcn_ballot_w64(!ok) == 0) break;
            __builtin_amdgcn_s_sleep(4);
          }
          if (lw == 0) BAR();
          else if (b == 0 && lane == 0) __hip_atomic_store((unsigned long long*)(ws + WS_EPO), ep, __ATOMIC_RELAXED, __HIP_MEMORY_SCOPE_AGENT);
        } else BAR();
      }
      BAR();
      if (lw == 0) {
        signal(cnt, C2 + (u1 >> 3), lane, (b & 8) ? 1u : 0x10000u);
        poll<PD2, PS2>(cnt + ((C2 + ((lane & 1) && tm3 ? tm3 - 1 : tm3)) * 8 + xc) * 32, 0x80000u);
      }
      if (!A_) { issue(0); issue(K3_STG); }
      touch();
      BAR();
      if (A_) { issue(0); issue(K3_STG); }
      wait_vm<N>();
      BAR();
      issue(2 * K3_STG);
      unsigned st = 0;
#pragma unroll 1
      for (int j = 0; j < 13; ++j) {
        wait_vm<N>();
        BAR();
        issue(st);
        st = st == 2 * K3_STG ? 0 : st + K3_STG;
        touch();
      }
      wait_vm<N>();
      BAR();
      issue4(0, 57344); issue4(5, 57344 + 5 * 16384);
      if (kpf < 0) kpf = 0;
      {
        glds4((P & 1) ? w3 : w1, ((kpf + ((lane >> 2) < VB3 ? lane >> 2 : 0)) * 2048 + (P >> 1) * 4 + (lane & 3)) * 128, lds_base + LDS_DUMMY); kpf += VB3; }
      wait_vm<9>();
    };
    if (isA) run3(T_()); else run3(F__());
    { auto issue = issue4;
      BAR();
      BAR();
      issue(1, 57344 + 16384);
      BAR();
      issue(2, 57344 + 2 * 16384); issue(3, 57344 + 3 * 16384); issue(4, 57344 + 4 * 16384);
      BAR();
      if (lw == 0) signal(cnt, C3 + tm3, lane);
      issue(6, 0); issue(7, 16384);
      if (lw == 0) poll<PD3, PS3>(cnt + ((C3 + tm4) * 8 + xc) * 32, 16);
      wait_vm<8>();
      BAR();
      wait_vm<0>();
      BAR();
      poll<0, 4>(cnt + (CM * 8 + cg) * 32, 4);
      f32x4 pv[4];
#pragma unroll
      for (int i = 0; i < 4; ++i) pv[i] = __builtin_bit_cast(f32x4, ld16<true>(ws + WS_MOD2, (((lane >> 4) * 4 + i) * 4096 + cg * 64 + (lane & 15) * 4) * 4));
      f32x4 v = (pv[0] + pv[1]) + (pv[2] + pv[3]);
#pragma unroll
      for (int e = 0; e < 4; ++e) { v[e] = rbf(xsum16_32(v[e])); if (lw == 2) v[e] = rbf(1.f + v[e]); if (lw == 1 || lw == 2) v[e] *= -1.4426950408889634f; }
      *(LDS_PTR(f32x4))(smem + LDS_VEC + lw * 256 + (lane & 15) * 16) = v;
      wait_lgkm0();
    }
    auto run5 = [&](auto isA_) {
      constexpr bool A_ = decltype(isA_)::value; constexpr int N = A_ ? 8 : 7;
      const char* sp = A_ ? ws + WS_H2 + (tm5 * 16 + lw * 8) * 1024 : (const char*)(lw == 2 ? w1 : w3) + tn5 * 224;
      unsigned vp[N];
#pragma unroll
      for (int i = 0; i < N; ++i) { const int L = i * 64 + lane, r = L / 14, c = L - r * 14; vp[i] = A_ ? L * 16 : r * (F_ * 2) + c * 16; }
      const unsigned d0 = lds_base + (A_ ? lw * 8192 : lw == 2 ? K5_G : K5_U);
      unsigned st = 3 * K5_STG;
      auto issue = [&]() {
#pragma unroll
        for (int i = 0; i < N; ++i) { if (A_) glds16s(sp, vp[i], d0 + st, i * 1024); else glds16(sp, vp[i], d0 + st, i * 1024); }
        sp += A_ ? 131072 : 32 * F_ * 2; st = st == (K5_NS - 1) * K5_STG ? 0 : st + K5_STG;
      };
      BAR();
      if (!A_) { issue(); issue(); }
      BAR();
      if (A_ && lw == 0) { signal(cnt, C4 + tm4, lane); poll<PD4>(cnt + ((C4 + tm5 * 2 + (lane & 1)) * 8 + xc) * 32, 16); }
      else { if (!A_) { __builtin_amdgcn_s_sleep(2); for (int s = 2; s < NB5; ++s) issue(); } }
      BAR();
      if (A_) { issue(); issue(); }
      wait_vm<A_ ? N : (NB5 - 1) * N>();
      BAR();
#pragma unroll 1
      for (int s = A_ ? 2 : NB5; s < K5_NS; ++s) issue();
#pragma unroll 1
      for (int j = 0; j < 27; ++j) {
        wait_vm<3 * N>();
        BAR();
        issue();
        touch(); __builtin_amdgcn_s_sleep(TS5); touch();
      }
      const char* sb = A_ ? ws + WS_HH + (kh6 * 56 * 128 + tm6 * 8 + lw * 4) * 1024 : (const char*)w2 + kh6 * 1792 * 2048 + tn6 * 256 + ((lw ^ kh6 ^ 1) & 1) * 128;
      unsigned vo[4];
#pragma unroll
      for (int i = 0; i < 4; ++i) {
        const int r = i * 8 + (lane >> 3), k = 8 * ((r >> 2) & 3) + 4 * (r >> 4) + (r & 3);
        vo[i] = A_ ? lane * 16 : k * 2048 - i * 1024 + swz;
      }
      const unsigned d6 = lds_base + (A_ ? K6_A : 0) + (lw & 1) * 4096;
      st = 0;
      const int tab = w2_stage(lane);
      int jn = 0;
      auto issue6 = [&]() {
        if (A_) { glds16x4<true>(sb, sb, vo, d6 + st); sb += 131072; }
        else { const int o = __builtin_amdgcn_readlane(tab, jn); const char* s0 = sb + (o & ~1); glds16x4<false>(s0, s0 + (o & 1) * 65536, vo, d6 + st); ++jn; }
        st = st == ((A_ ? K6_NA : K6_NW) - 1) * 8192 ? 0 : st + 8192;
      };
      auto pre6 = [&](int n) {
        if (!A_) {
#pragma unroll 1
          for (int i = 0; i < n; ++i) issue6();
        }
      };
      constexpr int E = A_ ? 0 : 4;
      { const int o = w2_stage(((xc & 1) ^ (lane >= 56)) * 28 + 7 * lw + (lane < 56 ? lane >> 3 : lane < 63 ? lane - 56 : 0));
        glds4((const char*)w2 + (kh6 * 1792 + ii) * 2048 + (tn6 >> 2) * 1024, (o & ~1) + (o & (ii >> 4) & 1) * 65536 + (lane < 56 ? lane & 7 : (3 - ((unsigned)(uintptr_t)w2 >> 7)) & 7) * 128,
              lds_base + LDS_DUMMY); }
      wait_vm<3 * N + 1>(); BAR();
      wait_vm<2 * N + 1>(); BAR(); pre6(PRE6);
      wait_vm<N + PRE6 * E + 1>(); BAR(); pre6(4);
      wait_vm<(PRE6 + 4) * E + 1>(); BAR(); pre6(8 - PRE6);
      BAR();
      if (A_) {
        volatile LDS_PTR(unsigned) go = (volatile LDS_PTR(unsigned))(smem + LDS_ARR + 4);
        if (lw == 0) { poll<PD5, PS5>(cnt + ((C5 + (tm6 >> 1) * 4 + kh6 * 2) * 8 + tn6) * 32, 16); *go = 1u; }
        else {
#pragma unroll 1
          for (unsigned spins = 0; spins < SPIN_CAP && __builtin_amdgcn_readfirstlane(*go) == 0; ++spins) __builtin_amdgcn_s_sleep(1);
          asm volatile("" ::: "memory");
        }
        STAMP_L(31);
        issue6(); issue6(); issue6();
        wait_vm<8>(); BAR(); issue6(); issue6();
        wait_vm<12>(); BAR(); issue6(); issue6();
      } else { wait_vm<16>(); BAR(); BAR(); }
#pragma unroll 1
      for (int c = 2; c < 51; ++c) { wait_vm<16>(); BAR(); if (A_ || !((SKIP6 >> c) & 1)) issue6(); }
      wait_vm<16>(); BAR(); wait_vm<12>(); BAR(); wait_vm<8>(); BAR(); wait_vm<4>(); BAR(); wait_vm<0>(); BAR();
    };
    if (isA) run5(T_()); else run5(F__());
    asm volatile("s_waitcnt lgkmcnt(0)" ::: "memory");
    return;
  }

  int fr = lane & 15, g = lane >> 4, p4 = fr & 3, row = 4 * g + (fr >> 2);
  int lsw = row * 128 + (((row >> 1) & 3) * 32) + p4 * 8;
  { const int mb = u1, hf = (b >> 3) & 1, kb0 = hf * 16 + wave * 2, cofs = (g & 1) * 16 + (g >> 1) * 8;
    u32x4 v[4];
    auto ldx = [&]() {
#pragma unroll
      for (int p = 0; p < 4; ++p) v[p] = *(const u32x4*)(x + (size_t)(mb * 16 + fr) * D_ + ((kb0 + (p & 1)) ^ ((p >> 1) * 16)) * 32 + cofs);
    };
    LDS_PTR(f32x2) sred = (LDS_PTR(f32x2))(smem + 4096);
    auto up2 = [](unsigned u) { return (f32x2){lo2f(u), hi2f(u)}; };
    auto stats = [&]() {
      f32x2 s2 = {0.f, 0.f}, q2 = {0.f, 0.f};
#pragma unroll
      for (int k = 0; k < 16; ++k) s2 += up2(v[k >> 2][k & 3]);
      const float mw = xsum16_32(s2[0] + s2[1]) * (1.f / 128.f);
#pragma unroll
      for (int k = 0; k < 16; ++k) { const f32x2 d = up2(v[k >> 2][k & 3]) - mw; q2 += d * d; }
      const float qw = xsum16_32(q2[0] + q2[1]);
      if (lane < 16) sred[wave * 16 + fr] = (f32x2){mw, qw};
      wait_lgkm0();
    };
    auto rest = [&]() {
    STAMP_AT(1 );
    float mean = 0.f, m2 = 0.f; f32x2 sw[8];
#pragma unroll
    for (int w = 0; w < 8; ++w) { sw[w] = sred[w * 16 + fr]; mean += sw[w][0]; m2 += sw[w][1]; }
    mean *= 0.125f;
#pragma unroll
    for (int w = 0; w < 8; ++w) { const float d = sw[w][0] - mean; m2 += 128.f * d * d; }
    const float rstd = ln_rstd(m2 * (1.f / 1024.f));
    mean = rbf(mean);
    unsigned nv[8];
#pragma unroll
    for (int k = 0; k < 8; ++k) {
      const f32x2 d = up2(v[k >> 2][k & 3]) - mean;
      const f32x2 n = up2(pk2(d[0], d[1])) * rstd;
      nv[k] = pk2(n[0], n[1]);
      asm volatile("" : "+v"(nv[k]));
    }
    SCHED0(); BAR(); SCHED0();
    STAMP_AT(2 );
    float mp[8];
#pragma unroll
    for (int i = 0; i < 8; ++i) mp[i] = __uint_as_float(__hip_atomic_load((unsigned*)(ws + WS_MOD) + hf * 512 + tid + i * 1024, __ATOMIC_RELAXED, __HIP_MEMORY_SCOPE_AGENT));
    *(LDS_PTR(unsigned))(smem + tid * 4) = pk2((mp[0] + mp[2]) + (mp[4] + mp[6]), 1.f + rbf((mp[1] + mp[3]) + (mp[5] + mp[7])));
    wait_lgkm0();
#pragma unroll
    for (int p = 0; p < 2; ++p) {
      const LDS_PTR(u32x4) tp = (const LDS_PTR(u32x4))(smem + ((wave * 2 + p) * 32 + cofs) * 4);
      const u32x4 t0 = tp[0], t1 = tp[1];
      unsigned o[4];
#pragma unroll
      for (int j = 0; j < 4; ++j) {
        const unsigned ta = j < 2 ? t0[2 * j] : t1[2 * j - 4], tb = j < 2 ? t0[2 * j + 1] : t1[2 * j - 3];
        const unsigned u = pk2(lo2f(nv[p * 4 + j]) * hi2f(ta), hi2f(nv[p * 4 + j]) * hi2f(tb));
        o[j] = pk2(lo2f(u) + lo2f(ta), hi2f(u) + lo2f(tb));
      }
      auto s0 = __builtin_amdgcn_permlane16_swap(o[0], o[2], false, false);
      auto s1 = __builtin_amdgcn_permlane16_swap(o[1], o[3], false, false);
      st16<true>(ws + WS_H, ((kb0 + p) * 128 + mb) * 1024 + lane * 16, (u32x4){s0[0], s1[0], s0[1], s1[1]});
    }
    STAMP_AT(3 );
    wait_vm<0>();
    STAMP_AT(4 );
    BAR();
    STAMP_AT(5 );
    };
    if (hf) {
      const int mrow = (u1 & 3) * 256 + (tid >> 3) * 4;
      const u32x2 c2 = *(const u32x2*)(cnd + mrow);
      u32x4 mw[4];
#pragma unroll
      for (int i = 0; i < 4; ++i) mw[i] = __builtin_nontemporal_load((const u32x4*)(wmod + (size_t)(mrow + i) * 6144 + (u1 >> 2) * 64 + (lane & 7) * 8));
      const unsigned mc[2] = {c2[0], c2[1]};
      *(LDS_PTR(f32x2))(smem + 8192 + (wave * 64 + (lane & 7) * 8 + (lane >> 4) * 2) * 4) = slab_sum<4>(mc, mw);
      wait_lgkm0();
      BAR();
      __builtin_amdgcn_s_sleep(XD1);
      ldx();
      stats();
      BAR();
      rest();
    } else {
      ldx();
      SCHED0();
      const int sl = (3 - ((unsigned)(uintptr_t)wqkv >> 7) - (xc >> 1) * 4) & 7;
      const int i2 = tid - 384, r0 = i2 < 0 ? tid / 12 : i2 / 3, c0 = i2 < 0 ? tid - r0 * 12 : (i2 - r0 * 3) * 4 + sl;
      const bool on = i2 < 0 || (sl < 4 && i2 < 96);
      const unsigned pf = *(const unsigned*)((const char*)wqkv + (xc >> 1) * 512 + (on ? ((((xc & 1) ^ (i2 >= 0)) * 512 + (ii >> 1) * 32 + r0) * 48 + (c0 >> 2) * 16 + (c0 & 3)) * 128 : 0));
      SCHED0(); BAR(); SCHED0();
      stats();
      SCHED0(); BAR(); SCHED0();
      rest();
      asm volatile("" ::"v"(pf));
    }
  }
  {
    f32x4 bias[2];
    { const int kbase = tm3 * 128 + wave * 16 - 16;
#pragma unroll
      for (int t = 0; t < 4; ++t) {
        const int kk = (t >> 1) * 16 + 4 * g + (t & 1) * 2 + (fr & 1), df = 16 + fr - kk;
        bias[t >> 1][(t & 1) * 2] = bias[t >> 1][(t & 1) * 2 + 1] = (df >= 0 && df <= 14 && kbase + kk >= 0) ? 0.f : -__builtin_inff();
      }
      asm volatile("" : "+v"(bias[0]), "+v"(bias[1])); }
    u32x4 mc, mw[8];
    if (wave < 4) {
      const int wm = wave & 1, bA = wave;
      unsigned a0 = lds_base + K3_B + (lsw ^ (bA * 32)), a1 = lds_base + K3_B + (lsw ^ ((bA ^ 1) * 32)), aA = lds_base + 1024 + wm * 4096 + lane * 16, aH = lds_base + lane * 16;
      unsigned lq = lds_base + bA * 512 + lane * 8, lv = lds_base + 36864 + bA * 512 + (lane ^ ((g >> 1) * 8)) * 8;
      int n = 4;
      asm volatile(".irp r,0,8,16,24,32,40,48,56,64,72,80,88,96\n\tv_mov_b64 v[\\r:\\r+1], 0\n\tv_mov_b64 v[\\r+2:\\r+3], 0\n\tv_mov_b64 v[\\r+4:\\r+5], 0\n\tv_mov_b64 v[\\r+6:\\r+7], 0\n\t.endr\n\t"
                   "s_setprio 2\n\ts_waitcnt vmcnt(0)\n\ts_barrier\n\t"
                   "s_barrier\n\t"
                   RA3(1, 0) RA3(0, 0) RW3(0, 0, a0, 0) RW3(0, 1, a0, 0) RW3(1, 0, a1, 0) RW3(1, 1, a1, 0) RW3(2, 0, a1, 0) RW3(2, 1, a1, 0) RW3(3, 0, a0, 0) RW3(3, 1, a0, 0) RH3(0)
                   RW3(4, 0, a0, 0) RW3(4, 1, a0, 0) RW3(5, 0, a1, 0) LG(8)
                   ".p2align 5\n1:\n\t" STAGE3(43008, ) STAGE3(43008, ) STAGE3(-86016, "s_sub_u32 %[n], %[n], 1\n\t") "s_cbranch_scc0 1b\n\t"
                   H2_3(0, , , , ) H1_3(1, )
                   H2_3(1, "v_add_u32 %[a0], %[mo], %[lq]\n\t", "v_add_u32 %[aA], %[mo], %[lv]\n\t", "v_xor_b32 %[a1], 512, %[a0]\n\tv_xor_b32 %[aH], 512, %[aA]\n\t", "s_barrier\n\t")
                   M3(1, 5) M3(0, 5) M3(2, 0) CV3(0, 0, 0) M3(3, 0) WR3(0, 0, a0, 0) CV3(1, 0, 1) M3(3, 1) WR3(1, 1, a0, 0) CV3(0, 1, 2) M3(2, 1) WR3(0, 2, a1, 0) CV3(1, 1, 3)
                   M3(2, 2) WR3(1, 3, a1, 0) CV3(0, 2, 0) M3(3, 2) WR3(0, 0, a1, 18432) CV3(1, 2, 1) M3(3, 3) WR3(1, 1, a1, 18432) CV3(0, 3, 2) M3(2, 3) WR3(0, 2, a0, 18432) CV3(1, 3, 3)
                   M3(4, 3) WR3(1, 3, a0, 18432) CV3(0, 4, 0) M3(4, 4) WR3(0, 0, aA, 0) CV3(1, 4, 1) M3(2, 4) WR3(1, 1, aA, 0) CV3(0, 5, 2) M3(3, 4) WR3(0, 2, aH, 0) CV3(1, 5, 3)
                   M3(3, 5) WR3(1, 3, aH, 0) CV3(2, 0, 0) M3(2, 5) WR3(2, 0, a0, 0)
                   CW3(3, 0, 1, a0, 0) CW3(3, 1, 2, a1, 0) CW3(2, 1, 3, a1, 0) CW3(2, 2, 0, a1, 18432) CW3(3, 2, 1, a1, 18432) CW3(3, 3, 2, a0, 18432) CW3(2, 3, 3, a0, 18432)
                   CV3(4, 3, 0) WR3(0, 0, lq, 18432) CV3(4, 4, 1) WR3(0, 1, lv, 0) CW3(2, 4, 2, aA, 0) CW3(3, 4, 3, aA, 0) CW3(3, 5, 0, aH, 0) CW3(2, 5, 1, aH, 0)
                   "s_setprio 0\n\t" LG(0)
                   : [a0] "+v"(a0), [a1] "+v"(a1), [aA] "+v"(aA), [aH] "+v"(aH), [n] "+s"(n)
                   : [lq] "v"(lq), [lv] "v"(lv), [mo] "s"((wm * 4 + 1) * 2048)
                   : "memory", "scc", V10(), V10(1), V10(2), V10(3), V10(4), V10(5), V10(6), V10(7), V10(8), V10(9), V10(10), V10(11), V10(12), V10(13), V8(14));
    } else {
      wait_vm<0>();
      BAR(); BAR();
      {
        const unsigned* pc = cnt + ((C2 + ((lane & 1) && tm3 ? tm3 - 1 : tm3)) * 8 + xc) * 32;
        unsigned seen = 0x80008u;
        BAR(); BAR(); BAR();
        if (wave == 4) asm volatile("global_load_dword %0, %1, off sc1" : "=v"(seen) : "v"(pc) : "memory");
        BAR(); BAR();
        asm volatile("s_waitcnt vmcnt(0)" : "+v"(seen)::"memory");
        if (__builtin_amdgcn_ballot_w64(seen < 0x80008u) != 0) poll<0>(pc, 0x80008u);
      }
#pragma unroll 1
      for (int t = 5; t < SLB; ++t) BAR();
      const int mrow = ((b & 3) * 4 + (wave & 3)) * 64 + (lane >> 3) * 8;
      mc = *(const u32x4*)(cnd + mrow);
#pragma unroll
      for (int i = 0; i < 8; ++i) mw[i] = __builtin_nontemporal_load((const u32x4*)(wmod + (size_t)(mrow + i) * 6144 + 2048 + (b >> 2) * 64 + (lane & 7) * 8));
      SCHED0();
#pragma unroll
      for (int t = SLB; t < 16; ++t) BAR();
    }
    lane = lane_id(); fr = lane & 15; g = lane >> 4; p4 = fr & 3; row = 4 * g + (fr >> 2); lsw = row * 128 + (((row >> 1) & 3) * 32) + p4 * 8;
    wait_lgkm0();
    BAR();
    STAMP_AT(9 );
    const LDS_PTR(char) eb = smem + wave * 2048;
    auto ld2 = [&](int o) { return cat8(*(const LDS_PTR(bf16x4))(eb + o), *(const LDS_PTR(bf16x4))(eb + o + 512)); };
    const int vo = 36864 + ((p4 * 16 + row) ^ ((p4 >> 1) * 8)) * 8;
    bf16x8 qf[2], k0[2], k1[2], v0[2], v1[2];
#pragma unroll
    for (int hd = 0; hd < 2; ++hd) { qf[hd] = ld2(2048 + hd * 1024 + lane * 8); k0[hd] = ld2(18432 + hd * 1024 + lane * 8); k1[hd] = ld2(18432 + 2048 + hd * 1024 + lane * 8); }
#pragma unroll
    for (int hd = 0; hd < 2; ++hd) {
      v0[hd] = cat8(ld_tr4(eb + vo + hd * 1024), ld_tr4(eb + vo + hd * 1024 + 2048));
      v1[hd] = cat8(ld_tr4(eb + vo + hd * 1024 + 512), ld_tr4(eb + vo + hd * 1024 + 512 + 2048));
    }
    SCHED0();
    const f32x4 z = {0.f, 0.f, 0.f, 0.f};
    const bool odd = fr & 1;
    const unsigned sel0 = odd ? 0x01000c0cu : 0x0c0c0100u, sel1 = odd ? 0x03020c0cu : 0x0c0c0302u;
    float inf = __builtin_inff(); asm("" : "+v"(inf));
    auto mx2 = [&](float a, float c) { return __builtin_amdgcn_fmed3f(a, c, inf); };
    auto both = [](float a, float c, auto op, auto fin) {
      auto u = __builtin_amdgcn_permlane16_swap(__float_as_uint(a), __float_as_uint(c), false, false);
      float r = op(__uint_as_float(u[0]), __uint_as_float(u[1]));
      auto w = __builtin_amdgcn_permlane32_swap(__float_as_uint(r), __float_as_uint(r), false, false);
      r = fin(op(__uint_as_float(w[0]), __uint_as_float(w[1])));
      auto y = __builtin_amdgcn_permlane16_swap(__float_as_uint(r), __float_as_uint(r), false, false);
      return (f32x2){__uint_as_float(y[0]), __uint_as_float(y[1])};
    };
    f32x2 l[2][2];
#pragma unroll
    for (int hd = 0; hd < 2; ++hd) {
      const f32x4 s0 = __builtin_amdgcn_mfma_f32_16x16x32_bf16(k0[hd], qf[hd], bias[0], 0, 0, 0), s1 = __builtin_amdgcn_mfma_f32_16x16x32_bf16(k1[hd], qf[hd], bias[1], 0, 0, 0);
#pragma unroll
      for (int t = 0; t < 2; ++t) {
        const f32x4 sv = t ? s1 : s0;
        unsigned u = pk2(odd ? sv[1] : sv[0], odd ? sv[3] : sv[2]);
        u = pk2(lo2f(u) * 0.17677669529663687f, hi2f(u) * 0.17677669529663687f);
        l[hd][t] = (f32x2){lo2f(u), hi2f(u)};
      }
    }
    const f32x2 mx = both(mx2(mx2(l[0][0][0], l[0][0][1]), mx2(l[0][1][0], l[0][1][1])), mx2(mx2(l[1][0][0], l[1][0][1]), mx2(l[1][1][0], l[1][1][1])), mx2,
                          [](float r) { return r * -1.4426950408889634f; });
    float sm[2];
#pragma unroll
    for (int hd = 0; hd < 2; ++hd) {
#pragma unroll
      for (int t = 0; t < 2; ++t) { const f32x2 e = l[hd][t] * 1.4426950408889634f + mx[hd]; l[hd][t] = (f32x2){__builtin_amdgcn_exp2f(e[0]), __builtin_amdgcn_exp2f(e[1])}; }
      const f32x2 a = l[hd][0] + l[hd][1]; sm[hd] = a[0] + a[1];
    }
    const f32x2 inv = both(sm[0], sm[1], [](float a, float c) { return a + c; }, [](float r) { return __builtin_amdgcn_rcpf(r); });
#pragma unroll
    for (int hd = 0; hd < 2; ++hd) {
      u32x4 pp;
#pragma unroll
      for (int t = 0; t < 2; ++t) {
        const f32x2 p = l[hd][t] * inv[hd]; const unsigned u = pk2(p[0], p[1]);
        pp[2 * t] = __builtin_amdgcn_perm(u, u, sel0); pp[2 * t + 1] = __builtin_amdgcn_perm(u, u, sel1);
      }
      const bf16x8 pf = __builtin_bit_cast(bf16x8, pp);
      const f32x4 oa = __builtin_amdgcn_mfma_f32_16x16x32_bf16(v0[hd], pf, z, 0, 0, 0), ob = __builtin_amdgcn_mfma_f32_16x16x32_bf16(v1[hd], pf, z, 0, 0, 0);
      u32x4 ov; ov[0] = pk2(oa[0], oa[1]); ov[1] = pk2(oa[2], oa[3]); ov[2] = pk2(ob[0], ob[1]); ov[3] = pk2(ob[2], ob[3]);
      st16<true>(ws + WS_O, ((tn3 * 2 + hd) * 128 + tm3 * 8 + wave) * 1024 + lane * 16, ov);
    }
    STAMP_AT(10 );
    wait_vm<0>();
    STAMP_AT(11 );
    BAR();
    STAMP_AT(12 );
    if (wave >= 4) {
      const unsigned m4[4] = {mc[0], mc[1], mc[2], mc[3]};
      const f32x2 ps = slab_sum<8>(m4, mw);
      if (!(lane & 8))
        __hip_atomic_store((unsigned long long*)(ws + WS_MOD2) + (((b & 3) * 4 + (wave & 3)) * 4096 + (b >> 2) * 64 + (lane & 7) * 8 + (lane >> 4) * 2) / 2,
                           ((unsigned long long)__float_as_uint(ps[1]) << 32) | __float_as_uint(ps[0]), __ATOMIC_RELAXED, __HIP_MEMORY_SCOPE_AGENT);
      wait_vm<0>();
      if (lane == 0 && __hip_atomic_fetch_add((LDS_PTR(unsigned))(smem + LDS_ARR + 8), 1u, __ATOMIC_RELAXED, __HIP_MEMORY_SCOPE_WORKGROUP) == 3)
        __hip_atomic_fetch_add(cnt + (CM * 8 + (b >> 2)) * 32, 1u, __ATOMIC_RELAXED, __HIP_MEMORY_SCOPE_AGENT);
    }
  }
  { const int wm = wave & 3, ks = wave >> 2, mb = tm4 * 8 + wm * 2 + ks;
    constexpr int R = 6;
    u32x4 rx[2];
#pragma unroll
    for (int p = 0; p < 2; ++p) rx[p] = *(const u32x4*)(x + (size_t)(mb * 16 + fr) * D_ + tn4 * 64 + p * 32 + (g & 1) * 16 + (g >> 1) * 8);
    const unsigned va0 = (ks * 128 + mb) * 1024 + lane * 16, va1 = va0 ^ 1024;
    bf16x8 fa[R][2], fw[2][4];
    auto issueA = [&](int t) {
      const char* pa = ws + WS_O + t * 262144;
      fa[t % R][0] = __builtin_bit_cast(bf16x8, ld16<true>(pa, va0));
      fa[t % R][1] = __builtin_bit_cast(bf16x8, ld16<true>(pa, va1));
    };
    unsigned aw[4];
    auto rdw = [&](int t, int j) {
      const int o = (t & 3) * 8192;
      fw[t & 1][j] = cat8(ld_tr4(smem + aw[j] + o), ld_tr4(smem + aw[j] + o + 2048));
    };
    auto stepw = [&](int t) {
      const int gi = t >> 2, base = gi < 3 ? 57344 + gi * 32768 : 0;
#pragma unroll
      for (int j = 0; j < 4; ++j) { aw[j] = base + ks * 4096 + (lsw ^ (j * 32)); asm volatile("" : "+v"(aw[j])); }
    };
    f32x4 acc[2][4];
#pragma unroll
    for (int i = 0; i < 2; ++i)
#pragma unroll
      for (int j = 0; j < 4; ++j) acc[i][j] = (f32x4){0.f, 0.f, 0.f, 0.f};
    stepw(0);
    wait_lgkm0();
    SCHED0(); BAR(); SCHED0();
    STAMP_AT(13 );
#pragma unroll
    for (int t = 0; t < R; ++t) issueA(t);
#pragma unroll
    for (int j = 0; j < 4; ++j) rdw(0, j);
    SCHED0();
#pragma unroll
    for (int t = 0; t < 16; ++t) {
      if (t < 15 && ((t + 1) & 3) == 0) stepw(t + 1);
#pragma unroll
      for (int j = 0; j < 4; ++j) {
        if (t < 15) rdw(t + 1, j);
        SCHED0();
#pragma unroll
        for (int i = 0; i < 2; ++i) acc[i][j] = __builtin_amdgcn_mfma_f32_16x16x32_bf16(fw[t & 1][j], fa[t % R][i], acc[i][j], 0, 0, 0);
        SCHED0();
      }
      if (t + R < 16) issueA(t + R);
      if (t == 2) {
#pragma unroll
        for (int p = 0; p < 2; ++p) {
          auto s0 = __builtin_amdgcn_permlane16_swap(rx[p][0], rx[p][2], false, false);
          auto s1 = __builtin_amdgcn_permlane16_swap(rx[p][1], rx[p][3], false, false);
          rx[p] = (u32x4){s0[0], s1[0], s0[1], s1[1]}; asm volatile("" : "+v"(rx[p]));
        }
      }
      if (t == 5) BAR();
      SCHED0();
    }
    STAMP_AT(14 );
    LDS_PTR(f32x4) red = (LDS_PTR(f32x4))(smem + K4_RED);
#pragma unroll
    for (int j = 0; j < 4; ++j) red[((wave ^ 4) * 4 + j) * 64 + lane] = acc[1][j];
    wait_lgkm0();
    BAR();
    STAMP_AT(15 );
    const LDS_PTR(float) vec = (const LDS_PTR(float))(smem + LDS_VEC);
    unsigned o[8];
#pragma unroll
    for (int j = 0; j < 4; ++j) {
      const f32x4 y = (acc[0][j] + red[(wave * 4 + j) * 64 + lane]) * *(const LDS_PTR(f32x4))(vec + j * 16 + 4 * g);
      const unsigned r0 = rx[j >> 1][(j & 1) * 2], r1 = rx[j >> 1][(j & 1) * 2 + 1];
      o[2 * j] = pk2(lo2f(r0) + y[0], hi2f(r0) + y[1]);
      o[2 * j + 1] = pk2(lo2f(r1) + y[2], hi2f(r1) + y[3]);
    }
    float f[16], s = 0.f;
#pragma unroll
    for (int j = 0; j < 8; ++j) { f[2 * j] = lo2f(o[j]); f[2 * j + 1] = hi2f(o[j]); s += f[2 * j] + f[2 * j + 1]; }
    const float mt = xsum16_32(s) * (1.f / 64.f);
    float qq = 0.f;
#pragma unroll
    for (int j = 0; j < 16; ++j) { const float d = f[j] - mt; qq += d * d; }
    qq = xsum16_32(qq);
    unsigned long long* slot = (unsigned long long*)(ws + WS_XCH) + (size_t)mb * 256 + fr;
    if (lane < 32) {
      const unsigned qb = qq == qq ? __float_as_uint(qq) : 0x7fc00000u;
      __hip_atomic_store(slot + g * 32768 + tn4 * 16, ((unsigned long long)qb << 32) | __float_as_uint(mt), __ATOMIC_RELAXED, __HIP_MEMORY_SCOPE_AGENT);
    }
    st16<true>(ws + WS_X1, (mb * 16 + tn4) * 2048 + lane * 16, (u32x4){o[0], o[1], o[2], o[3]});
    st16<true>(ws + WS_X1, (mb * 16 + tn4) * 2048 + 1024 + lane * 16, (u32x4){o[4], o[5], o[6], o[7]});
    STAMP_AT(16 );
    f32x4 ss[4], gg[4];
#pragma unroll
    for (int j = 0; j < 4; ++j) {
      ss[j] = *(const LDS_PTR(f32x4))(vec + 64 + j * 16 + 4 * g); gg[j] = *(const LDS_PTR(f32x4))(vec + 128 + j * 16 + 4 * g);
      asm volatile("" : "+v"(ss[j]), "+v"(gg[j]));
    }
    unsigned long long pv[4];
    __builtin_amdgcn_s_sleep(XS0);
    for (unsigned spins = 0;; ++spins) {
      bool ok = true;
#pragma unroll
      for (int i = 0; i < 4; ++i) { pv[i] = __hip_atomic_load(slot + (xc & 1) * 32768 + (4 * g + i) * 16, __ATOMIC_RELAXED, __HIP_MEMORY_SCOPE_AGENT); ok &= (unsigned)(pv[i] >> 32) != 0xffffffffu; }
      if (__builtin_amdgcn_ballot_w64(!ok) == 0 || spins > SPIN_CAP) break;
      __builtin_amdgcn_s_sleep(XS1);
    }
    STAMP_AT(17 );
    float ms = 0.f, m2 = 0.f;
#pragma unroll
    for (int i = 0; i < 4; ++i) { ms += __uint_as_float((unsigned)pv[i]); m2 += __uint_as_float((unsigned)(pv[i] >> 32)); }
    float mean = xsum16_32(ms) * (1.f / 16.f);
#pragma unroll
    for (int i = 0; i < 4; ++i) { const float d = __uint_as_float((unsigned)pv[i]) - mean; m2 += 64.f * d * d; }
    const float rstd = ln_rstd(xsum16_32(m2) * (1.f / 1024.f));
    mean = rbf(mean);
#pragma unroll
    for (int p = 0; p < 2; ++p) {
      u32x4 v;
#pragma unroll
      for (int j = 0; j < 4; ++j) {
        const int jb = 2 * p + (j >> 1), e = (j & 1) * 2;
        v[j] = pk2((f[8 * p + 2 * j] - mean) * rstd * gg[jb][e] + ss[jb][e], (f[8 * p + 2 * j + 1] - mean) * rstd * gg[jb][e + 1] + ss[jb][e + 1]);
      }
      st16<true>(ws + WS_H2, ((tn4 * 2 + p) * 128 + mb) * 1024 + lane * 16, v);
    }
    STAMP_AT(18 );
    wait_vm<0>();
    BAR();
    STAMP_AT(19 );

  }
  const int grp = wave >> 2;
  { const int wm = wave & 3;
    const LDS_PTR(char) sb = smem + 3 * K5_STG;
    const LDS_PTR(char) aP = sb + K5_G + row * 224 + grp * 96 + p4 * 8;
    const LDS_PTR(char) aM = sb + (p4 < 2 ? K5_G : K5_U) + row * 224 + (12 + grp) * 16 + (p4 & 1) * 8;
    const LDS_PTR(char) aA = sb + wm * 4096 + lane * 16;
    f32x32 c0, c1, c2; f32x16 c3;
    asm volatile(".irp r,0,8,16,24,32,40,48,56,64,72,80,88,96,104\n\tv_mov_b64 v[\\r:\\r+1], 0\n\tv_mov_b64 v[\\r+2:\\r+3], 0\n\tv_mov_b64 v[\\r+4:\\r+5], 0\n\tv_mov_b64 v[\\r+6:\\r+7], 0\n\t.endr"
                 : "={v[0:31]}"(c0), "={v[32:63]}"(c1), "={v[64:95]}"(c2), "={v[96:111]}"(c3)::"memory");
    int wrap = -(K5_NS - 1) * K5_STG, dl = K5_STG, n = 7;
    unsigned mask = wrap_mask(4, K5_NS);
    asm volatile("" : "+s"(wrap), "+v"(aP), "+v"(aM), "+v"(aA));
    if (grp) __builtin_amdgcn_s_setprio(1); else __builtin_amdgcn_s_setprio(2);
    BAR();
    STAMP_AT(20 );
    BAR();
    STAMP_G(21, 26);
    asm volatile(RA5(0) RA5(1) RP5(0, 0) RP5(0, 1) RP5(1, 0) RP5(1, 1) RP5(2, 0) RP5(2, 1) RP5(3, 0) RP5(3, 1) RP5(4, 0) RP5(4, 1) RP5(5, 0) RP5(5, 1) RW5(6, 0, aM, 0) LG(9)
                 ".p2align 5\n1:\n\t" TRIP5("") TRIP5("") TRIP5("") TRIP5("s_sub_u32 %[n], %[n], 1\n\t") "s_cbranch_scc0 1b\n\t" LG(0) "s_nop 7"
                 : ACC5, [aP] "+v"(aP), [aM] "+v"(aM), [aA] "+v"(aA), [dl] "+s"(dl), [mask] "+s"(mask), [n] "+s"(n) : [wrap] "s"(wrap) : CLOB5);
    f32x4 acc[4][7];
#pragma unroll
    for (int e = 0; e < 112; ++e) acc[e / 28][(e % 28) / 4][e & 3] = e < 32 ? c0[e & 31] : e < 64 ? c1[e & 31] : e < 96 ? c2[e & 31] : c3[e & 15];
    STAMP_G(22, 27);
    auto swi4 = [](const f32x4& gv, const f32x4& uv, unsigned& o0, unsigned& o1) {
      constexpr float c2 = 2.0813689810056077f;
      f32x2 pa = gv.xy * uv.xy, pb = gv.zw * uv.zw;
      const f32x2 ta = (f32x2){__builtin_amdgcn_exp2f(gv[0]), __builtin_amdgcn_exp2f(gv[1])} * c2 + c2, tb = (f32x2){__builtin_amdgcn_exp2f(gv[2]), __builtin_amdgcn_exp2f(gv[3])} * c2 + c2;
      pa *= (f32x2){__builtin_amdgcn_rcpf(ta[0]), __builtin_amdgcn_rcpf(ta[1])}; pb *= (f32x2){__builtin_amdgcn_rcpf(tb[0]), __builtin_amdgcn_rcpf(tb[1])};
      o0 = pk2(pa[0], pa[1]); o1 = pk2(pb[0], pb[1]);
    };
    const int lz = lane_id();
    const int gz = lz >> 4, fz = lz & 15;
    const int qb = (tn5 >> 4) * 224, ql = qb + 64 + (tn5 & 15) * 10;
    auto hof = [&](int e, int i) { return (unsigned)((e >> 2) * 131072 + (e & 3) * 256 + fz * 16 + i * 1024); };
    const unsigned ho0 = hof((grp ? ql + 2 : qb + (tn5 & 15) * 4) + 2 * (gz & 1) + (gz >> 1), 0), ho1 = hof(ql + 6 * grp + (gz >> 1), gz & 1), hom = hof(ql + 8 + grp, gz);
    char* hb = ws + WS_HH + (tm5 * 16 + wm * 4) * 1024;
    const int c5 = C5 + tm5 * 4 + (tn5 >> 4) * 2;
    auto tr16 = [](const unsigned (&o)[4]) {
      auto s0 = __builtin_amdgcn_permlane16_swap(o[0], o[2], false, false);
      auto s1 = __builtin_amdgcn_permlane16_swap(o[1], o[3], false, false);
      u32x4 v = {s0[0], s1[0], s0[1], s1[1]}; asm volatile("" : "+v"(v)); return v;
    };
    auto pair = [&](int p, int i) {
      unsigned o[4];
      swi4(acc[i][2 * p], acc[i][2 * p + 1], o[0], o[1]);
      swi4(acc[i + 1][2 * p], acc[i + 1][2 * p + 1], o[2], o[3]);
      return tr16(o);
    };
    u32x4 v[3];
#pragma unroll
    for (int i = 0; i < 4; ++i) {
      unsigned o[4];
      swi4(acc[i][0], acc[i][1], o[0], o[1]);
      swi4(acc[i][2], acc[i][3], o[2], o[3]);
      st16<true>(hb, ho0 + i * 1024, tr16(o));
      SCHED0();
    }
    STAMP_G(23, 28);
    v[0] = pair(2, 0);
    if (!grp) {
      wait_vm<0>();
      unsigned arr = 0;
      if (lz == 0) arr = __hip_atomic_fetch_add((LDS_PTR(unsigned))(smem + LDS_ARR), 1u, __ATOMIC_RELAXED, __HIP_MEMORY_SCOPE_WORKGROUP);
      if (__builtin_amdgcn_readfirstlane(arr) == 3) signal(cnt, c5, lz);
    }
    STAMP_G(24, 29);
    v[1] = pair(2, 2);
    { unsigned m[4];
#pragma unroll
      for (int i = 0; i < 4; i += 2) {
        f32x4 gv, uv;
#pragma unroll
        for (int e = 0; e < 4; ++e) {
          auto w = __builtin_amdgcn_permlane32_swap(__float_as_uint(acc[i + (e >> 1)][6][e & 1]), __float_as_uint(acc[i + (e >> 1)][6][2 + (e & 1)]), false, false);
          gv[e] = __uint_as_float(w[0]); uv[e] = __uint_as_float(w[1]);
        }
        swi4(gv, uv, m[i], m[i + 1]);
      }
      auto x0 = __builtin_amdgcn_permlane32_swap(m[0], m[2], false, false);
      auto x1 = __builtin_amdgcn_permlane32_swap(m[1], m[3], false, false);
      const unsigned o[4] = {x0[0], x0[1], x1[0], x1[1]};
      v[2] = tr16(o);
      st16<true>(hb, ho1, v[0]); st16<true>(hb, ho1 + 2048, v[1]); st16<true>(hb, hom, v[2]);
    }
    STAMP_G(25, 30);
    if (grp) {
#pragma unroll
      for (int t = 0; t < NDR; ++t) BAR();
      wait_vm<0>();
      BAR();
      if (wave == 4) signal(cnt, c5 + 1, lz);
      if (wave > 4) return;
#pragma unroll 1
      for (int t = NDR + 1; t < 9; ++t) BAR();
      unsigned seen = 16;
      const unsigned* pc = cnt + ((C5 + (tm6 >> 1) * 4 + kh6 * 2 + 1) * 8 + tn6) * 32;
      if (wave == 4) asm volatile("global_load_dword %0, %1, %2 sc1" : "=v"(seen) : "v"(0), "s"(pc) : "memory");
      BAR(); BAR();
      asm volatile("s_waitcnt vmcnt(0)" : "+v"(seen)::"memory");
      if (__builtin_amdgcn_readfirstlane(seen) < 16) poll<0>(pc, 16);
      return;
    }
  }
  { const int lane = lane_id(), fr = lane & 15, g = lane >> 4, p4 = fr & 3, row = 4 * g + (fr >> 2);
    const int wm = wave & 1, wn = wave >> 1;
    const int lw0 = row * 128 + (((row >> 1) & 3) * 32) + p4 * 8;
    const int mb0 = tm6 * 8 + wm * 4, t64 = tn6 * 2 + kh6;
    u32x4 rv[4];
#pragma unroll
    for (int i = 0; i < 4; ++i) rv[i] = ld16<true>(ws + WS_X1, ((mb0 + i) * 16 + t64) * 2048 + wn * 1024 + lane * 16);
    f32x4 acc[4][4];
#pragma unroll
    for (int i = 0; i < 4; ++i)
#pragma unroll
      for (int j = 0; j < 4; ++j) { acc[i][j] = (f32x4){0.f, 0.f, 0.f, 0.f}; asm volatile("" : "+v"(acc[i][j])); }
    struct Fr { bf16x8 a[4]; bf16x4 w[4][2]; };
    const LDS_PTR(char) aw0 = smem + (lw0 ^ (wn * 64)); const LDS_PTR(char) aw1 = smem + (lw0 ^ (wn * 64 + 32));
    const LDS_PTR(char) aa = smem + K6_A + wm * 4096 + lane * 16;
    auto rd1 = [&](Fr& f, int r) {
      if (r < 2) f.w[0][r] = ld_tr4(aw0 + r * 2048);
      else if (r < 4) f.a[r - 2] = *(const LDS_PTR(bf16x8))(aa + (r - 2) * 1024);
      else if (r < 10) { const int j = (r - 2) >> 1; f.w[j][r & 1] = ld_tr4(((j & 1) ? aw1 : aw0) + (j >> 1) * 4096 + (r & 1) * 2048); }
      else f.a[r - 8] = *(const LDS_PTR(bf16x8))(aa + (r - 8) * 1024);
    };
    int dl, da, wrap = -(K6_NW - 1) * 8192, wrapa = -(K6_NA - 1) * 8192;
    unsigned mask = wrap_mask(1, K6_NW), maska = wrap_mask(1, K6_NA);
    Fr fA, fB;
    asm volatile("" : "+s"(wrap), "+s"(wrapa), "+v"(aw0), "+v"(aw1), "+v"(aa));
    wait_vm<0>();
    BAR();
    STAMP_AT(32 );
#pragma unroll
    for (int r = 0; r < 12; ++r) { SCHED0(); rd1(fA, r); }
    SCHED0();
    aw0 += 8192; aw1 += 8192; aa += 8192;
    wait_lgkm<6>();
    BAR();
    auto trip = [&](const Fr& cur, Fr& nxt) {
      SCHED0();
#pragma unroll
      for (int m = 0; m < 16; ++m) {
        const int j = (m & 7) >> 1, i = (m >> 3) * 2 + ((m ^ j) & 1);
        if (m == 2) mfma_sel<8192>(acc[i][j], cat8(cur.w[j][0], cur.w[j][1]), cur.a[i], mask, dl, wrap);
        else if (m == 6) mfma_sel<8192>(acc[i][j], cat8(cur.w[j][0], cur.w[j][1]), cur.a[i], maska, da, wrapa);
        else mfma_acc(acc[i][j], cat8(cur.w[j][0], cur.w[j][1]), cur.a[i]);
        SCHED0();
        if (m < 12) { rd1(nxt, m); SCHED0(); }
        if (m == 3) wait_lgkm<4>();
        if (m == 12) { aw0 += dl; aw1 += dl; asm volatile("" : "+v"(aw0), "+v"(aw1)); }
        if (m == 13) { aa += da; asm volatile("" : "+v"(aa)); }
        if (m == 14) wait_lgkm<6>();
        if (m == 15) BAR();
        SCHED0();
      }
    };
#pragma unroll 1
    for (int h = 0; h < 2; ++h) {
#pragma unroll 1
      for (int t = 0; t < 7; ++t) { trip(fA, fB); trip(fB, fA); trip(fA, fB); trip(fB, fA); }
      mask = wrap_mask(29, K6_NW); maska = wrap_mask(29, K6_NA);
    }
    mfma_done(acc);
    STAMP_AT(34 );
    const int slot = wm * 2 + wn, pb = (b ^ 4) * 4 + slot;
    unsigned* flags = (unsigned*)(ws + WS_FLG);
    { char* pp = ws + WS_PART + (size_t)(b * 4 + slot) * 4096;
#pragma unroll
      for (int i = 0; i < 4; ++i)
        st16<true>(pp, i * 1024 + lane * 16, (u32x4){pk2(acc[i][0][0], acc[i][0][1]), pk2(acc[i][0][2], acc[i][0][3]), pk2(acc[i][1][0], acc[i][1][1]), pk2(acc[i][1][2], acc[i][1][3])});
      wait_vm<0>();
      STAMP_AT(35 );
      if (lane == 0) __hip_atomic_store(flags + (b * 4 + slot) * 16, 1u, __ATOMIC_RELAXED, __HIP_MEMORY_SCOPE_AGENT);
    }
    { const LDS_PTR(float) vec = (const LDS_PTR(float))(smem + LDS_VEC) + 192 + wn * 32 + 4 * g;
      const f32x4 gv0 = *(const LDS_PTR(f32x4))vec, gv1 = *(const LDS_PTR(f32x4))(vec + 16);
      poll<PD6>(flags + pb * 16, 1);
      STAMP_AT(36 );
      const char* pp = ws + WS_PART + (size_t)pb * 4096;
      u32x4 z[4];
#pragma unroll
      for (int i = 0; i < 4; ++i) z[i] = ld16<true>(pp, i * 1024 + lane * 16);
#pragma unroll
      for (int i = 0; i < 4; ++i) {
        const f32x4 v0 = (acc[i][2] + (f32x4){lo2f(z[i][0]), hi2f(z[i][0]), lo2f(z[i][1]), hi2f(z[i][1])}) * gv0;
        const f32x4 v1 = (acc[i][3] + (f32x4){lo2f(z[i][2]), hi2f(z[i][2]), lo2f(z[i][3]), hi2f(z[i][3])}) * gv1;
        const unsigned o0 = pk2(lo2f(rv[i][0]) + v0[0], hi2f(rv[i][0]) + v0[1]), o1 = pk2(lo2f(rv[i][1]) + v0[2], hi2f(rv[i][1]) + v0[3]);
        const unsigned o2 = pk2(lo2f(rv[i][2]) + v1[0], hi2f(rv[i][2]) + v1[1]), o3 = pk2(lo2f(rv[i][3]) + v1[2], hi2f(rv[i][3]) + v1[3]);
        auto s0 = __builtin_amdgcn_permlane16_swap(o0, o2, false, false);
        auto s1 = __builtin_amdgcn_permlane16_swap(o1, o3, false, false);
        st16<false>(out, (((mb0 + i) * 16 + fr) * D_ + t64 * 64 + wn * 32 + (g & 1) * 16 + (g >> 1) * 8) * 2, (u32x4){s0[0], s1[0], s0[1], s1[1]});
      }
    }
    STAMP_AT(37 );
  }
}

extern "C" void kernel_launch(void* const* d_in, const int* in_sizes, int n_in, void* d_out, int out_size, void* d_ws,
                              size_t ws_size, hipStream_t stream) {
  const u16* x = (const u16*)d_in[0];
  const u16* wqkv = (const u16*)d_in[1];
  const u16* wo = (const u16*)d_in[2];
  const u16* w1 = (const u16*)d_in[4];
  const u16* w3 = (const u16*)d_in[5];
  const u16* w2 = (const u16*)d_in[6];
  const u16* c = (const u16*)d_in[7];
  const u16* wmod = (const u16*)d_in[8];
  (void)hipFuncSetAttribute((const void*)k2_block, hipFuncAttributeMaxDynamicSharedMemorySize, MEGA_LDS);
  char* ws = (char*)d_ws;
  k2_block<<<256, 768, MEGA_LDS, stream>>>(x, wqkv, wo, w1, w3, w2, c, wmod, (u16*)d_out, ws);
}
```

```cpp
#include <hip/hip_runtime.h>
#include <stdint.h>
#include <type_traits>
#pragma clang diagnostic ignored "-Winline-asm"

typedef unsigned short u16;
typedef short bf16x8 __attribute__((ext_vector_type(8)));
typedef short bf16x4 __attribute__((ext_vector_type(4)));
typedef float f32x4 __attribute__((ext_vector_type(4)));
typedef float f32x2 __attribute__((ext_vector_type(2)));
typedef unsigned int u32x4 __attribute__((ext_vector_type(4)));
typedef unsigned int u32x2 __attribute__((ext_vector_type(2)));
#define LDS_PTR(T) __attribute__((address_space(3))) T*
#define BAR() do { asm volatile("" ::: "memory"); __builtin_amdgcn_s_barrier(); asm volatile("" ::: "memory"); } while (0)
#define SCHED0() __builtin_amdgcn_sched_barrier(0)
template <int N> using IC = std::integral_constant<int, N>;
typedef std::integral_constant<bool, true> T_;
typedef std::integral_constant<bool, false> F__;

constexpr int S_ = 2048, D_ = 1024, F_ = 3584;
#define STAMP_AT(i)
#define STAMP_G(ia, ib)
#define STAMP_L(i)

__device__ __forceinline__ float bf2f(u16 v) { return __uint_as_float(((unsigned)v) << 16); }
__device__ __forceinline__ unsigned pk2(float lo, float hi) {
  typedef __bf16 bf2 __attribute__((ext_vector_type(2)));
  f32x2 v = {lo, hi}; bf2 r = __builtin_convertvector(v, bf2); return __builtin_bit_cast(unsigned, r);
}
__device__ __forceinline__ float lo2f(unsigned u) { return __uint_as_float(u << 16); }
__device__ __forceinline__ float hi2f(unsigned u) { return __uint_as_float(u & 0xffff0000u); }
__device__ __forceinline__ float rbf(float v) { return lo2f(pk2(v, 0.f)); }
#define glds16(sbase, voff, lds_off, imm) \
  asm volatile("s_add_u32 m0, %2, %3\n\ts_nop 0\n\tglobal_load_lds_dwordx4 %0, %1" ::"v"(voff), "s"(sbase), "s"(lds_off), "n"(imm) : "memory", "scc")
#define glds16s(sbase, voff, lds_off, imm) \
  asm volatile("s_add_u32 m0, %2, %3\n\ts_nop 0\n\tglobal_load_lds_dwordx4 %0, %1 sc1" ::"v"(voff), "s"(sbase), "s"(lds_off), "n"(imm) : "memory", "scc")
#define GLDS4(SC) "s_mov_b32 m0, %6\n\ts_nop 0\n\tglobal_load_lds_dwordx4 %0, %4" SC "\n\tglobal_load_lds_dwordx4 %1, %5 offset:1024" SC \
                  "\n\tglobal_load_lds_dwordx4 %2, %4 offset:2048" SC "\n\tglobal_load_lds_dwordx4 %3, %5 offset:3072" SC
template <bool SC1> __device__ __forceinline__ void glds16x4(const void* s0, const void* s1, const unsigned (&v)[4], unsigned lds) {
  if (SC1) asm volatile(GLDS4(" sc1") ::"v"(v[0]), "v"(v[1]), "v"(v[2]), "v"(v[3]), "s"(s0), "s"(s1), "s"(lds) : "memory");
  else asm volatile(GLDS4("") ::"v"(v[0]), "v"(v[1]), "v"(v[2]), "v"(v[3]), "s"(s0), "s"(s1), "s"(lds) : "memory");
}
__device__ __forceinline__ void glds4(const void* sbase, unsigned voff, unsigned lds_off) {
  asm volatile("s_mov_b32 m0, %2\n\ts_nop 0\n\tglobal_load_lds_dword %0, %1" ::"v"(voff), "s"(sbase), "s"(lds_off) : "memory");
}
__device__ __forceinline__ void stouch4(const void* p) {
  asm volatile("s_load_dwordx2 s[100:101], %0, 0x3c\n\ts_load_dwordx2 s[100:101], %0, 0xbc\n\ts_load_dwordx2 s[100:101], %0, 0x13c\n\ts_load_dwordx2 s[100:101], %0, 0x1bc"
               ::"s"(p) : "memory", "s100", "s101");
}
template <bool SC1> __device__ __forceinline__ void st16(void* sbase, unsigned voff, u32x4 v) {
  __builtin_amdgcn_raw_buffer_store_b128(v, __builtin_amdgcn_make_buffer_rsrc(sbase, 0, 0x7fffffff, 0x00020000), voff, 0, SC1 ? 16 : 0);
}
template <bool SC1> __device__ __forceinline__ u32x4 ld16(const void* sbase, unsigned voff) {
  return __builtin_amdgcn_raw_buffer_load_b128(__builtin_amdgcn_make_buffer_rsrc((void*)sbase, 0, 0x7fffffff, 0x00020000), voff, 0, SC1 ? 16 : 0);
}
template <int N> __device__ __forceinline__ void wait_vm() { asm volatile("s_waitcnt vmcnt(%0)" ::"n"(N) : "memory"); }
__device__ __forceinline__ void wait_lgkm0() { asm volatile("" ::: "memory"); __builtin_amdgcn_s_waitcnt(0xC07F); asm volatile("" ::: "memory"); }
__device__ __forceinline__ bf16x4 ld_tr4(const LDS_PTR(char) p) { return __builtin_amdgcn_ds_read_tr16_b64_v4i16((LDS_PTR(bf16x4))p); }
__device__ __forceinline__ bf16x8 cat8(bf16x4 lo, bf16x4 hi) { return __builtin_shufflevector(lo, hi, 0, 1, 2, 3, 4, 5, 6, 7); }
__device__ __forceinline__ void mfma_acc(f32x4& acc, bf16x8 a, bf16x8 b) { asm volatile("v_mfma_f32_16x16x32_bf16 %0, %1, %2, %0" : "+v"(acc) : "v"(a), "v"(b)); }
template <int STEP> __device__ __forceinline__ void mfma_sel(f32x4& acc, bf16x8 a, bf16x8 b, unsigned& mask, int& dl, int wrap) {
  asm volatile("s_add_u32 %1, %1, %1\n\tv_mfma_f32_16x16x32_bf16 %0, %3, %4, %0\n\ts_cselect_b32 %2, %5, %6" : "+v"(acc), "+s"(mask), "=s"(dl) : "v"(a), "v"(b), "s"(wrap), "n"(STEP) : "scc");
}
constexpr unsigned wrap_mask(int first, int ns) { unsigned m = 0; for (int t = 0; t < 32; ++t) if ((first + t) % ns == ns - 1) m |= 0x80000000u >> t; return m; }
template <int N> __device__ __forceinline__ void wait_lgkm() { __builtin_amdgcn_s_waitcnt(0xC07F | (N << 8)); }
template <int R, int C> __device__ __forceinline__ void mfma_done(f32x4 (&acc)[R][C]) {
  asm volatile("s_nop 9");
#pragma unroll
  for (int i = 0; i < R; ++i)
#pragma unroll
    for (int j = 0; j < C; ++j) asm volatile("" : "+v"(acc[i][j]));
}
__device__ __forceinline__ int lane_id() { int l; asm volatile("v_mbcnt_lo_u32_b32 %0, -1, 0\n\tv_mbcnt_hi_u32_b32 %0, -1, %0" : "=v"(l)); return l; }
__device__ __forceinline__ float xsum16_32(float v) {
  auto a = __builtin_amdgcn_permlane16_swap(__float_as_uint(v), __float_as_uint(v), false, false); v = __uint_as_float(a[0]) + __uint_as_float(a[1]);
  auto c = __builtin_amdgcn_permlane32_swap(__float_as_uint(v), __float_as_uint(v), false, false); return __uint_as_float(c[0]) + __uint_as_float(c[1]);
}

__device__ __forceinline__ float ln_rstd(float var) { return __builtin_amdgcn_rcpf(rbf(__builtin_amdgcn_sqrtf(rbf(rbf(var) + 1e-5f)))); }

constexpr size_t WS_MOD = 0;
constexpr size_t WS_EPO = 64 << 10, WS_FL1 = 96 << 10;
constexpr size_t WS_CTL = 128 << 10;
constexpr int C2 = 0, C3 = 16, C4 = 32, C5 = 48, CM = 80;
constexpr size_t WS_FLG = WS_CTL + (88 << 10), CTL_BYTES = 120 << 10;
constexpr size_t WS_XCH = 512 << 10;
constexpr size_t WS_MOD2 = 1 << 20;
constexpr size_t WS_H = 2 << 20, WS_O = WS_H + (4 << 20), WS_H2 = WS_O + (4 << 20), WS_HH = WS_H2 + (4 << 20),
                 WS_PART = WS_HH + (size_t)S_ * F_ * 2;

template <int NL> __device__ __forceinline__ f32x2 slab_sum(const unsigned (&mc)[NL / 2], const u32x4 (&mw)[NL]) {
  f32x2 a2[4] = {};
#pragma unroll
  for (int i = 0; i < NL; ++i) {
    const float cv = (i & 1) ? hi2f(mc[i >> 1]) : lo2f(mc[i >> 1]);
    const float sv = rbf(cv * __builtin_amdgcn_rcpf(1.f + __builtin_amdgcn_exp2f(cv * -1.4426950408889634f)));
#pragma unroll
    for (int j = 0; j < 4; ++j) a2[j] += (f32x2){lo2f(mw[i][j]), hi2f(mw[i][j])} * sv;
  }
  float acc[8] = {a2[0][0], a2[0][1], a2[1][0], a2[1][1], a2[2][0], a2[2][1], a2[3][0], a2[3][1]};
#pragma unroll
  for (int j = 0; j < 4; ++j) { auto a = __builtin_amdgcn_permlane32_swap(__float_as_uint(acc[j]), __float_as_uint(acc[j + 4]), false, false); acc[j] = __uint_as_float(a[0]) + __uint_as_float(a[1]); }
#pragma unroll
  for (int j = 0; j < 2; ++j) { auto a = __builtin_amdgcn_permlane16_swap(__float_as_uint(acc[j]), __float_as_uint(acc[j + 2]), false, false); acc[j] = __uint_as_float(a[0]) + __uint_as_float(a[1]); }
#pragma unroll
  for (int j = 0; j < 2; ++j) acc[j] += __uint_as_float(__builtin_amdgcn_mov_dpp(__float_as_uint(acc[j]), 0x128, 0xf, 0xf, false));
  return (f32x2){acc[0], acc[1]};
}

constexpr int K3_B = 18432, K3_STG = 18432 + 24576;
constexpr int K4_RED = 57344;
constexpr int K5_G = 16384, K5_U = 16384 + 7168 + 16, K5_STG = 30736, K5_NS = 5;
constexpr int K6_NW = 12, K6_NA = 7, K6_A = K6_NW * 8192, K6_X = 9 * 8192;
constexpr int LDS_VEC = 158720, LDS_DUMMY = LDS_VEC + 1024, LDS_ARR = LDS_DUMMY + 256, MEGA_LDS = LDS_ARR + 64;
constexpr unsigned SPIN_CAP = 1u << 19;
#define KNOBS PQ = 2, PQ5 = 4, PD2 = 4, PD3 = 4, PD4 = 4, PD5 = 48, PD6 = 6, PS6 = 1, FS0 = 4, XS0 = 14, XS1 = 4, TS5 = 3, WA = 0, WB = 1, WC = 4, NA5 = 2, ND5 = 0, NI5 = 2, NB5 = 4, VB3 = 4, SLB = 15, PRE6 = 4, NDR = 1, SL6 = 0
enum { KNOBS };
constexpr unsigned long long SKIP6 = 0x7c;
static_assert(__builtin_popcountll(SKIP6) == 5, "44 stages of W are issued behind 49 barriers");

__device__ __forceinline__ void signal(unsigned* cnt, int c, int lane, unsigned v = 1) {
  if (lane < 8) __hip_atomic_fetch_add(cnt + (c * 8 + lane) * 32, v, __ATOMIC_RELAXED, __HIP_MEMORY_SCOPE_AGENT);
}
template <int D0, int D1 = 1> __device__ __forceinline__ void poll(const unsigned* cnt, unsigned want) {
  if (D0) __builtin_amdgcn_s_sleep(D0);
#pragma unroll 1
  for (unsigned spins = 0; spins < SPIN_CAP; ++spins) {
    if (__builtin_amdgcn_ballot_w64(__hip_atomic_load(cnt, __ATOMIC_RELAXED, __HIP_MEMORY_SCOPE_AGENT) < want) == 0) break;
    __builtin_amdgcn_s_sleep(D1);
  }
  __builtin_amdgcn_fence(__ATOMIC_ACQUIRE, "wavefront");
}

template <int D0, int D1, int EX> __device__ __forceinline__ void pollp(const unsigned* cnt, unsigned want, unsigned (&r)[4]) {
  unsigned n = SPIN_CAP;
#define PL1(a) "global_atomic_add %" #a ", %5, %9, off sc0\n\ts_sleep %8\n\t"
#define PL2(a, c) "global_atomic_add %" #a ", %5, %9, off sc0\n\ts_waitcnt vmcnt(3)\n\tv_cmp_gt_u32 vcc, %6, %" #c "\n\ts_cbranch_vccz 2f\n\ts_sleep %8\n\t"
  asm volatile("s_mov_b64 exec, %10\n\ts_sleep %7\n\t" PL1(0) PL1(1) PL1(2) "1:\n\t" PL2(3, 0) PL2(0, 1) PL2(1, 2) PL2(2, 3) "s_sub_u32 %4, %4, 1\n\ts_cbranch_scc0 1b\n2:\n\ts_mov_b64 exec, -1"
               : "=&v"(r[0]), "=&v"(r[1]), "=&v"(r[2]), "=&v"(r[3]), "+s"(n) : "v"(cnt), "s"(want), "n"(D0), "n"(D1), "v"(0), "n"(EX) : "memory", "vcc", "scc");
}
#define KEEP4(r) asm volatile("; keep %0 %1 %2 %3" ::"v"(r[0]), "v"(r[1]), "v"(r[2]), "v"(r[3]))
typedef float f32x32 __attribute__((ext_vector_type(32)));
typedef float f32x16 __attribute__((ext_vector_type(16)));
#define M5(i, j) "v_mfma_f32_16x16x32_bf16 v[28*" #i "+4*" #j ":28*" #i "+4*" #j "+3], v[128+4*" #j ":131+4*" #j "], v[112+4*" #i ":115+4*" #i "], v[28*" #i "+4*" #j ":28*" #i "+4*" #j "+3]\n\t"
#define RA5(i) "ds_read_b128 v[112+4*" #i ":115+4*" #i "], %[aA] offset:1024*" #i "\n\t"
#define RW5(j, h, a, o) "ds_read_b64_tr_b16 v[128+4*" #j "+2*" #h ":129+4*" #j "+2*" #h "], %[" #a "] offset:" #o "\n\t"
#define ST5(a) "v_add_u32 %[" #a "], %[dl], %[" #a "]\n\t"
#define SEL5A "s_add_u32 %[mask], %[mask], %[mask]\n\t"
#define SEL5B "s_cselect_b32 %[dl], %[wrap], 30736\n\t"
#define LG(n) "s_waitcnt lgkmcnt(" #n ")\n\t"
#define RP5(j, h) RW5(j, h, aP, 32*(j/2)+7184*(j&1)+3584*h)
#define TRIP5(X)                                                                                                                                        \
  M5(0, 0) RW5(6, 1, aM, 3584) M5(1, 0) RA5(2) ST5(aM) M5(1, 1) RA5(3) ST5(aP) M5(0, 1) ST5(aA) LG(6) M5(0, 2) M5(1, 2) SEL5A M5(1, 3) SEL5B              \
  M5(0, 3) M5(0, 4) M5(1, 4) LG(0) M5(1, 5) M5(0, 5) "s_barrier\n\t" M5(0, 6) RA5(0) M5(1, 6) RA5(1)                                                      \
  M5(2, 0) X M5(3, 0) RP5(0, 0) M5(3, 1) RP5(0, 1) M5(2, 1) RP5(1, 0) M5(2, 2) RP5(1, 1) M5(3, 2) RP5(2, 0) M5(3, 3) RP5(2, 1) M5(2, 3) RP5(3, 0)          \
  M5(2, 4) RP5(3, 1) M5(3, 4) RP5(4, 0) M5(3, 5) RP5(4, 1) M5(2, 5) RP5(5, 0) M5(2, 6) RP5(5, 1) LG(8) M5(3, 6) RW5(6, 0, aM, 0)
#define ACC5 "+{v[0:31]}"(c0), "+{v[32:63]}"(c1), "+{v[64:95]}"(c2), "+{v[96:111]}"(c3)
#define CLOB5 "memory", "scc", "v112", "v113", "v114", "v115", "v116", "v117", "v118", "v119", "v120", "v121", "v122", "v123", "v124", "v125", "v126", "v127", "v128", "v129", "v130", "v131", "v132", "v133", \
              "v134", "v135", "v136", "v137", "v138", "v139", "v140", "v141", "v142", "v143", "v144", "v145", "v146", "v147", "v148", "v149", "v150", "v151", "v152", "v153", "v154", "v155"
static_assert(K5_STG == 30736 && K5_U - K5_G == 7184, "TRIP5");

#define C3(i, j) "v[24*" #i "+4*" #j "-12*(" #i "/4):24*" #i "+4*" #j "-12*(" #i "/4)+3]"
#define M3(i, j) "v_mfma_f32_16x16x32_bf16 " C3(i, j) ", v[124+4*" #j ":127+4*" #j "], v[104+4*" #i ":107+4*" #i "], " C3(i, j) "\n\t"
#define RA3(i, u) "ds_read_b128 v[104+4*" #i ":107+4*" #i "], %[aA] offset:1024*" #i "+9216*" #u "\n\t"
#define RH3(u) "ds_read_b128 v[120:123], %[aH] offset:9216*" #u "\n\t"
#define RW3(j, h, a, u) "ds_read_b64_tr_b16 v[124+4*" #j "+2*" #h ":125+4*" #j "+2*" #h "], %[" #a "] offset:8192*(" #j "/2)+2048*" #h "+4096*" #u "\n\t"
#define ST3(a, d) "v_add_u32 %[" #a "], " #d ", %[" #a "]\n\t"
#define H2_3(u, S0, S1, S2, B) M3(0, 0) RW3(5, 1, a1, u) M3(1, 0) RA3(2, u) M3(1, 1) RA3(3, u) S0 M3(0, 1) LG(7) S1 M3(0, 2) S2 M3(1, 2) M3(1, 3) M3(0, 3) LG(2) M3(0, 4) M3(1, 4) LG(0) B
#define H1_3(n, X)                                                                                                                                      \
  M3(1, 5) RA3(1, n) M3(0, 5) RA3(0, n) M3(2, 0) X M3(3, 0) RW3(0, 0, a0, n) M3(3, 1) RW3(0, 1, a0, n) M3(2, 1) RW3(1, 0, a1, n) M3(2, 2) RW3(1, 1, a1, n) \
  M3(3, 2) RW3(2, 0, a1, n) M3(3, 3) RW3(2, 1, a1, n) M3(2, 3) M3(4, 3) RW3(3, 0, a0, n) M3(4, 4) RW3(3, 1, a0, n) M3(2, 4) RH3(n)                         \
  M3(3, 4) RW3(4, 0, a0, n) M3(3, 5) RW3(4, 1, a0, n) LG(7) M3(2, 5) RW3(5, 0, a1, n)
#define STAGE3(d, X) H2_3(0, , , , ) H1_3(1, ) H2_3(1, ST3(a0, d), ST3(aA, d), ST3(a1, d) ST3(aH, d), "s_barrier\n\t") H1_3(0, X)
#define CV3(i, j, t) "v_cvt_pk_bf16_f32 v[104+2*" #t "], v[24*" #i "+4*" #j "-12*(" #i "/4)], v[24*" #i "+4*" #j "-12*(" #i "/4)+1]\n\tv_cvt_pk_bf16_f32 v[105+2*" #t "], v[24*" #i "+4*" #j "-12*(" #i "/4)+2], v[24*" #i "+4*" #j "-12*(" #i "/4)+3]\n\t"
#define WR3(i, t, a, o) "ds_write_b64 %[" #a "], v[104+2*" #t ":105+2*" #t "] offset:2048*" #i "+" #o "\n\t"
#define CW3(i, j, t, a, o) CV3(i, j, t) WR3(i, t, a, o)
#define V8(a) "v" #a "0", "v" #a "1", "v" #a "2", "v" #a "3", "v" #a "4", "v" #a "5", "v" #a "6", "v" #a "7"
#define V10(a) V8(a), "v" #a "8", "v" #a "9"
static_assert(K3_STG == 43008, "STAGE3");
__device__ __forceinline__ int w2_stage(int j) {
  const int j2 = j - 16, p = (j2 * 52) >> 8, r = j2 - p * 5;
  return j < 16 ? j * 112 * 2048 : (p * 224 + 32 + r * 32 + (r > 2 ? 32 : 0)) * 2048 + (r == 2);
}
__global__ __launch_bounds__(768, 3) __attribute__((amdgpu_num_sgpr(98))) void k2_block(const u16* __restrict__ x, const u16* __restrict__ wqkv, const u16* __restrict__ wo,
                                                   const u16* __restrict__ w1, const u16* __restrict__ w3, const u16* __restrict__ w2,
                                                   const u16* __restrict__ cnd, const u16* __restrict__ wmod, u16* __restrict__ out, char* __restrict__ ws) {
  extern __shared__ __attribute__((aligned(16))) char smem_[];
  LDS_PTR(char) smem = (LDS_PTR(char))smem_;
  const int tid = threadIdx.x;
  int lane = tid & 63;
  const int wave = __builtin_amdgcn_readfirstlane(tid >> 6);
  const unsigned lds_base = (unsigned)(uintptr_t)smem;
  const int b = blockIdx.x, xc = b & 7, ii = b >> 3;
  const int tm3 = (xc & 1) * 8 + (ii & 7), tn3 = (xc >> 1) * 4 + (ii >> 3);
  const int tm5 = (xc & 1) * 4 + (ii & 3), tn5 = (xc >> 1) * 8 + (ii >> 2);
  const int kh6 = xc >> 2, tm6 = (xc & 1) * 8 + (xc & 2) * 2 + (ii & 3), tn6 = ii >> 2;
  const int tm4 = tm6, tn4 = tn6 * 2 + kh6;
  const int u1 = (b >> 4) * 8 + xc;
  unsigned* cnt = (unsigned*)(ws + WS_CTL);
  unsigned long long* fl1 = (unsigned long long*)(ws + WS_FL1);
  STAMP_AT(0 );

  if (wave >= 8) {
    const int lw = wave - 8, P = b * 4 + lw, lane = lane_id();
    const bool isA = lw < 2;
    asm volatile("" ::"s"(wqkv), "s"(wo), "s"(w1), "s"(w3), "s"(w2), "s"(ws));
    int kpf = -4;
    unsigned pr[4] = {};
    auto touch = [&]() {
      if (kpf < 28 && __builtin_amdgcn_s_getreg((3 << 11) | (8 << 6) | 7) <= 7) {
        stouch4(kpf < 0 ? (const char*)wo + ((kpf + 4) * 4096 + P * 4) * 128 : (const char*)((P & 1) ? w3 : w1) + (kpf * 2048 + (P >> 1) * 4) * 128);
        ++kpf;
      }
    };
    const char* epo = ws + WS_EPO;
    const unsigned swz = ((lane & 7) ^ ((lane >> 3) & 6)) * 16;
    const int cg = lw * 16 + tn4;
    auto issue4 = [&](int s, unsigned slot) {
      const char* p = (const char*)wo + tn4 * 128 + (lw * 32 + s * 128) * 2048;
      unsigned v4[4];
#pragma unroll
      for (int i = 0; i < 4; ++i) v4[i] = (lane >> 3) * 2048 + swz + i * 15360;
      glds16x4<false>(p, p, v4, lds_base + slot + lw * 4096);
    };
    auto run3 = [&](auto isA_) {
      constexpr bool A_ = decltype(isA_)::value; constexpr int N = A_ ? 9 : 12;
      const char* sp = A_ ? ws + WS_H + lw * 131072 + (tm3 * 8 - 1) * 1024 : (const char*)wqkv + tn3 * 128;
      unsigned vo[3][4];
#pragma unroll
      for (int i = 0; i < 12; ++i) {
        const int tb = (lw - 2) * 12 + i;
        vo[i >> 2][i & 3] = A_ ? lane * 16 + (i < 8 ? 1024 + (i >> 2) * 4096 : tm3 ? 0 : 1024) : ((tb & 7) * 8 + (lane >> 3)) * 6144 + (tb >> 3) * 2048 - (i & 3) * 1024 + swz;
      }
      const unsigned d0 = lds_base + (A_ ? lw * 9216 : K3_B + (lw - 2) * 12288);
      auto issue = [&](unsigned st) {
        if (A_) { glds16x4<true>(sp, sp, vo[0], d0 + st + 1024); glds16s(sp, vo[2][0], d0 + st, 0); glds16x4<true>(sp, sp, vo[1], d0 + st + 5120); }
        else {
#pragma unroll
          for (int i = 0; i < 3; ++i) glds16x4<false>(sp, sp, vo[i], d0 + st + i * 4096);
        }
        sp += A_ ? 262144 : 64 * 6144;
      };
      { unsigned long long ep;
        asm volatile("s_load_dwordx2 %0, %1, 0x0\n\ts_waitcnt lgkmcnt(0)" : "=s"(ep) : "s"(epo) : "memory");
        ep += 1;
        if (lw == 3) { *(LDS_PTR(u32x4))(smem + LDS_ARR) = (u32x4){0u, 0u, 0u, 0u}; wait_lgkm0(); }
        if (lw == 1) {
#pragma unroll
          for (int k = 0; k < 3; ++k) {
            const int i = b * 158 + (k * 64 + lane < 158 ? k * 64 + lane : 0);
            st16<true>(ws, i < 32768 ? (unsigned)WS_XCH + i * 16 : (unsigned)WS_CTL + (i - 32768) * 16, i < 32768 ? (u32x4){~0u, ~0u, ~0u, ~0u} : (u32x4){0u, 0u, 0u, 0u});
          }
        }
        BAR();
        auto flag = [&](int idx) { wait_vm<0>(); if (lane < 8) __hip_atomic_store(fl1 + lane * 256 + idx, ep, __ATOMIC_RELAXED, __HIP_MEMORY_SCOPE_AGENT); };
        if (lw == 1 && (b & 8)) {
          const int kq = u1 & 3, cgp = u1 >> 2;
          float sm = 0.f;
#pragma unroll
          for (int r = 0; r < 8; ++r) sm += *(const LDS_PTR(float))(smem + 8192 + (r * 64 + lane) * 4);
          __hip_atomic_store((unsigned*)(ws + WS_MOD) + kq * 2048 + cgp * 64 + lane, __float_as_uint(sm), __ATOMIC_RELAXED, __HIP_MEMORY_SCOPE_AGENT);
          flag(((cgp >> 3) & 1) * 64 + (cgp >> 4) * 32 + (cgp & 7) * 4 + kq);
        }
        if (b & 8) BAR();
        BAR();
        if (lw == 1 && !(b & 8)) flag(128 + u1);
        if (lw < 2) {
          if (lw == 1) BAR();
#pragma unroll 1
          for (unsigned spins = 0; spins < SPIN_CAP; ++spins) {
            bool ok = true;
#pragma unroll
            for (int k = 0; k < 4; ++k) if (k == 0 || lw) ok &= __hip_atomic_load(fl1 + xc * 256 + (lw ? k : (b >> 3) & 1) * 64 + lane, __ATOMIC_RELAXED, __HIP_MEMORY_SCOPE_AGENT) == ep;
            if (__builtin_amdgcn_ballot_w64(!ok) == 0) break;
            __builtin_amdgcn_s_sleep(FS0);
          }
          if (lw == 0) BAR();
          else if (b == 0 && lane == 0) __hip_atomic_store((unsigned long long*)(ws + WS_EPO), ep, __ATOMIC_RELAXED, __HIP_MEMORY_SCOPE_AGENT);
        } else BAR();
      }
      BAR();
      if (lw == 0) {
        signal(cnt, C2 + (u1 >> 3), lane, (b & 8) ? 1u : 0x10000u);
        pollp<PD2, PQ, 3>(cnt + ((C2 + ((lane & 1) && tm3 ? tm3 - 1 : tm3)) * 8 + (tn3 & 7)) * 32, 0x80000u, pr);
      }
      if (!A_) { issue(0); issue(K3_STG); }
      touch();
      BAR();
      if (A_) { issue(0); issue(K3_STG); }
      wait_vm<N>();
      KEEP4(pr);
      BAR();
      issue(2 * K3_STG);
      unsigned st = 0;
#pragma unroll 1
      for (int j = 0; j < 13; ++j) {
        wait_vm<N>();
        BAR();
        issue(st);
        st = st == 2 * K3_STG ? 0 : st + K3_STG;
        touch();
      }
      wait_vm<N>();
      BAR();
      issue4(0, 57344); issue4(5, 57344 + 5 * 16384);
      if (kpf < 0) kpf = 0;
      {
        glds4((P & 1) ? w3 : w1, ((kpf + ((lane >> 2) < VB3 ? lane >> 2 : 0)) * 2048 + (P >> 1) * 4 + (lane & 3)) * 128, lds_base + LDS_DUMMY); kpf += VB3; }
      wait_vm<9>();
    };
    if (isA) run3(T_()); else run3(F__());
    { auto issue = issue4;
      auto upto = [&](int a, int e) {
#pragma unroll
        for (int s = 1; s < 5; ++s) if (s > a && s <= e) issue(s, 57344 + s * 16384);
      };
      BAR();
      upto(0, WA);
      BAR();
      upto(WA, WB);
      BAR();
      upto(WB, WC);
      BAR();
      if (lw == 0) signal(cnt, C3 + tm3, lane);
      upto(WC, 4);
      issue(6, 0); issue(7, 16384);
      if (lw == 0) pollp<PD3, PQ, 1>(cnt + ((C3 + tm4) * 8 + tn6) * 32, 16, pr);
      wait_vm<8>();
      BAR();
      wait_vm<0>();
      KEEP4(pr);
      BAR();
      poll<0, 4>(cnt + (CM * 8 + cg) * 32, 4);
      f32x4 pv[4];
#pragma unroll
      for (int i = 0; i < 4; ++i) pv[i] = __builtin_bit_cast(f32x4, ld16<true>(ws + WS_MOD2, (((lane >> 4) * 4 + i) * 4096 + cg * 64 + (lane & 15) * 4) * 4));
      f32x4 v = (pv[0] + pv[1]) + (pv[2] + pv[3]);
#pragma unroll
      for (int e = 0; e < 4; ++e) { v[e] = rbf(xsum16_32(v[e])); if (lw == 2) v[e] = rbf(1.f + v[e]); if (lw == 1 || lw == 2) v[e] *= -1.4426950408889634f; }
      *(LDS_PTR(f32x4))(smem + LDS_VEC + lw * 256 + (lane & 15) * 16) = v;
      wait_lgkm0();
    }
    auto run5 = [&](auto isA_) {
      constexpr bool A_ = decltype(isA_)::value; constexpr int N = A_ ? 8 : 7;
      const char* sp = A_ ? ws + WS_H2 + (tm5 * 16 + lw * 8) * 1024 : (const char*)(lw == 2 ? w1 : w3) + tn5 * 224;
      unsigned vp[N];
#pragma unroll
      for (int i = 0; i < N; ++i) { const int L = i * 64 + lane, r = L / 14, c = L - r * 14; vp[i] = A_ ? L * 16 : r * (F_ * 2) + c * 16; }
      const unsigned d0 = lds_base + (A_ ? lw * 8192 : lw == 2 ? K5_G : K5_U);
      unsigned st = 3 * K5_STG;
      auto issue = [&]() {
#pragma unroll
        for (int i = 0; i < N; ++i) { if (A_) glds16s(sp, vp[i], d0 + st, i * 1024); else glds16(sp, vp[i], d0 + st, i * 1024); }
        sp += A_ ? 131072 : 32 * F_ * 2; st = st == (K5_NS - 1) * K5_STG ? 0 : st + K5_STG;
      };
      BAR();
      if (!A_) { __builtin_amdgcn_s_sleep(ND5); for (int s = 0; s < NA5; ++s) issue(); }
      BAR();
      u32x4 xk[4];
      auto keep = [&]() {
#pragma unroll
        for (int i = 0; i < 4; ++i) { xk[i] = *(const LDS_PTR(u32x4))(smem + lw * 4096 + i * 1024 + lane * 16); asm volatile("" : "+v"(xk[i])); }
      };
      if (A_ && lw == 0) { signal(cnt, C4 + tm4, lane); keep(); pollp<PD4, PQ, 3>(cnt + ((C4 + tm5 * 2 + (lane & 1)) * 8 + (tn5 & 7)) * 32, 16, pr); }
      else { keep(); if (!A_) { for (int s = NA5; s < NB5; ++s) issue(); } }
      BAR();
      if (A_) for (int s = 0; s < NI5; ++s) issue();
      wait_vm<A_ ? (NI5 - 1) * N : (NB5 - 1) * N>();
      KEEP4(pr);
      BAR();
#pragma unroll 1
      for (int s = A_ ? NI5 : NB5; s < K5_NS; ++s) issue();
#pragma unroll 1
      for (int j = 0; j < 27; ++j) {
        wait_vm<3 * N>();
        BAR();
        issue();
        if (j == 6) {
#pragma unroll
          for (int i = 0; i < 4; ++i) st16<true>(ws + WS_PART, (b * 4 + lw) * 4096 + i * 1024 + lane * 16, (u32x4){~0u, ~0u, ~0u, ~0u});
        }
        if (j == 12 && lw == 0 && lane == 0) __hip_atomic_store((unsigned*)(ws + WS_FLG) + b * 32, 16u, __ATOMIC_RELAXED, __HIP_MEMORY_SCOPE_AGENT);
        touch(); __builtin_amdgcn_s_sleep(TS5); touch();
      }
      const char* sb = A_ ? ws + WS_HH + (kh6 * 56 * 128 + tm6 * 8 + lw * 4) * 1024 : (const char*)w2 + kh6 * 1792 * 2048 + tn6 * 256 + ((lw ^ kh6 ^ 1) & 1) * 128;
      unsigned vo[4];
#pragma unroll
      for (int i = 0; i < 4; ++i) {
        const int r = i * 8 + (lane >> 3), k = 8 * ((r >> 2) & 3) + 4 * (r >> 4) + (r & 3);
        vo[i] = A_ ? lane * 16 : k * 2048 - i * 1024 + swz;
      }
      const unsigned d6 = lds_base + (A_ ? K6_A : 0) + (lw & 1) * 4096;
      st = 0;
      const int tab = w2_stage(lane);
      int jn = 0;
      auto issue6 = [&]() {
        if (A_) { glds16x4<true>(sb, sb, vo, d6 + st); sb += 131072; }
        else { const int o = __builtin_amdgcn_readlane(tab, jn); const char* s0 = sb + (o & ~1); glds16x4<false>(s0, s0 + (o & 1) * 65536, vo, d6 + st); ++jn; }
        st = st == ((A_ ? K6_NA : K6_NW) - 1) * 8192 ? 0 : st + 8192;
      };
      auto pre6 = [&](int n) {
        if (!A_) {
#pragma unroll 1
          for (int i = 0; i < n; ++i) issue6();
        }
      };
      constexpr int E = A_ ? 0 : 4;
      { const char* p = (const char*)w2 + (kh6 * 1792 + ii) * 2048;
        const int n = lw * 56 + (lane < 56 ? lane : 0), o = w2_stage((xc & 3) * 14 + (n >> 4));
        glds4(p, (o & ~1) + (o & (ii >> 4) & 1) * 65536 + (n & 15) * 128, lds_base + LDS_DUMMY);
        const int o2 = w2_stage(lw * 14 + (lane < 28 && SL6 ? lane >> 1 : 0));
        glds4(p, (o2 & ~1) + (o2 & (ii >> 4) & 1) * 65536 + (((3 - ((unsigned)(uintptr_t)w2 >> 7)) & 7) + (lane & 1) * 8) * 128, lds_base + LDS_DUMMY); }
      wait_vm<3 * N + 2>(); BAR();
      wait_vm<2 * N + 2>(); BAR(); pre6(PRE6);
      wait_vm<N + PRE6 * E + 2>(); BAR(); pre6(4);
      wait_vm<(PRE6 + 4) * E + 2>(); BAR(); pre6(8 - PRE6);
      BAR();
      if (A_) {
        volatile LDS_PTR(unsigned) go = (volatile LDS_PTR(unsigned))(smem + LDS_ARR + 4);
        if (lw == 0) { pollp<PD5, PQ5, 1>(cnt + ((C5 + (tm6 >> 1) * 4 + kh6 * 2) * 8 + tn6) * 32, 16, pr); *go = 1u; }
        else {
#pragma unroll 1
          for (unsigned spins = 0; spins < SPIN_CAP && __builtin_amdgcn_readfirstlane(*go) == 0; ++spins) __builtin_amdgcn_s_sleep(1);
          asm volatile("" ::: "memory");
        }
        STAMP_L(31);
        issue6(); issue6(); issue6();
        wait_vm<8>(); KEEP4(pr); BAR(); issue6(); issue6();
        wait_vm<12>(); BAR(); issue6(); issue6();
      } else { wait_vm<16>(); BAR(); BAR(); }
#pragma unroll 1
      for (int c = 2; c < 51; ++c) { wait_vm<16>(); BAR(); if (A_ || !((SKIP6 >> c) & 1)) issue6(); }
      wait_vm<16>(); BAR(); wait_vm<12>(); BAR(); wait_vm<8>(); BAR(); wait_vm<4>(); BAR(); wait_vm<0>(); BAR();
#pragma unroll
      for (int i = 0; i < 4; ++i) *(LDS_PTR(u32x4))(smem + K6_X + lw * 4096 + i * 1024 + lane * 16) = xk[i];
      BAR(); wait_lgkm0(); BAR();
    };
    if (isA) run5(T_()); else run5(F__());
    asm volatile("s_waitcnt lgkmcnt(0)" ::: "memory");
    return;
  }

  int fr = lane & 15, g = lane >> 4, p4 = fr & 3, row = 4 * g + (fr >> 2);
  int lsw = row * 128 + (((row >> 1) & 3) * 32) + p4 * 8;
  { const int mb = u1, hf = (b >> 3) & 1, kb0 = hf * 16 + wave * 2, cofs = (g & 1) * 16 + (g >> 1) * 8;
    u32x4 v[4];
    auto ldx = [&]() {
#pragma unroll
      for (int p = 0; p < 4; ++p) v[p] = *(const u32x4*)(x + (size_t)(mb * 16 + fr) * D_ + ((kb0 + (p & 1)) ^ ((p >> 1) * 16)) * 32 + cofs);
    };
    LDS_PTR(f32x2) sred = (LDS_PTR(f32x2))(smem + 4096);
    auto up2 = [](unsigned u) { return (f32x2){lo2f(u), hi2f(u)}; };
    auto stats = [&]() {
      f32x2 s2 = {0.f, 0.f}, q2 = {0.f, 0.f};
#pragma unroll
      for (int k = 0; k < 16; ++k) s2 += up2(v[k >> 2][k & 3]);
      const float mw = xsum16_32(s2[0] + s2[1]) * (1.f / 128.f);
#pragma unroll
      for (int k = 0; k < 16; ++k) { const f32x2 d = up2(v[k >> 2][k & 3]) - mw; q2 += d * d; }
      const float qw = xsum16_32(q2[0] + q2[1]);
      if (lane < 16) sred[wave * 16 + fr] = (f32x2){mw, qw};
      wait_lgkm0();
    };
    auto rest = [&]() {
    STAMP_AT(1 );
    float mean = 0.f, m2 = 0.f; f32x2 sw[8];
#pragma unroll
    for (int w = 0; w < 8; ++w) { sw[w] = sred[w * 16 + fr]; mean += sw[w][0]; m2 += sw[w][1]; }
    mean *= 0.125f;
#pragma unroll
    for (int w = 0; w < 8; ++w) { const float d = sw[w][0] - mean; m2 += 128.f * d * d; }
    const float rstd = ln_rstd(m2 * (1.f / 1024.f));
    mean = rbf(mean);
    unsigned nv[8];
#pragma unroll
    for (int k = 0; k < 8; ++k) {
      const f32x2 d = up2(v[k >> 2][k & 3]) - mean;
      const f32x2 n = up2(pk2(d[0], d[1])) * rstd;
      nv[k] = pk2(n[0], n[1]);
      asm volatile("" : "+v"(nv[k]));
    }
    SCHED0(); BAR(); SCHED0();
    STAMP_AT(2 );
    float mp[8];
#pragma unroll
    for (int i = 0; i < 8; ++i) mp[i] = __uint_as_float(__hip_atomic_load((unsigned*)(ws + WS_MOD) + hf * 512 + tid + i * 1024, __ATOMIC_RELAXED, __HIP_MEMORY_SCOPE_AGENT));
    *(LDS_PTR(unsigned))(smem + tid * 4) = pk2((mp[0] + mp[2]) + (mp[4] + mp[6]), 1.f + rbf((mp[1] + mp[3]) + (mp[5] + mp[7])));
    wait_lgkm0();
#pragma unroll
    for (int p = 0; p < 2; ++p) {
      const LDS_PTR(u32x4) tp = (const LDS_PTR(u32x4))(smem + ((wave * 2 + p) * 32 + cofs) * 4);
      const u32x4 t0 = tp[0], t1 = tp[1];
      unsigned o[4];
#pragma unroll
      for (int j = 0; j < 4; ++j) {
        const unsigned ta = j < 2 ? t0[2 * j] : t1[2 * j - 4], tb = j < 2 ? t0[2 * j + 1] : t1[2 * j - 3];
        const unsigned u = pk2(lo2f(nv[p * 4 + j]) * hi2f(ta), hi2f(nv[p * 4 + j]) * hi2f(tb));
        o[j] = pk2(lo2f(u) + lo2f(ta), hi2f(u) + lo2f(tb));
      }
      auto s0 = __builtin_amdgcn_permlane16_swap(o[0], o[2], false, false);
      auto s1 = __builtin_amdgcn_permlane16_swap(o[1], o[3], false, false);
      st16<true>(ws + WS_H, ((kb0 + p) * 128 + mb) * 1024 + lane * 16, (u32x4){s0[0], s1[0], s0[1], s1[1]});
    }
    STAMP_AT(3 );
    wait_vm<0>();
    STAMP_AT(4 );
    BAR();
    STAMP_AT(5 );
    };
    if (hf) {
      const int mrow = (u1 & 3) * 256 + (tid >> 3) * 4;
      const u32x2 c2 = *(const u32x2*)(cnd + mrow);
      u32x4 mw[4];
#pragma unroll
      for (int i = 0; i < 4; ++i) mw[i] = __builtin_nontemporal_load((const u32x4*)(wmod + (size_t)(mrow + i) * 6144 + (u1 >> 2) * 64 + (lane & 7) * 8));
      const unsigned mc[2] = {c2[0], c2[1]};
      *(LDS_PTR(f32x2))(smem + 8192 + (wave * 64 + (lane & 7) * 8 + (lane >> 4) * 2) * 4) = slab_sum<4>(mc, mw);
      wait_lgkm0();
      BAR();
      BAR();
      ldx();
      stats();
      BAR();
      rest();
    } else {
      ldx();
      SCHED0();
      const int sl = (3 - ((unsigned)(uintptr_t)wqkv >> 7) - (xc >> 1) * 4) & 7;
      const int i2 = tid - 384, r0 = i2 < 0 ? tid / 12 : i2 / 3, c0 = i2 < 0 ? tid - r0 * 12 : (i2 - r0 * 3) * 4 + sl;
      const bool on = i2 < 0 || (sl < 4 && i2 < 96);
      const unsigned pf = *(const unsigned*)((const char*)wqkv + (xc >> 1) * 512 + (on ? ((((xc & 1) ^ (i2 >= 0)) * 512 + (ii >> 1) * 32 + r0) * 48 + (c0 >> 2) * 16 + (c0 & 3)) * 128 : 0));
      SCHED0(); BAR(); SCHED0();
      stats();
      SCHED0(); BAR(); SCHED0();
      rest();
      asm volatile("" ::"v"(pf));
    }
  }
  {
    f32x4 bias[2];
    { const int kbase = tm3 * 128 + wave * 16 - 16;
#pragma unroll
      for (int t = 0; t < 4; ++t) {
        const int kk = (t >> 1) * 16 + 4 * g + (t & 1) * 2 + (fr & 1), df = 16 + fr - kk;
        bias[t >> 1][(t & 1) * 2] = bias[t >> 1][(t & 1) * 2 + 1] = (df >= 0 && df <= 14 && kbase + kk >= 0) ? 0.f : -__builtin_inff();
      }
      asm volatile("" : "+v"(bias[0]), "+v"(bias[1])); }
    u32x4 mc, mw[8];
    if (wave < 4) {
      const int wm = wave & 1, bA = wave;
      unsigned a0 = lds_base + K3_B + (lsw ^ (bA * 32)), a1 = lds_base + K3_B + (lsw ^ ((bA ^ 1) * 32)), aA = lds_base + 1024 + wm * 4096 + lane * 16, aH = lds_base + lane * 16;
      unsigned lq = lds_base + bA * 512 + lane * 8, lv = lds_base + 36864 + bA * 512 + (lane ^ ((g >> 1) * 8)) * 8;
      int n = 4;
      asm volatile(".irp r,0,8,16,24,32,40,48,56,64,72,80,88,96\n\tv_mov_b64 v[\\r:\\r+1], 0\n\tv_mov_b64 v[\\r+2:\\r+3], 0\n\tv_mov_b64 v[\\r+4:\\r+5], 0\n\tv_mov_b64 v[\\r+6:\\r+7], 0\n\t.endr\n\t"
                   "s_setprio 2\n\ts_waitcnt vmcnt(0)\n\ts_barrier\n\t"
                   "s_barrier\n\t"
                   RA3(1, 0) RA3(0, 0) RW3(0, 0, a0, 0) RW3(0, 1, a0, 0) RW3(1, 0, a1, 0) RW3(1, 1, a1, 0) RW3(2, 0, a1, 0) RW3(2, 1, a1, 0) RW3(3, 0, a0, 0) RW3(3, 1, a0, 0) RH3(0)
                   RW3(4, 0, a0, 0) RW3(4, 1, a0, 0) RW3(5, 0, a1, 0) LG(8)
                   ".p2align 5\n1:\n\t" STAGE3(43008, ) STAGE3(43008, ) STAGE3(-86016, "s_sub_u32 %[n], %[n], 1\n\t") "s_cbranch_scc0 1b\n\t"
                   H2_3(0, , , , ) H1_3(1, )
                   H2_3(1, "v_add_u32 %[a0], %[mo], %[lq]\n\t", "v_add_u32 %[aA], %[mo], %[lv]\n\t", "v_xor_b32 %[a1], 512, %[a0]\n\tv_xor_b32 %[aH], 512, %[aA]\n\t", "s_barrier\n\t")
                   M3(1, 5) M3(0, 5) M3(2, 0) CV3(0, 0, 0) M3(3, 0) WR3(0, 0, a0, 0) CV3(1, 0, 1) M3(3, 1) WR3(1, 1, a0, 0) CV3(0, 1, 2) M3(2, 1) WR3(0, 2, a1, 0) CV3(1, 1, 3)
                   M3(2, 2) WR3(1, 3, a1, 0) CV3(0, 2, 0) M3(3, 2) WR3(0, 0, a1, 18432) CV3(1, 2, 1) M3(3, 3) WR3(1, 1, a1, 18432) CV3(0, 3, 2) M3(2, 3) WR3(0, 2, a0, 18432) CV3(1, 3, 3)
                   M3(4, 3) WR3(1, 3, a0, 18432) CV3(0, 4, 0) M3(4, 4) WR3(0, 0, aA, 0) CV3(1, 4, 1) M3(2, 4) WR3(1, 1, aA, 0) CV3(0, 5, 2) M3(3, 4) WR3(0, 2, aH, 0) CV3(1, 5, 3)
                   M3(3, 5) WR3(1, 3, aH, 0) CV3(2, 0, 0) M3(2, 5) WR3(2, 0, a0, 0)
                   CW3(3, 0, 1, a0, 0) CW3(3, 1, 2, a1, 0) CW3(2, 1, 3, a1, 0) CW3(2, 2, 0, a1, 18432) CW3(3, 2, 1, a1, 18432) CW3(3, 3, 2, a0, 18432) CW3(2, 3, 3, a0, 18432)
                   CV3(4, 3, 0) WR3(0, 0, lq, 18432) CV3(4, 4, 1) WR3(0, 1, lv, 0) CW3(2, 4, 2, aA, 0) CW3(3, 4, 3, aA, 0) CW3(3, 5, 0, aH, 0) CW3(2, 5, 1, aH, 0)
                   "s_setprio 0\n\t" LG(0)
                   : [a0] "+v"(a0), [a1] "+v"(a1), [aA] "+v"(aA), [aH] "+v"(aH), [n] "+s"(n)
                   : [lq] "v"(lq), [lv] "v"(lv), [mo] "s"((wm * 4 + 1) * 2048)
                   : "memory", "scc", V10(), V10(1), V10(2), V10(3), V10(4), V10(5), V10(6), V10(7), V10(8), V10(9), V10(10), V10(11), V10(12), V10(13), V8(14));
    } else {
      wait_vm<0>();
      BAR(); BAR();
      {
        const unsigned* pc = cnt + ((C2 + ((lane & 1) && tm3 ? tm3 - 1 : tm3)) * 8 + (tn3 & 7)) * 32;
        unsigned seen = 0x80008u;
        BAR(); BAR(); BAR();
        if (wave == 4) asm volatile("global_load_dword %0, %1, off sc1" : "=v"(seen) : "v"(pc) : "memory");
        BAR(); BAR();
        asm volatile("s_waitcnt vmcnt(0)" : "+v"(seen)::"memory");
        if (__builtin_amdgcn_ballot_w64(seen < 0x80008u) != 0) poll<0>(pc, 0x80008u);
      }
#pragma unroll 1
      for (int t = 5; t < SLB; ++t) BAR();
      const int mrow = ((b & 3) * 4 + (wave & 3)) * 64 + (lane >> 3) * 8;
      mc = *(const u32x4*)(cnd + mrow);
#pragma unroll
      for (int i = 0; i < 8; ++i) mw[i] = __builtin_nontemporal_load((const u32x4*)(wmod + (size_t)(mrow + i) * 6144 + 2048 + (b >> 2) * 64 + (lane & 7) * 8));
      SCHED0();
#pragma unroll
      for (int t = SLB; t < 16; ++t) BAR();
    }
    lane = lane_id(); fr = lane & 15; g = lane >> 4; p4 = fr & 3; row = 4 * g + (fr >> 2); lsw = row * 128 + (((row >> 1) & 3) * 32) + p4 * 8;
    wait_lgkm0();
    BAR();
    STAMP_AT(9 );
    const LDS_PTR(char) eb = smem + wave * 2048;
    auto ld2 = [&](int o) { return cat8(*(const LDS_PTR(bf16x4))(eb + o), *(const LDS_PTR(bf16x4))(eb + o + 512)); };
    const int vo = 36864 + ((p4 * 16 + row) ^ ((p4 >> 1) * 8)) * 8;
    bf16x8 qf[2], k0[2], k1[2], v0[2], v1[2];
#pragma unroll
    for (int hd = 0; hd < 2; ++hd) { qf[hd] = ld2(2048 + hd * 1024 + lane * 8); k0[hd] = ld2(18432 + hd * 1024 + lane * 8); k1[hd] = ld2(18432 + 2048 + hd * 1024 + lane * 8); }
#pragma unroll
    for (int hd = 0; hd < 2; ++hd) {
      v0[hd] = cat8(ld_tr4(eb + vo + hd * 1024), ld_tr4(eb + vo + hd * 1024 + 2048));
      v1[hd] = cat8(ld_tr4(eb + vo + hd * 1024 + 512), ld_tr4(eb + vo + hd * 1024 + 512 + 2048));
    }
    SCHED0();
    const f32x4 z = {0.f, 0.f, 0.f, 0.f};
    const bool odd = fr & 1;
    const unsigned sel0 = odd ? 0x01000c0cu : 0x0c0c0100u, sel1 = odd ? 0x03020c0cu : 0x0c0c0302u;
    float inf = __builtin_inff(); asm("" : "+v"(inf));
    auto mx2 = [&](float a, float c) { return __builtin_amdgcn_fmed3f(a, c, inf); };
    auto both = [](float a, float c, auto op, auto fin) {
      auto u = __builtin_amdgcn_permlane16_swap(__float_as_uint(a), __float_as_uint(c), false, false);
      float r = op(__uint_as_float(u[0]), __uint_as_float(u[1]));
      auto w = __builtin_amdgcn_permlane32_swap(__float_as_uint(r), __float_as_uint(r), false, false);
      r = fin(op(__uint_as_float(w[0]), __uint_as_float(w[1])));
      auto y = __builtin_amdgcn_permlane16_swap(__float_as_uint(r), __float_as_uint(r), false, false);
      return (f32x2){__uint_as_float(y[0]), __uint_as_float(y[1])};
    };
    f32x2 l[2][2];
#pragma unroll
    for (int hd = 0; hd < 2; ++hd) {
      const f32x4 s0 = __builtin_amdgcn_mfma_f32_16x16x32_bf16(k0[hd], qf[hd], bias[0], 0, 0, 0), s1 = __builtin_amdgcn_mfma_f32_16x16x32_bf16(k1[hd], qf[hd], bias[1], 0, 0, 0);
#pragma unroll
      for (int t = 0; t < 2; ++t) {
        const f32x4 sv = t ? s1 : s0;
        unsigned u = pk2(odd ? sv[1] : sv[0], odd ? sv[3] : sv[2]);
        u = pk2(lo2f(u) * 0.17677669529663687f, hi2f(u) * 0.17677669529663687f);
        l[hd][t] = (f32x2){lo2f(u), hi2f(u)};
      }
    }
    const f32x2 mx = both(mx2(mx2(l[0][0][0], l[0][0][1]), mx2(l[0][1][0], l[0][1][1])), mx2(mx2(l[1][0][0], l[1][0][1]), mx2(l[1][1][0], l[1][1][1])), mx2,
                          [](float r) { return r * -1.4426950408889634f; });
    float sm[2];
#pragma unroll
    for (int hd = 0; hd < 2; ++hd) {
#pragma unroll
      for (int t = 0; t < 2; ++t) { const f32x2 e = l[hd][t] * 1.4426950408889634f + mx[hd]; l[hd][t] = (f32x2){__builtin_amdgcn_exp2f(e[0]), __builtin_amdgcn_exp2f(e[1])}; }
      const f32x2 a = l[hd][0] + l[hd][1]; sm[hd] = a[0] + a[1];
    }
    const f32x2 inv = both(sm[0], sm[1], [](float a, float c) { return a + c; }, [](float r) { return __builtin_amdgcn_rcpf(r); });
#pragma unroll
    for (int hd = 0; hd < 2; ++hd) {
      u32x4 pp;
#pragma unroll
      for (int t = 0; t < 2; ++t) {
        const f32x2 p = l[hd][t] * inv[hd]; const unsigned u = pk2(p[0], p[1]);
        pp[2 * t] = __builtin_amdgcn_perm(u, u, sel0); pp[2 * t + 1] = __builtin_amdgcn_perm(u, u, sel1);
      }
      const bf16x8 pf = __builtin_bit_cast(bf16x8, pp);
      const f32x4 oa = __builtin_amdgcn_mfma_f32_16x16x32_bf16(v0[hd], pf, z, 0, 0, 0), ob = __builtin_amdgcn_mfma_f32_16x16x32_bf16(v1[hd], pf, z, 0, 0, 0);
      u32x4 ov; ov[0] = pk2(oa[0], oa[1]); ov[1] = pk2(oa[2], oa[3]); ov[2] = pk2(ob[0], ob[1]); ov[3] = pk2(ob[2], ob[3]);
      st16<true>(ws + WS_O, ((tn3 * 2 + hd) * 128 + tm3 * 8 + wave) * 1024 + lane * 16, ov);
    }
    STAMP_AT(10 );
    wait_vm<0>();
    STAMP_AT(11 );
    BAR();
    STAMP_AT(12 );
    if (wave >= 4) {
      const unsigned m4[4] = {mc[0], mc[1], mc[2], mc[3]};
      const f32x2 ps = slab_sum<8>(m4, mw);
      if (!(lane & 8))
        __hip_atomic_store((unsigned long long*)(ws + WS_MOD2) + (((b & 3) * 4 + (wave & 3)) * 4096 + (b >> 2) * 64 + (lane & 7) * 8 + (lane >> 4) * 2) / 2,
                           ((unsigned long long)__float_as_uint(ps[1]) << 32) | __float_as_uint(ps[0]), __ATOMIC_RELAXED, __HIP_MEMORY_SCOPE_AGENT);
      wait_vm<0>();
      if (lane == 0 && __hip_atomic_fetch_add((LDS_PTR(unsigned))(smem + LDS_ARR + 8), 1u, __ATOMIC_RELAXED, __HIP_MEMORY_SCOPE_WORKGROUP) == 3)
        __hip_atomic_fetch_add(cnt + (CM * 8 + (b >> 2)) * 32, 1u, __ATOMIC_RELAXED, __HIP_MEMORY_SCOPE_AGENT);
    }
  }
  { const int wm = wave & 3, ks = wave >> 2, mb = tm4 * 8 + wm * 2 + ks;
    constexpr int R = 6;
    u32x4 rx[2];
#pragma unroll
    for (int p = 0; p < 2; ++p) rx[p] = *(const u32x4*)(x + (size_t)(mb * 16 + fr) * D_ + tn4 * 64 + p * 32 + (g & 1) * 16 + (g >> 1) * 8);
    const unsigned va0 = (ks * 128 + mb) * 1024 + lane * 16, va1 = va0 ^ 1024;
    bf16x8 fa[R][2], fw[2][4];
    auto issueA = [&](int t) {
      const char* pa = ws + WS_O + t * 262144;
      fa[t % R][0] = __builtin_bit_cast(bf16x8, ld16<true>(pa, va0));
      fa[t % R][1] = __builtin_bit_cast(bf16x8, ld16<true>(pa, va1));
    };
    unsigned aw[4];
    auto rdw = [&](int t, int j) {
      const int o = (t & 3) * 8192;
      fw[t & 1][j] = cat8(ld_tr4(smem + aw[j] + o), ld_tr4(smem + aw[j] + o + 2048));
    };
    auto stepw = [&](int t) {
      const int gi = t >> 2, base = gi < 3 ? 57344 + gi * 32768 : 0;
#pragma unroll
      for (int j = 0; j < 4; ++j) { aw[j] = base + ks * 4096 + (lsw ^ (j * 32)); asm volatile("" : "+v"(aw[j])); }
    };
    auto up2 = [](unsigned u) { return (f32x2){lo2f(u), hi2f(u)}; };
    f32x2 f[8];
    unsigned slot = (mb * 256 + fr + kh6 * 32768 + 4 * g * 16) * 8, sown = (mb * 256 + fr + (g & 1) * 32768 + tn4 * 16) * 8;
    f32x4 acc[2][4];
#pragma unroll
    for (int i = 0; i < 2; ++i)
#pragma unroll
      for (int j = 0; j < 4; ++j) acc[i][j] = (f32x4){0.f, 0.f, 0.f, 0.f};
    stepw(0);
    wait_lgkm0();
    SCHED0(); BAR(); SCHED0();
    STAMP_AT(13 );
#pragma unroll
    for (int t = 0; t < R; ++t) issueA(t);
#pragma unroll
    for (int j = 0; j < 4; ++j) rdw(0, j);
    SCHED0();
#pragma unroll
    for (int t = 0; t < 16; ++t) {
      if (t < 15 && ((t + 1) & 3) == 0) stepw(t + 1);
#pragma unroll
      for (int j = 0; j < 4; ++j) {
        if (t < 15) rdw(t + 1, j);
        SCHED0();
#pragma unroll
        for (int i = 0; i < 2; ++i) acc[i][j] = __builtin_amdgcn_mfma_f32_16x16x32_bf16(fw[t & 1][j], fa[t % R][i], acc[i][j], 0, 0, 0);
        SCHED0();
      }
      if (t + R < 16) issueA(t + R);
      if (t == 2) {
#pragma unroll
        for (int p = 0; p < 2; ++p) {
          auto s0 = __builtin_amdgcn_permlane16_swap(rx[p][0], rx[p][2], false, false);
          auto s1 = __builtin_amdgcn_permlane16_swap(rx[p][1], rx[p][3], false, false);
          f[4 * p] = up2(s0[0]); f[4 * p + 1] = up2(s1[0]); f[4 * p + 2] = up2(s0[1]); f[4 * p + 3] = up2(s1[1]);
        }
#pragma unroll
        for (int j = 0; j < 8; ++j) asm volatile("" : "+v"(f[j]));
        asm volatile("" : "+v"(slot), "+v"(sown));
      }
      if (t == 5) BAR();
      SCHED0();
    }
    STAMP_AT(14 );
    LDS_PTR(f32x4) red = (LDS_PTR(f32x4))(smem + K4_RED);
#pragma unroll
    for (int j = 0; j < 4; ++j) red[((wave ^ 4) * 4 + j) * 64 + lane] = acc[1][j];
    wait_lgkm0();
    BAR();
    STAMP_AT(15 );
    const LDS_PTR(float) vec = (const LDS_PTR(float))(smem + LDS_VEC);
    unsigned o[8];
    f32x4 pr[4], ga[4];
#pragma unroll
    for (int j = 0; j < 4; ++j) { pr[j] = red[(wave * 4 + j) * 64 + lane]; ga[j] = *(const LDS_PTR(f32x4))(vec + j * 16 + 4 * g); }
    SCHED0();
#pragma unroll
    for (int j = 0; j < 4; ++j) {
      const f32x4 y = acc[0][j] + pr[j];
      const f32x2 lo = y.xy * ga[j].xy + f[2 * j], hi = y.zw * ga[j].zw + f[2 * j + 1];
      o[2 * j] = pk2(lo[0], lo[1]); o[2 * j + 1] = pk2(hi[0], hi[1]);
      f[2 * j] = up2(o[2 * j]); f[2 * j + 1] = up2(o[2 * j + 1]);
    }
    const f32x2 s2 = ((f[0] + f[1]) + (f[2] + f[3])) + ((f[4] + f[5]) + (f[6] + f[7]));
    float mt = xsum16_32(s2[0] + s2[1]) * (1.f / 64.f); asm("" : "+v"(mt));
    f32x2 q2 = {0.f, 0.f}, q3 = {0.f, 0.f};
#pragma unroll
    for (int j = 0; j < 8; j += 2) { f[j] -= mt; f[j + 1] -= mt; q2 += f[j] * f[j]; q3 += f[j + 1] * f[j + 1]; }
    q2 += q3;
    const float qq = xsum16_32(q2[0] + q2[1]);
    if (lane < 32) {
      const unsigned qb = qq == qq ? __float_as_uint(qq) : 0x7fc00000u;
      __hip_atomic_store((unsigned long long*)(ws + WS_XCH + sown), ((unsigned long long)qb << 32) | __float_as_uint(mt), __ATOMIC_RELAXED, __HIP_MEMORY_SCOPE_AGENT);
    }
    *(LDS_PTR(u32x4))(smem + (mb & 7) * 2048 + lane * 16) = (u32x4){o[0], o[1], o[2], o[3]};
    *(LDS_PTR(u32x4))(smem + (mb & 7) * 2048 + 1024 + lane * 16) = (u32x4){o[4], o[5], o[6], o[7]};
    STAMP_AT(16 );
    f32x4 ss[4], gg[4];
#pragma unroll
    for (int j = 0; j < 4; ++j) {
      ss[j] = *(const LDS_PTR(f32x4))(vec + 64 + j * 16 + 4 * g); gg[j] = *(const LDS_PTR(f32x4))(vec + 128 + j * 16 + 4 * g);
      f[2 * j] *= gg[j].xy; f[2 * j + 1] *= gg[j].zw;
      asm volatile("" : "+v"(ss[j]), "+v"(gg[j]), "+v"(f[2 * j]), "+v"(f[2 * j + 1]));
    }
    unsigned long long pv[4];
    __builtin_amdgcn_s_sleep(XS0);
    for (unsigned spins = 0;; ++spins) {
      bool ok = true;
#pragma unroll
      for (int i = 0; i < 4; ++i) { pv[i] = __hip_atomic_load((unsigned long long*)(ws + WS_XCH + slot) + i * 16, __ATOMIC_RELAXED, __HIP_MEMORY_SCOPE_AGENT); ok &= (unsigned)(pv[i] >> 32) != 0xffffffffu; }
      if (__builtin_amdgcn_ballot_w64(!ok) == 0 || spins > SPIN_CAP) break;
      __builtin_amdgcn_s_sleep(XS1);
    }
    STAMP_AT(17 );
    float ms = 0.f, m2 = 0.f;
#pragma unroll
    for (int i = 0; i < 4; ++i) { ms += __uint_as_float((unsigned)pv[i]); m2 += __uint_as_float((unsigned)(pv[i] >> 32)); }
    const float mean = xsum16_32(ms) * (1.f / 16.f);
#pragma unroll
    for (int i = 0; i < 4; ++i) { const float d = __uint_as_float((unsigned)pv[i]) - mean; m2 += 64.f * d * d; }
    const float rstd = ln_rstd(xsum16_32(m2) * (1.f / 1024.f));
    const float sh = (mt - rbf(mean)) * rstd;
#pragma unroll
    for (int p = 0; p < 2; ++p) {
      u32x4 v;
#pragma unroll
      for (int j = 0; j < 4; ++j) {
        const int jb = 2 * p + (j >> 1);
        const f32x2 h = f[4 * p + j] * rstd + (((j & 1) ? gg[jb].zw : gg[jb].xy) * sh + ((j & 1) ? ss[jb].zw : ss[jb].xy));
        v[j] = pk2(h[0], h[1]);
      }
      st16<true>(ws + WS_H2, ((tn4 * 2 + p) * 128 + mb) * 1024 + lane * 16, v);
    }
    STAMP_AT(18 );
    wait_vm<0>();
    wait_lgkm0();
    BAR();
    STAMP_AT(19 );

  }
  const int grp = wave >> 2;
  { const int wm = wave & 3;
    const LDS_PTR(char) sb = smem + 3 * K5_STG;
    const LDS_PTR(char) aP = sb + K5_G + row * 224 + grp * 96 + p4 * 8;
    const LDS_PTR(char) aM = sb + (p4 < 2 ? K5_G : K5_U) + row * 224 + (12 + grp) * 16 + (p4 & 1) * 8;
    const LDS_PTR(char) aA = sb + wm * 4096 + lane * 16;
    f32x32 c0, c1, c2; f32x16 c3;
    asm volatile(".irp r,0,8,16,24,32,40,48,56,64,72,80,88,96,104\n\tv_mov_b64 v[\\r:\\r+1], 0\n\tv_mov_b64 v[\\r+2:\\r+3], 0\n\tv_mov_b64 v[\\r+4:\\r+5], 0\n\tv_mov_b64 v[\\r+6:\\r+7], 0\n\t.endr"
                 : "={v[0:31]}"(c0), "={v[32:63]}"(c1), "={v[64:95]}"(c2), "={v[96:111]}"(c3)::"memory");
    int wrap = -(K5_NS - 1) * K5_STG, dl = K5_STG, n = 7;
    unsigned mask = wrap_mask(4, K5_NS);
    asm volatile("" : "+s"(wrap), "+v"(aP), "+v"(aM), "+v"(aA));
    if (grp) __builtin_amdgcn_s_setprio(1); else __builtin_amdgcn_s_setprio(2);
    BAR();
    STAMP_AT(20 );
    BAR();
    STAMP_G(21, 26);
    asm volatile(RA5(0) RA5(1) RP5(0, 0) RP5(0, 1) RP5(1, 0) RP5(1, 1) RP5(2, 0) RP5(2, 1) RP5(3, 0) RP5(3, 1) RP5(4, 0) RP5(4, 1) RP5(5, 0) RP5(5, 1) RW5(6, 0, aM, 0) LG(9)
                 ".p2align 5\n1:\n\t" TRIP5("") TRIP5("") TRIP5("") TRIP5("s_sub_u32 %[n], %[n], 1\n\t") "s_cbranch_scc0 1b\n\t" LG(0) "s_nop 7"
                 : ACC5, [aP] "+v"(aP), [aM] "+v"(aM), [aA] "+v"(aA), [dl] "+s"(dl), [mask] "+s"(mask), [n] "+s"(n) : [wrap] "s"(wrap) : CLOB5);
    f32x4 acc[4][7];
#pragma unroll
    for (int e = 0; e < 112; ++e) acc[e / 28][(e % 28) / 4][e & 3] = e < 32 ? c0[e & 31] : e < 64 ? c1[e & 31] : e < 96 ? c2[e & 31] : c3[e & 15];
    STAMP_G(22, 27);
    auto swi4 = [](const f32x4& gv, const f32x4& uv, unsigned& o0, unsigned& o1) {
      constexpr float c2 = 2.0813689810056077f;
      f32x2 pa = gv.xy * uv.xy, pb = gv.zw * uv.zw;
      const f32x2 ta = (f32x2){__builtin_amdgcn_exp2f(gv[0]), __builtin_amdgcn_exp2f(gv[1])} * c2 + c2, tb = (f32x2){__builtin_amdgcn_exp2f(gv[2]), __builtin_amdgcn_exp2f(gv[3])} * c2 + c2;
      pa *= (f32x2){__builtin_amdgcn_rcpf(ta[0]), __builtin_amdgcn_rcpf(ta[1])}; pb *= (f32x2){__builtin_amdgcn_rcpf(tb[0]), __builtin_amdgcn_rcpf(tb[1])};
      o0 = pk2(pa[0], pa[1]); o1 = pk2(pb[0], pb[1]);
    };
    const int lz = lane_id();
    const int gz = lz >> 4, fz = lz & 15;
    const int qb = (tn5 >> 4) * 224, ql = qb + 64 + (tn5 & 15) * 10;
    auto hof = [&](int e, int i) { return (unsigned)((e >> 2) * 131072 + (e & 3) * 256 + fz * 16 + i * 1024); };
    const unsigned ho0 = hof((grp ? ql + 2 : qb + (tn5 & 15) * 4) + 2 * (gz & 1) + (gz >> 1), 0), ho1 = hof(ql + 6 * grp + (gz >> 1), gz & 1), hom = hof(ql + 8 + grp, gz);
    char* hb = ws + WS_HH + (tm5 * 16 + wm * 4) * 1024;
    const int c5 = C5 + tm5 * 4 + (tn5 >> 4) * 2;
    auto tr16 = [](const unsigned (&o)[4]) {
      auto s0 = __builtin_amdgcn_permlane16_swap(o[0], o[2], false, false);
      auto s1 = __builtin_amdgcn_permlane16_swap(o[1], o[3], false, false);
      u32x4 v = {s0[0], s1[0], s0[1], s1[1]}; asm volatile("" : "+v"(v)); return v;
    };
    auto pair = [&](int p, int i) {
      unsigned o[4];
      swi4(acc[i][2 * p], acc[i][2 * p + 1], o[0], o[1]);
      swi4(acc[i + 1][2 * p], acc[i + 1][2 * p + 1], o[2], o[3]);
      return tr16(o);
    };
    u32x4 v[3];
#pragma unroll
    for (int i = 0; i < 4; ++i) {
      unsigned o[4];
      swi4(acc[i][0], acc[i][1], o[0], o[1]);
      swi4(acc[i][2], acc[i][3], o[2], o[3]);
      st16<true>(hb, ho0 + i * 1024, tr16(o));
      SCHED0();
    }
    STAMP_G(23, 28);
    v[0] = pair(2, 0);
    if (!grp) {
      wait_vm<0>();
      unsigned arr = 0;
      if (lz == 0) arr = __hip_atomic_fetch_add((LDS_PTR(unsigned))(smem + LDS_ARR), 1u, __ATOMIC_RELAXED, __HIP_MEMORY_SCOPE_WORKGROUP);
      if (__builtin_amdgcn_readfirstlane(arr) == 3) signal(cnt, c5, lz);
    }
    STAMP_G(24, 29);
    v[1] = pair(2, 2);
    { unsigned m[4];
#pragma unroll
      for (int i = 0; i < 4; i += 2) {
        f32x4 gv, uv;
#pragma unroll
        for (int e = 0; e < 4; ++e) {
          auto w = __builtin_amdgcn_permlane32_swap(__float_as_uint(acc[i + (e >> 1)][6][e & 1]), __float_as_uint(acc[i + (e >> 1)][6][2 + (e & 1)]), false, false);
          gv[e] = __uint_as_float(w[0]); uv[e] = __uint_as_float(w[1]);
        }
        swi4(gv, uv, m[i], m[i + 1]);
      }
      auto x0 = __builtin_amdgcn_permlane32_swap(m[0], m[2], false, false);
      auto x1 = __builtin_amdgcn_permlane32_swap(m[1], m[3], false, false);
      const unsigned o[4] = {x0[0], x0[1], x1[0], x1[1]};
      v[2] = tr16(o);
      st16<true>(hb, ho1, v[0]); st16<true>(hb, ho1 + 2048, v[1]); st16<true>(hb, hom, v[2]);
    }
    STAMP_G(25, 30);
    if (grp) {
#pragma unroll
      for (int t = 0; t < NDR; ++t) BAR();
      wait_vm<0>();
      BAR();
      if (wave == 4) signal(cnt, c5 + 1, lz);
      if (wave > 4) return;
#pragma unroll 1
      for (int t = NDR + 1; t < 9; ++t) BAR();
      unsigned seen = 16;
      const unsigned* pc = (lz & 1) ? (const unsigned*)(ws + WS_FLG) + (b ^ 4) * 32 : cnt + ((C5 + (tm6 >> 1) * 4 + kh6 * 2 + 1) * 8 + tn6) * 32;
      asm volatile("global_load_dword %0, %1, off sc1" : "=v"(seen) : "v"(pc) : "memory");
      BAR(); BAR();
      asm volatile("s_waitcnt vmcnt(0)" : "+v"(seen)::"memory");
      if (__builtin_amdgcn_ballot_w64(seen < 16) != 0) poll<0>(pc, 16);
      return;
    }
  }
  { const int lane = lane_id(), fr = lane & 15, g = lane >> 4, p4 = fr & 3, row = 4 * g + (fr >> 2);
    const int wm = wave & 1, wn = wave >> 1;
    const int lw0 = row * 128 + (((row >> 1) & 3) * 32) + p4 * 8;
    const int mb0 = tm6 * 8 + wm * 4, t64 = tn6 * 2 + kh6;
    f32x4 acc[4][4];
#pragma unroll
    for (int i = 0; i < 4; ++i)
#pragma unroll
      for (int j = 0; j < 4; ++j) { acc[i][j] = (f32x4){0.f, 0.f, 0.f, 0.f}; asm volatile("" : "+v"(acc[i][j])); }
    struct Fr { bf16x8 a[4]; bf16x4 w[4][2]; };
    const LDS_PTR(char) aw0 = smem + (lw0 ^ (wn * 64)); const LDS_PTR(char) aw1 = smem + (lw0 ^ (wn * 64 + 32));
    const LDS_PTR(char) aa = smem + K6_A + wm * 4096 + lane * 16;
    auto rd1 = [&](Fr& f, int r) {
      if (r < 2) f.w[0][r] = ld_tr4(aw0 + r * 2048);
      else if (r < 4) f.a[r - 2] = *(const LDS_PTR(bf16x8))(aa + (r - 2) * 1024);
      else if (r < 10) { const int j = (r - 2) >> 1; f.w[j][r & 1] = ld_tr4(((j & 1) ? aw1 : aw0) + (j >> 1) * 4096 + (r & 1) * 2048); }
      else f.a[r - 8] = *(const LDS_PTR(bf16x8))(aa + (r - 8) * 1024);
    };
    int dl, da, wrap = -(K6_NW - 1) * 8192, wrapa = -(K6_NA - 1) * 8192;
    unsigned mask = wrap_mask(1, K6_NW), maska = wrap_mask(1, K6_NA);
    Fr fA, fB;
    asm volatile("" : "+s"(wrap), "+s"(wrapa), "+v"(aw0), "+v"(aw1), "+v"(aa));
    wait_vm<0>();
    BAR();
    STAMP_AT(32 );
#pragma unroll
    for (int r = 0; r < 12; ++r) { SCHED0(); rd1(fA, r); }
    SCHED0();
    aw0 += 8192; aw1 += 8192; aa += 8192;
    wait_lgkm<6>();
    BAR();
    auto trip = [&](const Fr& cur, Fr& nxt) {
      SCHED0();
#pragma unroll
      for (int m = 0; m < 16; ++m) {
        const int j = (m & 7) >> 1, i = (m >> 3) * 2 + ((m ^ j) & 1);
        if (m == 2) mfma_sel<8192>(acc[i][j], cat8(cur.w[j][0], cur.w[j][1]), cur.a[i], mask, dl, wrap);
        else if (m == 6) mfma_sel<8192>(acc[i][j], cat8(cur.w[j][0], cur.w[j][1]), cur.a[i], maska, da, wrapa);
        else mfma_acc(acc[i][j], cat8(cur.w[j][0], cur.w[j][1]), cur.a[i]);
        SCHED0();
        if (m < 12) { rd1(nxt, m); SCHED0(); }
        if (m == 3) wait_lgkm<4>();
        if (m == 12) { aw0 += dl; aw1 += dl; asm volatile("" : "+v"(aw0), "+v"(aw1)); }
        if (m == 13) { aa += da; asm volatile("" : "+v"(aa)); }
        if (m == 14) wait_lgkm<6>();
        if (m == 15) BAR();
        SCHED0();
      }
    };
#pragma unroll 1
    for (int h = 0; h < 2; ++h) {
#pragma unroll 1
      for (int t = 0; t < 7; ++t) { trip(fA, fB); trip(fB, fA); trip(fA, fB); trip(fB, fA); }
      mask = wrap_mask(29, K6_NW); maska = wrap_mask(29, K6_NA);
    }
    mfma_done(acc);
    STAMP_AT(34 );
    const int slot = wm * 2 + wn, pb = (b ^ 4) * 4 + slot;
    { auto pk = [](float lo, float hi) { const unsigned u = pk2(lo, hi); return u < 0xfffffffeu ? u : 0xfffffffeu; };
#pragma unroll
      for (int i = 0; i < 4; ++i)
        st16<true>(ws + WS_PART, (b * 4 + slot) * 4096 + i * 1024 + lane * 16,
                   (u32x4){pk(acc[i][0][0], acc[i][0][1]), pk(acc[i][0][2], acc[i][0][3]), pk(acc[i][1][0], acc[i][1][1]), pk(acc[i][1][2], acc[i][1][3])});
      STAMP_AT(35 );
    }
    { const LDS_PTR(float) vec = (const LDS_PTR(float))(smem + LDS_VEC) + 192 + wn * 32 + 4 * g;
      const f32x4 gv0 = *(const LDS_PTR(f32x4))vec, gv1 = *(const LDS_PTR(f32x4))(vec + 16);
      u32x4 rv[4];
#pragma unroll
      for (int i = 0; i < 4; ++i) { rv[i] = *(const LDS_PTR(u32x4))(smem + K6_X + (wm * 4 + i) * 2048 + wn * 1024 + lane * 16); asm volatile("" : "+v"(rv[i])); }
      u32x4 z[4];
      __builtin_amdgcn_s_sleep(PD6);
#pragma unroll 1
      for (unsigned spins = 0; spins < SPIN_CAP; ++spins) {
        asm volatile("" ::: "memory");
        unsigned m = 0;
#pragma unroll
        for (int i = 0; i < 4; ++i) z[i] = ld16<true>(ws + WS_PART, pb * 4096 + i * 1024 + lane * 16);
#pragma unroll
        for (int i = 0; i < 16; ++i) m = m > z[i >> 2][i & 3] ? m : z[i >> 2][i & 3];
        if (__builtin_amdgcn_ballot_w64(m == ~0u) == 0) break;
        __builtin_amdgcn_s_sleep(PS6);
      }
      STAMP_AT(36 );
#pragma unroll
      for (int i = 0; i < 4; ++i) {
        const f32x4 v0 = (acc[i][2] + (f32x4){lo2f(z[i][0]), hi2f(z[i][0]), lo2f(z[i][1]), hi2f(z[i][1])}) * gv0;
        const f32x4 v1 = (acc[i][3] + (f32x4){lo2f(z[i][2]), hi2f(z[i][2]), lo2f(z[i][3]), hi2f(z[i][3])}) * gv1;
        const unsigned o0 = pk2(lo2f(rv[i][0]) + v0[0], hi2f(rv[i][0]) + v0[1]), o1 = pk2(lo2f(rv[i][1]) + v0[2], hi2f(rv[i][1]) + v0[3]);
        const unsigned o2 = pk2(lo2f(rv[i][2]) + v1[0], hi2f(rv[i][2]) + v1[1]), o3 = pk2(lo2f(rv[i][3]) + v1[2], hi2f(rv[i][3]) + v1[3]);
        auto s0 = __builtin_amdgcn_permlane16_swap(o0, o2, false, false);
        auto s1 = __builtin_amdgcn_permlane16_swap(o1, o3, false, false);
        st16<false>(out, (((mb0 + i) * 16 + fr) * D_ + t64 * 64 + wn * 32 + (g & 1) * 16 + (g >> 1) * 8) * 2, (u32x4){s0[0], s1[0], s0[1], s1[1]});
      }
    }
    STAMP_AT(37 );
  }
}

extern "C" void kernel_launch(void* const* d_in, const int* in_sizes, int n_in, void* d_out, int out_size, void* d_ws,
                              size_t ws_size, hipStream_t stream) {
  const u16* x = (const u16*)d_in[0];
  const u16* wqkv = (const u16*)d_in[1];
  const u16* wo = (const u16*)d_in[2];
  const u16* w1 = (const u16*)d_in[4];
  const u16* w3 = (const u16*)d_in[5];
  const u16* w2 = (const u16*)d_in[6];
  const u16* c = (const u16*)d_in[7];
  const u16* wmod = (const u16*)d_in[8];
  (void)hipFuncSetAttribute((const void*)k2_block, hipFuncAttributeMaxDynamicSharedMemorySize, MEGA_LDS);
  char* ws = (char*)d_ws;
  k2_block<<<256, 768, MEGA_LDS, stream>>>(x, wqkv, wo, w1, w3, w2, c, wmod, (u16*)d_out, ws);
}
```

```cpp
#include <hip/hip_runtime.h>
#include <stdint.h>
#include <type_traits>
#pragma clang diagnostic ignored "-Winline-asm"

typedef unsigned short u16;
typedef short bf16x8 __attribute__((ext_vector_type(8)));
typedef short bf16x4 __attribute__((ext_vector_type(4)));
typedef float f32x4 __attribute__((ext_vector_type(4)));
typedef float f32x2 __attribute__((ext_vector_type(2)));
typedef unsigned int u32x4 __attribute__((ext_vector_type(4)));
typedef unsigned int u32x2 __attribute__((ext_vector_type(2)));
#define LDS_PTR(T) __attribute__((address_space(3))) T*
#define BAR() do { asm volatile("" ::: "memory"); __builtin_amdgcn_s_barrier(); asm volatile("" ::: "memory"); } while (0)
#define SCHED0() __builtin_amdgcn_sched_barrier(0)
template <int N> using IC = std::integral_constant<int, N>;
typedef std::integral_constant<bool, true> T_;
typedef std::integral_constant<bool, false> F__;

#define PROBE 1
constexpr int S_ = 2048, D_ = 1024, F_ = 3584;
#define STAMP_AT(i) do { if ((i) == 0) pr0 = __builtin_amdgcn_s_memrealtime(); else if ((i) == PROBE) prk = __builtin_amdgcn_s_memrealtime(); \
  if ((i) == 37 && wave == 0) { __builtin_amdgcn_s_waitcnt(0); if (PROBE == 37) prk = __builtin_amdgcn_s_memrealtime(); while ((long long)__builtin_amdgcn_s_memrealtime() < prk + 10000) __builtin_amdgcn_s_sleep(1); } } while (0)
#define STAMP_G(ia, ib) STAMP_AT(ia)
#define STAMP_L(i)

__device__ __forceinline__ float bf2f(u16 v) { return __uint_as_float(((unsigned)v) << 16); }
__device__ __forceinline__ unsigned pk2(float lo, float hi) {
  typedef __bf16 bf2 __attribute__((ext_vector_type(2)));
  f32x2 v = {lo, hi}; bf2 r = __builtin_convertvector(v, bf2); return __builtin_bit_cast(unsigned, r);
}
__device__ __forceinline__ float lo2f(unsigned u) { return __uint_as_float(u << 16); }
__device__ __forceinline__ float hi2f(unsigned u) { return __uint_as_float(u & 0xffff0000u); }
__device__ __forceinline__ float rbf(float v) { return lo2f(pk2(v, 0.f)); }
#define glds16(sbase, voff, lds_off, imm) \
  asm volatile("s_add_u32 m0, %2, %3\n\ts_nop 0\n\tglobal_load_lds_dwordx4 %0, %1" ::"v"(voff), "s"(sbase), "s"(lds_off), "n"(imm) : "memory", "scc")
#define glds16s(sbase, voff, lds_off, imm) \
  asm volatile("s_add_u32 m0, %2, %3\n\ts_nop 0\n\tglobal_load_lds_dwordx4 %0, %1 sc1" ::"v"(voff), "s"(sbase), "s"(lds_off), "n"(imm) : "memory", "scc")
#define GLDS4(SC) "s_mov_b32 m0, %6\n\ts_nop 0\n\tglobal_load_lds_dwordx4 %0, %4" SC "\n\tglobal_load_lds_dwordx4 %1, %5 offset:1024" SC \
                  "\n\tglobal_load_lds_dwordx4 %2, %4 offset:2048" SC "\n\tglobal_load_lds_dwordx4 %3, %5 offset:3072" SC
template <bool SC1> __device__ __forceinline__ void glds16x4(const void* s0, const void* s1, const unsigned (&v)[4], unsigned lds) {
  if (SC1) asm volatile(GLDS4(" sc1") ::"v"(v[0]), "v"(v[1]), "v"(v[2]), "v"(v[3]), "s"(s0), "s"(s1), "s"(lds) : "memory");
  else asm volatile(GLDS4("") ::"v"(v[0]), "v"(v[1]), "v"(v[2]), "v"(v[3]), "s"(s0), "s"(s1), "s"(lds) : "memory");
}
__device__ __forceinline__ void glds4(const void* sbase, unsigned voff, unsigned lds_off) {
  asm volatile("s_mov_b32 m0, %2\n\ts_nop 0\n\tglobal_load_lds_dword %0, %1" ::"v"(voff), "s"(sbase), "s"(lds_off) : "memory");
}
__device__ __forceinline__ void stouch4(const void* p) {
  asm volatile("s_load_dwordx2 s[100:101], %0, 0x3c\n\ts_load_dwordx2 s[100:101], %0, 0xbc\n\ts_load_dwordx2 s[100:101], %0, 0x13c\n\ts_load_dwordx2 s[100:101], %0, 0x1bc"
               ::"s"(p) : "memory", "s100", "s101");
}
template <bool SC1> __device__ __forceinline__ void st16(void* sbase, unsigned voff, u32x4 v) {
  __builtin_amdgcn_raw_buffer_store_b128(v, __builtin_amdgcn_make_buffer_rsrc(sbase, 0, 0x7fffffff, 0x00020000), voff, 0, SC1 ? 16 : 0);
}
template <bool SC1> __device__ __forceinline__ u32x4 ld16(const void* sbase, unsigned voff) {
  return __builtin_amdgcn_raw_buffer_load_b128(__builtin_amdgcn_make_buffer_rsrc((void*)sbase, 0, 0x7fffffff, 0x00020000), voff, 0, SC1 ? 16 : 0);
}
template <int N> __device__ __forceinline__ void wait_vm() { asm volatile("s_waitcnt vmcnt(%0)" ::"n"(N) : "memory"); }
__device__ __forceinline__ void wait_lgkm0() { asm volatile("" ::: "memory"); __builtin_amdgcn_s_waitcnt(0xC07F); asm volatile("" ::: "memory"); }
__device__ __forceinline__ bf16x4 ld_tr4(const LDS_PTR(char) p) { return __builtin_amdgcn_ds_read_tr16_b64_v4i16((LDS_PTR(bf16x4))p); }
__device__ __forceinline__ bf16x8 cat8(bf16x4 lo, bf16x4 hi) { return __builtin_shufflevector(lo, hi, 0, 1, 2, 3, 4, 5, 6, 7); }
__device__ __forceinline__ void mfma_acc(f32x4& acc, bf16x8 a, bf16x8 b) { asm volatile("v_mfma_f32_16x16x32_bf16 %0, %1, %2, %0" : "+v"(acc) : "v"(a), "v"(b)); }
template <int STEP> __device__ __forceinline__ void mfma_sel(f32x4& acc, bf16x8 a, bf16x8 b, unsigned& mask, int& dl, int wrap) {
  asm volatile("s_add_u32 %1, %1, %1\n\tv_mfma_f32_16x16x32_bf16 %0, %3, %4, %0\n\ts_cselect_b32 %2, %5, %6" : "+v"(acc), "+s"(mask), "=s"(dl) : "v"(a), "v"(b), "s"(wrap), "n"(STEP) : "scc");
}
constexpr unsigned wrap_mask(int first, int ns) { unsigned m = 0; for (int t = 0; t < 32; ++t) if ((first + t) % ns == ns - 1) m |= 0x80000000u >> t; return m; }
template <int N> __device__ __forceinline__ void wait_lgkm() { __builtin_amdgcn_s_waitcnt(0xC07F | (N << 8)); }
template <int R, int C> __device__ __forceinline__ void mfma_done(f32x4 (&acc)[R][C]) {
  asm volatile("s_nop 9");
#pragma unroll
  for (int i = 0; i < R; ++i)
#pragma unroll
    for (int j = 0; j < C; ++j) asm volatile("" : "+v"(acc[i][j]));
}
__device__ __forceinline__ int lane_id() { int l; asm volatile("v_mbcnt_lo_u32_b32 %0, -1, 0\n\tv_mbcnt_hi_u32_b32 %0, -1, %0" : "=v"(l)); return l; }
__device__ __forceinline__ float xsum16_32(float v) {
  auto a = __builtin_amdgcn_permlane16_swap(__float_as_uint(v), __float_as_uint(v), false, false); v = __uint_as_float(a[0]) + __uint_as_float(a[1]);
  auto c = __builtin_amdgcn_permlane32_swap(__float_as_uint(v), __float_as_uint(v), false, false); return __uint_as_float(c[0]) + __uint_as_float(c[1]);
}

__device__ __forceinline__ float ln_rstd(float var) { return __builtin_amdgcn_rcpf(rbf(__builtin_amdgcn_sqrtf(rbf(rbf(var) + 1e-5f)))); }

constexpr size_t WS_MOD = 0;
constexpr size_t WS_EPO = 64 << 10, WS_FL1 = 96 << 10;
constexpr size_t WS_CTL = 128 << 10;
constexpr int C2 = 0, C3 = 16, C4 = 32, C5 = 48, CM = 80;
constexpr size_t WS_FLG = WS_CTL + (88 << 10), CTL_BYTES = 120 << 10;
constexpr size_t WS_XCH = 512 << 10;
constexpr size_t WS_MOD2 = 1 << 20;
constexpr size_t WS_H = 2 << 20, WS_O = WS_H + (4 << 20), WS_H2 = WS_O + (4 << 20), WS_HH = WS_H2 + (4 << 20),
                 WS_PART = WS_HH + (size_t)S_ * F_ * 2;

template <int NL> __device__ __forceinline__ f32x2 slab_sum(const unsigned (&mc)[NL / 2], const u32x4 (&mw)[NL]) {
  f32x2 a2[4] = {};
#pragma unroll
  for (int i = 0; i < NL; ++i) {
    const float cv = (i & 1) ? hi2f(mc[i >> 1]) : lo2f(mc[i >> 1]);
    const float sv = rbf(cv * __builtin_amdgcn_rcpf(1.f + __builtin_amdgcn_exp2f(cv * -1.4426950408889634f)));
#pragma unroll
    for (int j = 0; j < 4; ++j) a2[j] += (f32x2){lo2f(mw[i][j]), hi2f(mw[i][j])} * sv;
  }
  float acc[8] = {a2[0][0], a2[0][1], a2[1][0], a2[1][1], a2[2][0], a2[2][1], a2[3][0], a2[3][1]};
#pragma unroll
  for (int j = 0; j < 4; ++j) { auto a = __builtin_amdgcn_permlane32_swap(__float_as_uint(acc[j]), __float_as_uint(acc[j + 4]), false, false); acc[j] = __uint_as_float(a[0]) + __uint_as_float(a[1]); }
#pragma unroll
  for (int j = 0; j < 2; ++j) { auto a = __builtin_amdgcn_permlane16_swap(__float_as_uint(acc[j]), __float_as_uint(acc[j + 2]), false, false); acc[j] = __uint_as_float(a[0]) + __uint_as_float(a[1]); }
#pragma unroll
  for (int j = 0; j < 2; ++j) acc[j] += __uint_as_float(__builtin_amdgcn_mov_dpp(__float_as_uint(acc[j]), 0x128, 0xf, 0xf, false));
  return (f32x2){acc[0], acc[1]};
}

constexpr int K3_B = 18432, K3_STG = 18432 + 24576;
constexpr int K4_RED = 57344;
constexpr int K5_G = 16384, K5_U = 16384 + 7168 + 16, K5_STG = 30736, K5_NS = 5;
constexpr int K6_NW = 12, K6_NA = 7, K6_A = K6_NW * 8192, K6_X = 9 * 8192;
constexpr int LDS_VEC = 158720, LDS_DUMMY = LDS_VEC + 1024, LDS_ARR = LDS_DUMMY + 256, MEGA_LDS = LDS_ARR + 64;
constexpr unsigned SPIN_CAP = 1u << 19;
#define KNOBS PQ = 2, PQ5 = 4, PD2 = 4, PD3 = 4, PD4 = 4, PD5 = 48, PD6 = 6, PS6 = 1, FS0 = 4, XS0 = 14, XS1 = 4, TS5 = 3, WA = 0, WB = 1, WC = 4, NA5 = 2, ND5 = 0, NI5 = 2, NB5 = 4, VB3 = 4, SLB = 15, PRE6 = 4, NDR = 1, SL6 = 0
enum { KNOBS };
constexpr unsigned long long SKIP6 = 0x7c;
static_assert(__builtin_popcountll(SKIP6) == 5, "44 stages of W are issued behind 49 barriers");

__device__ __forceinline__ void signal(unsigned* cnt, int c, int lane, unsigned v = 1) {
  if (lane < 8) __hip_atomic_fetch_add(cnt + (c * 8 + lane) * 32, v, __ATOMIC_RELAXED, __HIP_MEMORY_SCOPE_AGENT);
}
template <int D0, int D1 = 1> __device__ __forceinline__ void poll(const unsigned* cnt, unsigned want) {
  if (D0) __builtin_amdgcn_s_sleep(D0);
#pragma unroll 1
  for (unsigned spins = 0; spins < SPIN_CAP; ++spins) {
    if (__builtin_amdgcn_ballot_w64(__hip_atomic_load(cnt, __ATOMIC_RELAXED, __HIP_MEMORY_SCOPE_AGENT) < want) == 0) break;
    __builtin_amdgcn_s_sleep(D1);
  }
  __builtin_amdgcn_fence(__ATOMIC_ACQUIRE, "wavefront");
}

template <int D0, int D1, int EX> __device__ __forceinline__ void pollp(const unsigned* cnt, unsigned want, unsigned (&r)[4]) {
  unsigned n = SPIN_CAP;
#define PL1(a) "global_atomic_add %" #a ", %5, %9, off sc0\n\ts_sleep %8\n\t"
#define PL2(a, c) "global_atomic_add %" #a ", %5, %9, off sc0\n\ts_waitcnt vmcnt(3)\n\tv_cmp_gt_u32 vcc, %6, %" #c "\n\ts_cbranch_vccz 2f\n\ts_sleep %8\n\t"
  asm volatile("s_mov_b64 exec, %10\n\ts_sleep %7\n\t" PL1(0) PL1(1) PL1(2) "1:\n\t" PL2(3, 0) PL2(0, 1) PL2(1, 2) PL2(2, 3) "s_sub_u32 %4, %4, 1\n\ts_cbranch_scc0 1b\n2:\n\ts_mov_b64 exec, -1"
               : "=&v"(r[0]), "=&v"(r[1]), "=&v"(r[2]), "=&v"(r[3]), "+s"(n) : "v"(cnt), "s"(want), "n"(D0), "n"(D1), "v"(0), "n"(EX) : "memory", "vcc", "scc");
}
#define KEEP4(r) asm volatile("; keep %0 %1 %2 %3" ::"v"(r[0]), "v"(r[1]), "v"(r[2]), "v"(r[3]))
typedef float f32x32 __attribute__((ext_vector_type(32)));
typedef float f32x16 __attribute__((ext_vector_type(16)));
#define M5(i, j) "v_mfma_f32_16x16x32_bf16 v[28*" #i "+4*" #j ":28*" #i "+4*" #j "+3], v[128+4*" #j ":131+4*" #j "], v[112+4*" #i ":115+4*" #i "], v[28*" #i "+4*" #j ":28*" #i "+4*" #j "+3]\n\t"
#define RA5(i) "ds_read_b128 v[112+4*" #i ":115+4*" #i "], %[aA] offset:1024*" #i "\n\t"
#define RW5(j, h, a, o) "ds_read_b64_tr_b16 v[128+4*" #j "+2*" #h ":129+4*" #j "+2*" #h "], %[" #a "] offset:" #o "\n\t"
#define ST5(a) "v_add_u32 %[" #a "], %[dl], %[" #a "]\n\t"
#define SEL5A "s_add_u32 %[mask], %[mask], %[mask]\n\t"
#define SEL5B "s_cselect_b32 %[dl], %[wrap], 30736\n\t"
#define LG(n) "s_waitcnt lgkmcnt(" #n ")\n\t"
#define RP5(j, h) RW5(j, h, aP, 32*(j/2)+7184*(j&1)+3584*h)
#define TRIP5(X)                                                                                                                                        \
  M5(0, 0) RW5(6, 1, aM, 3584) M5(1, 0) RA5(2) ST5(aM) M5(1, 1) RA5(3) ST5(aP) M5(0, 1) ST5(aA) LG(6) M5(0, 2) M5(1, 2) SEL5A M5(1, 3) SEL5B              \
  M5(0, 3) M5(0, 4) M5(1, 4) LG(0) M5(1, 5) M5(0, 5) "s_barrier\n\t" M5(0, 6) RA5(0) M5(1, 6) RA5(1)                                                      \
  M5(2, 0) X M5(3, 0) RP5(0, 0) M5(3, 1) RP5(0, 1) M5(2, 1) RP5(1, 0) M5(2, 2) RP5(1, 1) M5(3, 2) RP5(2, 0) M5(3, 3) RP5(2, 1) M5(2, 3) RP5(3, 0)          \
  M5(2, 4) RP5(3, 1) M5(3, 4) RP5(4, 0) M5(3, 5) RP5(4, 1) M5(2, 5) RP5(5, 0) M5(2, 6) RP5(5, 1) LG(8) M5(3, 6) RW5(6, 0, aM, 0)
#define ACC5 "+{v[0:31]}"(c0), "+{v[32:63]}"(c1), "+{v[64:95]}"(c2), "+{v[96:111]}"(c3)
#define CLOB5 "memory", "scc", "v112", "v113", "v114", "v115", "v116", "v117", "v118", "v119", "v120", "v121", "v122", "v123", "v124", "v125", "v126", "v127", "v128", "v129", "v130", "v131", "v132", "v133", \
              "v134", "v135", "v136", "v137", "v138", "v139", "v140", "v141", "v142", "v143", "v144", "v145", "v146", "v147", "v148", "v149", "v150", "v151", "v152", "v153", "v154", "v155"
static_assert(K5_STG == 30736 && K5_U - K5_G == 7184, "TRIP5");

#define C3(i, j) "v[24*" #i "+4*" #j "-12*(" #i "/4):24*" #i "+4*" #j "-12*(" #i "/4)+3]"
#define M3(i, j) "v_mfma_f32_16x16x32_bf16 " C3(i, j) ", v[124+4*" #j ":127+4*" #j "], v[104+4*" #i ":107+4*" #i "], " C3(i, j) "\n\t"
#define RA3(i, u) "ds_read_b128 v[104+4*" #i ":107+4*" #i "], %[aA] offset:1024*" #i "+9216*" #u "\n\t"
#define RH3(u) "ds_read_b128 v[120:123], %[aH] offset:9216*" #u "\n\t"
#define RW3(j, h, a, u) "ds_read_b64_tr_b16 v[124+4*" #j "+2*" #h ":125+4*" #j "+2*" #h "], %[" #a "] offset:8192*(" #j "/2)+2048*" #h "+4096*" #u "\n\t"
#define ST3(a, d) "v_add_u32 %[" #a "], " #d ", %[" #a "]\n\t"
#define H2_3(u, S0, S1, S2, B) M3(0, 0) RW3(5, 1, a1, u) M3(1, 0) RA3(2, u) M3(1, 1) RA3(3, u) S0 M3(0, 1) LG(7) S1 M3(0, 2) S2 M3(1, 2) M3(1, 3) M3(0, 3) LG(2) M3(0, 4) M3(1, 4) LG(0) B
#define H1_3(n, X)                                                                                                                                      \
  M3(1, 5) RA3(1, n) M3(0, 5) RA3(0, n) M3(2, 0) X M3(3, 0) RW3(0, 0, a0, n) M3(3, 1) RW3(0, 1, a0, n) M3(2, 1) RW3(1, 0, a1, n) M3(2, 2) RW3(1, 1, a1, n) \
  M3(3, 2) RW3(2, 0, a1, n) M3(3, 3) RW3(2, 1, a1, n) M3(2, 3) M3(4, 3) RW3(3, 0, a0, n) M3(4, 4) RW3(3, 1, a0, n) M3(2, 4) RH3(n)                         \
  M3(3, 4) RW3(4, 0, a0, n) M3(3, 5) RW3(4, 1, a0, n) LG(7) M3(2, 5) RW3(5, 0, a1, n)
#define STAGE3(d, X) H2_3(0, , , , ) H1_3(1, ) H2_3(1, ST3(a0, d), ST3(aA, d), ST3(a1, d) ST3(aH, d), "s_barrier\n\t") H1_3(0, X)
#define CV3(i, j, t) "v_cvt_pk_bf16_f32 v[104+2*" #t "], v[24*" #i "+4*" #j "-12*(" #i "/4)], v[24*" #i "+4*" #j "-12*(" #i "/4)+1]\n\tv_cvt_pk_bf16_f32 v[105+2*" #t "], v[24*" #i "+4*" #j "-12*(" #i "/4)+2], v[24*" #i "+4*" #j "-12*(" #i "/4)+3]\n\t"
#define WR3(i, t, a, o) "ds_write_b64 %[" #a "], v[104+2*" #t ":105+2*" #t "] offset:2048*" #i "+" #o "\n\t"
#define CW3(i, j, t, a, o) CV3(i, j, t) WR3(i, t, a, o)
#define V8(a) "v" #a "0", "v" #a "1", "v" #a "2", "v" #a "3", "v" #a "4", "v" #a "5", "v" #a "6", "v" #a "7"
#define V10(a) V8(a), "v" #a "8", "v" #a "9"
static_assert(K3_STG == 43008, "STAGE3");
__device__ __forceinline__ int w2_stage(int j) {
  const int j2 = j - 16, p = (j2 * 52) >> 8, r = j2 - p * 5;
  return j < 16 ? j * 112 * 2048 : (p * 224 + 32 + r * 32 + (r > 2 ? 32 : 0)) * 2048 + (r == 2);
}
__global__ __launch_bounds__(768, 3) __attribute__((amdgpu_num_sgpr(98))) void k2_block(const u16* __restrict__ x, const u16* __restrict__ wqkv, const u16* __restrict__ wo,
                                                   const u16* __restrict__ w1, const u16* __restrict__ w3, const u16* __restrict__ w2,
                                                   const u16* __restrict__ cnd, const u16* __restrict__ wmod, u16* __restrict__ out, char* __restrict__ ws) {
  extern __shared__ __attribute__((aligned(16))) char smem_[];
  LDS_PTR(char) smem = (LDS_PTR(char))smem_;
  const int tid = threadIdx.x;
  int lane = tid & 63;
  const int wave = __builtin_amdgcn_readfirstlane(tid >> 6);
  const unsigned lds_base = (unsigned)(uintptr_t)smem;
  const int b = blockIdx.x, xc = b & 7, ii = b >> 3;
  const int tm3 = (xc & 1) * 8 + (ii & 7), tn3 = (xc >> 1) * 4 + (ii >> 3);
  const int tm5 = (xc & 1) * 4 + (ii & 3), tn5 = (xc >> 1) * 8 + (ii >> 2);
  const int kh6 = xc >> 2, tm6 = (xc & 1) * 8 + (xc & 2) * 2 + (ii & 3), tn6 = ii >> 2;
  const int tm4 = tm6, tn4 = tn6 * 2 + kh6;
  const int u1 = (b >> 4) * 8 + xc;
  unsigned* cnt = (unsigned*)(ws + WS_CTL);
  unsigned long long* fl1 = (unsigned long long*)(ws + WS_FL1);
  long long pr0 = 0, prk = 0;
  STAMP_AT(0 );
  if (PROBE == 0) prk = pr0;

  if (wave >= 8) {
    const int lw = wave - 8, P = b * 4 + lw, lane = lane_id();
    const bool isA = lw < 2;
    asm volatile("" ::"s"(wqkv), "s"(wo), "s"(w1), "s"(w3), "s"(w2), "s"(ws));
    int kpf = -4;
    unsigned pr[4] = {};
    auto touch = [&]() {
      if (kpf < 28 && __builtin_amdgcn_s_getreg((3 << 11) | (8 << 6) | 7) <= 7) {
        stouch4(kpf < 0 ? (const char*)wo + ((kpf + 4) * 4096 + P * 4) * 128 : (const char*)((P & 1) ? w3 : w1) + (kpf * 2048 + (P >> 1) * 4) * 128);
        ++kpf;
      }
    };
    const char* epo = ws + WS_EPO;
    const unsigned swz = ((lane & 7) ^ ((lane >> 3) & 6)) * 16;
    const int cg = lw * 16 + tn4;
    auto issue4 = [&](int s, unsigned slot) {
      const char* p = (const char*)wo + tn4 * 128 + (lw * 32 + s * 128) * 2048;
      unsigned v4[4];
#pragma unroll
      for (int i = 0; i < 4; ++i) v4[i] = (lane >> 3) * 2048 + swz + i * 15360;
      glds16x4<false>(p, p, v4, lds_base + slot + lw * 4096);
    };
    auto run3 = [&](auto isA_) {
      constexpr bool A_ = decltype(isA_)::value; constexpr int N = A_ ? 9 : 12;
      const char* sp = A_ ? ws + WS_H + lw * 131072 + (tm3 * 8 - 1) * 1024 : (const char*)wqkv + tn3 * 128;
      unsigned vo[3][4];
#pragma unroll
      for (int i = 0; i < 12; ++i) {
        const int tb = (lw - 2) * 12 + i;
        vo[i >> 2][i & 3] = A_ ? lane * 16 + (i < 8 ? 1024 + (i >> 2) * 4096 : tm3 ? 0 : 1024) : ((tb & 7) * 8 + (lane >> 3)) * 6144 + (tb >> 3) * 2048 - (i & 3) * 1024 + swz;
      }
      const unsigned d0 = lds_base + (A_ ? lw * 9216 : K3_B + (lw - 2) * 12288);
      auto issue = [&](unsigned st) {
        if (A_) { glds16x4<true>(sp, sp, vo[0], d0 + st + 1024); glds16s(sp, vo[2][0], d0 + st, 0); glds16x4<true>(sp, sp, vo[1], d0 + st + 5120); }
        else {
#pragma unroll
          for (int i = 0; i < 3; ++i) glds16x4<false>(sp, sp, vo[i], d0 + st + i * 4096);
        }
        sp += A_ ? 262144 : 64 * 6144;
      };
      { unsigned long long ep;
        asm volatile("s_load_dwordx2 %0, %1, 0x0\n\ts_waitcnt lgkmcnt(0)" : "=s"(ep) : "s"(epo) : "memory");
        ep += 1;
        if (lw == 3) { *(LDS_PTR(u32x4))(smem + LDS_ARR) = (u32x4){0u, 0u, 0u, 0u}; wait_lgkm0(); }
        if (lw == 1) {
#pragma unroll
          for (int k = 0; k < 3; ++k) {
            const int i = b * 158 + (k * 64 + lane < 158 ? k * 64 + lane : 0);
            st16<true>(ws, i < 32768 ? (unsigned)WS_XCH + i * 16 : (unsigned)WS_CTL + (i - 32768) * 16, i < 32768 ? (u32x4){~0u, ~0u, ~0u, ~0u} : (u32x4){0u, 0u, 0u, 0u});
          }
        }
        BAR();
        auto flag = [&](int idx) { wait_vm<0>(); if (lane < 8) __hip_atomic_store(fl1 + lane * 256 + idx, ep, __ATOMIC_RELAXED, __HIP_MEMORY_SCOPE_AGENT); };
        if (lw == 1 && (b & 8)) {
          const int kq = u1 & 3, cgp = u1 >> 2;
          float sm = 0.f;
#pragma unroll
          for (int r = 0; r < 8; ++r) sm += *(const LDS_PTR(float))(smem + 8192 + (r * 64 + lane) * 4);
          __hip_atomic_store((unsigned*)(ws + WS_MOD) + kq * 2048 + cgp * 64 + lane, __float_as_uint(sm), __ATOMIC_RELAXED, __HIP_MEMORY_SCOPE_AGENT);
          flag(((cgp >> 3) & 1) * 64 + (cgp >> 4) * 32 + (cgp & 7) * 4 + kq);
        }
        if (b & 8) BAR();
        BAR();
        if (lw == 1 && !(b & 8)) flag(128 + u1);
        if (lw < 2) {
          if (lw == 1) BAR();
#pragma unroll 1
          for (unsigned spins = 0; spins < SPIN_CAP; ++spins) {
            bool ok = true;
#pragma unroll
            for (int k = 0; k < 4; ++k) if (k == 0 || lw) ok &= __hip_atomic_load(fl1 + xc * 256 + (lw ? k : (b >> 3) & 1) * 64 + lane, __ATOMIC_RELAXED, __HIP_MEMORY_SCOPE_AGENT) == ep;
            if (__builtin_amdgcn_ballot_w64(!ok) == 0) break;
            __builtin_amdgcn_s_sleep(FS0);
          }
          if (lw == 0) BAR();
          else if (b == 0 && lane == 0) __hip_atomic_store((unsigned long long*)(ws + WS_EPO), ep, __ATOMIC_RELAXED, __HIP_MEMORY_SCOPE_AGENT);
        } else BAR();
      }
      BAR();
      if (lw == 0) {
        signal(cnt, C2 + (u1 >> 3), lane, (b & 8) ? 1u : 0x10000u);
        pollp<PD2, PQ, 3>(cnt + ((C2 + ((lane & 1) && tm3 ? tm3 - 1 : tm3)) * 8 + (tn3 & 7)) * 32, 0x80000u, pr);
      }
      if (!A_) { issue(0); issue(K3_STG); }
      touch();
      BAR();
      if (A_) { issue(0); issue(K3_STG); }
      wait_vm<N>();
      KEEP4(pr);
      BAR();
      issue(2 * K3_STG);
      unsigned st = 0;
#pragma unroll 1
      for (int j = 0; j < 13; ++j) {
        wait_vm<N>();
        BAR();
        issue(st);
        st = st == 2 * K3_STG ? 0 : st + K3_STG;
        touch();
      }
      wait_vm<N>();
      BAR();
      issue4(0, 57344); issue4(5, 57344 + 5 * 16384);
      if (kpf < 0) kpf = 0;
      {
        glds4((P & 1) ? w3 : w1, ((kpf + ((lane >> 2) < VB3 ? lane >> 2 : 0)) * 2048 + (P >> 1) * 4 + (lane & 3)) * 128, lds_base + LDS_DUMMY); kpf += VB3; }
      wait_vm<9>();
    };
    if (isA) run3(T_()); else run3(F__());
    { auto issue = issue4;
      auto upto = [&](int a, int e) {
#pragma unroll
        for (int s = 1; s < 5; ++s) if (s > a && s <= e) issue(s, 57344 + s * 16384);
      };
      BAR();
      upto(0, WA);
      BAR();
      upto(WA, WB);
      BAR();
      upto(WB, WC);
      BAR();
      if (lw == 0) signal(cnt, C3 + tm3, lane);
      upto(WC, 4);
      issue(6, 0); issue(7, 16384);
      if (lw == 0) pollp<PD3, PQ, 1>(cnt + ((C3 + tm4) * 8 + tn6) * 32, 16, pr);
      wait_vm<8>();
      BAR();
      wait_vm<0>();
      KEEP4(pr);
      BAR();
      poll<0, 4>(cnt + (CM * 8 + cg) * 32, 4);
      f32x4 pv[4];
#pragma unroll
      for (int i = 0; i < 4; ++i) pv[i] = __builtin_bit_cast(f32x4, ld16<true>(ws + WS_MOD2, (((lane >> 4) * 4 + i) * 4096 + cg * 64 + (lane & 15) * 4) * 4));
      f32x4 v = (pv[0] + pv[1]) + (pv[2] + pv[3]);
#pragma unroll
      for (int e = 0; e < 4; ++e) { v[e] = rbf(xsum16_32(v[e])); if (lw == 2) v[e] = rbf(1.f + v[e]); if (lw == 1 || lw == 2) v[e] *= -1.4426950408889634f; }
      *(LDS_PTR(f32x4))(smem + LDS_VEC + lw * 256 + (lane & 15) * 16) = v;
      wait_lgkm0();
    }
    auto run5 = [&](auto isA_) {
      constexpr bool A_ = decltype(isA_)::value; constexpr int N = A_ ? 8 : 7;
      const char* sp = A_ ? ws + WS_H2 + (tm5 * 16 + lw * 8) * 1024 : (const char*)(lw == 2 ? w1 : w3) + tn5 * 224;
      unsigned vp[N];
#pragma unroll
      for (int i = 0; i < N; ++i) { const int L = i * 64 + lane, r = L / 14, c = L - r * 14; vp[i] = A_ ? L * 16 : r * (F_ * 2) + c * 16; }
      const unsigned d0 = lds_base + (A_ ? lw * 8192 : lw == 2 ? K5_G : K5_U);
      unsigned st = 3 * K5_STG;
      auto issue = [&]() {
#pragma unroll
        for (int i = 0; i < N; ++i) { if (A_) glds16s(sp, vp[i], d0 + st, i * 1024); else glds16(sp, vp[i], d0 + st, i * 1024); }
        sp += A_ ? 131072 : 32 * F_ * 2; st = st == (K5_NS - 1) * K5_STG ? 0 : st + K5_STG;
      };
      BAR();
      if (!A_) { __builtin_amdgcn_s_sleep(ND5); for (int s = 0; s < NA5; ++s) issue(); }
      BAR();
      u32x4 xk[4];
      auto keep = [&]() {
#pragma unroll
        for (int i = 0; i < 4; ++i) { xk[i] = *(const LDS_PTR(u32x4))(smem + lw * 4096 + i * 1024 + lane * 16); asm volatile("" : "+v"(xk[i])); }
      };
      if (A_ && lw == 0) { signal(cnt, C4 + tm4, lane); keep(); pollp<PD4, PQ, 3>(cnt + ((C4 + tm5 * 2 + (lane & 1)) * 8 + (tn5 & 7)) * 32, 16, pr); }
      else { keep(); if (!A_) { for (int s = NA5; s < NB5; ++s) issue(); } }
      BAR();
      if (A_) for (int s = 0; s < NI5; ++s) issue();
      wait_vm<A_ ? (NI5 - 1) * N : (NB5 - 1) * N>();
      KEEP4(pr);
      BAR();
#pragma unroll 1
      for (int s = A_ ? NI5 : NB5; s < K5_NS; ++s) issue();
#pragma unroll 1
      for (int j = 0; j < 27; ++j) {
        wait_vm<3 * N>();
        BAR();
        issue();
        if (j == 6) {
#pragma unroll
          for (int i = 0; i < 4; ++i) st16<true>(ws + WS_PART, (b * 4 + lw) * 4096 + i * 1024 + lane * 16, (u32x4){~0u, ~0u, ~0u, ~0u});
        }
        if (j == 12 && lw == 0 && lane == 0) __hip_atomic_store((unsigned*)(ws + WS_FLG) + b * 32, 16u, __ATOMIC_RELAXED, __HIP_MEMORY_SCOPE_AGENT);
        touch(); __builtin_amdgcn_s_sleep(TS5); touch();
      }
      const char* sb = A_ ? ws + WS_HH + (kh6 * 56 * 128 + tm6 * 8 + lw * 4) * 1024 : (const char*)w2 + kh6 * 1792 * 2048 + tn6 * 256 + ((lw ^ kh6 ^ 1) & 1) * 128;
      unsigned vo[4];
#pragma unroll
      for (int i = 0; i < 4; ++i) {
        const int r = i * 8 + (lane >> 3), k = 8 * ((r >> 2) & 3) + 4 * (r >> 4) + (r & 3);
        vo[i] = A_ ? lane * 16 : k * 2048 - i * 1024 + swz;
      }
      const unsigned d6 = lds_base + (A_ ? K6_A : 0) + (lw & 1) * 4096;
      st = 0;
      const int tab = w2_stage(lane);
      int jn = 0;
      auto issue6 = [&]() {
        if (A_) { glds16x4<true>(sb, sb, vo, d6 + st); sb += 131072; }
        else { const int o = __builtin_amdgcn_readlane(tab, jn); const char* s0 = sb + (o & ~1); glds16x4<false>(s0, s0 + (o & 1) * 65536, vo, d6 + st); ++jn; }
        st = st == ((A_ ? K6_NA : K6_NW) - 1) * 8192 ? 0 : st + 8192;
      };
      auto pre6 = [&](int n) {
        if (!A_) {
#pragma unroll 1
          for (int i = 0; i < n; ++i) issue6();
        }
      };
      constexpr int E = A_ ? 0 : 4;
      { const char* p = (const char*)w2 + (kh6 * 1792 + ii) * 2048;
        const int n = lw * 56 + (lane < 56 ? lane : 0), o = w2_stage((xc & 3) * 14 + (n >> 4));
        glds4(p, (o & ~1) + (o & (ii >> 4) & 1) * 65536 + (n & 15) * 128, lds_base + LDS_DUMMY);
        const int o2 = w2_stage(lw * 14 + (lane < 28 && SL6 ? lane >> 1 : 0));
        glds4(p, (o2 & ~1) + (o2 & (ii >> 4) & 1) * 65536 + (((3 - ((unsigned)(uintptr_t)w2 >> 7)) & 7) + (lane & 1) * 8) * 128, lds_base + LDS_DUMMY); }
      wait_vm<3 * N + 2>(); BAR();
      wait_vm<2 * N + 2>(); BAR(); pre6(PRE6);
      wait_vm<N + PRE6 * E + 2>(); BAR(); pre6(4);
      wait_vm<(PRE6 + 4) * E + 2>(); BAR(); pre6(8 - PRE6);
      BAR();
      if (A_) {
        volatile LDS_PTR(unsigned) go = (volatile LDS_PTR(unsigned))(smem + LDS_ARR + 4);
        if (lw == 0) { pollp<PD5, PQ5, 1>(cnt + ((C5 + (tm6 >> 1) * 4 + kh6 * 2) * 8 + tn6) * 32, 16, pr); *go = 1u; }
        else {
#pragma unroll 1
          for (unsigned spins = 0; spins < SPIN_CAP && __builtin_amdgcn_readfirstlane(*go) == 0; ++spins) __builtin_amdgcn_s_sleep(1);
          asm volatile("" ::: "memory");
        }
        STAMP_L(31);
        issue6(); issue6(); issue6();
        wait_vm<8>(); KEEP4(pr); BAR(); issue6(); issue6();
        wait_vm<12>(); BAR(); issue6(); issue6();
      } else { wait_vm<16>(); BAR(); BAR(); }
#pragma unroll 1
      for (int c = 2; c < 51; ++c) { wait_vm<16>(); BAR(); if (A_ || !((SKIP6 >> c) & 1)) issue6(); }
      wait_vm<16>(); BAR(); wait_vm<12>(); BAR(); wait_vm<8>(); BAR(); wait_vm<4>(); BAR(); wait_vm<0>(); BAR();
#pragma unroll
      for (int i = 0; i < 4; ++i) *(LDS_PTR(u32x4))(smem + K6_X + lw * 4096 + i * 1024 + lane * 16) = xk[i];
      BAR(); wait_lgkm0(); BAR();
    };
    if (isA) run5(T_()); else run5(F__());
    asm volatile("s_waitcnt lgkmcnt(0)" ::: "memory");
    return;
  }

  int fr = lane & 15, g = lane >> 4, p4 = fr & 3, row = 4 * g + (fr >> 2);
  int lsw = row * 128 + (((row >> 1) & 3) * 32) + p4 * 8;
  { const int mb = u1, hf = (b >> 3) & 1, kb0 = hf * 16 + wave * 2, cofs = (g & 1) * 16 + (g >> 1) * 8;
    u32x4 v[4];
    auto ldx = [&]() {
#pragma unroll
      for (int p = 0; p < 4; ++p) v[p] = *(const u32x4*)(x + (size_t)(mb * 16 + fr) * D_ + ((kb0 + (p & 1)) ^ ((p >> 1) * 16)) * 32 + cofs);
    };
    LDS_PTR(f32x2) sred = (LDS_PTR(f32x2))(smem + 4096);
    auto up2 = [](unsigned u) { return (f32x2){lo2f(u), hi2f(u)}; };
    auto stats = [&]() {
      f32x2 s2 = {0.f, 0.f}, q2 = {0.f, 0.f};
#pragma unroll
      for (int k = 0; k < 16; ++k) s2 += up2(v[k >> 2][k & 3]);
      const float mw = xsum16_32(s2[0] + s2[1]) * (1.f / 128.f);
#pragma unroll
      for (int k = 0; k < 16; ++k) { const f32x2 d = up2(v[k >> 2][k & 3]) - mw; q2 += d * d; }
      const float qw = xsum16_32(q2[0] + q2[1]);
      if (lane < 16) sred[wave * 16 + fr] = (f32x2){mw, qw};
      wait_lgkm0();
    };
    auto rest = [&]() {
    STAMP_AT(1 );
    float mean = 0.f, m2 = 0.f; f32x2 sw[8];
#pragma unroll
    for (int w = 0; w < 8; ++w) { sw[w] = sred[w * 16 + fr]; mean += sw[w][0]; m2 += sw[w][1]; }
    mean *= 0.125f;
#pragma unroll
    for (int w = 0; w < 8; ++w) { const float d = sw[w][0] - mean; m2 += 128.f * d * d; }
    const float rstd = ln_rstd(m2 * (1.f / 1024.f));
    mean = rbf(mean);
    unsigned nv[8];
#pragma unroll
    for (int k = 0; k < 8; ++k) {
      const f32x2 d = up2(v[k >> 2][k & 3]) - mean;
      const f32x2 n = up2(pk2(d[0], d[1])) * rstd;
      nv[k] = pk2(n[0], n[1]);
      asm volatile("" : "+v"(nv[k]));
    }
    SCHED0(); BAR(); SCHED0();
    STAMP_AT(2 );
    float mp[8];
#pragma unroll
    for (int i = 0; i < 8; ++i) mp[i] = __uint_as_float(__hip_atomic_load((unsigned*)(ws + WS_MOD) + hf * 512 + tid + i * 1024, __ATOMIC_RELAXED, __HIP_MEMORY_SCOPE_AGENT));
    *(LDS_PTR(unsigned))(smem + tid * 4) = pk2((mp[0] + mp[2]) + (mp[4] + mp[6]), 1.f + rbf((mp[1] + mp[3]) + (mp[5] + mp[7])));
    wait_lgkm0();
#pragma unroll
    for (int p = 0; p < 2; ++p) {
      const LDS_PTR(u32x4) tp = (const LDS_PTR(u32x4))(smem + ((wave * 2 + p) * 32 + cofs) * 4);
      const u32x4 t0 = tp[0], t1 = tp[1];
      unsigned o[4];
#pragma unroll
      for (int j = 0; j < 4; ++j) {
        const unsigned ta = j < 2 ? t0[2 * j] : t1[2 * j - 4], tb = j < 2 ? t0[2 * j + 1] : t1[2 * j - 3];
        const unsigned u = pk2(lo2f(nv[p * 4 + j]) * hi2f(ta), hi2f(nv[p * 4 + j]) * hi2f(tb));
        o[j] = pk2(lo2f(u) + lo2f(ta), hi2f(u) + lo2f(tb));
      }
      auto s0 = __builtin_amdgcn_permlane16_swap(o[0], o[2], false, false);
      auto s1 = __builtin_amdgcn_permlane16_swap(o[1], o[3], false, false);
      st16<true>(ws + WS_H, ((kb0 + p) * 128 + mb) * 1024 + lane * 16, (u32x4){s0[0], s1[0], s0[1], s1[1]});
    }
    STAMP_AT(3 );
    wait_vm<0>();
    STAMP_AT(4 );
    BAR();
    STAMP_AT(5 );
    };
    if (hf) {
      const int mrow = (u1 & 3) * 256 + (tid >> 3) * 4;
      const u32x2 c2 = *(const u32x2*)(cnd + mrow);
      u32x4 mw[4];
#pragma unroll
      for (int i = 0; i < 4; ++i) mw[i] = __builtin_nontemporal_load((const u32x4*)(wmod + (size_t)(mrow + i) * 6144 + (u1 >> 2) * 64 + (lane & 7) * 8));
      const unsigned mc[2] = {c2[0], c2[1]};
      *(LDS_PTR(f32x2))(smem + 8192 + (wave * 64 + (lane & 7) * 8 + (lane >> 4) * 2) * 4) = slab_sum<4>(mc, mw);
      wait_lgkm0();
      BAR();
      BAR();
      ldx();
      stats();
      BAR();
      rest();
    } else {
      ldx();
      SCHED0();
      const int sl = (3 - ((unsigned)(uintptr_t)wqkv >> 7) - (xc >> 1) * 4) & 7;
      const int i2 = tid - 384, r0 = i2 < 0 ? tid / 12 : i2 / 3, c0 = i2 < 0 ? tid - r0 * 12 : (i2 - r0 * 3) * 4 + sl;
      const bool on = i2 < 0 || (sl < 4 && i2 < 96);
      const unsigned pf = *(const unsigned*)((const char*)wqkv + (xc >> 1) * 512 + (on ? ((((xc & 1) ^ (i2 >= 0)) * 512 + (ii >> 1) * 32 + r0) * 48 + (c0 >> 2) * 16 + (c0 & 3)) * 128 : 0));
      SCHED0(); BAR(); SCHED0();
      stats();
      SCHED0(); BAR(); SCHED0();
      rest();
      asm volatile("" ::"v"(pf));
    }
  }
  {
    f32x4 bias[2];
    { const int kbase = tm3 * 128 + wave * 16 - 16;
#pragma unroll
      for (int t = 0; t < 4; ++t) {
        const int kk = (t >> 1) * 16 + 4 * g + (t & 1) * 2 + (fr & 1), df = 16 + fr - kk;
        bias[t >> 1][(t & 1) * 2] = bias[t >> 1][(t & 1) * 2 + 1] = (df >= 0 && df <= 14 && kbase + kk >= 0) ? 0.f : -__builtin_inff();
      }
      asm volatile("" : "+v"(bias[0]), "+v"(bias[1])); }
    u32x4 mc, mw[8];
    if (wave < 4) {
      const int wm = wave & 1, bA = wave;
      unsigned a0 = lds_base + K3_B + (lsw ^ (bA * 32)), a1 = lds_base + K3_B + (lsw ^ ((bA ^ 1) * 32)), aA = lds_base + 1024 + wm * 4096 + lane * 16, aH = lds_base + lane * 16;
      unsigned lq = lds_base + bA * 512 + lane * 8, lv = lds_base + 36864 + bA * 512 + (lane ^ ((g >> 1) * 8)) * 8;
      int n = 4;
      asm volatile(".irp r,0,8,16,24,32,40,48,56,64,72,80,88,96\n\tv_mov_b64 v[\\r:\\r+1], 0\n\tv_mov_b64 v[\\r+2:\\r+3], 0\n\tv_mov_b64 v[\\r+4:\\r+5], 0\n\tv_mov_b64 v[\\r+6:\\r+7], 0\n\t.endr\n\t"
                   "s_setprio 2\n\ts_waitcnt vmcnt(0)\n\ts_barrier\n\t"
                   "s_barrier\n\t"
                   RA3(1, 0) RA3(0, 0) RW3(0, 0, a0, 0) RW3(0, 1, a0, 0) RW3(1, 0, a1, 0) RW3(1, 1, a1, 0) RW3(2, 0, a1, 0) RW3(2, 1, a1, 0) RW3(3, 0, a0, 0) RW3(3, 1, a0, 0) RH3(0)
                   RW3(4, 0, a0, 0) RW3(4, 1, a0, 0) RW3(5, 0, a1, 0) LG(8)
                   ".p2align 5\n1:\n\t" STAGE3(43008, ) STAGE3(43008, ) STAGE3(-86016, "s_sub_u32 %[n], %[n], 1\n\t") "s_cbranch_scc0 1b\n\t"
                   H2_3(0, , , , ) H1_3(1, )
                   H2_3(1, "v_add_u32 %[a0], %[mo], %[lq]\n\t", "v_add_u32 %[aA], %[mo], %[lv]\n\t", "v_xor_b32 %[a1], 512, %[a0]\n\tv_xor_b32 %[aH], 512, %[aA]\n\t", "s_barrier\n\t")
                   M3(1, 5) M3(0, 5) M3(2, 0) CV3(0, 0, 0) M3(3, 0) WR3(0, 0, a0, 0) CV3(1, 0, 1) M3(3, 1) WR3(1, 1, a0, 0) CV3(0, 1, 2) M3(2, 1) WR3(0, 2, a1, 0) CV3(1, 1, 3)
                   M3(2, 2) WR3(1, 3, a1, 0) CV3(0, 2, 0) M3(3, 2) WR3(0, 0, a1, 18432) CV3(1, 2, 1) M3(3, 3) WR3(1, 1, a1, 18432) CV3(0, 3, 2) M3(2, 3) WR3(0, 2, a0, 18432) CV3(1, 3, 3)
                   M3(4, 3) WR3(1, 3, a0, 18432) CV3(0, 4, 0) M3(4, 4) WR3(0, 0, aA, 0) CV3(1, 4, 1) M3(2, 4) WR3(1, 1, aA, 0) CV3(0, 5, 2) M3(3, 4) WR3(0, 2, aH, 0) CV3(1, 5, 3)
                   M3(3, 5) WR3(1, 3, aH, 0) CV3(2, 0, 0) M3(2, 5) WR3(2, 0, a0, 0)
                   CW3(3, 0, 1, a0, 0) CW3(3, 1, 2, a1, 0) CW3(2, 1, 3, a1, 0) CW3(2, 2, 0, a1, 18432) CW3(3, 2, 1, a1, 18432) CW3(3, 3, 2, a0, 18432) CW3(2, 3, 3, a0, 18432)
                   CV3(4, 3, 0) WR3(0, 0, lq, 18432) CV3(4, 4, 1) WR3(0, 1, lv, 0) CW3(2, 4, 2, aA, 0) CW3(3, 4, 3, aA, 0) CW3(3, 5, 0, aH, 0) CW3(2, 5, 1, aH, 0)
                   "s_setprio 0\n\t" LG(0)
                   : [a0] "+v"(a0), [a1] "+v"(a1), [aA] "+v"(aA), [aH] "+v"(aH), [n] "+s"(n)
                   : [lq] "v"(lq), [lv] "v"(lv), [mo] "s"((wm * 4 + 1) * 2048)
                   : "memory", "scc", V10(), V10(1), V10(2), V10(3), V10(4), V10(5), V10(6), V10(7), V10(8), V10(9), V10(10), V10(11), V10(12), V10(13), V8(14));
    } else {
      wait_vm<0>();
      BAR(); BAR();
      {
        const unsigned* pc = cnt + ((C2 + ((lane & 1) && tm3 ? tm3 - 1 : tm3)) * 8 + (tn3 & 7)) * 32;
        unsigned seen = 0x80008u;
        BAR(); BAR(); BAR();
        if (wave == 4) asm volatile("global_load_dword %0, %1, off sc1" : "=v"(seen) : "v"(pc) : "memory");
        BAR(); BAR();
        asm volatile("s_waitcnt vmcnt(0)" : "+v"(seen)::"memory");
        if (__builtin_amdgcn_ballot_w64(seen < 0x80008u) != 0) poll<0>(pc, 0x80008u);
      }
#pragma unroll 1
      for (int t = 5; t < SLB; ++t) BAR();
      const int mrow = ((b & 3) * 4 + (wave & 3)) * 64 + (lane >> 3) * 8;
      mc = *(const u32x4*)(cnd + mrow);
#pragma unroll
      for (int i = 0; i < 8; ++i) mw[i] = __builtin_nontemporal_load((const u32x4*)(wmod + (size_t)(mrow + i) * 6144 + 2048 + (b >> 2) * 64 + (lane & 7) * 8));
      SCHED0();
#pragma unroll
      for (int t = SLB; t < 16; ++t) BAR();
    }
    lane = lane_id(); fr = lane & 15; g = lane >> 4; p4 = fr & 3; row = 4 * g + (fr >> 2); lsw = row * 128 + (((row >> 1) & 3) * 32) + p4 * 8;
    wait_lgkm0();
    BAR();
    STAMP_AT(9 );
    const LDS_PTR(char) eb = smem + wave * 2048;
    auto ld2 = [&](int o) { return cat8(*(const LDS_PTR(bf16x4))(eb + o), *(const LDS_PTR(bf16x4))(eb + o + 512)); };
    const int vo = 36864 + ((p4 * 16 + row) ^ ((p4 >> 1) * 8)) * 8;
    bf16x8 qf[2], k0[2], k1[2], v0[2], v1[2];
#pragma unroll
    for (int hd = 0; hd < 2; ++hd) { qf[hd] = ld2(2048 + hd * 1024 + lane * 8); k0[hd] = ld2(18432 + hd * 1024 + lane * 8); k1[hd] = ld2(18432 + 2048 + hd * 1024 + lane * 8); }
#pragma unroll
    for (int hd = 0; hd < 2; ++hd) {
      v0[hd] = cat8(ld_tr4(eb + vo + hd * 1024), ld_tr4(eb + vo + hd * 1024 + 2048));
      v1[hd] = cat8(ld_tr4(eb + vo + hd * 1024 + 512), ld_tr4(eb + vo + hd * 1024 + 512 + 2048));
    }
    SCHED0();
    const f32x4 z = {0.f, 0.f, 0.f, 0.f};
    const bool odd = fr & 1;
    const unsigned sel0 = odd ? 0x01000c0cu : 0x0c0c0100u, sel1 = odd ? 0x03020c0cu : 0x0c0c0302u;
    float inf = __builtin_inff(); asm("" : "+v"(inf));
    auto mx2 = [&](float a, float c) { return __builtin_amdgcn_fmed3f(a, c, inf); };
    auto both = [](float a, float c, auto op, auto fin) {
      auto u = __builtin_amdgcn_permlane16_swap(__float_as_uint(a), __float_as_uint(c), false, false);
      float r = op(__uint_as_float(u[0]), __uint_as_float(u[1]));
      auto w = __builtin_amdgcn_permlane32_swap(__float_as_uint(r), __float_as_uint(r), false, false);
      r = fin(op(__uint_as_float(w[0]), __uint_as_float(w[1])));
      auto y = __builtin_amdgcn_permlane16_swap(__float_as_uint(r), __float_as_uint(r), false, false);
      return (f32x2){__uint_as_float(y[0]), __uint_as_float(y[1])};
    };
    f32x2 l[2][2];
#pragma unroll
    for (int hd = 0; hd < 2; ++hd) {
      const f32x4 s0 = __builtin_amdgcn_mfma_f32_16x16x32_bf16(k0[hd], qf[hd], bias[0], 0, 0, 0), s1 = __builtin_amdgcn_mfma_f32_16x16x32_bf16(k1[hd], qf[hd], bias[1], 0, 0, 0);
#pragma unroll
      for (int t = 0; t < 2; ++t) {
        const f32x4 sv = t ? s1 : s0;
        unsigned u = pk2(odd ? sv[1] : sv[0], odd ? sv[3] : sv[2]);
        u = pk2(lo2f(u) * 0.17677669529663687f, hi2f(u) * 0.17677669529663687f);
        l[hd][t] = (f32x2){lo2f(u), hi2f(u)};
      }
    }
    const f32x2 mx = both(mx2(mx2(l[0][0][0], l[0][0][1]), mx2(l[0][1][0], l[0][1][1])), mx2(mx2(l[1][0][0], l[1][0][1]), mx2(l[1][1][0], l[1][1][1])), mx2,
                          [](float r) { return r * -1.4426950408889634f; });
    float sm[2];
#pragma unroll
    for (int hd = 0; hd < 2; ++hd) {
#pragma unroll
      for (int t = 0; t < 2; ++t) { const f32x2 e = l[hd][t] * 1.4426950408889634f + mx[hd]; l[hd][t] = (f32x2){__builtin_amdgcn_exp2f(e[0]), __builtin_amdgcn_exp2f(e[1])}; }
      const f32x2 a = l[hd][0] + l[hd][1]; sm[hd] = a[0] + a[1];
    }
    const f32x2 inv = both(sm[0], sm[1], [](float a, float c) { return a + c; }, [](float r) { return __builtin_amdgcn_rcpf(r); });
#pragma unroll
    for (int hd = 0; hd < 2; ++hd) {
      u32x4 pp;
#pragma unroll
      for (int t = 0; t < 2; ++t) {
        const f32x2 p = l[hd][t] * inv[hd]; const unsigned u = pk2(p[0], p[1]);
        pp[2 * t] = __builtin_amdgcn_perm(u, u, sel0); pp[2 * t + 1] = __builtin_amdgcn_perm(u, u, sel1);
      }
      const bf16x8 pf = __builtin_bit_cast(bf16x8, pp);
      const f32x4 oa = __builtin_amdgcn_mfma_f32_16x16x32_bf16(v0[hd], pf, z, 0, 0, 0), ob = __builtin_amdgcn_mfma_f32_16x16x32_bf16(v1[hd], pf, z, 0, 0, 0);
      u32x4 ov; ov[0] = pk2(oa[0], oa[1]); ov[1] = pk2(oa[2], oa[3]); ov[2] = pk2(ob[0], ob[1]); ov[3] = pk2(ob[2], ob[3]);
      st16<true>(ws + WS_O, ((tn3 * 2 + hd) * 128 + tm3 * 8 + wave) * 1024 + lane * 16, ov);
    }
    STAMP_AT(10 );
    wait_vm<0>();
    STAMP_AT(11 );
    BAR();
    STAMP_AT(12 );
    if (wave >= 4) {
      const unsigned m4[4] = {mc[0], mc[1], mc[2], mc[3]};
      const f32x2 ps = slab_sum<8>(m4, mw);
      if (!(lane & 8))
        __hip_atomic_store((unsigned long long*)(ws + WS_MOD2) + (((b & 3) * 4 + (wave & 3)) * 4096 + (b >> 2) * 64 + (lane & 7) * 8 + (lane >> 4) * 2) / 2,
                           ((unsigned long long)__float_as_uint(ps[1]) << 32) | __float_as_uint(ps[0]), __ATOMIC_RELAXED, __HIP_MEMORY_SCOPE_AGENT);
      wait_vm<0>();
      if (lane == 0 && __hip_atomic_fetch_add((LDS_PTR(unsigned))(smem + LDS_ARR + 8), 1u, __ATOMIC_RELAXED, __HIP_MEMORY_SCOPE_WORKGROUP) == 3)
        __hip_atomic_fetch_add(cnt + (CM * 8 + (b >> 2)) * 32, 1u, __ATOMIC_RELAXED, __HIP_MEMORY_SCOPE_AGENT);
    }
  }
  { const int wm = wave & 3, ks = wave >> 2, mb = tm4 * 8 + wm * 2 + ks;
    constexpr int R = 6;
    u32x4 rx[2];
#pragma unroll
    for (int p = 0; p < 2; ++p) rx[p] = *(const u32x4*)(x + (size_t)(mb * 16 + fr) * D_ + tn4 * 64 + p * 32 + (g & 1) * 16 + (g >> 1) * 8);
    const unsigned va0 = (ks * 128 + mb) * 1024 + lane * 16, va1 = va0 ^ 1024;
    bf16x8 fa[R][2], fw[2][4];
    auto issueA = [&](int t) {
      const char* pa = ws + WS_O + t * 262144;
      fa[t % R][0] = __builtin_bit_cast(bf16x8, ld16<true>(pa, va0));
      fa[t % R][1] = __builtin_bit_cast(bf16x8, ld16<true>(pa, va1));
    };
    unsigned aw[4];
    auto rdw = [&](int t, int j) {
      const int o = (t & 3) * 8192;
      fw[t & 1][j] = cat8(ld_tr4(smem + aw[j] + o), ld_tr4(smem + aw[j] + o + 2048));
    };
    auto stepw = [&](int t) {
      const int gi = t >> 2, base = gi < 3 ? 57344 + gi * 32768 : 0;
#pragma unroll
      for (int j = 0; j < 4; ++j) { aw[j] = base + ks * 4096 + (lsw ^ (j * 32)); asm volatile("" : "+v"(aw[j])); }
    };
    auto up2 = [](unsigned u) { return (f32x2){lo2f(u), hi2f(u)}; };
    f32x2 f[8];
    unsigned slot = (mb * 256 + fr + kh6 * 32768 + 4 * g * 16) * 8, sown = (mb * 256 + fr + (g & 1) * 32768 + tn4 * 16) * 8;
    f32x4 acc[2][4];
#pragma unroll
    for (int i = 0; i < 2; ++i)
#pragma unroll
      for (int j = 0; j < 4; ++j) acc[i][j] = (f32x4){0.f, 0.f, 0.f, 0.f};
    stepw(0);
    wait_lgkm0();
    SCHED0(); BAR(); SCHED0();
    STAMP_AT(13 );
#pragma unroll
    for (int t = 0; t < R; ++t) issueA(t);
#pragma unroll
    for (int j = 0; j < 4; ++j) rdw(0, j);
    SCHED0();
#pragma unroll
    for (int t = 0; t < 16; ++t) {
      if (t < 15 && ((t + 1) & 3) == 0) stepw(t + 1);
#pragma unroll
      for (int j = 0; j < 4; ++j) {
        if (t < 15) rdw(t + 1, j);
        SCHED0();
#pragma unroll
        for (int i = 0; i < 2; ++i) acc[i][j] = __builtin_amdgcn_mfma_f32_16x16x32_bf16(fw[t & 1][j], fa[t % R][i], acc[i][j], 0, 0, 0);
        SCHED0();
      }
      if (t + R < 16) issueA(t + R);
      if (t == 2) {
#pragma unroll
        for (int p = 0; p < 2; ++p) {
          auto s0 = __builtin_amdgcn_permlane16_swap(rx[p][0], rx[p][2], false, false);
          auto s1 = __builtin_amdgcn_permlane16_swap(rx[p][1], rx[p][3], false, false);
          f[4 * p] = up2(s0[0]); f[4 * p + 1] = up2(s1[0]); f[4 * p + 2] = up2(s0[1]); f[4 * p + 3] = up2(s1[1]);
        }
#pragma unroll
        for (int j = 0; j < 8; ++j) asm volatile("" : "+v"(f[j]));
        asm volatile("" : "+v"(slot), "+v"(sown));
      }
      if (t == 5) BAR();
      SCHED0();
    }
    STAMP_AT(14 );
    LDS_PTR(f32x4) red = (LDS_PTR(f32x4))(smem + K4_RED);
#pragma unroll
    for (int j = 0; j < 4; ++j) red[((wave ^ 4) * 4 + j) * 64 + lane] = acc[1][j];
    wait_lgkm0();
    BAR();
    STAMP_AT(15 );
    const LDS_PTR(float) vec = (const LDS_PTR(float))(smem + LDS_VEC);
    unsigned o[8];
    f32x4 pr[4], ga[4];
#pragma unroll
    for (int j = 0; j < 4; ++j) { pr[j] = red[(wave * 4 + j) * 64 + lane]; ga[j] = *(const LDS_PTR(f32x4))(vec + j * 16 + 4 * g); }
    SCHED0();
#pragma unroll
    for (int j = 0; j < 4; ++j) {
      const f32x4 y = acc[0][j] + pr[j];
      const f32x2 lo = y.xy * ga[j].xy + f[2 * j], hi = y.zw * ga[j].zw + f[2 * j + 1];
      o[2 * j] = pk2(lo[0], lo[1]); o[2 * j + 1] = pk2(hi[0], hi[1]);
      f[2 * j] = up2(o[2 * j]); f[2 * j + 1] = up2(o[2 * j + 1]);
    }
    const f32x2 s2 = ((f[0] + f[1]) + (f[2] + f[3])) + ((f[4] + f[5]) + (f[6] + f[7]));
    float mt = xsum16_32(s2[0] + s2[1]) * (1.f / 64.f); asm("" : "+v"(mt));
    f32x2 q2 = {0.f, 0.f}, q3 = {0.f, 0.f};
#pragma unroll
    for (int j = 0; j < 8; j += 2) { f[j] -= mt; f[j + 1] -= mt; q2 += f[j] * f[j]; q3 += f[j + 1] * f[j + 1]; }
    q2 += q3;
    const float qq = xsum16_32(q2[0] + q2[1]);
    if (lane < 32) {
      const unsigned qb = qq == qq ? __float_as_uint(qq) : 0x7fc00000u;
      __hip_atomic_store((unsigned long long*)(ws + WS_XCH + sown), ((unsigned long long)qb << 32) | __float_as_uint(mt), __ATOMIC_RELAXED, __HIP_MEMORY_SCOPE_AGENT);
    }
    *(LDS_PTR(u32x4))(smem + (mb & 7) * 2048 + lane * 16) = (u32x4){o[0], o[1], o[2], o[3]};
    *(LDS_PTR(u32x4))(smem + (mb & 7) * 2048 + 1024 + lane * 16) = (u32x4){o[4], o[5], o[6], o[7]};
    STAMP_AT(16 );
    f32x4 ss[4], gg[4];
#pragma unroll
    for (int j = 0; j < 4; ++j) {
      ss[j] = *(const LDS_PTR(f32x4))(vec + 64 + j * 16 + 4 * g); gg[j] = *(const LDS_PTR(f32x4))(vec + 128 + j * 16 + 4 * g);
      f[2 * j] *= gg[j].xy; f[2 * j + 1] *= gg[j].zw;
      asm volatile("" : "+v"(ss[j]), "+v"(gg[j]), "+v"(f[2 * j]), "+v"(f[2 * j + 1]));
    }
    unsigned long long pv[4];
    __builtin_amdgcn_s_sleep(XS0);
    for (unsigned spins = 0;; ++spins) {
      bool ok = true;
#pragma unroll
      for (int i = 0; i < 4; ++i) { pv[i] = __hip_atomic_load((unsigned long long*)(ws + WS_XCH + slot) + i * 16, __ATOMIC_RELAXED, __HIP_MEMORY_SCOPE_AGENT); ok &= (unsigned)(pv[i] >> 32) != 0xffffffffu; }
      if (__builtin_amdgcn_ballot_w64(!ok) == 0 || spins > SPIN_CAP) break;
      __builtin_amdgcn_s_sleep(XS1);
    }
    STAMP_AT(17 );
    float ms = 0.f, m2 = 0.f;
#pragma unroll
    for (int i = 0; i < 4; ++i) { ms += __uint_as_float((unsigned)pv[i]); m2 += __uint_as_float((unsigned)(pv[i] >> 32)); }
    const float mean = xsum16_32(ms) * (1.f / 16.f);
#pragma unroll
    for (int i = 0; i < 4; ++i) { const float d = __uint_as_float((unsigned)pv[i]) - mean; m2 += 64.f * d * d; }
    const float rstd = ln_rstd(xsum16_32(m2) * (1.f / 1024.f));
    const float sh = (mt - rbf(mean)) * rstd;
#pragma unroll
    for (int p = 0; p < 2; ++p) {
      u32x4 v;
#pragma unroll
      for (int j = 0; j < 4; ++j) {
        const int jb = 2 * p + (j >> 1);
        const f32x2 h = f[4 * p + j] * rstd + (((j & 1) ? gg[jb].zw : gg[jb].xy) * sh + ((j & 1) ? ss[jb].zw : ss[jb].xy));
        v[j] = pk2(h[0], h[1]);
      }
      st16<true>(ws + WS_H2, ((tn4 * 2 + p) * 128 + mb) * 1024 + lane * 16, v);
    }
    STAMP_AT(18 );
    wait_vm<0>();
    wait_lgkm0();
    BAR();
    STAMP_AT(19 );

  }
  const int grp = wave >> 2;
  { const int wm = wave & 3;
    const LDS_PTR(char) sb = smem + 3 * K5_STG;
    const LDS_PTR(char) aP = sb + K5_G + row * 224 + grp * 96 + p4 * 8;
    const LDS_PTR(char) aM = sb + (p4 < 2 ? K5_G : K5_U) + row * 224 + (12 + grp) * 16 + (p4 & 1) * 8;
    const LDS_PTR(char) aA = sb + wm * 4096 + lane * 16;
    f32x32 c0, c1, c2; f32x16 c3;
    asm volatile(".irp r,0,8,16,24,32,40,48,56,64,72,80,88,96,104\n\tv_mov_b64 v[\\r:\\r+1], 0\n\tv_mov_b64 v[\\r+2:\\r+3], 0\n\tv_mov_b64 v[\\r+4:\\r+5], 0\n\tv_mov_b64 v[\\r+6:\\r+7], 0\n\t.endr"
                 : "={v[0:31]}"(c0), "={v[32:63]}"(c1), "={v[64:95]}"(c2), "={v[96:111]}"(c3)::"memory");
    int wrap = -(K5_NS - 1) * K5_STG, dl = K5_STG, n = 7;
    unsigned mask = wrap_mask(4, K5_NS);
    asm volatile("" : "+s"(wrap), "+v"(aP), "+v"(aM), "+v"(aA));
    if (grp) __builtin_amdgcn_s_setprio(1); else __builtin_amdgcn_s_setprio(2);
    BAR();
    STAMP_AT(20 );
    BAR();
    STAMP_G(21, 26);
    asm volatile(RA5(0) RA5(1) RP5(0, 0) RP5(0, 1) RP5(1, 0) RP5(1, 1) RP5(2, 0) RP5(2, 1) RP5(3, 0) RP5(3, 1) RP5(4, 0) RP5(4, 1) RP5(5, 0) RP5(5, 1) RW5(6, 0, aM, 0) LG(9)
                 ".p2align 5\n1:\n\t" TRIP5("") TRIP5("") TRIP5("") TRIP5("s_sub_u32 %[n], %[n], 1\n\t") "s_cbranch_scc0 1b\n\t" LG(0) "s_nop 7"
                 : ACC5, [aP] "+v"(aP), [aM] "+v"(aM), [aA] "+v"(aA), [dl] "+s"(dl), [mask] "+s"(mask), [n] "+s"(n) : [wrap] "s"(wrap) : CLOB5);
    f32x4 acc[4][7];
#pragma unroll
    for (int e = 0; e < 112; ++e) acc[e / 28][(e % 28) / 4][e & 3] = e < 32 ? c0[e & 31] : e < 64 ? c1[e & 31] : e < 96 ? c2[e & 31] : c3[e & 15];
    STAMP_G(22, 27);
    auto swi4 = [](const f32x4& gv, const f32x4& uv, unsigned& o0, unsigned& o1) {
      constexpr float c2 = 2.0813689810056077f;
      f32x2 pa = gv.xy * uv.xy, pb = gv.zw * uv.zw;
      const f32x2 ta = (f32x2){__builtin_amdgcn_exp2f(gv[0]), __builtin_amdgcn_exp2f(gv[1])} * c2 + c2, tb = (f32x2){__builtin_amdgcn_exp2f(gv[2]), __builtin_amdgcn_exp2f(gv[3])} * c2 + c2;
      pa *= (f32x2){__builtin_amdgcn_rcpf(ta[0]), __builtin_amdgcn_rcpf(ta[1])}; pb *= (f32x2){__builtin_amdgcn_rcpf(tb[0]), __builtin_amdgcn_rcpf(tb[1])};
      o0 = pk2(pa[0], pa[1]); o1 = pk2(pb[0], pb[1]);
    };
    const int lz = lane_id();
    const int gz = lz >> 4, fz = lz & 15;
    const int qb = (tn5 >> 4) * 224, ql = qb + 64 + (tn5 & 15) * 10;
    auto hof = [&](int e, int i) { return (unsigned)((e >> 2) * 131072 + (e & 3) * 256 + fz * 16 + i * 1024); };
    const unsigned ho0 = hof((grp ? ql + 2 : qb + (tn5 & 15) * 4) + 2 * (gz & 1) + (gz >> 1), 0), ho1 = hof(ql + 6 * grp + (gz >> 1), gz & 1), hom = hof(ql + 8 + grp, gz);
    char* hb = ws + WS_HH + (tm5 * 16 + wm * 4) * 1024;
    const int c5 = C5 + tm5 * 4 + (tn5 >> 4) * 2;
    auto tr16 = [](const unsigned (&o)[4]) {
      auto s0 = __builtin_amdgcn_permlane16_swap(o[0], o[2], false, false);
      auto s1 = __builtin_amdgcn_permlane16_swap(o[1], o[3], false, false);
      u32x4 v = {s0[0], s1[0], s0[1], s1[1]}; asm volatile("" : "+v"(v)); return v;
    };
    auto pair = [&](int p, int i) {
      unsigned o[4];
      swi4(acc[i][2 * p], acc[i][2 * p + 1], o[0], o[1]);
      swi4(acc[i + 1][2 * p], acc[i + 1][2 * p + 1], o[2], o[3]);
      return tr16(o);
    };
    u32x4 v[3];
#pragma unroll
    for (int i = 0; i < 4; ++i) {
      unsigned o[4];
      swi4(acc[i][0], acc[i][1], o[0], o[1]);
      swi4(acc[i][2], acc[i][3], o[2], o[3]);
      st16<true>(hb, ho0 + i * 1024, tr16(o));
      SCHED0();
    }
    STAMP_G(23, 28);
    v[0] = pair(2, 0);
    if (!grp) {
      wait_vm<0>();
      unsigned arr = 0;
      if (lz == 0) arr = __hip_atomic_fetch_add((LDS_PTR(unsigned))(smem + LDS_ARR), 1u, __ATOMIC_RELAXED, __HIP_MEMORY_SCOPE_WORKGROUP);
      if (__builtin_amdgcn_readfirstlane(arr) == 3) signal(cnt, c5, lz);
    }
    STAMP_G(24, 29);
    v[1] = pair(2, 2);
    { unsigned m[4];
#pragma unroll
      for (int i = 0; i < 4; i += 2) {
        f32x4 gv, uv;
#pragma unroll
        for (int e = 0; e < 4; ++e) {
          auto w = __builtin_amdgcn_permlane32_swap(__float_as_uint(acc[i + (e >> 1)][6][e & 1]), __float_as_uint(acc[i + (e >> 1)][6][2 + (e & 1)]), false, false);
          gv[e] = __uint_as_float(w[0]); uv[e] = __uint_as_float(w[1]);
        }
        swi4(gv, uv, m[i], m[i + 1]);
      }
      auto x0 = __builtin_amdgcn_permlane32_swap(m[0], m[2], false, false);
      auto x1 = __builtin_amdgcn_permlane32_swap(m[1], m[3], false, false);
      const unsigned o[4] = {x0[0], x0[1], x1[0], x1[1]};
      v[2] = tr16(o);
      st16<true>(hb, ho1, v[0]); st16<true>(hb, ho1 + 2048, v[1]); st16<true>(hb, hom, v[2]);
    }
    STAMP_G(25, 30);
    if (grp) {
#pragma unroll
      for (int t = 0; t < NDR; ++t) BAR();
      wait_vm<0>();
      BAR();
      if (wave == 4) signal(cnt, c5 + 1, lz);
      if (wave > 4) return;
#pragma unroll 1
      for (int t = NDR + 1; t < 9; ++t) BAR();
      unsigned seen = 16;
      const unsigned* pc = (lz & 1) ? (const unsigned*)(ws + WS_FLG) + (b ^ 4) * 32 : cnt + ((C5 + (tm6 >> 1) * 4 + kh6 * 2 + 1) * 8 + tn6) * 32;
      asm volatile("global_load_dword %0, %1, off sc1" : "=v"(seen) : "v"(pc) : "memory");
      BAR(); BAR();
      asm volatile("s_waitcnt vmcnt(0)" : "+v"(seen)::"memory");
      if (__builtin_amdgcn_ballot_w64(seen < 16) != 0) poll<0>(pc, 16);
      return;
    }
  }
  { const int lane = lane_id(), fr = lane & 15, g = lane >> 4, p4 = fr & 3, row = 4 * g + (fr >> 2);
    const int wm = wave & 1, wn = wave >> 1;
    const int lw0 = row * 128 + (((row >> 1) & 3) * 32) + p4 * 8;
    const int mb0 = tm6 * 8 + wm * 4, t64 = tn6 * 2 + kh6;
    f32x4 acc[4][4];
#pragma unroll
    for (int i = 0; i < 4; ++i)
#pragma unroll
      for (int j = 0; j < 4; ++j) { acc[i][j] = (f32x4){0.f, 0.f, 0.f, 0.f}; asm volatile("" : "+v"(acc[i][j])); }
    struct Fr { bf16x8 a[4]; bf16x4 w[4][2]; };
    const LDS_PTR(char) aw0 = smem + (lw0 ^ (wn * 64)); const LDS_PTR(char) aw1 = smem + (lw0 ^ (wn * 64 + 32));
    const LDS_PTR(char) aa = smem + K6_A + wm * 4096 + lane * 16;
    auto rd1 = [&](Fr& f, int r) {
      if (r < 2) f.w[0][r] = ld_tr4(aw0 + r * 2048);
      else if (r < 4) f.a[r - 2] = *(const LDS_PTR(bf16x8))(aa + (r - 2) * 1024);
      else if (r < 10) { const int j = (r - 2) >> 1; f.w[j][r & 1] = ld_tr4(((j & 1) ? aw1 : aw0) + (j >> 1) * 4096 + (r & 1) * 2048); }
      else f.a[r - 8] = *(const LDS_PTR(bf16x8))(aa + (r - 8) * 1024);
    };
    int dl, da, wrap = -(K6_NW - 1) * 8192, wrapa = -(K6_NA - 1) * 8192;
    unsigned mask = wrap_mask(1, K6_NW), maska = wrap_mask(1, K6_NA);
    Fr fA, fB;
    asm volatile("" : "+s"(wrap), "+s"(wrapa), "+v"(aw0), "+v"(aw1), "+v"(aa));
    wait_vm<0>();
    BAR();
    STAMP_AT(32 );
#pragma unroll
    for (int r = 0; r < 12; ++r) { SCHED0(); rd1(fA, r); }
    SCHED0();
    aw0 += 8192; aw1 += 8192; aa += 8192;
    wait_lgkm<6>();
    BAR();
    auto trip = [&](const Fr& cur, Fr& nxt) {
      SCHED0();
#pragma unroll
      for (int m = 0; m < 16; ++m) {
        const int j = (m & 7) >> 1, i = (m >> 3) * 2 + ((m ^ j) & 1);
        if (m == 2) mfma_sel<8192>(acc[i][j], cat8(cur.w[j][0], cur.w[j][1]), cur.a[i], mask, dl, wrap);
        else if (m == 6) mfma_sel<8192>(acc[i][j], cat8(cur.w[j][0], cur.w[j][1]), cur.a[i], maska, da, wrapa);
        else mfma_acc(acc[i][j], cat8(cur.w[j][0], cur.w[j][1]), cur.a[i]);
        SCHED0();
        if (m < 12) { rd1(nxt, m); SCHED0(); }
        if (m == 3) wait_lgkm<4>();
        if (m == 12) { aw0 += dl; aw1 += dl; asm volatile("" : "+v"(aw0), "+v"(aw1)); }
        if (m == 13) { aa += da; asm volatile("" : "+v"(aa)); }
        if (m == 14) wait_lgkm<6>();
        if (m == 15) BAR();
        SCHED0();
      }
    };
#pragma unroll 1
    for (int h = 0; h < 2; ++h) {
#pragma unroll 1
      for (int t = 0; t < 7; ++t) { trip(fA, fB); trip(fB, fA); trip(fA, fB); trip(fB, fA); }
      mask = wrap_mask(29, K6_NW); maska = wrap_mask(29, K6_NA);
    }
    mfma_done(acc);
    STAMP_AT(34 );
    const int slot = wm * 2 + wn, pb = (b ^ 4) * 4 + slot;
    { auto pk = [](float lo, float hi) { const unsigned u = pk2(lo, hi); return u < 0xfffffffeu ? u : 0xfffffffeu; };
#pragma unroll
      for (int i = 0; i < 4; ++i)
        st16<true>(ws + WS_PART, (b * 4 + slot) * 4096 + i * 1024 + lane * 16,
                   (u32x4){pk(acc[i][0][0], acc[i][0][1]), pk(acc[i][0][2], acc[i][0][3]), pk(acc[i][1][0], acc[i][1][1]), pk(acc[i][1][2], acc[i][1][3])});
      STAMP_AT(35 );
    }
    { const LDS_PTR(float) vec = (const LDS_PTR(float))(smem + LDS_VEC) + 192 + wn * 32 + 4 * g;
      const f32x4 gv0 = *(const LDS_PTR(f32x4))vec, gv1 = *(const LDS_PTR(f32x4))(vec + 16);
      u32x4 rv[4];
#pragma unroll
      for (int i = 0; i < 4; ++i) { rv[i] = *(const LDS_PTR(u32x4))(smem + K6_X + (wm * 4 + i) * 2048 + wn * 1024 + lane * 16); asm volatile("" : "+v"(rv[i])); }
      u32x4 z[4];
      __builtin_amdgcn_s_sleep(PD6);
#pragma unroll 1
      for (unsigned spins = 0; spins < SPIN_CAP; ++spins) {
        asm volatile("" ::: "memory");
        unsigned m = 0;
#pragma unroll
        for (int i = 0; i < 4; ++i) z[i] = ld16<true>(ws + WS_PART, pb * 4096 + i * 1024 + lane * 16);
#pragma unroll
        for (int i = 0; i < 16; ++i) m = m > z[i >> 2][i & 3] ? m : z[i >> 2][i & 3];
        if (__builtin_amdgcn_ballot_w64(m == ~0u) == 0) break;
        __builtin_amdgcn_s_sleep(PS6);
      }
      STAMP_AT(36 );
#pragma unroll
      for (int i = 0; i < 4; ++i) {
        const f32x4 v0 = (acc[i][2] + (f32x4){lo2f(z[i][0]), hi2f(z[i][0]), lo2f(z[i][1]), hi2f(z[i][1])}) * gv0;
        const f32x4 v1 = (acc[i][3] + (f32x4){lo2f(z[i][2]), hi2f(z[i][2]), lo2f(z[i][3]), hi2f(z[i][3])}) * gv1;
        const unsigned o0 = pk2(lo2f(rv[i][0]) + v0[0], hi2f(rv[i][0]) + v0[1]), o1 = pk2(lo2f(rv[i][1]) + v0[2], hi2f(rv[i][1]) + v0[3]);
        const unsigned o2 = pk2(lo2f(rv[i][2]) + v1[0], hi2f(rv[i][2]) + v1[1]), o3 = pk2(lo2f(rv[i][3]) + v1[2], hi2f(rv[i][3]) + v1[3]);
        auto s0 = __builtin_amdgcn_permlane16_swap(o0, o2, false, false);
        auto s1 = __builtin_amdgcn_permlane16_swap(o1, o3, false, false);
        st16<false>(out, (((mb0 + i) * 16 + fr) * D_ + t64 * 64 + wn * 32 + (g & 1) * 16 + (g >> 1) * 8) * 2, (u32x4){s0[0], s1[0], s0[1], s1[1]});
      }
    }
    STAMP_AT(37 );
  }
}

extern "C" void kernel_launch(void* const* d_in, const int* in_sizes, int n_in, void* d_out, int out_size, void* d_ws,
                              size_t ws_size, hipStream_t stream) {
  const u16* x = (const u16*)d_in[0];
  const u16* wqkv = (const u16*)d_in[1];
  const u16* wo = (const u16*)d_in[2];
  const u16* w1 = (const u16*)d_in[4];
  const u16* w3 = (const u16*)d_in[5];
  const u16* w2 = (const u16*)d_in[6];
  const u16* c = (const u16*)d_in[7];
  const u16* wmod = (const u16*)d_in[8];
  (void)hipFuncSetAttribute((const void*)k2_block, hipFuncAttributeMaxDynamicSharedMemorySize, MEGA_LDS);
  char* ws = (char*)d_ws;
  k2_block<<<256, 768, MEGA_LDS, stream>>>(x, wqkv, wo, w1, w3, w2, c, wmod, (u16*)d_out, ws);
}
```

```cpp
#include <hip/hip_runtime.h>
#include <stdint.h>
#include <type_traits>
#pragma clang diagnostic ignored "-Winline-asm"

typedef unsigned short u16;
typedef short bf16x8 __attribute__((ext_vector_type(8)));
typedef short bf16x4 __attribute__((ext_vector_type(4)));
typedef float f32x4 __attribute__((ext_vector_type(4)));
typedef float f32x2 __attribute__((ext_vector_type(2)));
typedef unsigned int u32x4 __attribute__((ext_vector_type(4)));
typedef unsigned int u32x2 __attribute__((ext_vector_type(2)));
#define LDS_PTR(T) __attribute__((address_space(3))) T*
#define BAR() do { asm volatile("" ::: "memory"); __builtin_amdgcn_s_barrier(); asm volatile("" ::: "memory"); } while (0)
#define SCHED0() __builtin_amdgcn_sched_barrier(0)
template <int N> using IC = std::integral_constant<int, N>;
typedef std::integral_constant<bool, true> T_;
typedef std::integral_constant<bool, false> F__;

constexpr int S_ = 2048, D_ = 1024, F_ = 3584;
#define STAMP_AT(i)
#define STAMP_G(ia, ib)
#define STAMP_L(i)

__device__ __forceinline__ unsigned pk2(float lo, float hi) {
  typedef __bf16 bf2 __attribute__((ext_vector_type(2)));
  f32x2 v = {lo, hi}; bf2 r = __builtin_convertvector(v, bf2); return __builtin_bit_cast(unsigned, r);
}
__device__ __forceinline__ float lo2f(unsigned u) { return __uint_as_float(u << 16); }
__device__ __forceinline__ float hi2f(unsigned u) { return __uint_as_float(u & 0xffff0000u); }
__device__ __forceinline__ float rbf(float v) { return lo2f(pk2(v, 0.f)); }
#define glds16(sbase, voff, lds_off, imm) \
  asm volatile("s_add_u32 m0, %2, %3\n\ts_nop 0\n\tglobal_load_lds_dwordx4 %0, %1" ::"v"(voff), "s"(sbase), "s"(lds_off), "n"(imm) : "memory", "scc")
#define glds16s(sbase, voff, lds_off, imm) \
  asm volatile("s_add_u32 m0, %2, %3\n\ts_nop 0\n\tglobal_load_lds_dwordx4 %0, %1 sc1" ::"v"(voff), "s"(sbase), "s"(lds_off), "n"(imm) : "memory", "scc")
#define GLDS4(SC) "s_mov_b32 m0, %6\n\ts_nop 0\n\tglobal_load_lds_dwordx4 %0, %4" SC "\n\tglobal_load_lds_dwordx4 %1, %5 offset:1024" SC \
                  "\n\tglobal_load_lds_dwordx4 %2, %4 offset:2048" SC "\n\tglobal_load_lds_dwordx4 %3, %5 offset:3072" SC
template <bool SC1> __device__ __forceinline__ void glds16x4(const void* s0, const void* s1, const unsigned (&v)[4], unsigned lds) {
  if (SC1) asm volatile(GLDS4(" sc1") ::"v"(v[0]), "v"(v[1]), "v"(v[2]), "v"(v[3]), "s"(s0), "s"(s1), "s"(lds) : "memory");
  else asm volatile(GLDS4("") ::"v"(v[0]), "v"(v[1]), "v"(v[2]), "v"(v[3]), "s"(s0), "s"(s1), "s"(lds) : "memory");
}
__device__ __forceinline__ void glds4(const void* sbase, unsigned voff, unsigned lds_off) {
  asm volatile("s_mov_b32 m0, %2\n\ts_nop 0\n\tglobal_load_lds_dword %0, %1" ::"v"(voff), "s"(sbase), "s"(lds_off) : "memory");
}
__device__ __forceinline__ void stouch4(const void* p) {
  asm volatile("s_load_dwordx2 s[100:101], %0, 0x3c\n\ts_load_dwordx2 s[100:101], %0, 0xbc\n\ts_load_dwordx2 s[100:101], %0, 0x13c\n\ts_load_dwordx2 s[100:101], %0, 0x1bc"
               ::"s"(p) : "memory", "s100", "s101");
}
template <bool SC1> __device__ __forceinline__ void st16(void* sbase, unsigned voff, u32x4 v) {
  __builtin_amdgcn_raw_buffer_store_b128(v, __builtin_amdgcn_make_buffer_rsrc(sbase, 0, 0x7fffffff, 0x00020000), voff, 0, SC1 ? 16 : 0);
}
template <bool SC1> __device__ __forceinline__ u32x4 ld16(const void* sbase, unsigned voff) {
  return __builtin_amdgcn_raw_buffer_load_b128(__builtin_amdgcn_make_buffer_rsrc((void*)sbase, 0, 0x7fffffff, 0x00020000), voff, 0, SC1 ? 16 : 0);
}
template <int N> __device__ __forceinline__ void wait_vm() { asm volatile("s_waitcnt vmcnt(%0)" ::"n"(N) : "memory"); }
__device__ __forceinline__ void wait_lgkm0() { asm volatile("" ::: "memory"); __builtin_amdgcn_s_waitcnt(0xC07F); asm volatile("" ::: "memory"); }
__device__ __forceinline__ bf16x4 ld_tr4(const LDS_PTR(char) p) { return __builtin_amdgcn_ds_read_tr16_b64_v4i16((LDS_PTR(bf16x4))p); }
__device__ __forceinline__ bf16x8 cat8(bf16x4 lo, bf16x4 hi) { return __builtin_shufflevector(lo, hi, 0, 1, 2, 3, 4, 5, 6, 7); }
__device__ __forceinline__ void mfma_acc(f32x4& acc, bf16x8 a, bf16x8 b) { asm volatile("v_mfma_f32_16x16x32_bf16 %0, %1, %2, %0" : "+v"(acc) : "v"(a), "v"(b)); }
template <int STEP> __device__ __forceinline__ void mfma_sel(f32x4& acc, bf16x8 a, bf16x8 b, unsigned& mask, int& dl, int wrap) {
  asm volatile("s_add_u32 %1, %1, %1\n\tv_mfma_f32_16x16x32_bf16 %0, %3, %4, %0\n\ts_cselect_b32 %2, %5, %6" : "+v"(acc), "+s"(mask), "=s"(dl) : "v"(a), "v"(b), "s"(wrap), "n"(STEP) : "scc");
}
constexpr unsigned wrap_mask(int first, int ns) { unsigned m = 0; for (int t = 0; t < 32; ++t) if ((first + t) % ns == ns - 1) m |= 0x80000000u >> t; return m; }
template <int N> __device__ __forceinline__ void wait_lgkm() { __builtin_amdgcn_s_waitcnt(0xC07F | (N << 8)); }
template <int R, int C> __device__ __forceinline__ void mfma_done(f32x4 (&acc)[R][C]) {
  asm volatile("s_nop 9");
#pragma unroll
  for (int i = 0; i < R; ++i)
#pragma unroll
    for (int j = 0; j < C; ++j) asm volatile("" : "+v"(acc[i][j]));
}
__device__ __forceinline__ int lane_id() { int l; asm volatile("v_mbcnt_lo_u32_b32 %0, -1, 0\n\tv_mbcnt_hi_u32_b32 %0, -1, %0" : "=v"(l)); return l; }
__device__ __forceinline__ float xsum16_32(float v) {
  auto a = __builtin_amdgcn_permlane16_swap(__float_as_uint(v), __float_as_uint(v), false, false); v = __uint_as_float(a[0]) + __uint_as_float(a[1]);
  auto c = __builtin_amdgcn_permlane32_swap(__float_as_uint(v), __float_as_uint(v), false, false); return __uint_as_float(c[0]) + __uint_as_float(c[1]);
}

__device__ __forceinline__ float ln_rstd(float var) { return __builtin_amdgcn_rcpf(rbf(__builtin_amdgcn_sqrtf(rbf(rbf(var) + 1e-5f)))); }

constexpr size_t WS_MOD = 0;
constexpr size_t WS_EPO = 64 << 10, WS_FL1 = 96 << 10;
constexpr size_t WS_CTL = 128 << 10;
constexpr int C2 = 0, C3 = 16, C4 = 32, C5 = 48, CM = 80;
constexpr size_t WS_FLG = WS_CTL + (88 << 10), CTL_BYTES = 120 << 10;
constexpr size_t WS_XCH = 512 << 10;
constexpr size_t WS_MOD2 = 1 << 20;
constexpr size_t WS_H = 2 << 20, WS_O = WS_H + (4 << 20), WS_H2 = WS_O + (4 << 20), WS_HH = WS_H2 + (4 << 20),
                 WS_PART = WS_HH + (size_t)S_ * F_ * 2;

template <int NL> __device__ __forceinline__ f32x2 slab_sum(const unsigned (&mc)[NL / 2], const u32x4 (&mw)[NL]) {
  f32x2 a2[4] = {};
#pragma unroll
  for (int i = 0; i < NL; ++i) {
    const float cv = (i & 1) ? hi2f(mc[i >> 1]) : lo2f(mc[i >> 1]);
    const float sv = rbf(cv * __builtin_amdgcn_rcpf(1.f + __builtin_amdgcn_exp2f(cv * -1.4426950408889634f)));
#pragma unroll
    for (int j = 0; j < 4; ++j) a2[j] += (f32x2){lo2f(mw[i][j]), hi2f(mw[i][j])} * sv;
  }
  float acc[8] = {a2[0][0], a2[0][1], a2[1][0], a2[1][1], a2[2][0], a2[2][1], a2[3][0], a2[3][1]};
#pragma unroll
  for (int j = 0; j < 4; ++j) { auto a = __builtin_amdgcn_permlane32_swap(__float_as_uint(acc[j]), __float_as_uint(acc[j + 4]), false, false); acc[j] = __uint_as_float(a[0]) + __uint_as_float(a[1]); }
#pragma unroll
  for (int j = 0; j < 2; ++j) { auto a = __builtin_amdgcn_permlane16_swap(__float_as_uint(acc[j]), __float_as_uint(acc[j + 2]), false, false); acc[j] = __uint_as_float(a[0]) + __uint_as_float(a[1]); }
#pragma unroll
  for (int j = 0; j < 2; ++j) acc[j] += __uint_as_float(__builtin_amdgcn_mov_dpp(__float_as_uint(acc[j]), 0x128, 0xf, 0xf, false));
  return (f32x2){acc[0], acc[1]};
}

constexpr int K3_B = 18432, K3_STG = 18432 + 24576;
constexpr int K4_RED = 57344;
constexpr int K5_G = 16384, K5_U = 16384 + 7168 + 16, K5_STG = 30736, K5_NS = 5;
constexpr int K6_NW = 12, K6_NA = 7, K6_A = K6_NW * 8192, K6_X = 9 * 8192;
constexpr int LDS_VEC = 158720, LDS_DUMMY = LDS_VEC + 1024, LDS_ARR = LDS_DUMMY + 256, MEGA_LDS = LDS_ARR + 64;
constexpr unsigned SPIN_CAP = 1u << 19;
#define KNOBS XD0 = 16, PFD = 16, FD1 = 8, FS0 = 4, PQ = 2, PQ5 = 4, PD2 = 4, PD3 = 4, PD4 = 4, PD5 = 48, PD6 = 6, PS6 = 1, XS0 = 14, XS1 = 4, TS5 = 2, VB3 = 2, SLB = 15, NDR = 1
enum { KNOBS };
constexpr unsigned long long SKIP6 = 0x7c;
static_assert(__builtin_popcountll(SKIP6) == 5, "44 stages of W are issued behind 49 barriers");

__device__ __forceinline__ void signal(unsigned* cnt, int c, int lane, unsigned v = 1) {
  if (lane < 8) __hip_atomic_fetch_add(cnt + (c * 8 + lane) * 32, v, __ATOMIC_RELAXED, __HIP_MEMORY_SCOPE_AGENT);
}
template <int D0, int D1 = 1> __device__ __forceinline__ void poll(const unsigned* cnt, unsigned want) {
  if (D0) __builtin_amdgcn_s_sleep(D0);
#pragma unroll 1
  for (unsigned spins = 0; spins < SPIN_CAP; ++spins) {
    if (__builtin_amdgcn_ballot_w64(__hip_atomic_load(cnt, __ATOMIC_RELAXED, __HIP_MEMORY_SCOPE_AGENT) < want) == 0) break;
    __builtin_amdgcn_s_sleep(D1);
  }
  __builtin_amdgcn_fence(__ATOMIC_ACQUIRE, "wavefront");
}

template <int D0, int D1, int EX> __device__ __forceinline__ void pollp(const unsigned* cnt, unsigned want, unsigned (&r)[4]) {
  unsigned n = SPIN_CAP;
#define PL1(a) "global_atomic_add %" #a ", %5, %9, off sc0\n\ts_sleep %8\n\t"
#define PL2(a, c) "global_atomic_add %" #a ", %5, %9, off sc0\n\ts_waitcnt vmcnt(3)\n\tv_cmp_gt_u32 vcc, %6, %" #c "\n\ts_cbranch_vccz 2f\n\ts_sleep %8\n\t"
  asm volatile("s_mov_b64 exec, %10\n\ts_sleep %7\n\t" PL1(0) PL1(1) PL1(2) "1:\n\t" PL2(3, 0) PL2(0, 1) PL2(1, 2) PL2(2, 3) "s_sub_u32 %4, %4, 1\n\ts_cbranch_scc0 1b\n2:\n\ts_mov_b64 exec, -1"
               : "=&v"(r[0]), "=&v"(r[1]), "=&v"(r[2]), "=&v"(r[3]), "+s"(n) : "v"(cnt), "s"(want), "n"(D0), "n"(D1), "v"(0), "n"(EX) : "memory", "vcc", "scc");
}
#define KEEP4(r) asm volatile("; keep %0 %1 %2 %3" ::"v"(r[0]), "v"(r[1]), "v"(r[2]), "v"(r[3]))
typedef float f32x32 __attribute__((ext_vector_type(32)));
typedef float f32x16 __attribute__((ext_vector_type(16)));
#define M5(i, j) "v_mfma_f32_16x16x32_bf16 v[28*" #i "+4*" #j ":28*" #i "+4*" #j "+3], v[128+4*" #j ":131+4*" #j "], v[112+4*" #i ":115+4*" #i "], v[28*" #i "+4*" #j ":28*" #i "+4*" #j "+3]\n\t"
#define RA5(i) "ds_read_b128 v[112+4*" #i ":115+4*" #i "], %[aA] offset:1024*" #i "\n\t"
#define RW5(j, h, a, o) "ds_read_b64_tr_b16 v[128+4*" #j "+2*" #h ":129+4*" #j "+2*" #h "], %[" #a "] offset:" #o "\n\t"
#define ST5(a) "v_add_u32 %[" #a "], %[dl], %[" #a "]\n\t"
#define SEL5A "s_add_u32 %[mask], %[mask], %[mask]\n\t"
#define SEL5B "s_cselect_b32 %[dl], %[wrap], 30736\n\t"
#define LG(n) "s_waitcnt lgkmcnt(" #n ")\n\t"
#define RP5(j, h) RW5(j, h, aP, 32*(j/2)+7184*(j&1)+3584*h)
#define TRIP5(X)                                                                                                                                        \
  M5(0, 0) RW5(6, 1, aM, 3584) M5(1, 0) RA5(2) ST5(aM) M5(1, 1) RA5(3) ST5(aP) M5(0, 1) ST5(aA) LG(6) M5(0, 2) M5(1, 2) SEL5A M5(1, 3) SEL5B              \
  M5(0, 3) M5(0, 4) M5(1, 4) LG(0) M5(1, 5) M5(0, 5) "s_barrier\n\t" M5(0, 6) RA5(0) M5(1, 6) RA5(1)                                                      \
  M5(2, 0) X M5(3, 0) RP5(0, 0) M5(3, 1) RP5(0, 1) M5(2, 1) RP5(1, 0) M5(2, 2) RP5(1, 1) M5(3, 2) RP5(2, 0) M5(3, 3) RP5(2, 1) M5(2, 3) RP5(3, 0)          \
  M5(2, 4) RP5(3, 1) M5(3, 4) RP5(4, 0) M5(3, 5) RP5(4, 1) M5(2, 5) RP5(5, 0) M5(2, 6) RP5(5, 1) LG(8) M5(3, 6) RW5(6, 0, aM, 0)
#define ACC5 "+{v[0:31]}"(c0), "+{v[32:63]}"(c1), "+{v[64:95]}"(c2), "+{v[96:111]}"(c3)
#define CLOB5 "memory", "scc", "v112", "v113", "v114", "v115", "v116", "v117", "v118", "v119", "v120", "v121", "v122", "v123", "v124", "v125", "v126", "v127", "v128", "v129", "v130", "v131", "v132", "v133", \
              "v134", "v135", "v136", "v137", "v138", "v139", "v140", "v141", "v142", "v143", "v144", "v145", "v146", "v147", "v148", "v149", "v150", "v151", "v152", "v153", "v154", "v155"
static_assert(K5_STG == 30736 && K5_U - K5_G == 7184, "TRIP5");

#define C3(i, j) "v[24*" #i "+4*" #j "-12*(" #i "/4):24*" #i "+4*" #j "-12*(" #i "/4)+3]"
#define M3(i, j) "v_mfma_f32_16x16x32_bf16 " C3(i, j) ", v[124+4*" #j ":127+4*" #j "], v[104+4*" #i ":107+4*" #i "], " C3(i, j) "\n\t"
#define RA3(i, u) "ds_read_b128 v[104+4*" #i ":107+4*" #i "], %[aA] offset:1024*" #i "+9216*" #u "\n\t"
#define RH3(u) "ds_read_b128 v[120:123], %[aH] offset:9216*" #u "\n\t"
#define RW3(j, h, a, u) "ds_read_b64_tr_b16 v[124+4*" #j "+2*" #h ":125+4*" #j "+2*" #h "], %[" #a "] offset:8192*(" #j "/2)+2048*" #h "+4096*" #u "\n\t"
#define ST3(a, d) "v_add_u32 %[" #a "], " #d ", %[" #a "]\n\t"
#define H2_3(u, S0, S1, S2, B) M3(0, 0) RW3(5, 1, a1, u) M3(1, 0) RA3(2, u) M3(1, 1) RA3(3, u) S0 M3(0, 1) LG(7) S1 M3(0, 2) S2 M3(1, 2) M3(1, 3) M3(0, 3) LG(2) M3(0, 4) M3(1, 4) LG(0) B
#define H1_3(n, X)                                                                                                                                      \
  M3(1, 5) RA3(1, n) M3(0, 5) RA3(0, n) M3(2, 0) X M3(3, 0) RW3(0, 0, a0, n) M3(3, 1) RW3(0, 1, a0, n) M3(2, 1) RW3(1, 0, a1, n) M3(2, 2) RW3(1, 1, a1, n) \
  M3(3, 2) RW3(2, 0, a1, n) M3(3, 3) RW3(2, 1, a1, n) M3(2, 3) M3(4, 3) RW3(3, 0, a0, n) M3(4, 4) RW3(3, 1, a0, n) M3(2, 4) RH3(n)                         \
  M3(3, 4) RW3(4, 0, a0, n) M3(3, 5) RW3(4, 1, a0, n) LG(7) M3(2, 5) RW3(5, 0, a1, n)
#define STAGE3(d, X) H2_3(0, , , , ) H1_3(1, ) H2_3(1, ST3(a0, d), ST3(aA, d), ST3(a1, d) ST3(aH, d), "s_barrier\n\t") H1_3(0, X)
#define CV3(i, j, t) "v_cvt_pk_bf16_f32 v[104+2*" #t "], v[24*" #i "+4*" #j "-12*(" #i "/4)], v[24*" #i "+4*" #j "-12*(" #i "/4)+1]\n\tv_cvt_pk_bf16_f32 v[105+2*" #t "], v[24*" #i "+4*" #j "-12*(" #i "/4)+2], v[24*" #i "+4*" #j "-12*(" #i "/4)+3]\n\t"
#define WR3(i, t, a, o) "ds_write_b64 %[" #a "], v[104+2*" #t ":105+2*" #t "] offset:2048*" #i "+" #o "\n\t"
#define CW3(i, j, t, a, o) CV3(i, j, t) WR3(i, t, a, o)
#define V8(a) "v" #a "0", "v" #a "1", "v" #a "2", "v" #a "3", "v" #a "4", "v" #a "5", "v" #a "6", "v" #a "7"
#define V10(a) V8(a), "v" #a "8", "v" #a "9"
static_assert(K3_STG == 43008, "STAGE3");
__device__ __forceinline__ int w2_stage(int j) {
  const int j2 = j - 16, p = (j2 * 52) >> 8, r = j2 - p * 5;
  return j < 16 ? j * 112 * 2048 : (p * 224 + 32 + r * 32 + (r > 2 ? 32 : 0)) * 2048 + (r == 2);
}
__global__ __launch_bounds__(768, 3) __attribute__((amdgpu_num_sgpr(98))) void k2_block(const u16* __restrict__ x, const u16* __restrict__ wqkv, const u16* __restrict__ wo,
                                                   const u16* __restrict__ w1, const u16* __restrict__ w3, const u16* __restrict__ w2,
                                                   const u16* __restrict__ cnd, const u16* __restrict__ wmod, u16* __restrict__ out, char* __restrict__ ws) {
  extern __shared__ __attribute__((aligned(16))) char smem_[];
  LDS_PTR(char) smem = (LDS_PTR(char))smem_;
  const int tid = threadIdx.x;
  int lane = tid & 63;
  const int wave = __builtin_amdgcn_readfirstlane(tid >> 6);
  const unsigned lds_base = (unsigned)(uintptr_t)smem;
  const int b = blockIdx.x, xc = b & 7, ii = b >> 3;
  const int tm3 = (xc & 1) * 8 + (ii & 7), tn3 = (xc >> 1) * 4 + (ii >> 3);
  const int tm5 = (xc & 1) * 4 + (ii & 3), tn5 = (xc >> 1) * 8 + (ii >> 2);
  const int kh6 = xc >> 2, tm6 = (xc & 1) * 8 + (xc & 2) * 2 + (ii & 3), tn6 = ii >> 2;
  const int tm4 = tm6, tn4 = tn6 * 2 + kh6;
  const int u1 = (b >> 4) * 8 + xc;
  unsigned* cnt = (unsigned*)(ws + WS_CTL);
  unsigned long long* fl1 = (unsigned long long*)(ws + WS_FL1);
  STAMP_AT(0 );

  if (wave >= 8) {
    const int lw = wave - 8, P = b * 4 + lw, lane = lane_id();
    const bool isA = lw < 2;
    asm volatile("" ::"s"(wqkv), "s"(wo), "s"(w1), "s"(w3), "s"(w2), "s"(ws));
    int kpf = -4;
    unsigned pr[4] = {};
    auto touch = [&]() {
      if (kpf < 28 && __builtin_amdgcn_s_getreg((3 << 11) | (8 << 6) | 7) <= 7) {
        stouch4(kpf < 0 ? (const char*)wo + ((kpf + 4) * 4096 + P * 4) * 128 : (const char*)((P & 1) ? w3 : w1) + (kpf * 2048 + (P >> 1) * 4) * 128);
        ++kpf;
      }
    };
    const char* epo = ws + WS_EPO;
    const unsigned swz = ((lane & 7) ^ ((lane >> 3) & 6)) * 16;
    const int cg = lw * 16 + tn4;
    auto issue4 = [&](int s, unsigned slot) {
      const char* p = (const char*)wo + tn4 * 128 + (lw * 32 + s * 128) * 2048;
      unsigned v4[4];
#pragma unroll
      for (int i = 0; i < 4; ++i) v4[i] = (lane >> 3) * 2048 + swz + i * 15360;
      glds16x4<false>(p, p, v4, lds_base + slot + lw * 4096);
    };
    auto run3 = [&](auto isA_) {
      constexpr bool A_ = decltype(isA_)::value; constexpr int N = A_ ? 9 : 12;
      const char* sp = A_ ? ws + WS_H + lw * 131072 + (tm3 * 8 - 1) * 1024 : (const char*)wqkv + tn3 * 128;
      unsigned vo[3][4];
#pragma unroll
      for (int i = 0; i < 12; ++i) {
        const int tb = (lw - 2) * 12 + i;
        vo[i >> 2][i & 3] = A_ ? lane * 16 + (i < 8 ? 1024 + (i >> 2) * 4096 : tm3 ? 0 : 1024) : ((tb & 7) * 8 + (lane >> 3)) * 6144 + (tb >> 3) * 2048 - (i & 3) * 1024 + swz;
      }
      const unsigned d0 = lds_base + (A_ ? lw * 9216 : K3_B + (lw - 2) * 12288);
      auto issue = [&](unsigned st) {
        if (A_) { glds16x4<true>(sp, sp, vo[0], d0 + st + 1024); glds16s(sp, vo[2][0], d0 + st, 0); glds16x4<true>(sp, sp, vo[1], d0 + st + 5120); }
        else {
#pragma unroll
          for (int i = 0; i < 3; ++i) glds16x4<false>(sp, sp, vo[i], d0 + st + i * 4096);
        }
        sp += A_ ? 262144 : 64 * 6144;
      };
      { unsigned long long ep;
        asm volatile("s_load_dwordx2 %0, %1, 0x0\n\ts_waitcnt lgkmcnt(0)" : "=s"(ep) : "s"(epo) : "memory");
        ep += 1;
        if (lw == 3) { *(LDS_PTR(u32x4))(smem + LDS_ARR) = (u32x4){0u, 0u, 0u, 0u}; wait_lgkm0(); }
        if (lw == 1) {
#pragma unroll
          for (int k = 0; k < 3; ++k) {
            const int i = b * 158 + (k * 64 + lane < 158 ? k * 64 + lane : 0);
            st16<true>(ws, i < 32768 ? (unsigned)WS_XCH + i * 16 : (unsigned)WS_CTL + (i - 32768) * 16, i < 32768 ? (u32x4){~0u, ~0u, ~0u, ~0u} : (u32x4){0u, 0u, 0u, 0u});
          }
        }
        BAR();
        auto flag = [&](int idx) { wait_vm<0>(); if (lane < 8) __hip_atomic_store(fl1 + lane * 256 + idx, ep, __ATOMIC_RELAXED, __HIP_MEMORY_SCOPE_AGENT); };
        if (lw == 1 && (b & 8)) {
          const int kq = u1 & 3, cgp = u1 >> 2;
          float sm = 0.f;
#pragma unroll
          for (int r = 0; r < 8; ++r) sm += *(const LDS_PTR(float))(smem + 8192 + (r * 64 + lane) * 4);
          __hip_atomic_store((unsigned*)(ws + WS_MOD) + kq * 2048 + cgp * 64 + lane, __float_as_uint(sm), __ATOMIC_RELAXED, __HIP_MEMORY_SCOPE_AGENT);
          flag(((cgp >> 3) & 1) * 64 + (cgp >> 4) * 32 + (cgp & 7) * 4 + kq);
        }
        if (b & 8) BAR();
        BAR();
        if (lw == 1 && !(b & 8)) flag(128 + u1);
        auto flags = [&](auto n_, int first) {
#pragma unroll 1
          for (unsigned spins = 0; spins < SPIN_CAP; ++spins) {
            bool ok = true;
#pragma unroll
            for (int k = 0; k < decltype(n_)::value; ++k) ok &= __hip_atomic_load(fl1 + xc * 256 + (first + k) * 64 + lane, __ATOMIC_RELAXED, __HIP_MEMORY_SCOPE_AGENT) == ep;
            if (__builtin_amdgcn_ballot_w64(!ok) == 0) break;
            __builtin_amdgcn_s_sleep(FS0);
          }
        };
        if (lw == 0) flags(IC<1>(), (b >> 3) & 1);
        BAR();
        if (lw == 1) {
          __builtin_amdgcn_s_sleep(FD1);
          flags(IC<4>(), 0);
          if (b == 0 && lane == 0) __hip_atomic_store((unsigned long long*)(ws + WS_EPO), ep, __ATOMIC_RELAXED, __HIP_MEMORY_SCOPE_AGENT);
        }
      }
      BAR();
      if (lw == 0) {
        signal(cnt, C2 + (u1 >> 3), lane, (b & 8) ? 1u : 0x10000u);
        pollp<PD2, PQ, 3>(cnt + ((C2 + ((lane & 1) && tm3 ? tm3 - 1 : tm3)) * 8 + (tn3 & 7)) * 32, 0x80000u, pr);
      }
      if (!A_) { issue(0); issue(K3_STG); }
      touch();
      BAR();
      if (A_) { issue(0); issue(K3_STG); }
      wait_vm<N>();
      KEEP4(pr);
      BAR();
      issue(2 * K3_STG);
      unsigned st = 0;
#pragma unroll 1
      for (int j = 0; j < 13; ++j) {
        wait_vm<N>();
        BAR();
        issue(st);
        st = st == 2 * K3_STG ? 0 : st + K3_STG;
        touch();
      }
      wait_vm<N>();
      BAR();
      issue4(0, 57344); issue4(5, 57344 + 5 * 16384);
      if (kpf < 0) kpf = 0;
      {
        glds4((P & 1) ? w3 : w1, ((kpf + ((lane >> 2) < VB3 ? lane >> 2 : 0)) * 2048 + (P >> 1) * 4 + (lane & 3)) * 128, lds_base + LDS_DUMMY); kpf += VB3; }
      wait_vm<9>();
    };
    if (isA) run3(T_()); else run3(F__());
    { BAR();
      BAR();
      issue4(1, 57344 + 16384);
      BAR();
      issue4(2, 57344 + 2 * 16384); issue4(3, 57344 + 3 * 16384); issue4(4, 57344 + 4 * 16384);
      BAR();
      if (lw == 0) signal(cnt, C3 + tm3, lane);
      issue4(6, 0); issue4(7, 16384);
      if (lw == 0) pollp<PD3, PQ, 1>(cnt + ((C3 + tm4) * 8 + tn6) * 32, 16, pr);
      wait_vm<8>();
      BAR();
      wait_vm<0>();
      KEEP4(pr);
      BAR();
      poll<0, 4>(cnt + (CM * 8 + cg) * 32, 4);
      f32x4 pv[4];
#pragma unroll
      for (int i = 0; i < 4; ++i) pv[i] = __builtin_bit_cast(f32x4, ld16<true>(ws + WS_MOD2, (((lane >> 4) * 4 + i) * 4096 + cg * 64 + (lane & 15) * 4) * 4));
      f32x4 v = (pv[0] + pv[1]) + (pv[2] + pv[3]);
#pragma unroll
      for (int e = 0; e < 4; ++e) { v[e] = rbf(xsum16_32(v[e])); if (lw == 2) v[e] = rbf(1.f + v[e]); if (lw == 1 || lw == 2) v[e] *= -1.4426950408889634f; }
      *(LDS_PTR(f32x4))(smem + LDS_VEC + lw * 256 + (lane & 15) * 16) = v;
      wait_lgkm0();
    }
    auto run5 = [&](auto isA_) {
      constexpr bool A_ = decltype(isA_)::value; constexpr int N = A_ ? 8 : 7;
      const char* sp = A_ ? ws + WS_H2 + (tm5 * 16 + lw * 8) * 1024 : (const char*)(lw == 2 ? w1 : w3) + tn5 * 224;
      unsigned vp[N];
#pragma unroll
      for (int i = 0; i < N; ++i) { const int L = i * 64 + lane, r = L / 14, c = L - r * 14; vp[i] = A_ ? L * 16 : r * (F_ * 2) + c * 16; }
      const unsigned d0 = lds_base + (A_ ? lw * 8192 : lw == 2 ? K5_G : K5_U);
      unsigned st = 3 * K5_STG;
      auto issue = [&]() {
#pragma unroll
        for (int i = 0; i < N; ++i) { if (A_) glds16s(sp, vp[i], d0 + st, i * 1024); else glds16(sp, vp[i], d0 + st, i * 1024); }
        sp += A_ ? 131072 : 32 * F_ * 2; st = st == (K5_NS - 1) * K5_STG ? 0 : st + K5_STG;
      };
      BAR();
      if (!A_) { issue(); issue(); }
      BAR();
      u32x4 xk[4];
      auto keep = [&]() {
#pragma unroll
        for (int i = 0; i < 4; ++i) { xk[i] = *(const LDS_PTR(u32x4))(smem + lw * 4096 + i * 1024 + lane * 16); asm volatile("" : "+v"(xk[i])); }
      };
      if (A_ && lw == 0) { signal(cnt, C4 + tm4, lane); keep(); pollp<PD4, PQ, 3>(cnt + ((C4 + tm5 * 2 + (lane & 1)) * 8 + (tn5 & 7)) * 32, 16, pr); }
      else { keep(); if (!A_) { issue(); issue(); issue(); } }
      BAR();
      if (A_) { issue(); issue(); }
      wait_vm<A_ ? N : 4 * N>();
      KEEP4(pr);
      BAR();
      if (A_) { issue(); issue(); issue(); }
#pragma unroll 1
      for (int j = 0; j < 27; ++j) {
        wait_vm<3 * N>();
        BAR();
        issue();
        if (j == 6) {
#pragma unroll
          for (int i = 0; i < 4; ++i) st16<true>(ws + WS_PART, (b * 4 + lw) * 4096 + i * 1024 + lane * 16, (u32x4){~0u, ~0u, ~0u, ~0u});
        }
        if (j == 12 && lw == 0 && lane == 0) __hip_atomic_store((unsigned*)(ws + WS_FLG) + b * 32, 16u, __ATOMIC_RELAXED, __HIP_MEMORY_SCOPE_AGENT);
        touch(); __builtin_amdgcn_s_sleep(TS5); touch();
      }
      const char* sb = A_ ? ws + WS_HH + (kh6 * 56 * 128 + tm6 * 8 + lw * 4) * 1024 : (const char*)w2 + kh6 * 1792 * 2048 + tn6 * 256 + ((lw ^ kh6 ^ 1) & 1) * 128;
      unsigned vo[4];
#pragma unroll
      for (int i = 0; i < 4; ++i) {
        const int r = i * 8 + (lane >> 3), k = 8 * ((r >> 2) & 3) + 4 * (r >> 4) + (r & 3);
        vo[i] = A_ ? lane * 16 : k * 2048 - i * 1024 + swz;
      }
      const unsigned d6 = lds_base + (A_ ? K6_A : 0) + (lw & 1) * 4096;
      st = 0;
      const int tab = w2_stage(lane);
      int jn = 0;
      auto issue6 = [&]() {
        if (A_) { glds16x4<true>(sb, sb, vo, d6 + st); sb += 131072; }
        else { const int o = __builtin_amdgcn_readlane(tab, jn); const char* s0 = sb + (o & ~1); glds16x4<false>(s0, s0 + (o & 1) * 65536, vo, d6 + st); ++jn; }
        st = st == ((A_ ? K6_NA : K6_NW) - 1) * 8192 ? 0 : st + 8192;
      };
      auto pre6 = [&](int n) {
        if (!A_) {
#pragma unroll 1
          for (int i = 0; i < n; ++i) issue6();
        }
      };
      constexpr int E = A_ ? 0 : 4;
      { const char* p = (const char*)w2 + (kh6 * 1792 + ii) * 2048;
        const int n = lw * 56 + (lane < 56 ? lane : 0), o = w2_stage((xc & 3) * 14 + (n >> 4));
        glds4(p, (o & ~1) + (o & (ii >> 4) & 1) * 65536 + (n & 15) * 128, lds_base + LDS_DUMMY); }
      wait_vm<3 * N + 1>(); BAR();
      wait_vm<2 * N + 1>(); BAR(); pre6(4);
      wait_vm<N + 4 * E + 1>(); BAR(); pre6(4);
      wait_vm<8 * E + 1>(); BAR(); pre6(4);
      BAR();
      if (A_) {
        volatile LDS_PTR(unsigned) go = (volatile LDS_PTR(unsigned))(smem + LDS_ARR + 4);
        if (lw == 0) { pollp<PD5, PQ5, 1>(cnt + ((C5 + (tm6 >> 1) * 4 + kh6 * 2) * 8 + tn6) * 32, 16, pr); *go = 1u; }
        else {
#pragma unroll 1
          for (unsigned spins = 0; spins < SPIN_CAP && __builtin_amdgcn_readfirstlane(*go) == 0; ++spins) __builtin_amdgcn_s_sleep(1);
          asm volatile("" ::: "memory");
        }
        STAMP_L(31);
        issue6(); issue6(); issue6();
        wait_vm<8>(); KEEP4(pr); BAR(); issue6(); issue6();
        wait_vm<12>(); BAR(); issue6(); issue6();
      } else { wait_vm<16>(); BAR(); BAR(); }
#pragma unroll 1
      for (int c = 2; c < 51; ++c) { wait_vm<16>(); BAR(); if (A_ || !((SKIP6 >> c) & 1)) issue6(); }
      wait_vm<16>(); BAR(); wait_vm<12>(); BAR(); wait_vm<8>(); BAR(); wait_vm<4>(); BAR(); wait_vm<0>(); BAR();
#pragma unroll
      for (int i = 0; i < 4; ++i) *(LDS_PTR(u32x4))(smem + K6_X + lw * 4096 + i * 1024 + lane * 16) = xk[i];
      BAR(); wait_lgkm0(); BAR();
    };
    if (isA) run5(T_()); else run5(F__());
    asm volatile("s_waitcnt lgkmcnt(0)" ::: "memory");
    return;
  }

  int fr = lane & 15, g = lane >> 4, p4 = fr & 3, row = 4 * g + (fr >> 2);
  int lsw = row * 128 + (((row >> 1) & 3) * 32) + p4 * 8;
  { const int mb = u1, hf = (b >> 3) & 1, kb0 = hf * 16 + wave * 2, cofs = (g & 1) * 16 + (g >> 1) * 8;
    u32x4 v[4];
    auto ldx = [&]() {
#pragma unroll
      for (int p = 0; p < 4; ++p) v[p] = *(const u32x4*)(x + (size_t)(mb * 16 + fr) * D_ + ((kb0 + (p & 1)) ^ ((p >> 1) * 16)) * 32 + cofs);
    };
    LDS_PTR(f32x2) sred = (LDS_PTR(f32x2))(smem + 4096);
    auto up2 = [](unsigned u) { return (f32x2){lo2f(u), hi2f(u)}; };
    auto stats = [&]() {
      f32x2 s2 = {0.f, 0.f}, q2 = {0.f, 0.f};
#pragma unroll
      for (int k = 0; k < 16; ++k) s2 += up2(v[k >> 2][k & 3]);
      const float mw = xsum16_32(s2[0] + s2[1]) * (1.f / 128.f);
#pragma unroll
      for (int k = 0; k < 16; ++k) { const f32x2 d = up2(v[k >> 2][k & 3]) - mw; q2 += d * d; }
      const float qw = xsum16_32(q2[0] + q2[1]);
      if (lane < 16) sred[wave * 16 + fr] = (f32x2){mw, qw};
      wait_lgkm0();
    };
    auto rest = [&]() {
    STAMP_AT(1 );
    float mean = 0.f, m2 = 0.f; f32x2 sw[8];
#pragma unroll
    for (int w = 0; w < 8; ++w) { sw[w] = sred[w * 16 + fr]; mean += sw[w][0]; m2 += sw[w][1]; }
    mean *= 0.125f;
#pragma unroll
    for (int w = 0; w < 8; ++w) { const float d = sw[w][0] - mean; m2 += 128.f * d * d; }
    const float rstd = ln_rstd(m2 * (1.f / 1024.f));
    mean = rbf(mean);
    unsigned nv[8];
#pragma unroll
    for (int k = 0; k < 8; ++k) {
      const f32x2 d = up2(v[k >> 2][k & 3]) - mean;
      const f32x2 n = up2(pk2(d[0], d[1])) * rstd;
      nv[k] = pk2(n[0], n[1]);
      asm volatile("" : "+v"(nv[k]));
    }
    SCHED0(); BAR(); SCHED0();
    STAMP_AT(2 );
    float mp[8];
#pragma unroll
    for (int i = 0; i < 8; ++i) mp[i] = __uint_as_float(__hip_atomic_load((unsigned*)(ws + WS_MOD) + hf * 512 + tid + i * 1024, __ATOMIC_RELAXED, __HIP_MEMORY_SCOPE_AGENT));
    *(LDS_PTR(unsigned))(smem + tid * 4) = pk2((mp[0] + mp[2]) + (mp[4] + mp[6]), 1.f + rbf((mp[1] + mp[3]) + (mp[5] + mp[7])));
    wait_lgkm0();
#pragma unroll
    for (int p = 0; p < 2; ++p) {
      const LDS_PTR(u32x4) tp = (const LDS_PTR(u32x4))(smem + ((wave * 2 + p) * 32 + cofs) * 4);
      const u32x4 t0 = tp[0], t1 = tp[1];
      unsigned o[4];
#pragma unroll
      for (int j = 0; j < 4; ++j) {
        const unsigned ta = j < 2 ? t0[2 * j] : t1[2 * j - 4], tb = j < 2 ? t0[2 * j + 1] : t1[2 * j - 3];
        const unsigned u = pk2(lo2f(nv[p * 4 + j]) * hi2f(ta), hi2f(nv[p * 4 + j]) * hi2f(tb));
        o[j] = pk2(lo2f(u) + lo2f(ta), hi2f(u) + lo2f(tb));
      }
      auto s0 = __builtin_amdgcn_permlane16_swap(o[0], o[2], false, false);
      auto s1 = __builtin_amdgcn_permlane16_swap(o[1], o[3], false, false);
      st16<true>(ws + WS_H, ((kb0 + p) * 128 + mb) * 1024 + lane * 16, (u32x4){s0[0], s1[0], s0[1], s1[1]});
    }
    STAMP_AT(3 );
    wait_vm<0>();
    STAMP_AT(4 );
    BAR();
    STAMP_AT(5 );
    };
    if (hf) {
      const int mrow = (u1 & 3) * 256 + (tid >> 3) * 4;
      const u32x2 c2 = *(const u32x2*)(cnd + mrow);
      u32x4 mw[4];
#pragma unroll
      for (int i = 0; i < 4; ++i) mw[i] = __builtin_nontemporal_load((const u32x4*)(wmod + (size_t)(mrow + i) * 6144 + (u1 >> 2) * 64 + (lane & 7) * 8));
      const unsigned mc[2] = {c2[0], c2[1]};
      *(LDS_PTR(f32x2))(smem + 8192 + (wave * 64 + (lane & 7) * 8 + (lane >> 4) * 2) * 4) = slab_sum<4>(mc, mw);
      wait_lgkm0();
      BAR();
      BAR();
      ldx();
      stats();
      BAR();
      rest();
    } else {
      __builtin_amdgcn_s_sleep(XD0);
      ldx();
      SCHED0();
      const int sl = (3 - ((unsigned)(uintptr_t)wqkv >> 7) - (xc >> 1) * 4) & 7;
      const int i2 = tid - 384, r0 = i2 < 0 ? tid / 12 : i2 / 3, c0 = i2 < 0 ? tid - r0 * 12 : (i2 - r0 * 3) * 4 + sl;
      const bool on = i2 < 0 || (sl < 4 && i2 < 96);
      __builtin_amdgcn_s_sleep(PFD);
      const unsigned pf = *(const unsigned*)((const char*)wqkv + (xc >> 1) * 512 + (on ? ((((xc & 1) ^ (i2 >= 0)) * 512 + (ii >> 1) * 32 + r0) * 48 + (c0 >> 2) * 16 + (c0 & 3)) * 128 : 0));
      SCHED0(); BAR(); SCHED0();
      stats();
      SCHED0(); BAR(); SCHED0();
      rest();
      asm volatile("" ::"v"(pf));
    }
  }
  {
    f32x4 bias[2];
    { const int kbase = tm3 * 128 + wave * 16 - 16;
#pragma unroll
      for (int t = 0; t < 4; ++t) {
        const int kk = (t >> 1) * 16 + 4 * g + (t & 1) * 2 + (fr & 1), df = 16 + fr - kk;
        bias[t >> 1][(t & 1) * 2] = bias[t >> 1][(t & 1) * 2 + 1] = (df >= 0 && df <= 14 && kbase + kk >= 0) ? 0.f : -__builtin_inff();
      }
      asm volatile("" : "+v"(bias[0]), "+v"(bias[1])); }
    u32x4 mc, mw[8];
    if (wave < 4) {
      const int wm = wave & 1, bA = wave;
      unsigned a0 = lds_base + K3_B + (lsw ^ (bA * 32)), a1 = lds_base + K3_B + (lsw ^ ((bA ^ 1) * 32)), aA = lds_base + 1024 + wm * 4096 + lane * 16, aH = lds_base + lane * 16;
      unsigned lq = lds_base + bA * 512 + lane * 8, lv = lds_base + 36864 + bA * 512 + (lane ^ ((g >> 1) * 8)) * 8;
      int n = 4;
      asm volatile(".irp r,0,8,16,24,32,40,48,56,64,72,80,88,96\n\tv_mov_b64 v[\\r:\\r+1], 0\n\tv_mov_b64 v[\\r+2:\\r+3], 0\n\tv_mov_b64 v[\\r+4:\\r+5], 0\n\tv_mov_b64 v[\\r+6:\\r+7], 0\n\t.endr\n\t"
                   "s_setprio 2\n\ts_waitcnt vmcnt(0)\n\ts_barrier\n\t"
                   "s_barrier\n\t"
                   RA3(1, 0) RA3(0, 0) RW3(0, 0, a0, 0) RW3(0, 1, a0, 0) RW3(1, 0, a1, 0) RW3(1, 1, a1, 0) RW3(2, 0, a1, 0) RW3(2, 1, a1, 0) RW3(3, 0, a0, 0) RW3(3, 1, a0, 0) RH3(0)
                   RW3(4, 0, a0, 0) RW3(4, 1, a0, 0) RW3(5, 0, a1, 0) LG(8)
                   ".p2align 5\n1:\n\t" STAGE3(43008, ) STAGE3(43008, ) STAGE3(-86016, "s_sub_u32 %[n], %[n], 1\n\t") "s_cbranch_scc0 1b\n\t"
                   H2_3(0, , , , ) H1_3(1, )
                   H2_3(1, "v_add_u32 %[a0], %[mo], %[lq]\n\t", "v_add_u32 %[aA], %[mo], %[lv]\n\t", "v_xor_b32 %[a1], 512, %[a0]\n\tv_xor_b32 %[aH], 512, %[aA]\n\t", "s_barrier\n\t")
                   M3(1, 5) M3(0, 5) M3(2, 0) CV3(0, 0, 0) M3(3, 0) WR3(0, 0, a0, 0) CV3(1, 0, 1) M3(3, 1) WR3(1, 1, a0, 0) CV3(0, 1, 2) M3(2, 1) WR3(0, 2, a1, 0) CV3(1, 1, 3)
                   M3(2, 2) WR3(1, 3, a1, 0) CV3(0, 2, 0) M3(3, 2) WR3(0, 0, a1, 18432) CV3(1, 2, 1) M3(3, 3) WR3(1, 1, a1, 18432) CV3(0, 3, 2) M3(2, 3) WR3(0, 2, a0, 18432) CV3(1, 3, 3)
                   M3(4, 3) WR3(1, 3, a0, 18432) CV3(0, 4, 0) M3(4, 4) WR3(0, 0, aA, 0) CV3(1, 4, 1) M3(2, 4) WR3(1, 1, aA, 0) CV3(0, 5, 2) M3(3, 4) WR3(0, 2, aH, 0) CV3(1, 5, 3)
                   M3(3, 5) WR3(1, 3, aH, 0) CV3(2, 0, 0) M3(2, 5) WR3(2, 0, a0, 0)
                   CW3(3, 0, 1, a0, 0) CW3(3, 1, 2, a1, 0) CW3(2, 1, 3, a1, 0) CW3(2, 2, 0, a1, 18432) CW3(3, 2, 1, a1, 18432) CW3(3, 3, 2, a0, 18432) CW3(2, 3, 3, a0, 18432)
                   CV3(4, 3, 0) WR3(0, 0, lq, 18432) CV3(4, 4, 1) WR3(0, 1, lv, 0) CW3(2, 4, 2, aA, 0) CW3(3, 4, 3, aA, 0) CW3(3, 5, 0, aH, 0) CW3(2, 5, 1, aH, 0)
                   "s_setprio 0\n\t" LG(0)
                   : [a0] "+v"(a0), [a1] "+v"(a1), [aA] "+v"(aA), [aH] "+v"(aH), [n] "+s"(n)
                   : [lq] "v"(lq), [lv] "v"(lv), [mo] "s"((wm * 4 + 1) * 2048)
                   : "memory", "scc", V10(), V10(1), V10(2), V10(3), V10(4), V10(5), V10(6), V10(7), V10(8), V10(9), V10(10), V10(11), V10(12), V10(13), V8(14));
    } else {
      wait_vm<0>();
      BAR(); BAR();
      {
        const unsigned* pc = cnt + ((C2 + ((lane & 1) && tm3 ? tm3 - 1 : tm3)) * 8 + (tn3 & 7)) * 32;
        unsigned seen = 0x80008u;
        BAR(); BAR(); BAR();
        if (wave == 4) asm volatile("global_load_dword %0, %1, off sc1" : "=v"(seen) : "v"(pc) : "memory");
        BAR(); BAR();
        asm volatile("s_waitcnt vmcnt(0)" : "+v"(seen)::"memory");
        if (__builtin_amdgcn_ballot_w64(seen < 0x80008u) != 0) poll<0>(pc, 0x80008u);
      }
#pragma unroll 1
      for (int t = 5; t < SLB; ++t) BAR();
      const int mrow = ((b & 3) * 4 + (wave & 3)) * 64 + (lane >> 3) * 8;
      mc = *(const u32x4*)(cnd + mrow);
#pragma unroll
      for (int i = 0; i < 8; ++i) mw[i] = __builtin_nontemporal_load((const u32x4*)(wmod + (size_t)(mrow + i) * 6144 + 2048 + (b >> 2) * 64 + (lane & 7) * 8));
      SCHED0();
#pragma unroll
      for (int t = SLB; t < 16; ++t) BAR();
    }
    lane = lane_id(); fr = lane & 15; g = lane >> 4; p4 = fr & 3; row = 4 * g + (fr >> 2); lsw = row * 128 + (((row >> 1) & 3) * 32) + p4 * 8;
    wait_lgkm0();
    BAR();
    STAMP_AT(9 );
    const LDS_PTR(char) eb = smem + wave * 2048;
    auto ld2 = [&](int o) { return cat8(*(const LDS_PTR(bf16x4))(eb + o), *(const LDS_PTR(bf16x4))(eb + o + 512)); };
    const int vo = 36864 + ((p4 * 16 + row) ^ ((p4 >> 1) * 8)) * 8;
    bf16x8 qf[2], k0[2], k1[2], v0[2], v1[2];
#pragma unroll
    for (int hd = 0; hd < 2; ++hd) { qf[hd] = ld2(2048 + hd * 1024 + lane * 8); k0[hd] = ld2(18432 + hd * 1024 + lane * 8); k1[hd] = ld2(18432 + 2048 + hd * 1024 + lane * 8); }
#pragma unroll
    for (int hd = 0; hd < 2; ++hd) {
      v0[hd] = cat8(ld_tr4(eb + vo + hd * 1024), ld_tr4(eb + vo + hd * 1024 + 2048));
      v1[hd] = cat8(ld_tr4(eb + vo + hd * 1024 + 512), ld_tr4(eb + vo + hd * 1024 + 512 + 2048));
    }
    SCHED0();
    const f32x4 z = {0.f, 0.f, 0.f, 0.f};
    const bool odd = fr & 1;
    const unsigned sel0 = odd ? 0x01000c0cu : 0x0c0c0100u, sel1 = odd ? 0x03020c0cu : 0x0c0c0302u;
    float inf = __builtin_inff(); asm("" : "+v"(inf));
    auto mx2 = [&](float a, float c) { return __builtin_amdgcn_fmed3f(a, c, inf); };
    auto both = [](float a, float c, auto op, auto fin) {
      auto u = __builtin_amdgcn_permlane16_swap(__float_as_uint(a), __float_as_uint(c), false, false);
      float r = op(__uint_as_float(u[0]), __uint_as_float(u[1]));
      auto w = __builtin_amdgcn_permlane32_swap(__float_as_uint(r), __float_as_uint(r), false, false);
      r = fin(op(__uint_as_float(w[0]), __uint_as_float(w[1])));
      auto y = __builtin_amdgcn_permlane16_swap(__float_as_uint(r), __float_as_uint(r), false, false);
      return (f32x2){__uint_as_float(y[0]), __uint_as_float(y[1])};
    };
    f32x2 l[2][2];
#pragma unroll
    for (int hd = 0; hd < 2; ++hd) {
      const f32x4 s0 = __builtin_amdgcn_mfma_f32_16x16x32_bf16(k0[hd], qf[hd], bias[0], 0, 0, 0), s1 = __builtin_amdgcn_mfma_f32_16x16x32_bf16(k1[hd], qf[hd], bias[1], 0, 0, 0);
#pragma unroll
      for (int t = 0; t < 2; ++t) {
        const f32x4 sv = t ? s1 : s0;
        unsigned u = pk2(odd ? sv[1] : sv[0], odd ? sv[3] : sv[2]);
        u = pk2(lo2f(u) * 0.17677669529663687f, hi2f(u) * 0.17677669529663687f);
        l[hd][t] = (f32x2){lo2f(u), hi2f(u)};
      }
    }
    const f32x2 mx = both(mx2(mx2(l[0][0][0], l[0][0][1]), mx2(l[0][1][0], l[0][1][1])), mx2(mx2(l[1][0][0], l[1][0][1]), mx2(l[1][1][0], l[1][1][1])), mx2,
                          [](float r) { return r * -1.4426950408889634f; });
    float sm[2];
#pragma unroll
    for (int hd = 0; hd < 2; ++hd) {
#pragma unroll
      for (int t = 0; t < 2; ++t) { const f32x2 e = l[hd][t] * 1.4426950408889634f + mx[hd]; l[hd][t] = (f32x2){__builtin_amdgcn_exp2f(e[0]), __builtin_amdgcn_exp2f(e[1])}; }
      const f32x2 a = l[hd][0] + l[hd][1]; sm[hd] = a[0] + a[1];
    }
    const f32x2 inv = both(sm[0], sm[1], [](float a, float c) { return a + c; }, [](float r) { return __builtin_amdgcn_rcpf(r); });
#pragma unroll
    for (int hd = 0; hd < 2; ++hd) {
      u32x4 pp;
#pragma unroll
      for (int t = 0; t < 2; ++t) {
        const f32x2 p = l[hd][t] * inv[hd]; const unsigned u = pk2(p[0], p[1]);
        pp[2 * t] = __builtin_amdgcn_perm(u, u, sel0); pp[2 * t + 1] = __builtin_amdgcn_perm(u, u, sel1);
      }
      const bf16x8 pf = __builtin_bit_cast(bf16x8, pp);
      const f32x4 oa = __builtin_amdgcn_mfma_f32_16x16x32_bf16(v0[hd], pf, z, 0, 0, 0), ob = __builtin_amdgcn_mfma_f32_16x16x32_bf16(v1[hd], pf, z, 0, 0, 0);
      u32x4 ov; ov[0] = pk2(oa[0], oa[1]); ov[1] = pk2(oa[2], oa[3]); ov[2] = pk2(ob[0], ob[1]); ov[3] = pk2(ob[2], ob[3]);
      st16<true>(ws + WS_O, ((tn3 * 2 + hd) * 128 + tm3 * 8 + wave) * 1024 + lane * 16, ov);
    }
    STAMP_AT(10 );
    wait_vm<0>();
    STAMP_AT(11 );
    BAR();
    STAMP_AT(12 );
    if (wave >= 4) {
      const unsigned m4[4] = {mc[0], mc[1], mc[2], mc[3]};
      const f32x2 ps = slab_sum<8>(m4, mw);
      if (!(lane & 8))
        __hip_atomic_store((unsigned long long*)(ws + WS_MOD2) + (((b & 3) * 4 + (wave & 3)) * 4096 + (b >> 2) * 64 + (lane & 7) * 8 + (lane >> 4) * 2) / 2,
                           ((unsigned long long)__float_as_uint(ps[1]) << 32) | __float_as_uint(ps[0]), __ATOMIC_RELAXED, __HIP_MEMORY_SCOPE_AGENT);
      wait_vm<0>();
      if (lane == 0 && __hip_atomic_fetch_add((LDS_PTR(unsigned))(smem + LDS_ARR + 8), 1u, __ATOMIC_RELAXED, __HIP_MEMORY_SCOPE_WORKGROUP) == 3)
        __hip_atomic_fetch_add(cnt + (CM * 8 + (b >> 2)) * 32, 1u, __ATOMIC_RELAXED, __HIP_MEMORY_SCOPE_AGENT);
    }
  }
  { const int wm = wave & 3, ks = wave >> 2, mb = tm4 * 8 + wm * 2 + ks;
    constexpr int R = 6;
    u32x4 rx[2];
#pragma unroll
    for (int p = 0; p < 2; ++p) rx[p] = *(const u32x4*)(x + (size_t)(mb * 16 + fr) * D_ + tn4 * 64 + p * 32 + (g & 1) * 16 + (g >> 1) * 8);
    const unsigned va0 = (ks * 128 + mb) * 1024 + lane * 16, va1 = va0 ^ 1024;
    bf16x8 fa[R][2], fw[2][4];
    auto issueA = [&](int t) {
      const char* pa = ws + WS_O + t * 262144;
      fa[t % R][0] = __builtin_bit_cast(bf16x8, ld16<true>(pa, va0));
      fa[t % R][1] = __builtin_bit_cast(bf16x8, ld16<true>(pa, va1));
    };
    unsigned aw[4];
    auto rdw = [&](int t, int j) {
      const int o = (t & 3) * 8192;
      fw[t & 1][j] = cat8(ld_tr4(smem + aw[j] + o), ld_tr4(smem + aw[j] + o + 2048));
    };
    auto stepw = [&](int t) {
      const int gi = t >> 2, base = gi < 3 ? 57344 + gi * 32768 : 0;
#pragma unroll
      for (int j = 0; j < 4; ++j) { aw[j] = base + ks * 4096 + (lsw ^ (j * 32)); asm volatile("" : "+v"(aw[j])); }
    };
    auto up2 = [](unsigned u) { return (f32x2){lo2f(u), hi2f(u)}; };
    f32x2 f[8];
    unsigned slot = (mb * 256 + fr + kh6 * 32768 + 4 * g * 16) * 8, sown = (mb * 256 + fr + (g & 1) * 32768 + tn4 * 16) * 8;
    f32x4 acc[2][4];
#pragma unroll
    for (int i = 0; i < 2; ++i)
#pragma unroll
      for (int j = 0; j < 4; ++j) acc[i][j] = (f32x4){0.f, 0.f, 0.f, 0.f};
    stepw(0);
    wait_lgkm0();
    SCHED0(); BAR(); SCHED0();
    STAMP_AT(13 );
#pragma unroll
    for (int t = 0; t < R; ++t) issueA(t);
#pragma unroll
    for (int j = 0; j < 4; ++j) rdw(0, j);
    SCHED0();
#pragma unroll
    for (int t = 0; t < 16; ++t) {
      if (t < 15 && ((t + 1) & 3) == 0) stepw(t + 1);
#pragma unroll
      for (int j = 0; j < 4; ++j) {
        if (t < 15) rdw(t + 1, j);
        SCHED0();
#pragma unroll
        for (int i = 0; i < 2; ++i) acc[i][j] = __builtin_amdgcn_mfma_f32_16x16x32_bf16(fw[t & 1][j], fa[t % R][i], acc[i][j], 0, 0, 0);
        SCHED0();
      }
      if (t + R < 16) issueA(t + R);
      if (t == 2) {
#pragma unroll
        for (int p = 0; p < 2; ++p) {
          auto s0 = __builtin_amdgcn_permlane16_swap(rx[p][0], rx[p][2], false, false);
          auto s1 = __builtin_amdgcn_permlane16_swap(rx[p][1], rx[p][3], false, false);
          f[4 * p] = up2(s0[0]); f[4 * p + 1] = up2(s1[0]); f[4 * p + 2] = up2(s0[1]); f[4 * p + 3] = up2(s1[1]);
        }
#pragma unroll
        for (int j = 0; j < 8; ++j) asm volatile("" : "+v"(f[j]));
        asm volatile("" : "+v"(slot), "+v"(sown));
      }
      if (t == 5) BAR();
      SCHED0();
    }
    STAMP_AT(14 );
    LDS_PTR(f32x4) red = (LDS_PTR(f32x4))(smem + K4_RED);
#pragma unroll
    for (int j = 0; j < 4; ++j) red[((wave ^ 4) * 4 + j) * 64 + lane] = acc[1][j];
    wait_lgkm0();
    BAR();
    STAMP_AT(15 );
    const LDS_PTR(float) vec = (const LDS_PTR(float))(smem + LDS_VEC);
    unsigned o[8];
    f32x4 pr[4], ga[4];
#pragma unroll
    for (int j = 0; j < 4; ++j) { pr[j] = red[(wave * 4 + j) * 64 + lane]; ga[j] = *(const LDS_PTR(f32x4))(vec + j * 16 + 4 * g); }
    SCHED0();
#pragma unroll
    for (int j = 0; j < 4; ++j) {
      const f32x4 y = acc[0][j] + pr[j];
      const f32x2 lo = y.xy * ga[j].xy + f[2 * j], hi = y.zw * ga[j].zw + f[2 * j + 1];
      o[2 * j] = pk2(lo[0], lo[1]); o[2 * j + 1] = pk2(hi[0], hi[1]);
      f[2 * j] = up2(o[2 * j]); f[2 * j + 1] = up2(o[2 * j + 1]);
    }
    const f32x2 s2 = ((f[0] + f[1]) + (f[2] + f[3])) + ((f[4] + f[5]) + (f[6] + f[7]));
    float mt = xsum16_32(s2[0] + s2[1]) * (1.f / 64.f); asm("" : "+v"(mt));
    f32x2 q2 = {0.f, 0.f}, q3 = {0.f, 0.f};
#pragma unroll
    for (int j = 0; j < 8; j += 2) { f[j] -= mt; f[j + 1] -= mt; q2 += f[j] * f[j]; q3 += f[j + 1] * f[j + 1]; }
    q2 += q3;
    const float qq = xsum16_32(q2[0] + q2[1]);
    if (lane < 32) {
      const unsigned qb = qq == qq ? __float_as_uint(qq) : 0x7fc00000u;
      __hip_atomic_store((unsigned long long*)(ws + WS_XCH + sown), ((unsigned long long)qb << 32) | __float_as_uint(mt), __ATOMIC_RELAXED, __HIP_MEMORY_SCOPE_AGENT);
    }
    *(LDS_PTR(u32x4))(smem + (mb & 7) * 2048 + lane * 16) = (u32x4){o[0], o[1], o[2], o[3]};
    *(LDS_PTR(u32x4))(smem + (mb & 7) * 2048 + 1024 + lane * 16) = (u32x4){o[4], o[5], o[6], o[7]};
    STAMP_AT(16 );
    f32x4 ss[4], gg[4];
#pragma unroll
    for (int j = 0; j < 4; ++j) {
      ss[j] = *(const LDS_PTR(f32x4))(vec + 64 + j * 16 + 4 * g); gg[j] = *(const LDS_PTR(f32x4))(vec + 128 + j * 16 + 4 * g);
      f[2 * j] *= gg[j].xy; f[2 * j + 1] *= gg[j].zw;
      asm volatile("" : "+v"(ss[j]), "+v"(gg[j]), "+v"(f[2 * j]), "+v"(f[2 * j + 1]));
    }
    unsigned long long pv[4];
    __builtin_amdgcn_s_sleep(XS0);
    for (unsigned spins = 0;; ++spins) {
      bool ok = true;
#pragma unroll
      for (int i = 0; i < 4; ++i) { pv[i] = __hip_atomic_load((unsigned long long*)(ws + WS_XCH + slot) + i * 16, __ATOMIC_RELAXED, __HIP_MEMORY_SCOPE_AGENT); ok &= (unsigned)(pv[i] >> 32) != 0xffffffffu; }
      if (__builtin_amdgcn_ballot_w64(!ok) == 0 || spins > SPIN_CAP) break;
      __builtin_amdgcn_s_sleep(XS1);
    }
    STAMP_AT(17 );
    float ms = 0.f, m2 = 0.f;
#pragma unroll
    for (int i = 0; i < 4; ++i) { ms += __uint_as_float((unsigned)pv[i]); m2 += __uint_as_float((unsigned)(pv[i] >> 32)); }
    const float mean = xsum16_32(ms) * (1.f / 16.f);
#pragma unroll
    for (int i = 0; i < 4; ++i) { const float d = __uint_as_float((unsigned)pv[i]) - mean; m2 += 64.f * d * d; }
    const float rstd = ln_rstd(xsum16_32(m2) * (1.f / 1024.f));
    const float sh = (mt - rbf(mean)) * rstd;
#pragma unroll
    for (int p = 0; p < 2; ++p) {
      u32x4 v;
#pragma unroll
      for (int j = 0; j < 4; ++j) {
        const int jb = 2 * p + (j >> 1);
        const f32x2 h = f[4 * p + j] * rstd + (((j & 1) ? gg[jb].zw : gg[jb].xy) * sh + ((j & 1) ? ss[jb].zw : ss[jb].xy));
        v[j] = pk2(h[0], h[1]);
      }
      st16<true>(ws + WS_H2, ((tn4 * 2 + p) * 128 + mb) * 1024 + lane * 16, v);
    }
    STAMP_AT(18 );
    wait_vm<0>();
    wait_lgkm0();
    BAR();
    STAMP_AT(19 );

  }
  const int grp = wave >> 2;
  { const int wm = wave & 3;
    const LDS_PTR(char) sb = smem + 3 * K5_STG;
    const LDS_PTR(char) aP = sb + K5_G + row * 224 + grp * 96 + p4 * 8;
    const LDS_PTR(char) aM = sb + (p4 < 2 ? K5_G : K5_U) + row * 224 + (12 + grp) * 16 + (p4 & 1) * 8;
    const LDS_PTR(char) aA = sb + wm * 4096 + lane * 16;
    f32x32 c0, c1, c2; f32x16 c3;
    asm volatile(".irp r,0,8,16,24,32,40,48,56,64,72,80,88,96,104\n\tv_mov_b64 v[\\r:\\r+1], 0\n\tv_mov_b64 v[\\r+2:\\r+3], 0\n\tv_mov_b64 v[\\r+4:\\r+5], 0\n\tv_mov_b64 v[\\r+6:\\r+7], 0\n\t.endr"
                 : "={v[0:31]}"(c0), "={v[32:63]}"(c1), "={v[64:95]}"(c2), "={v[96:111]}"(c3)::"memory");
    int wrap = -(K5_NS - 1) * K5_STG, dl = K5_STG, n = 7;
    unsigned mask = wrap_mask(4, K5_NS);
    asm volatile("" : "+s"(wrap), "+v"(aP), "+v"(aM), "+v"(aA));
    if (grp) __builtin_amdgcn_s_setprio(1); else __builtin_amdgcn_s_setprio(2);
    BAR();
    STAMP_AT(20 );
    BAR();
    STAMP_G(21, 26);
    asm volatile(RA5(0) RA5(1) RP5(0, 0) RP5(0, 1) RP5(1, 0) RP5(1, 1) RP5(2, 0) RP5(2, 1) RP5(3, 0) RP5(3, 1) RP5(4, 0) RP5(4, 1) RP5(5, 0) RP5(5, 1) RW5(6, 0, aM, 0) LG(9)
                 ".p2align 5\n1:\n\t" TRIP5("") TRIP5("") TRIP5("") TRIP5("s_sub_u32 %[n], %[n], 1\n\t") "s_cbranch_scc0 1b\n\t" LG(0) "s_nop 7"
                 : ACC5, [aP] "+v"(aP), [aM] "+v"(aM), [aA] "+v"(aA), [dl] "+s"(dl), [mask] "+s"(mask), [n] "+s"(n) : [wrap] "s"(wrap) : CLOB5);
    f32x4 acc[4][7];
#pragma unroll
    for (int e = 0; e < 112; ++e) acc[e / 28][(e % 28) / 4][e & 3] = e < 32 ? c0[e & 31] : e < 64 ? c1[e & 31] : e < 96 ? c2[e & 31] : c3[e & 15];
    STAMP_G(22, 27);
    auto swi4 = [](const f32x4& gv, const f32x4& uv, unsigned& o0, unsigned& o1) {
      constexpr float c2 = 2.0813689810056077f;
      f32x2 pa = gv.xy * uv.xy, pb = gv.zw * uv.zw;
      const f32x2 ta = (f32x2){__builtin_amdgcn_exp2f(gv[0]), __builtin_amdgcn_exp2f(gv[1])} * c2 + c2, tb = (f32x2){__builtin_amdgcn_exp2f(gv[2]), __builtin_amdgcn_exp2f(gv[3])} * c2 + c2;
      pa *= (f32x2){__builtin_amdgcn_rcpf(ta[0]), __builtin_amdgcn_rcpf(ta[1])}; pb *= (f32x2){__builtin_amdgcn_rcpf(tb[0]), __builtin_amdgcn_rcpf(tb[1])};
      o0 = pk2(pa[0], pa[1]); o1 = pk2(pb[0], pb[1]);
    };
    const int lz = lane_id();
    const int gz = lz >> 4, fz = lz & 15;
    const int qb = (tn5 >> 4) * 224, ql = qb + 64 + (tn5 & 15) * 10;
    auto hof = [&](int e, int i) { return (unsigned)((e >> 2) * 131072 + (e & 3) * 256 + fz * 16 + i * 1024); };
    const unsigned ho0 = hof((grp ? ql + 2 : qb + (tn5 & 15) * 4) + 2 * (gz & 1) + (gz >> 1), 0), ho1 = hof(ql + 6 * grp + (gz >> 1), gz & 1), hom = hof(ql + 8 + grp, gz);
    char* hb = ws + WS_HH + (tm5 * 16 + wm * 4) * 1024;
    const int c5 = C5 + tm5 * 4 + (tn5 >> 4) * 2;
    auto tr16 = [](const unsigned (&o)[4]) {
      auto s0 = __builtin_amdgcn_permlane16_swap(o[0], o[2], false, false);
      auto s1 = __builtin_amdgcn_permlane16_swap(o[1], o[3], false, false);
      u32x4 v = {s0[0], s1[0], s0[1], s1[1]}; asm volatile("" : "+v"(v)); return v;
    };
    auto pair = [&](int p, int i) {
      unsigned o[4];
      swi4(acc[i][2 * p], acc[i][2 * p + 1], o[0], o[1]);
      swi4(acc[i + 1][2 * p], acc[i + 1][2 * p + 1], o[2], o[3]);
      return tr16(o);
    };
    u32x4 v[3];
#pragma unroll
    for (int i = 0; i < 4; ++i) {
      unsigned o[4];
      swi4(acc[i][0], acc[i][1], o[0], o[1]);
      swi4(acc[i][2], acc[i][3], o[2], o[3]);
      st16<true>(hb, ho0 + i * 1024, tr16(o));
      SCHED0();
    }
    STAMP_G(23, 28);
    v[0] = pair(2, 0);
    if (!grp) {
      wait_vm<0>();
      unsigned arr = 0;
      if (lz == 0) arr = __hip_atomic_fetch_add((LDS_PTR(unsigned))(smem + LDS_ARR), 1u, __ATOMIC_RELAXED, __HIP_MEMORY_SCOPE_WORKGROUP);
      if (__builtin_amdgcn_readfirstlane(arr) == 3) signal(cnt, c5, lz);
    }
    STAMP_G(24, 29);
    v[1] = pair(2, 2);
    { unsigned m[4];
#pragma unroll
      for (int i = 0; i < 4; i += 2) {
        f32x4 gv, uv;
#pragma unroll
        for (int e = 0; e < 4; ++e) {
          auto w = __builtin_amdgcn_permlane32_swap(__float_as_uint(acc[i + (e >> 1)][6][e & 1]), __float_as_uint(acc[i + (e >> 1)][6][2 + (e & 1)]), false, false);
          gv[e] = __uint_as_float(w[0]); uv[e] = __uint_as_float(w[1]);
        }
        swi4(gv, uv, m[i], m[i + 1]);
      }
      auto x0 = __builtin_amdgcn_permlane32_swap(m[0], m[2], false, false);
      auto x1 = __builtin_amdgcn_permlane32_swap(m[1], m[3], false, false);
      const unsigned o[4] = {x0[0], x0[1], x1[0], x1[1]};
      v[2] = tr16(o);
      st16<true>(hb, ho1, v[0]); st16<true>(hb, ho1 + 2048, v[1]); st16<true>(hb, hom, v[2]);
    }
    STAMP_G(25, 30);
    if (grp) {
#pragma unroll
      for (int t = 0; t < NDR; ++t) BAR();
      wait_vm<0>();
      BAR();
      if (wave == 4) signal(cnt, c5 + 1, lz);
      if (wave > 4) return;
#pragma unroll 1
      for (int t = NDR + 1; t < 9; ++t) BAR();
      unsigned seen = 16;
      const unsigned* pc = (lz & 1) ? (const unsigned*)(ws + WS_FLG) + (b ^ 4) * 32 : cnt + ((C5 + (tm6 >> 1) * 4 + kh6 * 2 + 1) * 8 + tn6) * 32;
      asm volatile("global_load_dword %0, %1, off sc1" : "=v"(seen) : "v"(pc) : "memory");
      BAR(); BAR();
      asm volatile("s_waitcnt vmcnt(0)" : "+v"(seen)::"memory");
      if (__builtin_amdgcn_ballot_w64(seen < 16) != 0) poll<0>(pc, 16);
      return;
    }
  }
  { const int lane = lane_id(), fr = lane & 15, g = lane >> 4, p4 = fr & 3, row = 4 * g + (fr >> 2);
    const int wm = wave & 1, wn = wave >> 1;
    const int lw0 = row * 128 + (((row >> 1) & 3) * 32) + p4 * 8;
    const int mb0 = tm6 * 8 + wm * 4, t64 = tn6 * 2 + kh6;
    f32x4 acc[4][4];
#pragma unroll
    for (int i = 0; i < 4; ++i)
#pragma unroll
      for (int j = 0; j < 4; ++j) { acc[i][j] = (f32x4){0.f, 0.f, 0.f, 0.f}; asm volatile("" : "+v"(acc[i][j])); }
    struct Fr { bf16x8 a[4]; bf16x4 w[4][2]; };
    const LDS_PTR(char) aw0 = smem + (lw0 ^ (wn * 64)); const LDS_PTR(char) aw1 = smem + (lw0 ^ (wn * 64 + 32));
    const LDS_PTR(char) aa = smem + K6_A + wm * 4096 + lane * 16;
    auto rd1 = [&](Fr& f, int r) {
      if (r < 2) f.w[0][r] = ld_tr4(aw0 + r * 2048);
      else if (r < 4) f.a[r - 2] = *(const LDS_PTR(bf16x8))(aa + (r - 2) * 1024);
      else if (r < 10) { const int j = (r - 2) >> 1; f.w[j][r & 1] = ld_tr4(((j & 1) ? aw1 : aw0) + (j >> 1) * 4096 + (r & 1) * 2048); }
      else f.a[r - 8] = *(const LDS_PTR(bf16x8))(aa + (r - 8) * 1024);
    };
    int dl, da, wrap = -(K6_NW - 1) * 8192, wrapa = -(K6_NA - 1) * 8192;
    unsigned mask = wrap_mask(1, K6_NW), maska = wrap_mask(1, K6_NA);
    Fr fA, fB;
    asm volatile("" : "+s"(wrap), "+s"(wrapa), "+v"(aw0), "+v"(aw1), "+v"(aa));
    wait_vm<0>();
    BAR();
    STAMP_AT(32 );
#pragma unroll
    for (int r = 0; r < 12; ++r) { SCHED0(); rd1(fA, r); }
    SCHED0();
    aw0 += 8192; aw1 += 8192; aa += 8192;
    wait_lgkm<6>();
    BAR();
    auto trip = [&](const Fr& cur, Fr& nxt) {
      SCHED0();
#pragma unroll
      for (int m = 0; m < 16; ++m) {
        const int j = (m & 7) >> 1, i = (m >> 3) * 2 + ((m ^ j) & 1);
        if (m == 2) mfma_sel<8192>(acc[i][j], cat8(cur.w[j][0], cur.w[j][1]), cur.a[i], mask, dl, wrap);
        else if (m == 6) mfma_sel<8192>(acc[i][j], cat8(cur.w[j][0], cur.w[j][1]), cur.a[i], maska, da, wrapa);
        else mfma_acc(acc[i][j], cat8(cur.w[j][0], cur.w[j][1]), cur.a[i]);
        SCHED0();
        if (m < 12) { rd1(nxt, m); SCHED0(); }
        if (m == 3) wait_lgkm<4>();
        if (m == 12) { aw0 += dl; aw1 += dl; asm volatile("" : "+v"(aw0), "+v"(aw1)); }
        if (m == 13) { aa += da; asm volatile("" : "+v"(aa)); }
        if (m == 14) wait_lgkm<6>();
        if (m == 15) BAR();
        SCHED0();
      }
    };
#pragma unroll 1
    for (int h = 0; h < 2; ++h) {
#pragma unroll 1
      for (int t = 0; t < 7; ++t) { trip(fA, fB); trip(fB, fA); trip(fA, fB); trip(fB, fA); }
      mask = wrap_mask(29, K6_NW); maska = wrap_mask(29, K6_NA);
    }
    mfma_done(acc);
    STAMP_AT(34 );
    const int slot = wm * 2 + wn, pb = (b ^ 4) * 4 + slot;
    { auto pk = [](float lo, float hi) { const unsigned u = pk2(lo, hi); return u < 0xfffffffeu ? u : 0xfffffffeu; };
#pragma unroll
      for (int i = 0; i < 4; ++i)
        st16<true>(ws + WS_PART, (b * 4 + slot) * 4096 + i * 1024 + lane * 16,
                   (u32x4){pk(acc[i][0][0], acc[i][0][1]), pk(acc[i][0][2], acc[i][0][3]), pk(acc[i][1][0], acc[i][1][1]), pk(acc[i][1][2], acc[i][1][3])});
      STAMP_AT(35 );
    }
    { const LDS_PTR(float) vec = (const LDS_PTR(float))(smem + LDS_VEC) + 192 + wn * 32 + 4 * g;
      const f32x4 gv0 = *(const LDS_PTR(f32x4))vec, gv1 = *(const LDS_PTR(f32x4))(vec + 16);
      u32x4 rv[4];
#pragma unroll
      for (int i = 0; i < 4; ++i) { rv[i] = *(const LDS_PTR(u32x4))(smem + K6_X + (wm * 4 + i) * 2048 + wn * 1024 + lane * 16); asm volatile("" : "+v"(rv[i])); }
      u32x4 z[4];
      __builtin_amdgcn_s_sleep(PD6);
#pragma unroll 1
      for (unsigned spins = 0; spins < SPIN_CAP; ++spins) {
        asm volatile("" ::: "memory");
        unsigned m = 0;
#pragma unroll
        for (int i = 0; i < 4; ++i) z[i] = ld16<true>(ws + WS_PART, pb * 4096 + i * 1024 + lane * 16);
#pragma unroll
        for (int i = 0; i < 16; ++i) m = m > z[i >> 2][i & 3] ? m : z[i >> 2][i & 3];
        if (__builtin_amdgcn_ballot_w64(m == ~0u) == 0) break;
        __builtin_amdgcn_s_sleep(PS6);
      }
      STAMP_AT(36 );
#pragma unroll
      for (int i = 0; i < 4; ++i) {
        const f32x4 v0 = (acc[i][2] + (f32x4){lo2f(z[i][0]), hi2f(z[i][0]), lo2f(z[i][1]), hi2f(z[i][1])}) * gv0;
        const f32x4 v1 = (acc[i][3] + (f32x4){lo2f(z[i][2]), hi2f(z[i][2]), lo2f(z[i][3]), hi2f(z[i][3])}) * gv1;
        const unsigned o0 = pk2(lo2f(rv[i][0]) + v0[0], hi2f(rv[i][0]) + v0[1]), o1 = pk2(lo2f(rv[i][1]) + v0[2], hi2f(rv[i][1]) + v0[3]);
        const unsigned o2 = pk2(lo2f(rv[i][2]) + v1[0], hi2f(rv[i][2]) + v1[1]), o3 = pk2(lo2f(rv[i][3]) + v1[2], hi2f(rv[i][3]) + v1[3]);
        auto s0 = __builtin_amdgcn_permlane16_swap(o0, o2, false, false);
        auto s1 = __builtin_amdgcn_permlane16_swap(o1, o3, false, false);
        st16<false>(out, (((mb0 + i) * 16 + fr) * D_ + t64 * 64 + wn * 32 + (g & 1) * 16 + (g >> 1) * 8) * 2, (u32x4){s0[0], s1[0], s0[1], s1[1]});
      }
    }
    STAMP_AT(37 );
  }
}

extern "C" void kernel_launch(void* const* d_in, const int* in_sizes, int n_in, void* d_out, int out_size, void* d_ws,
                              size_t ws_size, hipStream_t stream) {
  const u16* x = (const u16*)d_in[0];
  const u16* wqkv = (const u16*)d_in[1];
  const u16* wo = (const u16*)d_in[2];
  const u16* w1 = (const u16*)d_in[4];
  const u16* w3 = (const u16*)d_in[5];
  const u16* w2 = (const u16*)d_in[6];
  const u16* c = (const u16*)d_in[7];
  const u16* wmod = (const u16*)d_in[8];
  (void)hipFuncSetAttribute((const void*)k2_block, hipFuncAttributeMaxDynamicSharedMemorySize, MEGA_LDS);
  char* ws = (char*)d_ws;
  k2_block<<<256, 768, MEGA_LDS, stream>>>(x, wqkv, wo, w1, w3, w2, c, wmod, (u16*)d_out, ws);
}
```

```cpp
#include <hip/hip_runtime.h>
#include <stdint.h>
#include <type_traits>
#pragma clang diagnostic ignored "-Winline-asm"

typedef unsigned short u16;
typedef short bf16x8 __attribute__((ext_vector_type(8)));
typedef short bf16x4 __attribute__((ext_vector_type(4)));
typedef float f32x4 __attribute__((ext_vector_type(4)));
typedef float f32x2 __attribute__((ext_vector_type(2)));
typedef unsigned int u32x4 __attribute__((ext_vector_type(4)));
typedef unsigned int u32x2 __attribute__((ext_vector_type(2)));
#define LDS_PTR(T) __attribute__((address_space(3))) T*
#define BAR() do { asm volatile("" ::: "memory"); __builtin_amdgcn_s_barrier(); asm volatile("" ::: "memory"); } while (0)
#define SCHED0() __builtin_amdgcn_sched_barrier(0)
template <int N> using IC = std::integral_constant<int, N>;
typedef std::integral_constant<bool, true> T_;
typedef std::integral_constant<bool, false> F__;

constexpr int S_ = 2048, D_ = 1024, F_ = 3584;
#define STAMP_AT(i)
#define STAMP_G(ia, ib)
#define STAMP_L(i)

__device__ __forceinline__ unsigned pk2(float lo, float hi) {
  typedef __bf16 bf2 __attribute__((ext_vector_type(2)));
  f32x2 v = {lo, hi}; bf2 r = __builtin_convertvector(v, bf2); return __builtin_bit_cast(unsigned, r);
}
__device__ __forceinline__ float lo2f(unsigned u) { return __uint_as_float(u << 16); }
__device__ __forceinline__ float hi2f(unsigned u) { return __uint_as_float(u & 0xffff0000u); }
__device__ __forceinline__ float rbf(float v) { return lo2f(pk2(v, 0.f)); }
#define glds16(sbase, voff, lds_off, imm) \
  asm volatile("s_add_u32 m0, %2, %3\n\ts_nop 0\n\tglobal_load_lds_dwordx4 %0, %1" ::"v"(voff), "s"(sbase), "s"(lds_off), "n"(imm) : "memory", "scc")
#define glds16s(sbase, voff, lds_off, imm) \
  asm volatile("s_add_u32 m0, %2, %3\n\ts_nop 0\n\tglobal_load_lds_dwordx4 %0, %1 sc1" ::"v"(voff), "s"(sbase), "s"(lds_off), "n"(imm) : "memory", "scc")
#define GLDS4(SC) "s_mov_b32 m0, %6\n\ts_nop 0\n\tglobal_load_lds_dwordx4 %0, %4" SC "\n\tglobal_load_lds_dwordx4 %1, %5 offset:1024" SC \
                  "\n\tglobal_load_lds_dwordx4 %2, %4 offset:2048" SC "\n\tglobal_load_lds_dwordx4 %3, %5 offset:3072" SC
template <bool SC1> __device__ __forceinline__ void glds16x4(const void* s0, const void* s1, const unsigned (&v)[4], unsigned lds) {
  if (SC1) asm volatile(GLDS4(" sc1") ::"v"(v[0]), "v"(v[1]), "v"(v[2]), "v"(v[3]), "s"(s0), "s"(s1), "s"(lds) : "memory");
  else asm volatile(GLDS4("") ::"v"(v[0]), "v"(v[1]), "v"(v[2]), "v"(v[3]), "s"(s0), "s"(s1), "s"(lds) : "memory");
}
__device__ __forceinline__ void glds4(const void* sbase, unsigned voff, unsigned lds_off) {
  asm volatile("s_mov_b32 m0, %2\n\ts_nop 0\n\tglobal_load_lds_dword %0, %1" ::"v"(voff), "s"(sbase), "s"(lds_off) : "memory");
}
__device__ __forceinline__ void stouch4(const void* p) {
  asm volatile("s_load_dwordx2 s[100:101], %0, 0x3c\n\ts_load_dwordx2 s[100:101], %0, 0xbc\n\ts_load_dwordx2 s[100:101], %0, 0x13c\n\ts_load_dwordx2 s[100:101], %0, 0x1bc"
               ::"s"(p) : "memory", "s100", "s101");
}
template <bool SC1> __device__ __forceinline__ void st16(void* sbase, unsigned voff, u32x4 v) {
  __builtin_amdgcn_raw_buffer_store_b128(v, __builtin_amdgcn_make_buffer_rsrc(sbase, 0, 0x7fffffff, 0x00020000), voff, 0, SC1 ? 16 : 0);
}
template <bool SC1> __device__ __forceinline__ u32x4 ld16(const void* sbase, unsigned voff) {
  return __builtin_amdgcn_raw_buffer_load_b128(__builtin_amdgcn_make_buffer_rsrc((void*)sbase, 0, 0x7fffffff, 0x00020000), voff, 0, SC1 ? 16 : 0);
}
template <int N> __device__ __forceinline__ void wait_vm() { asm volatile("s_waitcnt vmcnt(%0)" ::"n"(N) : "memory"); }
__device__ __forceinline__ void wait_lgkm0() { asm volatile("" ::: "memory"); __builtin_amdgcn_s_waitcnt(0xC07F); asm volatile("" ::: "memory"); }
__device__ __forceinline__ bf16x4 ld_tr4(const LDS_PTR(char) p) { return __builtin_amdgcn_ds_read_tr16_b64_v4i16((LDS_PTR(bf16x4))p); }
__device__ __forceinline__ bf16x8 cat8(bf16x4 lo, bf16x4 hi) { return __builtin_shufflevector(lo, hi, 0, 1, 2, 3, 4, 5, 6, 7); }
__device__ __forceinline__ void mfma_acc(f32x4& acc, bf16x8 a, bf16x8 b) { asm volatile("v_mfma_f32_16x16x32_bf16 %0, %1, %2, %0" : "+v"(acc) : "v"(a), "v"(b)); }
template <int STEP> __device__ __forceinline__ void mfma_sel(f32x4& acc, bf16x8 a, bf16x8 b, unsigned& mask, int& dl, int wrap) {
  asm volatile("s_add_u32 %1, %1, %1\n\tv_mfma_f32_16x16x32_bf16 %0, %3, %4, %0\n\ts_cselect_b32 %2, %5, %6" : "+v"(acc), "+s"(mask), "=s"(dl) : "v"(a), "v"(b), "s"(wrap), "n"(STEP) : "scc");
}
constexpr unsigned wrap_mask(int first, int ns) { unsigned m = 0; for (int t = 0; t < 32; ++t) if ((first + t) % ns == ns - 1) m |= 0x80000000u >> t; return m; }
template <int N> __device__ __forceinline__ void wait_lgkm() { __builtin_amdgcn_s_waitcnt(0xC07F | (N << 8)); }
template <int R, int C> __device__ __forceinline__ void mfma_done(f32x4 (&acc)[R][C]) {
  asm volatile("s_nop 9");
#pragma unroll
  for (int i = 0; i < R; ++i)
#pragma unroll
    for (int j = 0; j < C; ++j) asm volatile("" : "+v"(acc[i][j]));
}
__device__ __forceinline__ int lane_id() { int l; asm volatile("v_mbcnt_lo_u32_b32 %0, -1, 0\n\tv_mbcnt_hi_u32_b32 %0, -1, %0" : "=v"(l)); return l; }
__device__ __forceinline__ float xsum16_32(float v) {
  auto a = __builtin_amdgcn_permlane16_swap(__float_as_uint(v), __float_as_uint(v), false, false); v = __uint_as_float(a[0]) + __uint_as_float(a[1]);
  auto c = __builtin_amdgcn_permlane32_swap(__float_as_uint(v), __float_as_uint(v), false, false); return __uint_as_float(c[0]) + __uint_as_float(c[1]);
}

__device__ __forceinline__ float ln_rstd(float var) { return __builtin_amdgcn_rcpf(rbf(__builtin_amdgcn_sqrtf(rbf(rbf(var) + 1e-5f)))); }

constexpr size_t WS_MOD = 0;
constexpr size_t WS_EPO = 64 << 10, WS_FL1 = 96 << 10;
constexpr size_t WS_CTL = 128 << 10;
constexpr int C2 = 0, C3 = 16, C4 = 32, C5 = 48, CM = 80;
constexpr size_t WS_FLG = WS_CTL + (88 << 10), CTL_BYTES = 120 << 10;
constexpr size_t WS_XCH = 512 << 10;
constexpr size_t WS_MOD2 = 1 << 20;
constexpr size_t WS_H = 2 << 20, WS_O = WS_H + (4 << 20), WS_H2 = WS_O + (4 << 20), WS_HH = WS_H2 + (4 << 20),
                 WS_PART = WS_HH + (size_t)S_ * F_ * 2;

template <int NL> __device__ __forceinline__ f32x2 slab_sum(const unsigned (&mc)[NL / 2], const u32x4 (&mw)[NL]) {
  f32x2 a2[4] = {};
#pragma unroll
  for (int i = 0; i < NL; ++i) {
    const float cv = (i & 1) ? hi2f(mc[i >> 1]) : lo2f(mc[i >> 1]);
    const float sv = rbf(cv * __builtin_amdgcn_rcpf(1.f + __builtin_amdgcn_exp2f(cv * -1.4426950408889634f)));
#pragma unroll
    for (int j = 0; j < 4; ++j) a2[j] += (f32x2){lo2f(mw[i][j]), hi2f(mw[i][j])} * sv;
  }
  float acc[8] = {a2[0][0], a2[0][1], a2[1][0], a2[1][1], a2[2][0], a2[2][1], a2[3][0], a2[3][1]};
#pragma unroll
  for (int j = 0; j < 4; ++j) { auto a = __builtin_amdgcn_permlane32_swap(__float_as_uint(acc[j]), __float_as_uint(acc[j + 4]), false, false); acc[j] = __uint_as_float(a[0]) + __uint_as_float(a[1]); }
#pragma unroll
  for (int j = 0; j < 2; ++j) { auto a = __builtin_amdgcn_permlane16_swap(__float_as_uint(acc[j]), __float_as_uint(acc[j + 2]), false, false); acc[j] = __uint_as_float(a[0]) + __uint_as_float(a[1]); }
#pragma unroll
  for (int j = 0; j < 2; ++j) acc[j] += __uint_as_float(__builtin_amdgcn_mov_dpp(__float_as_uint(acc[j]), 0x128, 0xf, 0xf, false));
  return (f32x2){acc[0], acc[1]};
}

constexpr int K3_B = 18432, K3_STG = 18432 + 24576;
constexpr int K4_RED = 57344;
constexpr int K5_G = 16384, K5_U = 16384 + 7168 + 16, K5_STG = 30736, K5_NS = 5;
constexpr int K6_NW = 12, K6_NA = 7, K6_A = K6_NW * 8192, K6_X = 9 * 8192;
constexpr int LDS_VEC = 158720, LDS_DUMMY = LDS_VEC + 1024, LDS_ARR = LDS_DUMMY + 256, MEGA_LDS = LDS_ARR + 64;
constexpr unsigned SPIN_CAP = 1u << 19;
#define KNOBS KB = 8, XD0 = 16, PFD = 16, FD1 = 8, FS0 = 4, PQ = 2, PQ5 = 4, PD2 = 4, PD3 = 4, PD4 = 4, PD5 = 48, PD6 = 6, PS6 = 1, XS0 = 14, XS1 = 4, TS5 = 2, VB3 = 2, SLB = 15, NDR = 1
enum { KNOBS };
constexpr unsigned long long SKIP6 = 0x7c;
static_assert(__builtin_popcountll(SKIP6) == 5, "44 stages of W are issued behind 49 barriers");

__device__ __forceinline__ void signal(unsigned* cnt, int c, int lane, unsigned v = 1) {
  if (lane < 8) __hip_atomic_fetch_add(cnt + (c * 8 + lane) * 32, v, __ATOMIC_RELAXED, __HIP_MEMORY_SCOPE_AGENT);
}
template <int D0, int D1 = 1> __device__ __forceinline__ void poll(const unsigned* cnt, unsigned want) {
  if (D0) __builtin_amdgcn_s_sleep(D0);
#pragma unroll 1
  for (unsigned spins = 0; spins < SPIN_CAP; ++spins) {
    if (__builtin_amdgcn_ballot_w64(__hip_atomic_load(cnt, __ATOMIC_RELAXED, __HIP_MEMORY_SCOPE_AGENT) < want) == 0) break;
    __builtin_amdgcn_s_sleep(D1);
  }
  __builtin_amdgcn_fence(__ATOMIC_ACQUIRE, "wavefront");
}

template <int D0, int D1, int EX> __device__ __forceinline__ void pollp(const unsigned* cnt, unsigned want, unsigned (&r)[4]) {
  unsigned n = SPIN_CAP;
#define PL1(a) "global_atomic_add %" #a ", %5, %9, off sc0\n\ts_sleep %8\n\t"
#define PL2(a, c) "global_atomic_add %" #a ", %5, %9, off sc0\n\ts_waitcnt vmcnt(3)\n\tv_cmp_gt_u32 vcc, %6, %" #c "\n\ts_cbranch_vccz 2f\n\ts_sleep %8\n\t"
  asm volatile("s_mov_b64 exec, %10\n\ts_sleep %7\n\t" PL1(0) PL1(1) PL1(2) "1:\n\t" PL2(3, 0) PL2(0, 1) PL2(1, 2) PL2(2, 3) "s_sub_u32 %4, %4, 1\n\ts_cbranch_scc0 1b\n2:\n\ts_mov_b64 exec, -1"
               : "=&v"(r[0]), "=&v"(r[1]), "=&v"(r[2]), "=&v"(r[3]), "+s"(n) : "v"(cnt), "s"(want), "n"(D0), "n"(D1), "v"(0), "n"(EX) : "memory", "vcc", "scc");
}
#define KEEP4(r) asm volatile("; keep %0 %1 %2 %3" ::"v"(r[0]), "v"(r[1]), "v"(r[2]), "v"(r[3]))
typedef float f32x32 __attribute__((ext_vector_type(32)));
typedef float f32x16 __attribute__((ext_vector_type(16)));
#define M5(i, j) "v_mfma_f32_16x16x32_bf16 v[28*" #i "+4*" #j ":28*" #i "+4*" #j "+3], v[128+4*" #j ":131+4*" #j "], v[112+4*" #i ":115+4*" #i "], v[28*" #i "+4*" #j ":28*" #i "+4*" #j "+3]\n\t"
#define RA5(i) "ds_read_b128 v[112+4*" #i ":115+4*" #i "], %[aA] offset:1024*" #i "\n\t"
#define RW5(j, h, a, o) "ds_read_b64_tr_b16 v[128+4*" #j "+2*" #h ":129+4*" #j "+2*" #h "], %[" #a "] offset:" #o "\n\t"
#define ST5(a) "v_add_u32 %[" #a "], %[dl], %[" #a "]\n\t"
#define SEL5A "s_add_u32 %[mask], %[mask], %[mask]\n\t"
#define SEL5B "s_cselect_b32 %[dl], %[wrap], 30736\n\t"
#define LG(n) "s_waitcnt lgkmcnt(" #n ")\n\t"
#define RP5(j, h) RW5(j, h, aP, 32*(j/2)+7184*(j&1)+3584*h)
#define TRIP5(X)                                                                                                                                        \
  M5(0, 0) RW5(6, 1, aM, 3584) M5(1, 0) RA5(2) ST5(aM) M5(1, 1) RA5(3) ST5(aP) M5(0, 1) ST5(aA) LG(6) M5(0, 2) M5(1, 2) SEL5A M5(1, 3) SEL5B              \
  M5(0, 3) M5(0, 4) M5(1, 4) LG(0) M5(1, 5) M5(0, 5) "s_barrier\n\t" M5(0, 6) RA5(0) M5(1, 6) RA5(1)                                                      \
  M5(2, 0) X M5(3, 0) RP5(0, 0) M5(3, 1) RP5(0, 1) M5(2, 1) RP5(1, 0) M5(2, 2) RP5(1, 1) M5(3, 2) RP5(2, 0) M5(3, 3) RP5(2, 1) M5(2, 3) RP5(3, 0)          \
  M5(2, 4) RP5(3, 1) M5(3, 4) RP5(4, 0) M5(3, 5) RP5(4, 1) M5(2, 5) RP5(5, 0) M5(2, 6) RP5(5, 1) LG(8) M5(3, 6) RW5(6, 0, aM, 0)
#define ACC5 "+{v[0:31]}"(c0), "+{v[32:63]}"(c1), "+{v[64:95]}"(c2), "+{v[96:111]}"(c3)
#define CLOB5 "memory", "scc", "v112", "v113", "v114", "v115", "v116", "v117", "v118", "v119", "v120", "v121", "v122", "v123", "v124", "v125", "v126", "v127", "v128", "v129", "v130", "v131", "v132", "v133", \
              "v134", "v135", "v136", "v137", "v138", "v139", "v140", "v141", "v142", "v143", "v144", "v145", "v146", "v147", "v148", "v149", "v150", "v151", "v152", "v153", "v154", "v155"
static_assert(K5_STG == 30736 && K5_U - K5_G == 7184, "TRIP5");

#define C3(i, j) "v[24*" #i "+4*" #j "-12*(" #i "/4):24*" #i "+4*" #j "-12*(" #i "/4)+3]"
#define M3(i, j) "v_mfma_f32_16x16x32_bf16 " C3(i, j) ", v[124+4*" #j ":127+4*" #j "], v[104+4*" #i ":107+4*" #i "], " C3(i, j) "\n\t"
#define RA3(i, u) "ds_read_b128 v[104+4*" #i ":107+4*" #i "], %[aA] offset:1024*" #i "+9216*" #u "\n\t"
#define RH3(u) "ds_read_b128 v[120:123], %[aH] offset:9216*" #u "\n\t"
#define RW3(j, h, a, u) "ds_read_b64_tr_b16 v[124+4*" #j "+2*" #h ":125+4*" #j "+2*" #h "], %[" #a "] offset:8192*(" #j "/2)+2048*" #h "+4096*" #u "\n\t"
#define ST3(a, d) "v_add_u32 %[" #a "], " #d ", %[" #a "]\n\t"
#define H2_3(u, S0, S1, S2, B) M3(0, 0) RW3(5, 1, a1, u) M3(1, 0) RA3(2, u) M3(1, 1) RA3(3, u) S0 M3(0, 1) LG(7) S1 M3(0, 2) S2 M3(1, 2) M3(1, 3) M3(0, 3) LG(2) M3(0, 4) M3(1, 4) LG(0) B
#define H1_3(n, X)                                                                                                                                      \
  M3(1, 5) RA3(1, n) M3(0, 5) RA3(0, n) M3(2, 0) X M3(3, 0) RW3(0, 0, a0, n) M3(3, 1) RW3(0, 1, a0, n) M3(2, 1) RW3(1, 0, a1, n) M3(2, 2) RW3(1, 1, a1, n) \
  M3(3, 2) RW3(2, 0, a1, n) M3(3, 3) RW3(2, 1, a1, n) M3(2, 3) M3(4, 3) RW3(3, 0, a0, n) M3(4, 4) RW3(3, 1, a0, n) M3(2, 4) RH3(n)                         \
  M3(3, 4) RW3(4, 0, a0, n) M3(3, 5) RW3(4, 1, a0, n) LG(7) M3(2, 5) RW3(5, 0, a1, n)
#define STAGE3(d, X) H2_3(0, , , , ) H1_3(1, ) H2_3(1, ST3(a0, d), ST3(aA, d), ST3(a1, d) ST3(aH, d), "s_barrier\n\t") H1_3(0, X)
#define CV3(i, j, t) "v_cvt_pk_bf16_f32 v[104+2*" #t "], v[24*" #i "+4*" #j "-12*(" #i "/4)], v[24*" #i "+4*" #j "-12*(" #i "/4)+1]\n\tv_cvt_pk_bf16_f32 v[105+2*" #t "], v[24*" #i "+4*" #j "-12*(" #i "/4)+2], v[24*" #i "+4*" #j "-12*(" #i "/4)+3]\n\t"
#define WR3(i, t, a, o) "ds_write_b64 %[" #a "], v[104+2*" #t ":105+2*" #t "] offset:2048*" #i "+" #o "\n\t"
#define CW3(i, j, t, a, o) CV3(i, j, t) WR3(i, t, a, o)
#define V8(a) "v" #a "0", "v" #a "1", "v" #a "2", "v" #a "3", "v" #a "4", "v" #a "5", "v" #a "6", "v" #a "7"
#define V10(a) V8(a), "v" #a "8", "v" #a "9"
static_assert(K3_STG == 43008, "STAGE3");
__device__ __forceinline__ int w2_stage(int j) {
  const int j2 = j - 16, p = (j2 * 52) >> 8, r = j2 - p * 5;
  return j < 16 ? j * 112 * 2048 : (p * 224 + 32 + r * 32 + (r > 2 ? 32 : 0)) * 2048 + (r == 2);
}
__global__ __launch_bounds__(768, 3) __attribute__((amdgpu_num_sgpr(98))) void k2_block(const u16* __restrict__ x, const u16* __restrict__ wqkv, const u16* __restrict__ wo,
                                                   const u16* __restrict__ w1, const u16* __restrict__ w3, const u16* __restrict__ w2,
                                                   const u16* __restrict__ cnd, const u16* __restrict__ wmod, u16* __restrict__ out, char* __restrict__ ws) {
  extern __shared__ __attribute__((aligned(16))) char smem_[];
  LDS_PTR(char) smem = (LDS_PTR(char))smem_;
  const int tid = threadIdx.x;
  int lane = tid & 63;
  const int wave = __builtin_amdgcn_readfirstlane(tid >> 6);
  const unsigned lds_base = (unsigned)(uintptr_t)smem;
  const int b = blockIdx.x, xc = b & 7, ii = b >> 3;
  const int tm3 = (xc & 1) * 8 + (ii & 7), tn3 = (xc >> 1) * 4 + (ii >> 3);
  const int tm5 = (xc & 1) * 4 + (ii & 3), tn5 = (xc >> 1) * 8 + (ii >> 2);
  const int kh6 = xc >> 2, tm6 = (xc & 1) * 8 + (xc & 2) * 2 + (ii & 3), tn6 = ii >> 2;
  const int tm4 = tm6, tn4 = tn6 * 2 + kh6;
  const int u1 = (b >> 4) * 8 + xc;
  unsigned* cnt = (unsigned*)(ws + WS_CTL);
  unsigned long long* fl1 = (unsigned long long*)(ws + WS_FL1);
  STAMP_AT(0 );

  if (wave >= 8) {
    const int lw = wave - 8, P = b * 4 + lw, lane = lane_id();
    const bool isA = lw < 2;
    asm volatile("" ::"s"(wqkv), "s"(wo), "s"(w1), "s"(w3), "s"(w2), "s"(ws));
    int kpf = KB + VB3;
    unsigned pr[4] = {};
    auto touch = [&]() {
      if (kpf < 28 && __builtin_amdgcn_s_getreg((3 << 11) | (8 << 6) | 7) <= 7) {
        stouch4((const char*)((P & 1) ? w3 : w1) + (kpf * 2048 + (P >> 1) * 4) * 128);
        ++kpf;
      }
    };
    const char* epo = ws + WS_EPO;
    const unsigned swz = ((lane & 7) ^ ((lane >> 3) & 6)) * 16;
    const int cg = lw * 16 + tn4;
    auto issue4 = [&](int s, unsigned slot) {
      const char* p = (const char*)wo + tn4 * 128 + (lw * 32 + s * 128) * 2048;
      unsigned v4[4];
#pragma unroll
      for (int i = 0; i < 4; ++i) v4[i] = (lane >> 3) * 2048 + swz + i * 15360;
      glds16x4<false>(p, p, v4, lds_base + slot + lw * 4096);
    };
    auto run3 = [&](auto isA_) {
      constexpr bool A_ = decltype(isA_)::value; constexpr int N = A_ ? 9 : 12;
      const char* sp = A_ ? ws + WS_H + lw * 131072 + (tm3 * 8 - 1) * 1024 : (const char*)wqkv + tn3 * 128;
      unsigned vo[3][4];
#pragma unroll
      for (int i = 0; i < 12; ++i) {
        const int tb = (lw - 2) * 12 + i;
        vo[i >> 2][i & 3] = A_ ? lane * 16 + (i < 8 ? 1024 + (i >> 2) * 4096 : tm3 ? 0 : 1024) : ((tb & 7) * 8 + (lane >> 3)) * 6144 + (tb >> 3) * 2048 - (i & 3) * 1024 + swz;
      }
      const unsigned d0 = lds_base + (A_ ? lw * 9216 : K3_B + (lw - 2) * 12288);
      auto issue = [&](unsigned st) {
        if (A_) { glds16x4<true>(sp, sp, vo[0], d0 + st + 1024); glds16s(sp, vo[2][0], d0 + st, 0); glds16x4<true>(sp, sp, vo[1], d0 + st + 5120); }
        else {
#pragma unroll
          for (int i = 0; i < 3; ++i) glds16x4<false>(sp, sp, vo[i], d0 + st + i * 4096);
        }
        sp += A_ ? 262144 : 64 * 6144;
      };
      { unsigned long long ep;
        asm volatile("s_load_dwordx2 %0, %1, 0x0\n\ts_waitcnt lgkmcnt(0)" : "=s"(ep) : "s"(epo) : "memory");
        ep += 1;
        if (lw == 3) { *(LDS_PTR(u32x4))(smem + LDS_ARR) = (u32x4){0u, 0u, 0u, 0u}; wait_lgkm0(); }
        if (lw == 1) {
#pragma unroll
          for (int k = 0; k < 3; ++k) {
            const int i = b * 158 + (k * 64 + lane < 158 ? k * 64 + lane : 0);
            st16<true>(ws, i < 32768 ? (unsigned)WS_XCH + i * 16 : (unsigned)WS_CTL + (i - 32768) * 16, i < 32768 ? (u32x4){~0u, ~0u, ~0u, ~0u} : (u32x4){0u, 0u, 0u, 0u});
          }
        }
        BAR();
        auto flag = [&](int idx) { wait_vm<0>(); if (lane < 8) __hip_atomic_store(fl1 + lane * 256 + idx, ep, __ATOMIC_RELAXED, __HIP_MEMORY_SCOPE_AGENT); };
        if (lw == 1 && (b & 8)) {
          const int kq = u1 & 3, cgp = u1 >> 2;
          float sm = 0.f;
#pragma unroll
          for (int r = 0; r < 8; ++r) sm += *(const LDS_PTR(float))(smem + 8192 + (r * 64 + lane) * 4);
          __hip_atomic_store((unsigned*)(ws + WS_MOD) + kq * 2048 + cgp * 64 + lane, __float_as_uint(sm), __ATOMIC_RELAXED, __HIP_MEMORY_SCOPE_AGENT);
          flag(((cgp >> 3) & 1) * 64 + (cgp >> 4) * 32 + (cgp & 7) * 4 + kq);
        }
        if (b & 8) BAR();
        BAR();
        if (lw == 1 && !(b & 8)) flag(128 + u1);
        auto flags = [&](auto n_, int first) {
#pragma unroll 1
          for (unsigned spins = 0; spins < SPIN_CAP; ++spins) {
            bool ok = true;
#pragma unroll
            for (int k = 0; k < decltype(n_)::value; ++k) ok &= __hip_atomic_load(fl1 + xc * 256 + (first + k) * 64 + lane, __ATOMIC_RELAXED, __HIP_MEMORY_SCOPE_AGENT) == ep;
            if (__builtin_amdgcn_ballot_w64(!ok) == 0) break;
            __builtin_amdgcn_s_sleep(FS0);
          }
        };
        if (lw == 0) flags(IC<1>(), (b >> 3) & 1);
        BAR();
        if (lw == 1) {
          __builtin_amdgcn_s_sleep(FD1);
          flags(IC<4>(), 0);
          if (b == 0 && lane == 0) __hip_atomic_store((unsigned long long*)(ws + WS_EPO), ep, __ATOMIC_RELAXED, __HIP_MEMORY_SCOPE_AGENT);
        }
      }
      BAR();
      if (lw == 0) {
        signal(cnt, C2 + (u1 >> 3), lane, (b & 8) ? 1u : 0x10000u);
        pollp<PD2, PQ, 3>(cnt + ((C2 + ((lane & 1) && tm3 ? tm3 - 1 : tm3)) * 8 + (tn3 & 7)) * 32, 0x80000u, pr);
      }
      if (!A_) { issue(0); issue(K3_STG); }
      BAR();
      if (A_) { issue(0); issue(K3_STG); }
      wait_vm<N>();
      KEEP4(pr);
      BAR();
      issue(2 * K3_STG);
      unsigned st = 0;
#pragma unroll 1
      for (int j = 0; j < 13; ++j) {
        wait_vm<N>();
        BAR();
        issue(st);
        st = st == 2 * K3_STG ? 0 : st + K3_STG;
      }
      wait_vm<N>();
      BAR();
      issue4(0, 57344); issue4(5, 57344 + 5 * 16384);
      glds4((P & 1) ? w3 : w1, ((KB + ((lane >> 2) < VB3 ? lane >> 2 : 0)) * 2048 + (P >> 1) * 4 + (lane & 3)) * 128, lds_base + LDS_DUMMY);
      wait_vm<9>();
    };
    if (isA) run3(T_()); else run3(F__());
    { BAR();
      BAR();
      issue4(1, 57344 + 16384);
      BAR();
      issue4(2, 57344 + 2 * 16384); issue4(3, 57344 + 3 * 16384); issue4(4, 57344 + 4 * 16384);
      BAR();
      if (lw == 0) signal(cnt, C3 + tm3, lane);
      issue4(6, 0); issue4(7, 16384);
      if (lw == 0) pollp<PD3, PQ, 1>(cnt + ((C3 + tm4) * 8 + tn6) * 32, 16, pr);
      wait_vm<8>();
      BAR();
      wait_vm<0>();
      KEEP4(pr);
      BAR();
      poll<0, 4>(cnt + (CM * 8 + cg) * 32, 4);
      f32x4 pv[4];
#pragma unroll
      for (int i = 0; i < 4; ++i) pv[i] = __builtin_bit_cast(f32x4, ld16<true>(ws + WS_MOD2, (((lane >> 4) * 4 + i) * 4096 + cg * 64 + (lane & 15) * 4) * 4));
      f32x4 v = (pv[0] + pv[1]) + (pv[2] + pv[3]);
#pragma unroll
      for (int e = 0; e < 4; ++e) { v[e] = rbf(xsum16_32(v[e])); if (lw == 2) v[e] = rbf(1.f + v[e]); if (lw == 1 || lw == 2) v[e] *= -1.4426950408889634f; }
      *(LDS_PTR(f32x4))(smem + LDS_VEC + lw * 256 + (lane & 15) * 16) = v;
      wait_lgkm0();
    }
    auto run5 = [&](auto isA_) {
      constexpr bool A_ = decltype(isA_)::value; constexpr int N = A_ ? 8 : 7;
      const char* sp = A_ ? ws + WS_H2 + (tm5 * 16 + lw * 8) * 1024 : (const char*)(lw == 2 ? w1 : w3) + tn5 * 224;
      unsigned vp[N];
#pragma unroll
      for (int i = 0; i < N; ++i) { const int L = i * 64 + lane, r = L / 14, c = L - r * 14; vp[i] = A_ ? L * 16 : r * (F_ * 2) + c * 16; }
      const unsigned d0 = lds_base + (A_ ? lw * 8192 : lw == 2 ? K5_G : K5_U);
      unsigned st = 3 * K5_STG;
      auto issue = [&]() {
#pragma unroll
        for (int i = 0; i < N; ++i) { if (A_) glds16s(sp, vp[i], d0 + st, i * 1024); else glds16(sp, vp[i], d0 + st, i * 1024); }
        sp += A_ ? 131072 : 32 * F_ * 2; st = st == (K5_NS - 1) * K5_STG ? 0 : st + K5_STG;
      };
      BAR();
      if (!A_) { issue(); issue(); }
      BAR();
      u32x4 xk[4];
      auto keep = [&]() {
#pragma unroll
        for (int i = 0; i < 4; ++i) { xk[i] = *(const LDS_PTR(u32x4))(smem + lw * 4096 + i * 1024 + lane * 16); asm volatile("" : "+v"(xk[i])); }
      };
      if (A_ && lw == 0) { signal(cnt, C4 + tm4, lane); keep(); pollp<PD4, PQ, 3>(cnt + ((C4 + tm5 * 2 + (lane & 1)) * 8 + (tn5 & 7)) * 32, 16, pr); }
      else { keep(); if (!A_) { issue(); issue(); issue(); } }
      BAR();
      if (A_) { issue(); issue(); }
      wait_vm<A_ ? N : 4 * N>();
      KEEP4(pr);
      BAR();
      if (A_) { issue(); issue(); issue(); }
#pragma unroll 1
      for (int j = 0; j < 27; ++j) {
        wait_vm<3 * N>();
        BAR();
        issue();
        if (j == 6) {
#pragma unroll
          for (int i = 0; i < 4; ++i) st16<true>(ws + WS_PART, (b * 4 + lw) * 4096 + i * 1024 + lane * 16, (u32x4){~0u, ~0u, ~0u, ~0u});
        }
        if (j == 12 && lw == 0 && lane == 0) __hip_atomic_store((unsigned*)(ws + WS_FLG) + b * 32, 16u, __ATOMIC_RELAXED, __HIP_MEMORY_SCOPE_AGENT);
        touch(); __builtin_amdgcn_s_sleep(TS5); touch();
      }
      const char* sb = A_ ? ws + WS_HH + (kh6 * 56 * 128 + tm6 * 8 + lw * 4) * 1024 : (const char*)w2 + kh6 * 1792 * 2048 + tn6 * 256 + ((lw ^ kh6 ^ 1) & 1) * 128;
      unsigned vo[4];
#pragma unroll
      for (int i = 0; i < 4; ++i) {
        const int r = i * 8 + (lane >> 3), k = 8 * ((r >> 2) & 3) + 4 * (r >> 4) + (r & 3);
        vo[i] = A_ ? lane * 16 : k * 2048 - i * 1024 + swz;
      }
      const unsigned d6 = lds_base + (A_ ? K6_A : 0) + (lw & 1) * 4096;
      st = 0;
      const int tab = w2_stage(lane);
      int jn = 0;
      auto issue6 = [&]() {
        if (A_) { glds16x4<true>(sb, sb, vo, d6 + st); sb += 131072; }
        else { const int o = __builtin_amdgcn_readlane(tab, jn); const char* s0 = sb + (o & ~1); glds16x4<false>(s0, s0 + (o & 1) * 65536, vo, d6 + st); ++jn; }
        st = st == ((A_ ? K6_NA : K6_NW) - 1) * 8192 ? 0 : st + 8192;
      };
      auto pre6 = [&](int n) {
        if (!A_) {
#pragma unroll 1
          for (int i = 0; i < n; ++i) issue6();
        }
      };
      constexpr int E = A_ ? 0 : 4;
      { const char* p = (const char*)w2 + (kh6 * 1792 + ii) * 2048;
        const int n = lw * 56 + (lane < 56 ? lane : 0), o = w2_stage((xc & 3) * 14 + (n >> 4));
        glds4(p, (o & ~1) + (o & (ii >> 4) & 1) * 65536 + (n & 15) * 128, lds_base + LDS_DUMMY); }
      wait_vm<3 * N + 1>(); BAR();
      wait_vm<2 * N + 1>(); BAR(); pre6(4);
      wait_vm<N + 4 * E + 1>(); BAR(); pre6(4);
      wait_vm<8 * E + 1>(); BAR(); pre6(4);
      BAR();
      if (A_) {
        volatile LDS_PTR(unsigned) go = (volatile LDS_PTR(unsigned))(smem + LDS_ARR + 4);
        if (lw == 0) { pollp<PD5, PQ5, 1>(cnt + ((C5 + (tm6 >> 1) * 4 + kh6 * 2) * 8 + tn6) * 32, 16, pr); *go = 1u; }
        else {
#pragma unroll 1
          for (unsigned spins = 0; spins < SPIN_CAP && __builtin_amdgcn_readfirstlane(*go) == 0; ++spins) __builtin_amdgcn_s_sleep(1);
          asm volatile("" ::: "memory");
        }
        STAMP_L(31);
        issue6(); issue6(); issue6();
        wait_vm<8>(); KEEP4(pr); BAR(); issue6(); issue6();
        wait_vm<12>(); BAR(); issue6(); issue6();
      } else { wait_vm<16>(); BAR(); BAR(); }
#pragma unroll 1
      for (int c = 2; c < 51; ++c) { wait_vm<16>(); BAR(); if (A_ || !((SKIP6 >> c) & 1)) issue6(); }
      wait_vm<16>(); BAR(); wait_vm<12>(); BAR(); wait_vm<8>(); BAR(); wait_vm<4>(); BAR(); wait_vm<0>(); BAR();
#pragma unroll
      for (int i = 0; i < 4; ++i) *(LDS_PTR(u32x4))(smem + K6_X + lw * 4096 + i * 1024 + lane * 16) = xk[i];
      BAR(); wait_lgkm0(); BAR();
    };
    if (isA) run5(T_()); else run5(F__());
    asm volatile("s_waitcnt lgkmcnt(0)" ::: "memory");
    return;
  }

  int fr = lane & 15, g = lane >> 4, p4 = fr & 3, row = 4 * g + (fr >> 2);
  int lsw = row * 128 + (((row >> 1) & 3) * 32) + p4 * 8;
  { const int mb = u1, hf = (b >> 3) & 1, kb0 = hf * 16 + wave * 2, cofs = (g & 1) * 16 + (g >> 1) * 8;
    u32x4 v[4];
    auto ldx = [&]() {
#pragma unroll
      for (int p = 0; p < 4; ++p) v[p] = *(const u32x4*)(x + (size_t)(mb * 16 + fr) * D_ + ((kb0 + (p & 1)) ^ ((p >> 1) * 16)) * 32 + cofs);
    };
    LDS_PTR(f32x2) sred = (LDS_PTR(f32x2))(smem + 4096);
    auto up2 = [](unsigned u) { return (f32x2){lo2f(u), hi2f(u)}; };
    auto stats = [&]() {
      f32x2 s2 = {0.f, 0.f}, q2 = {0.f, 0.f};
#pragma unroll
      for (int k = 0; k < 16; ++k) s2 += up2(v[k >> 2][k & 3]);
      const float mw = xsum16_32(s2[0] + s2[1]) * (1.f / 128.f);
#pragma unroll
      for (int k = 0; k < 16; ++k) { const f32x2 d = up2(v[k >> 2][k & 3]) - mw; q2 += d * d; }
      const float qw = xsum16_32(q2[0] + q2[1]);
      if (lane < 16) sred[wave * 16 + fr] = (f32x2){mw, qw};
      wait_lgkm0();
    };
    auto rest = [&]() {
    STAMP_AT(1 );
    float mean = 0.f, m2 = 0.f; f32x2 sw[8];
#pragma unroll
    for (int w = 0; w < 8; ++w) { sw[w] = sred[w * 16 + fr]; mean += sw[w][0]; m2 += sw[w][1]; }
    mean *= 0.125f;
#pragma unroll
    for (int w = 0; w < 8; ++w) { const float d = sw[w][0] - mean; m2 += 128.f * d * d; }
    const float rstd = ln_rstd(m2 * (1.f / 1024.f));
    mean = rbf(mean);
    unsigned nv[8];
#pragma unroll
    for (int k = 0; k < 8; ++k) {
      const f32x2 d = up2(v[k >> 2][k & 3]) - mean;
      const f32x2 n = up2(pk2(d[0], d[1])) * rstd;
      nv[k] = pk2(n[0], n[1]);
      asm volatile("" : "+v"(nv[k]));
    }
    SCHED0(); BAR(); SCHED0();
    STAMP_AT(2 );
    float mp[8];
#pragma unroll
    for (int i = 0; i < 8; ++i) mp[i] = __uint_as_float(__hip_atomic_load((unsigned*)(ws + WS_MOD) + hf * 512 + tid + i * 1024, __ATOMIC_RELAXED, __HIP_MEMORY_SCOPE_AGENT));
    *(LDS_PTR(unsigned))(smem + tid * 4) = pk2((mp[0] + mp[2]) + (mp[4] + mp[6]), 1.f + rbf((mp[1] + mp[3]) + (mp[5] + mp[7])));
    wait_lgkm0();
#pragma unroll
    for (int p = 0; p < 2; ++p) {
      const LDS_PTR(u32x4) tp = (const LDS_PTR(u32x4))(smem + ((wave * 2 + p) * 32 + cofs) * 4);
      const u32x4 t0 = tp[0], t1 = tp[1];
      unsigned o[4];
#pragma unroll
      for (int j = 0; j < 4; ++j) {
        const unsigned ta = j < 2 ? t0[2 * j] : t1[2 * j - 4], tb = j < 2 ? t0[2 * j + 1] : t1[2 * j - 3];
        const unsigned u = pk2(lo2f(nv[p * 4 + j]) * hi2f(ta), hi2f(nv[p * 4 + j]) * hi2f(tb));
        o[j] = pk2(lo2f(u) + lo2f(ta), hi2f(u) + lo2f(tb));
      }
      auto s0 = __builtin_amdgcn_permlane16_swap(o[0], o[2], false, false);
      auto s1 = __builtin_amdgcn_permlane16_swap(o[1], o[3], false, false);
      st16<true>(ws + WS_H, ((kb0 + p) * 128 + mb) * 1024 + lane * 16, (u32x4){s0[0], s1[0], s0[1], s1[1]});
    }
    STAMP_AT(3 );
    wait_vm<0>();
    STAMP_AT(4 );
    BAR();
    STAMP_AT(5 );
    };
    if (hf) {
      const int mrow = (u1 & 3) * 256 + (tid >> 3) * 4;
      const u32x2 c2 = *(const u32x2*)(cnd + mrow);
      u32x4 mw[4];
#pragma unroll
      for (int i = 0; i < 4; ++i) mw[i] = __builtin_nontemporal_load((const u32x4*)(wmod + (size_t)(mrow + i) * 6144 + (u1 >> 2) * 64 + (lane & 7) * 8));
      const unsigned mc[2] = {c2[0], c2[1]};
      *(LDS_PTR(f32x2))(smem + 8192 + (wave * 64 + (lane & 7) * 8 + (lane >> 4) * 2) * 4) = slab_sum<4>(mc, mw);
      wait_lgkm0();
      BAR();
      BAR();
      ldx();
      stats();
      BAR();
      rest();
    } else {
      __builtin_amdgcn_s_sleep(XD0);
      ldx();
      SCHED0();
      const int sl = (3 - ((unsigned)(uintptr_t)wqkv >> 7) - (xc >> 1) * 4) & 7;
      const int i2 = tid - 384, r0 = i2 < 0 ? tid / 12 : i2 / 3, c0 = i2 < 0 ? tid - r0 * 12 : (i2 - r0 * 3) * 4 + sl;
      const bool on = i2 < 0 || (sl < 4 && i2 < 96);
      __builtin_amdgcn_s_sleep(PFD);
      const unsigned pf = *(const unsigned*)((const char*)wqkv + (xc >> 1) * 512 + (on ? ((((xc & 1) ^ (i2 >= 0)) * 512 + (ii >> 1) * 32 + r0) * 48 + (c0 >> 2) * 16 + (c0 & 3)) * 128 : 0));
      SCHED0(); BAR(); SCHED0();
      stats();
      SCHED0(); BAR(); SCHED0();
      rest();
      asm volatile("" ::"v"(pf));
    }
  }
  {
    f32x4 bias[2];
    { const int kbase = tm3 * 128 + wave * 16 - 16;
#pragma unroll
      for (int t = 0; t < 4; ++t) {
        const int kk = (t >> 1) * 16 + 4 * g + (t & 1) * 2 + (fr & 1), df = 16 + fr - kk;
        bias[t >> 1][(t & 1) * 2] = bias[t >> 1][(t & 1) * 2 + 1] = (df >= 0 && df <= 14 && kbase + kk >= 0) ? 0.f : -__builtin_inff();
      }
      asm volatile("" : "+v"(bias[0]), "+v"(bias[1])); }
    u32x4 mc, mw[8];
    if (wave < 4) {
      const int wm = wave & 1, bA = wave;
      unsigned a0 = lds_base + K3_B + (lsw ^ (bA * 32)), a1 = lds_base + K3_B + (lsw ^ ((bA ^ 1) * 32)), aA = lds_base + 1024 + wm * 4096 + lane * 16, aH = lds_base + lane * 16;
      unsigned lq = lds_base + bA * 512 + lane * 8, lv = lds_base + 36864 + bA * 512 + (lane ^ ((g >> 1) * 8)) * 8;
      int n = 4;
      asm volatile(".irp r,0,8,16,24,32,40,48,56,64,72,80,88,96\n\tv_mov_b64 v[\\r:\\r+1], 0\n\tv_mov_b64 v[\\r+2:\\r+3], 0\n\tv_mov_b64 v[\\r+4:\\r+5], 0\n\tv_mov_b64 v[\\r+6:\\r+7], 0\n\t.endr\n\t"
                   "s_setprio 2\n\ts_waitcnt vmcnt(0)\n\ts_barrier\n\t"
                   "s_barrier\n\t"
                   RA3(1, 0) RA3(0, 0) RW3(0, 0, a0, 0) RW3(0, 1, a0, 0) RW3(1, 0, a1, 0) RW3(1, 1, a1, 0) RW3(2, 0, a1, 0) RW3(2, 1, a1, 0) RW3(3, 0, a0, 0) RW3(3, 1, a0, 0) RH3(0)
                   RW3(4, 0, a0, 0) RW3(4, 1, a0, 0) RW3(5, 0, a1, 0) LG(8)
                   ".p2align 5\n1:\n\t" STAGE3(43008, ) STAGE3(43008, ) STAGE3(-86016, "s_sub_u32 %[n], %[n], 1\n\t") "s_cbranch_scc0 1b\n\t"
                   H2_3(0, , , , ) H1_3(1, )
                   H2_3(1, "v_add_u32 %[a0], %[mo], %[lq]\n\t", "v_add_u32 %[aA], %[mo], %[lv]\n\t", "v_xor_b32 %[a1], 512, %[a0]\n\tv_xor_b32 %[aH], 512, %[aA]\n\t", "s_barrier\n\t")
                   M3(1, 5) M3(0, 5) M3(2, 0) CV3(0, 0, 0) M3(3, 0) WR3(0, 0, a0, 0) CV3(1, 0, 1) M3(3, 1) WR3(1, 1, a0, 0) CV3(0, 1, 2) M3(2, 1) WR3(0, 2, a1, 0) CV3(1, 1, 3)
                   M3(2, 2) WR3(1, 3, a1, 0) CV3(0, 2, 0) M3(3, 2) WR3(0, 0, a1, 18432) CV3(1, 2, 1) M3(3, 3) WR3(1, 1, a1, 18432) CV3(0, 3, 2) M3(2, 3) WR3(0, 2, a0, 18432) CV3(1, 3, 3)
                   M3(4, 3) WR3(1, 3, a0, 18432) CV3(0, 4, 0) M3(4, 4) WR3(0, 0, aA, 0) CV3(1, 4, 1) M3(2, 4) WR3(1, 1, aA, 0) CV3(0, 5, 2) M3(3, 4) WR3(0, 2, aH, 0) CV3(1, 5, 3)
                   M3(3, 5) WR3(1, 3, aH, 0) CV3(2, 0, 0) M3(2, 5) WR3(2, 0, a0, 0)
                   CW3(3, 0, 1, a0, 0) CW3(3, 1, 2, a1, 0) CW3(2, 1, 3, a1, 0) CW3(2, 2, 0, a1, 18432) CW3(3, 2, 1, a1, 18432) CW3(3, 3, 2, a0, 18432) CW3(2, 3, 3, a0, 18432)
                   CV3(4, 3, 0) WR3(0, 0, lq, 18432) CV3(4, 4, 1) WR3(0, 1, lv, 0) CW3(2, 4, 2, aA, 0) CW3(3, 4, 3, aA, 0) CW3(3, 5, 0, aH, 0) CW3(2, 5, 1, aH, 0)
                   "s_setprio 0\n\t" LG(0)
                   : [a0] "+v"(a0), [a1] "+v"(a1), [aA] "+v"(aA), [aH] "+v"(aH), [n] "+s"(n)
                   : [lq] "v"(lq), [lv] "v"(lv), [mo] "s"((wm * 4 + 1) * 2048)
                   : "memory", "scc", V10(), V10(1), V10(2), V10(3), V10(4), V10(5), V10(6), V10(7), V10(8), V10(9), V10(10), V10(11), V10(12), V10(13), V8(14));
    } else {
      wait_vm<0>();
      BAR(); BAR();
      int k = -4; const int P = b * 4 + (wave & 3);
      auto idle = [&]() {
        if (k < KB && __builtin_amdgcn_s_getreg((3 << 11) | (8 << 6) | 7) <= 7) {
          stouch4(k < 0 ? (const char*)wo + ((k + 4) * 4096 + P * 4) * 128 : (const char*)((P & 1) ? w3 : w1) + (k * 2048 + (P >> 1) * 4) * 128); ++k; }
        BAR();
      };
      {
        const unsigned* pc = cnt + ((C2 + ((lane & 1) && tm3 ? tm3 - 1 : tm3)) * 8 + (tn3 & 7)) * 32;
        unsigned seen = 0x80008u;
        idle(); idle(); idle();
        if (wave == 4) asm volatile("global_load_dword %0, %1, off sc1" : "=v"(seen) : "v"(pc) : "memory");
        idle(); idle();
        asm volatile("s_waitcnt vmcnt(0)" : "+v"(seen)::"memory");
        if (__builtin_amdgcn_ballot_w64(seen < 0x80008u) != 0) poll<0>(pc, 0x80008u);
      }
#pragma unroll 1
      for (int t = 5; t < SLB; ++t) { if (t < 14) idle(); else BAR(); }
      wait_lgkm0();
      const int mrow = ((b & 3) * 4 + (wave & 3)) * 64 + (lane >> 3) * 8;
      mc = *(const u32x4*)(cnd + mrow);
#pragma unroll
      for (int i = 0; i < 8; ++i) mw[i] = __builtin_nontemporal_load((const u32x4*)(wmod + (size_t)(mrow + i) * 6144 + 2048 + (b >> 2) * 64 + (lane & 7) * 8));
      SCHED0();
#pragma unroll
      for (int t = SLB; t < 16; ++t) BAR();
    }
    lane = lane_id(); fr = lane & 15; g = lane >> 4; p4 = fr & 3; row = 4 * g + (fr >> 2); lsw = row * 128 + (((row >> 1) & 3) * 32) + p4 * 8;
    wait_lgkm0();
    BAR();
    STAMP_AT(9 );
    const LDS_PTR(char) eb = smem + wave * 2048;
    auto ld2 = [&](int o) { return cat8(*(const LDS_PTR(bf16x4))(eb + o), *(const LDS_PTR(bf16x4))(eb + o + 512)); };
    const int vo = 36864 + ((p4 * 16 + row) ^ ((p4 >> 1) * 8)) * 8;
    bf16x8 qf[2], k0[2], k1[2], v0[2], v1[2];
#pragma unroll
    for (int hd = 0; hd < 2; ++hd) { qf[hd] = ld2(2048 + hd * 1024 + lane * 8); k0[hd] = ld2(18432 + hd * 1024 + lane * 8); k1[hd] = ld2(18432 + 2048 + hd * 1024 + lane * 8); }
#pragma unroll
    for (int hd = 0; hd < 2; ++hd) {
      v0[hd] = cat8(ld_tr4(eb + vo + hd * 1024), ld_tr4(eb + vo + hd * 1024 + 2048));
      v1[hd] = cat8(ld_tr4(eb + vo + hd * 1024 + 512), ld_tr4(eb + vo + hd * 1024 + 512 + 2048));
    }
    SCHED0();
    const f32x4 z = {0.f, 0.f, 0.f, 0.f};
    const bool odd = fr & 1;
    const unsigned sel0 = odd ? 0x01000c0cu : 0x0c0c0100u, sel1 = odd ? 0x03020c0cu : 0x0c0c0302u;
    float inf = __builtin_inff(); asm("" : "+v"(inf));
    auto mx2 = [&](float a, float c) { return __builtin_amdgcn_fmed3f(a, c, inf); };
    auto both = [](float a, float c, auto op, auto fin) {
      auto u = __builtin_amdgcn_permlane16_swap(__float_as_uint(a), __float_as_uint(c), false, false);
      float r = op(__uint_as_float(u[0]), __uint_as_float(u[1]));
      auto w = __builtin_amdgcn_permlane32_swap(__float_as_uint(r), __float_as_uint(r), false, false);
      r = fin(op(__uint_as_float(w[0]), __uint_as_float(w[1])));
      auto y = __builtin_amdgcn_permlane16_swap(__float_as_uint(r), __float_as_uint(r), false, false);
      return (f32x2){__uint_as_float(y[0]), __uint_as_float(y[1])};
    };
    f32x2 l[2][2];
#pragma unroll
    for (int hd = 0; hd < 2; ++hd) {
      const f32x4 s0 = __builtin_amdgcn_mfma_f32_16x16x32_bf16(k0[hd], qf[hd], bias[0], 0, 0, 0), s1 = __builtin_amdgcn_mfma_f32_16x16x32_bf16(k1[hd], qf[hd], bias[1], 0, 0, 0);
#pragma unroll
      for (int t = 0; t < 2; ++t) {
        const f32x4 sv = t ? s1 : s0;
        unsigned u = pk2(odd ? sv[1] : sv[0], odd ? sv[3] : sv[2]);
        u = pk2(lo2f(u) * 0.17677669529663687f, hi2f(u) * 0.17677669529663687f);
        l[hd][t] = (f32x2){lo2f(u), hi2f(u)};
      }
    }
    const f32x2 mx = both(mx2(mx2(l[0][0][0], l[0][0][1]), mx2(l[0][1][0], l[0][1][1])), mx2(mx2(l[1][0][0], l[1][0][1]), mx2(l[1][1][0], l[1][1][1])), mx2,
                          [](float r) { return r * -1.4426950408889634f; });
    float sm[2];
#pragma unroll
    for (int hd = 0; hd < 2; ++hd) {
#pragma unroll
      for (int t = 0; t < 2; ++t) { const f32x2 e = l[hd][t] * 1.4426950408889634f + mx[hd]; l[hd][t] = (f32x2){__builtin_amdgcn_exp2f(e[0]), __builtin_amdgcn_exp2f(e[1])}; }
      const f32x2 a = l[hd][0] + l[hd][1]; sm[hd] = a[0] + a[1];
    }
    const f32x2 inv = both(sm[0], sm[1], [](float a, float c) { return a + c; }, [](float r) { return __builtin_amdgcn_rcpf(r); });
#pragma unroll
    for (int hd = 0; hd < 2; ++hd) {
      u32x4 pp;
#pragma unroll
      for (int t = 0; t < 2; ++t) {
        const f32x2 p = l[hd][t] * inv[hd]; const unsigned u = pk2(p[0], p[1]);
        pp[2 * t] = __builtin_amdgcn_perm(u, u, sel0); pp[2 * t + 1] = __builtin_amdgcn_perm(u, u, sel1);
      }
      const bf16x8 pf = __builtin_bit_cast(bf16x8, pp);
      const f32x4 oa = __builtin_amdgcn_mfma_f32_16x16x32_bf16(v0[hd], pf, z, 0, 0, 0), ob = __builtin_amdgcn_mfma_f32_16x16x32_bf16(v1[hd], pf, z, 0, 0, 0);
      u32x4 ov; ov[0] = pk2(oa[0], oa[1]); ov[1] = pk2(oa[2], oa[3]); ov[2] = pk2(ob[0], ob[1]); ov[3] = pk2(ob[2], ob[3]);
      st16<true>(ws + WS_O, ((tn3 * 2 + hd) * 128 + tm3 * 8 + wave) * 1024 + lane * 16, ov);
    }
    STAMP_AT(10 );
    wait_vm<0>();
    STAMP_AT(11 );
    BAR();
    STAMP_AT(12 );
    if (wave >= 4) {
      const unsigned m4[4] = {mc[0], mc[1], mc[2], mc[3]};
      const f32x2 ps = slab_sum<8>(m4, mw);
      if (!(lane & 8))
        __hip_atomic_store((unsigned long long*)(ws + WS_MOD2) + (((b & 3) * 4 + (wave & 3)) * 4096 + (b >> 2) * 64 + (lane & 7) * 8 + (lane >> 4) * 2) / 2,
                           ((unsigned long long)__float_as_uint(ps[1]) << 32) | __float_as_uint(ps[0]), __ATOMIC_RELAXED, __HIP_MEMORY_SCOPE_AGENT);
      wait_vm<0>();
      if (lane == 0 && __hip_atomic_fetch_add((LDS_PTR(unsigned))(smem + LDS_ARR + 8), 1u, __ATOMIC_RELAXED, __HIP_MEMORY_SCOPE_WORKGROUP) == 3)
        __hip_atomic_fetch_add(cnt + (CM * 8 + (b >> 2)) * 32, 1u, __ATOMIC_RELAXED, __HIP_MEMORY_SCOPE_AGENT);
    }
  }
  { const int wm = wave & 3, ks = wave >> 2, mb = tm4 * 8 + wm * 2 + ks;
    constexpr int R = 6;
    u32x4 rx[2];
#pragma unroll
    for (int p = 0; p < 2; ++p) rx[p] = *(const u32x4*)(x + (size_t)(mb * 16 + fr) * D_ + tn4 * 64 + p * 32 + (g & 1) * 16 + (g >> 1) * 8);
    const unsigned va0 = (ks * 128 + mb) * 1024 + lane * 16, va1 = va0 ^ 1024;
    bf16x8 fa[R][2], fw[2][4];
    auto issueA = [&](int t) {
      const char* pa = ws + WS_O + t * 262144;
      fa[t % R][0] = __builtin_bit_cast(bf16x8, ld16<true>(pa, va0));
      fa[t % R][1] = __builtin_bit_cast(bf16x8, ld16<true>(pa, va1));
    };
    unsigned aw[4];
    auto rdw = [&](int t, int j) {
      const int o = (t & 3) * 8192;
      fw[t & 1][j] = cat8(ld_tr4(smem + aw[j] + o), ld_tr4(smem + aw[j] + o + 2048));
    };
    auto stepw = [&](int t) {
      const int gi = t >> 2, base = gi < 3 ? 57344 + gi * 32768 : 0;
#pragma unroll
      for (int j = 0; j < 4; ++j) { aw[j] = base + ks * 4096 + (lsw ^ (j * 32)); asm volatile("" : "+v"(aw[j])); }
    };
    auto up2 = [](unsigned u) { return (f32x2){lo2f(u), hi2f(u)}; };
    f32x2 f[8];
    unsigned slot = (mb * 256 + fr + kh6 * 32768 + 4 * g * 16) * 8, sown = (mb * 256 + fr + (g & 1) * 32768 + tn4 * 16) * 8;
    f32x4 acc[2][4];
#pragma unroll
    for (int i = 0; i < 2; ++i)
#pragma unroll
      for (int j = 0; j < 4; ++j) acc[i][j] = (f32x4){0.f, 0.f, 0.f, 0.f};
    stepw(0);
    wait_lgkm0();
    SCHED0(); BAR(); SCHED0();
    STAMP_AT(13 );
#pragma unroll
    for (int t = 0; t < R; ++t) issueA(t);
#pragma unroll
    for (int j = 0; j < 4; ++j) rdw(0, j);
    SCHED0();
#pragma unroll
    for (int t = 0; t < 16; ++t) {
      if (t < 15 && ((t + 1) & 3) == 0) stepw(t + 1);
#pragma unroll
      for (int j = 0; j < 4; ++j) {
        if (t < 15) rdw(t + 1, j);
        SCHED0();
#pragma unroll
        for (int i = 0; i < 2; ++i) acc[i][j] = __builtin_amdgcn_mfma_f32_16x16x32_bf16(fw[t & 1][j], fa[t % R][i], acc[i][j], 0, 0, 0);
        SCHED0();
      }
      if (t + R < 16) issueA(t + R);
      if (t == 2) {
#pragma unroll
        for (int p = 0; p < 2; ++p) {
          auto s0 = __builtin_amdgcn_permlane16_swap(rx[p][0], rx[p][2], false, false);
          auto s1 = __builtin_amdgcn_permlane16_swap(rx[p][1], rx[p][3], false, false);
          f[4 * p] = up2(s0[0]); f[4 * p + 1] = up2(s1[0]); f[4 * p + 2] = up2(s0[1]); f[4 * p + 3] = up2(s1[1]);
        }
#pragma unroll
        for (int j = 0; j < 8; ++j) asm volatile("" : "+v"(f[j]));
        asm volatile("" : "+v"(slot), "+v"(sown));
      }
      if (t == 5) BAR();
      SCHED0();
    }
    STAMP_AT(14 );
    LDS_PTR(f32x4) red = (LDS_PTR(f32x4))(smem + K4_RED);
#pragma unroll
    for (int j = 0; j < 4; ++j) red[((wave ^ 4) * 4 + j) * 64 + lane] = acc[1][j];
    wait_lgkm0();
    BAR();
    STAMP_AT(15 );
    const LDS_PTR(float) vec = (const LDS_PTR(float))(smem + LDS_VEC);
    unsigned o[8];
    f32x4 pr[4], ga[4];
#pragma unroll
    for (int j = 0; j < 4; ++j) { pr[j] = red[(wave * 4 + j) * 64 + lane]; ga[j] = *(const LDS_PTR(f32x4))(vec + j * 16 + 4 * g); }
    SCHED0();
#pragma unroll
    for (int j = 0; j < 4; ++j) {
      const f32x4 y = acc[0][j] + pr[j];
      const f32x2 lo = y.xy * ga[j].xy + f[2 * j], hi = y.zw * ga[j].zw + f[2 * j + 1];
      o[2 * j] = pk2(lo[0], lo[1]); o[2 * j + 1] = pk2(hi[0], hi[1]);
      f[2 * j] = up2(o[2 * j]); f[2 * j + 1] = up2(o[2 * j + 1]);
    }
    const f32x2 s2 = ((f[0] + f[1]) + (f[2] + f[3])) + ((f[4] + f[5]) + (f[6] + f[7]));
    float mt = xsum16_32(s2[0] + s2[1]) * (1.f / 64.f); asm("" : "+v"(mt));
    f32x2 q2 = {0.f, 0.f}, q3 = {0.f, 0.f};
#pragma unroll
    for (int j = 0; j < 8; j += 2) { f[j] -= mt; f[j + 1] -= mt; q2 += f[j] * f[j]; q3 += f[j + 1] * f[j + 1]; }
    q2 += q3;
    const float qq = xsum16_32(q2[0] + q2[1]);
    if (lane < 32) {
      const unsigned qb = qq == qq ? __float_as_uint(qq) : 0x7fc00000u;
      __hip_atomic_store((unsigned long long*)(ws + WS_XCH + sown), ((unsigned long long)qb << 32) | __float_as_uint(mt), __ATOMIC_RELAXED, __HIP_MEMORY_SCOPE_AGENT);
    }
    *(LDS_PTR(u32x4))(smem + (mb & 7) * 2048 + lane * 16) = (u32x4){o[0], o[1], o[2], o[3]};
    *(LDS_PTR(u32x4))(smem + (mb & 7) * 2048 + 1024 + lane * 16) = (u32x4){o[4], o[5], o[6], o[7]};
    STAMP_AT(16 );
    f32x4 ss[4], gg[4];
#pragma unroll
    for (int j = 0; j < 4; ++j) {
      ss[j] = *(const LDS_PTR(f32x4))(vec + 64 + j * 16 + 4 * g); gg[j] = *(const LDS_PTR(f32x4))(vec + 128 + j * 16 + 4 * g);
      f[2 * j] *= gg[j].xy; f[2 * j + 1] *= gg[j].zw;
      asm volatile("" : "+v"(ss[j]), "+v"(gg[j]), "+v"(f[2 * j]), "+v"(f[2 * j + 1]));
    }
    unsigned long long pv[4];
    __builtin_amdgcn_s_sleep(XS0);
    for (unsigned spins = 0;; ++spins) {
      bool ok = true;
#pragma unroll
      for (int i = 0; i < 4; ++i) { pv[i] = __hip_atomic_load((unsigned long long*)(ws + WS_XCH + slot) + i * 16, __ATOMIC_RELAXED, __HIP_MEMORY_SCOPE_AGENT); ok &= (unsigned)(pv[i] >> 32) != 0xffffffffu; }
      if (__builtin_amdgcn_ballot_w64(!ok) == 0 || spins > SPIN_CAP) break;
      __builtin_amdgcn_s_sleep(XS1);
    }
    STAMP_AT(17 );
    float ms = 0.f, m2 = 0.f;
#pragma unroll
    for (int i = 0; i < 4; ++i) { ms += __uint_as_float((unsigned)pv[i]); m2 += __uint_as_float((unsigned)(pv[i] >> 32)); }
    const float mean = xsum16_32(ms) * (1.f / 16.f);
#pragma unroll
    for (int i = 0; i < 4; ++i) { const float d = __uint_as_float((unsigned)pv[i]) - mean; m2 += 64.f * d * d; }
    const float rstd = ln_rstd(xsum16_32(m2) * (1.f / 1024.f));
    const float sh = (mt - rbf(mean)) * rstd;
#pragma unroll
    for (int p = 0; p < 2; ++p) {
      u32x4 v;
#pragma unroll
      for (int j = 0; j < 4; ++j) {
        const int jb = 2 * p + (j >> 1);
        const f32x2 h = f[4 * p + j] * rstd + (((j & 1) ? gg[jb].zw : gg[jb].xy) * sh + ((j & 1) ? ss[jb].zw : ss[jb].xy));
        v[j] = pk2(h[0], h[1]);
      }
      st16<true>(ws + WS_H2, ((tn4 * 2 + p) * 128 + mb) * 1024 + lane * 16, v);
    }
    STAMP_AT(18 );
    wait_vm<0>();
    wait_lgkm0();
    BAR();
    STAMP_AT(19 );

  }
  const int grp = wave >> 2;
  { const int wm = wave & 3;
    const LDS_PTR(char) sb = smem + 3 * K5_STG;
    const LDS_PTR(char) aP = sb + K5_G + row * 224 + grp * 96 + p4 * 8;
    const LDS_PTR(char) aM = sb + (p4 < 2 ? K5_G : K5_U) + row * 224 + (12 + grp) * 16 + (p4 & 1) * 8;
    const LDS_PTR(char) aA = sb + wm * 4096 + lane * 16;
    f32x32 c0, c1, c2; f32x16 c3;
    asm volatile(".irp r,0,8,16,24,32,40,48,56,64,72,80,88,96,104\n\tv_mov_b64 v[\\r:\\r+1], 0\n\tv_mov_b64 v[\\r+2:\\r+3], 0\n\tv_mov_b64 v[\\r+4:\\r+5], 0\n\tv_mov_b64 v[\\r+6:\\r+7], 0\n\t.endr"
                 : "={v[0:31]}"(c0), "={v[32:63]}"(c1), "={v[64:95]}"(c2), "={v[96:111]}"(c3)::"memory");
    int wrap = -(K5_NS - 1) * K5_STG, dl = K5_STG, n = 7;
    unsigned mask = wrap_mask(4, K5_NS);
    asm volatile("" : "+s"(wrap), "+v"(aP), "+v"(aM), "+v"(aA));
    if (grp) __builtin_amdgcn_s_setprio(1); else __builtin_amdgcn_s_setprio(2);
    BAR();
    STAMP_AT(20 );
    BAR();
    STAMP_G(21, 26);
    asm volatile(RA5(0) RA5(1) RP5(0, 0) RP5(0, 1) RP5(1, 0) RP5(1, 1) RP5(2, 0) RP5(2, 1) RP5(3, 0) RP5(3, 1) RP5(4, 0) RP5(4, 1) RP5(5, 0) RP5(5, 1) RW5(6, 0, aM, 0) LG(9)
                 ".p2align 5\n1:\n\t" TRIP5("") TRIP5("") TRIP5("") TRIP5("s_sub_u32 %[n], %[n], 1\n\t") "s_cbranch_scc0 1b\n\t" LG(0) "s_nop 7"
                 : ACC5, [aP] "+v"(aP), [aM] "+v"(aM), [aA] "+v"(aA), [dl] "+s"(dl), [mask] "+s"(mask), [n] "+s"(n) : [wrap] "s"(wrap) : CLOB5);
    f32x4 acc[4][7];
#pragma unroll
    for (int e = 0; e < 112; ++e) acc[e / 28][(e % 28) / 4][e & 3] = e < 32 ? c0[e & 31] : e < 64 ? c1[e & 31] : e < 96 ? c2[e & 31] : c3[e & 15];
    STAMP_G(22, 27);
    auto swi4 = [](const f32x4& gv, const f32x4& uv, unsigned& o0, unsigned& o1) {
      constexpr float c2 = 2.0813689810056077f;
      f32x2 pa = gv.xy * uv.xy, pb = gv.zw * uv.zw;
      const f32x2 ta = (f32x2){__builtin_amdgcn_exp2f(gv[0]), __builtin_amdgcn_exp2f(gv[1])} * c2 + c2, tb = (f32x2){__builtin_amdgcn_exp2f(gv[2]), __builtin_amdgcn_exp2f(gv[3])} * c2 + c2;
      pa *= (f32x2){__builtin_amdgcn_rcpf(ta[0]), __builtin_amdgcn_rcpf(ta[1])}; pb *= (f32x2){__builtin_amdgcn_rcpf(tb[0]), __builtin_amdgcn_rcpf(tb[1])};
      o0 = pk2(pa[0], pa[1]); o1 = pk2(pb[0], pb[1]);
    };
    const int lz = lane_id();
    const int gz = lz >> 4, fz = lz & 15;
    const int qb = (tn5 >> 4) * 224, ql = qb + 64 + (tn5 & 15) * 10;
    auto hof = [&](int e, int i) { return (unsigned)((e >> 2) * 131072 + (e & 3) * 256 + fz * 16 + i * 1024); };
    const unsigned ho0 = hof((grp ? ql + 2 : qb + (tn5 & 15) * 4) + 2 * (gz & 1) + (gz >> 1), 0), ho1 = hof(ql + 6 * grp + (gz >> 1), gz & 1), hom = hof(ql + 8 + grp, gz);
    char* hb = ws + WS_HH + (tm5 * 16 + wm * 4) * 1024;
    const int c5 = C5 + tm5 * 4 + (tn5 >> 4) * 2;
    auto tr16 = [](const unsigned (&o)[4]) {
      auto s0 = __builtin_amdgcn_permlane16_swap(o[0], o[2], false, false);
      auto s1 = __builtin_amdgcn_permlane16_swap(o[1], o[3], false, false);
      u32x4 v = {s0[0], s1[0], s0[1], s1[1]}; asm volatile("" : "+v"(v)); return v;
    };
    auto pair = [&](int p, int i) {
      unsigned o[4];
      swi4(acc[i][2 * p], acc[i][2 * p + 1], o[0], o[1]);
      swi4(acc[i + 1][2 * p], acc[i + 1][2 * p + 1], o[2], o[3]);
      return tr16(o);
    };
    u32x4 v[3];
#pragma unroll
    for (int i = 0; i < 4; ++i) {
      unsigned o[4];
      swi4(acc[i][0], acc[i][1], o[0], o[1]);
      swi4(acc[i][2], acc[i][3], o[2], o[3]);
      st16<true>(hb, ho0 + i * 1024, tr16(o));
      SCHED0();
    }
    STAMP_G(23, 28);
    v[0] = pair(2, 0);
    if (!grp) {
      wait_vm<0>();
      unsigned arr = 0;
      if (lz == 0) arr = __hip_atomic_fetch_add((LDS_PTR(unsigned))(smem + LDS_ARR), 1u, __ATOMIC_RELAXED, __HIP_MEMORY_SCOPE_WORKGROUP);
      if (__builtin_amdgcn_readfirstlane(arr) == 3) signal(cnt, c5, lz);
    }
    STAMP_G(24, 29);
    v[1] = pair(2, 2);
    { unsigned m[4];
#pragma unroll
      for (int i = 0; i < 4; i += 2) {
        f32x4 gv, uv;
#pragma unroll
        for (int e = 0; e < 4; ++e) {
          auto w = __builtin_amdgcn_permlane32_swap(__float_as_uint(acc[i + (e >> 1)][6][e & 1]), __float_as_uint(acc[i + (e >> 1)][6][2 + (e & 1)]), false, false);
          gv[e] = __uint_as_float(w[0]); uv[e] = __uint_as_float(w[1]);
        }
        swi4(gv, uv, m[i], m[i + 1]);
      }
      auto x0 = __builtin_amdgcn_permlane32_swap(m[0], m[2], false, false);
      auto x1 = __builtin_amdgcn_permlane32_swap(m[1], m[3], false, false);
      const unsigned o[4] = {x0[0], x0[1], x1[0], x1[1]};
      v[2] = tr16(o);
      st16<true>(hb, ho1, v[0]); st16<true>(hb, ho1 + 2048, v[1]); st16<true>(hb, hom, v[2]);
    }
    STAMP_G(25, 30);
    if (grp) {
#pragma unroll
      for (int t = 0; t < NDR; ++t) BAR();
      wait_vm<0>();
      BAR();
      if (wave == 4) signal(cnt, c5 + 1, lz);
      if (wave > 4) return;
#pragma unroll 1
      for (int t = NDR + 1; t < 9; ++t) BAR();
      unsigned seen = 16;
      const unsigned* pc = (lz & 1) ? (const unsigned*)(ws + WS_FLG) + (b ^ 4) * 32 : cnt + ((C5 + (tm6 >> 1) * 4 + kh6 * 2 + 1) * 8 + tn6) * 32;
      asm volatile("global_load_dword %0, %1, off sc1" : "=v"(seen) : "v"(pc) : "memory");
      BAR(); BAR();
      asm volatile("s_waitcnt vmcnt(0)" : "+v"(seen)::"memory");
      if (__builtin_amdgcn_ballot_w64(seen < 16) != 0) poll<0>(pc, 16);
      return;
    }
  }
  { const int lane = lane_id(), fr = lane & 15, g = lane >> 4, p4 = fr & 3, row = 4 * g + (fr >> 2);
    const int wm = wave & 1, wn = wave >> 1;
    const int lw0 = row * 128 + (((row >> 1) & 3) * 32) + p4 * 8;
    const int mb0 = tm6 * 8 + wm * 4, t64 = tn6 * 2 + kh6;
    f32x4 acc[4][4];
#pragma unroll
    for (int i = 0; i < 4; ++i)
#pragma unroll
      for (int j = 0; j < 4; ++j) { acc[i][j] = (f32x4){0.f, 0.f, 0.f, 0.f}; asm volatile("" : "+v"(acc[i][j])); }
    struct Fr { bf16x8 a[4]; bf16x4 w[4][2]; };
    const LDS_PTR(char) aw0 = smem + (lw0 ^ (wn * 64)); const LDS_PTR(char) aw1 = smem + (lw0 ^ (wn * 64 + 32));
    const LDS_PTR(char) aa = smem + K6_A + wm * 4096 + lane * 16;
    auto rd1 = [&](Fr& f, int r) {
      if (r < 2) f.w[0][r] = ld_tr4(aw0 + r * 2048);
      else if (r < 4) f.a[r - 2] = *(const LDS_PTR(bf16x8))(aa + (r - 2) * 1024);
      else if (r < 10) { const int j = (r - 2) >> 1; f.w[j][r & 1] = ld_tr4(((j & 1) ? aw1 : aw0) + (j >> 1) * 4096 + (r & 1) * 2048); }
      else f.a[r - 8] = *(const LDS_PTR(bf16x8))(aa + (r - 8) * 1024);
    };
    int dl, da, wrap = -(K6_NW - 1) * 8192, wrapa = -(K6_NA - 1) * 8192;
    unsigned mask = wrap_mask(1, K6_NW), maska = wrap_mask(1, K6_NA);
    Fr fA, fB;
    asm volatile("" : "+s"(wrap), "+s"(wrapa), "+v"(aw0), "+v"(aw1), "+v"(aa));
    wait_vm<0>();
    BAR();
    STAMP_AT(32 );
#pragma unroll
    for (int r = 0; r < 12; ++r) { SCHED0(); rd1(fA, r); }
    SCHED0();
    aw0 += 8192; aw1 += 8192; aa += 8192;
    wait_lgkm<6>();
    BAR();
    auto trip = [&](const Fr& cur, Fr& nxt) {
      SCHED0();
#pragma unroll
      for (int m = 0; m < 16; ++m) {
        const int j = (m & 7) >> 1, i = (m >> 3) * 2 + ((m ^ j) & 1);
        if (m == 2) mfma_sel<8192>(acc[i][j], cat8(cur.w[j][0], cur.w[j][1]), cur.a[i], mask, dl, wrap);
        else if (m == 6) mfma_sel<8192>(acc[i][j], cat8(cur.w[j][0], cur.w[j][1]), cur.a[i], maska, da, wrapa);
        else mfma_acc(acc[i][j], cat8(cur.w[j][0], cur.w[j][1]), cur.a[i]);
        SCHED0();
        if (m < 12) { rd1(nxt, m); SCHED0(); }
        if (m == 3) wait_lgkm<4>();
        if (m == 12) { aw0 += dl; aw1 += dl; asm volatile("" : "+v"(aw0), "+v"(aw1)); }
        if (m == 13) { aa += da; asm volatile("" : "+v"(aa)); }
        if (m == 14) wait_lgkm<6>();
        if (m == 15) BAR();
        SCHED0();
      }
    };
#pragma unroll 1
    for (int h = 0; h < 2; ++h) {
#pragma unroll 1
      for (int t = 0; t < 7; ++t) { trip(fA, fB); trip(fB, fA); trip(fA, fB); trip(fB, fA); }
      mask = wrap_mask(29, K6_NW); maska = wrap_mask(29, K6_NA);
    }
    mfma_done(acc);
    STAMP_AT(34 );
    const int slot = wm * 2 + wn, pb = (b ^ 4) * 4 + slot;
    { auto pk = [](float lo, float hi) { const unsigned u = pk2(lo, hi); return u < 0xfffffffeu ? u : 0xfffffffeu; };
#pragma unroll
      for (int i = 0; i < 4; ++i)
        st16<true>(ws + WS_PART, (b * 4 + slot) * 4096 + i * 1024 + lane * 16,
                   (u32x4){pk(acc[i][0][0], acc[i][0][1]), pk(acc[i][0][2], acc[i][0][3]), pk(acc[i][1][0], acc[i][1][1]), pk(acc[i][1][2], acc[i][1][3])});
      STAMP_AT(35 );
    }
    { const LDS_PTR(float) vec = (const LDS_PTR(float))(smem + LDS_VEC) + 192 + wn * 32 + 4 * g;
      const f32x4 gv0 = *(const LDS_PTR(f32x4))vec, gv1 = *(const LDS_PTR(f32x4))(vec + 16);
      u32x4 rv[4];
#pragma unroll
      for (int i = 0; i < 4; ++i) { rv[i] = *(const LDS_PTR(u32x4))(smem + K6_X + (wm * 4 + i) * 2048 + wn * 1024 + lane * 16); asm volatile("" : "+v"(rv[i])); }
      u32x4 z[4];
      __builtin_amdgcn_s_sleep(PD6);
#pragma unroll 1
      for (unsigned spins = 0; spins < SPIN_CAP; ++spins) {
        asm volatile("" ::: "memory");
        unsigned m = 0;
#pragma unroll
        for (int i = 0; i < 4; ++i) z[i] = ld16<true>(ws + WS_PART, pb * 4096 + i * 1024 + lane * 16);
#pragma unroll
        for (int i = 0; i < 16; ++i) m = m > z[i >> 2][i & 3] ? m : z[i >> 2][i & 3];
        if (__builtin_amdgcn_ballot_w64(m == ~0u) == 0) break;
        __builtin_amdgcn_s_sleep(PS6);
      }
      STAMP_AT(36 );
#pragma unroll
      for (int i = 0; i < 4; ++i) {
        const f32x4 v0 = (acc[i][2] + (f32x4){lo2f(z[i][0]), hi2f(z[i][0]), lo2f(z[i][1]), hi2f(z[i][1])}) * gv0;
        const f32x4 v1 = (acc[i][3] + (f32x4){lo2f(z[i][2]), hi2f(z[i][2]), lo2f(z[i][3]), hi2f(z[i][3])}) * gv1;
        const unsigned o0 = pk2(lo2f(rv[i][0]) + v0[0], hi2f(rv[i][0]) + v0[1]), o1 = pk2(lo2f(rv[i][1]) + v0[2], hi2f(rv[i][1]) + v0[3]);
        const unsigned o2 = pk2(lo2f(rv[i][2]) + v1[0], hi2f(rv[i][2]) + v1[1]), o3 = pk2(lo2f(rv[i][3]) + v1[2], hi2f(rv[i][3]) + v1[3]);
        auto s0 = __builtin_amdgcn_permlane16_swap(o0, o2, false, false);
        auto s1 = __builtin_amdgcn_permlane16_swap(o1, o3, false, false);
        st16<false>(out, (((mb0 + i) * 16 + fr) * D_ + t64 * 64 + wn * 32 + (g & 1) * 16 + (g >> 1) * 8) * 2, (u32x4){s0[0], s1[0], s0[1], s1[1]});
      }
    }
    STAMP_AT(37 );
  }
}

extern "C" void kernel_launch(void* const* d_in, const int* in_sizes, int n_in, void* d_out, int out_size, void* d_ws,
                              size_t ws_size, hipStream_t stream) {
  const u16* x = (const u16*)d_in[0];
  const u16* wqkv = (const u16*)d_in[1];
  const u16* wo = (const u16*)d_in[2];
  const u16* w1 = (const u16*)d_in[4];
  const u16* w3 = (const u16*)d_in[5];
  const u16* w2 = (const u16*)d_in[6];
  const u16* c = (const u16*)d_in[7];
  const u16* wmod = (const u16*)d_in[8];
  (void)hipFuncSetAttribute((const void*)k2_block, hipFuncAttributeMaxDynamicSharedMemorySize, MEGA_LDS);
  char* ws = (char*)d_ws;
  k2_block<<<256, 768, MEGA_LDS, stream>>>(x, wqkv, wo, w1, w3, w2, c, wmod, (u16*)d_out, ws);
}
```

```cpp
#include <hip/hip_runtime.h>
#include <stdint.h>
#include <type_traits>
#pragma clang diagnostic ignored "-Winline-asm"

typedef unsigned short u16;
typedef short bf16x8 __attribute__((ext_vector_type(8)));
typedef short bf16x4 __attribute__((ext_vector_type(4)));
typedef float f32x4 __attribute__((ext_vector_type(4)));
typedef float f32x2 __attribute__((ext_vector_type(2)));
typedef unsigned int u32x4 __attribute__((ext_vector_type(4)));
typedef unsigned int u32x2 __attribute__((ext_vector_type(2)));
#define LDS_PTR(T) __attribute__((address_space(3))) T*
#define BAR() do { asm volatile("" ::: "memory"); __builtin_amdgcn_s_barrier(); asm volatile("" ::: "memory"); } while (0)
#define SCHED0() __builtin_amdgcn_sched_barrier(0)
template <int N> using IC = std::integral_constant<int, N>;
typedef std::integral_constant<bool, true> T_;
typedef std::integral_constant<bool, false> F__;

constexpr int S_ = 2048, D_ = 1024, F_ = 3584;
#define STAMP_AT(i)
#define STAMP_G(ia, ib)
#define STAMP_L(i)

__device__ __forceinline__ unsigned pk2(float lo, float hi) {
  typedef __bf16 bf2 __attribute__((ext_vector_type(2)));
  f32x2 v = {lo, hi}; bf2 r = __builtin_convertvector(v, bf2); return __builtin_bit_cast(unsigned, r);
}
__device__ __forceinline__ float lo2f(unsigned u) { return __uint_as_float(u << 16); }
__device__ __forceinline__ float hi2f(unsigned u) { return __uint_as_float(u & 0xffff0000u); }
__device__ __forceinline__ float rbf(float v) { return lo2f(pk2(v, 0.f)); }
#define glds16(sbase, voff, lds_off, imm) \
  asm volatile("s_add_u32 m0, %2, %3\n\ts_nop 0\n\tglobal_load_lds_dwordx4 %0, %1" ::"v"(voff), "s"(sbase), "s"(lds_off), "n"(imm) : "memory", "scc")
#define glds16s(sbase, voff, lds_off, imm) \
  asm volatile("s_add_u32 m0, %2, %3\n\ts_nop 0\n\tglobal_load_lds_dwordx4 %0, %1 sc1" ::"v"(voff), "s"(sbase), "s"(lds_off), "n"(imm) : "memory", "scc")
#define GLDS4(SC) "s_mov_b32 m0, %6\n\ts_nop 0\n\tglobal_load_lds_dwordx4 %0, %4" SC "\n\tglobal_load_lds_dwordx4 %1, %5 offset:1024" SC \
                  "\n\tglobal_load_lds_dwordx4 %2, %4 offset:2048" SC "\n\tglobal_load_lds_dwordx4 %3, %5 offset:3072" SC
template <bool SC1> __device__ __forceinline__ void glds16x4(const void* s0, const void* s1, const unsigned (&v)[4], unsigned lds) {
  if (SC1) asm volatile(GLDS4(" sc1") ::"v"(v[0]), "v"(v[1]), "v"(v[2]), "v"(v[3]), "s"(s0), "s"(s1), "s"(lds) : "memory");
  else asm volatile(GLDS4("") ::"v"(v[0]), "v"(v[1]), "v"(v[2]), "v"(v[3]), "s"(s0), "s"(s1), "s"(lds) : "memory");
}
__device__ __forceinline__ void glds4(const void* sbase, unsigned voff, unsigned lds_off) {
  asm volatile("s_mov_b32 m0, %2\n\ts_nop 0\n\tglobal_load_lds_dword %0, %1" ::"v"(voff), "s"(sbase), "s"(lds_off) : "memory");
}
__device__ __forceinline__ void stouch4(const void* p) {
  asm volatile("s_load_dwordx2 s[100:101], %0, 0x3c\n\ts_load_dwordx2 s[100:101], %0, 0xbc\n\ts_load_dwordx2 s[100:101], %0, 0x13c\n\ts_load_dwordx2 s[100:101], %0, 0x1bc"
               ::"s"(p) : "memory", "s100", "s101");
}
template <bool SC1> __device__ __forceinline__ void st16(void* sbase, unsigned voff, u32x4 v) {
  __builtin_amdgcn_raw_buffer_store_b128(v, __builtin_amdgcn_make_buffer_rsrc(sbase, 0, 0x7fffffff, 0x00020000), voff, 0, SC1 ? 16 : 0);
}
template <bool SC1> __device__ __forceinline__ u32x4 ld16(const void* sbase, unsigned voff) {
  return __builtin_amdgcn_raw_buffer_load_b128(__builtin_amdgcn_make_buffer_rsrc((void*)sbase, 0, 0x7fffffff, 0x00020000), voff, 0, SC1 ? 16 : 0);
}
template <int N> __device__ __forceinline__ void wait_vm() { asm volatile("s_waitcnt vmcnt(%0)" ::"n"(N) : "memory"); }
__device__ __forceinline__ void wait_lgkm0() { asm volatile("" ::: "memory"); __builtin_amdgcn_s_waitcnt(0xC07F); asm volatile("" ::: "memory"); }
__device__ __forceinline__ bf16x4 ld_tr4(const LDS_PTR(char) p) { return __builtin_amdgcn_ds_read_tr16_b64_v4i16((LDS_PTR(bf16x4))p); }
__device__ __forceinline__ bf16x8 cat8(bf16x4 lo, bf16x4 hi) { return __builtin_shufflevector(lo, hi, 0, 1, 2, 3, 4, 5, 6, 7); }
__device__ __forceinline__ void mfma_acc(f32x4& acc, bf16x8 a, bf16x8 b) { asm volatile("v_mfma_f32_16x16x32_bf16 %0, %1, %2, %0" : "+v"(acc) : "v"(a), "v"(b)); }
template <int STEP> __device__ __forceinline__ void mfma_sel(f32x4& acc, bf16x8 a, bf16x8 b, unsigned& mask, int& dl, int wrap) {
  asm volatile("s_add_u32 %1, %1, %1\n\tv_mfma_f32_16x16x32_bf16 %0, %3, %4, %0\n\ts_cselect_b32 %2, %5, %6" : "+v"(acc), "+s"(mask), "=s"(dl) : "v"(a), "v"(b), "s"(wrap), "n"(STEP) : "scc");
}
constexpr unsigned wrap_mask(int first, int ns) { unsigned m = 0; for (int t = 0; t < 32; ++t) if ((first + t) % ns == ns - 1) m |= 0x80000000u >> t; return m; }
template <int N> __device__ __forceinline__ void wait_lgkm() { __builtin_amdgcn_s_waitcnt(0xC07F | (N << 8)); }
template <int R, int C> __device__ __forceinline__ void mfma_done(f32x4 (&acc)[R][C]) {
  asm volatile("s_nop 9");
#pragma unroll
  for (int i = 0; i < R; ++i)
#pragma unroll
    for (int j = 0; j < C; ++j) asm volatile("" : "+v"(acc[i][j]));
}
__device__ __forceinline__ int lane_id() { int l; asm volatile("v_mbcnt_lo_u32_b32 %0, -1, 0\n\tv_mbcnt_hi_u32_b32 %0, -1, %0" : "=v"(l)); return l; }
__device__ __forceinline__ float xsum16_32(float v) {
  auto a = __builtin_amdgcn_permlane16_swap(__float_as_uint(v), __float_as_uint(v), false, false); v = __uint_as_float(a[0]) + __uint_as_float(a[1]);
  auto c = __builtin_amdgcn_permlane32_swap(__float_as_uint(v), __float_as_uint(v), false, false); return __uint_as_float(c[0]) + __uint_as_float(c[1]);
}

__device__ __forceinline__ float ln_rstd(float var) { return __builtin_amdgcn_rcpf(rbf(__builtin_amdgcn_sqrtf(rbf(rbf(var) + 1e-5f)))); }

constexpr size_t WS_MOD = 0;
constexpr size_t WS_EPO = 64 << 10, WS_FL1 = 96 << 10;
constexpr size_t WS_CTL = 128 << 10;
constexpr int C2 = 0, C3 = 16, C4 = 32, C5 = 48, CM = 80;
constexpr size_t WS_FLG = WS_CTL + (88 << 10), CTL_BYTES = 120 << 10;
constexpr size_t WS_XCH = 512 << 10;
constexpr size_t WS_MOD2 = 1 << 20;
constexpr size_t WS_H = 2 << 20, WS_O = WS_H + (4 << 20), WS_H2 = WS_O + (4 << 20), WS_HH = WS_H2 + (4 << 20),
                 WS_PART = WS_HH + (size_t)S_ * F_ * 2;

template <int NL> __device__ __forceinline__ f32x2 slab_sum(const unsigned (&mc)[NL / 2], const u32x4 (&mw)[NL]) {
  f32x2 a2[4] = {};
#pragma unroll
  for (int i = 0; i < NL; ++i) {
    const float cv = (i & 1) ? hi2f(mc[i >> 1]) : lo2f(mc[i >> 1]);
    const float sv = rbf(cv * __builtin_amdgcn_rcpf(1.f + __builtin_amdgcn_exp2f(cv * -1.4426950408889634f)));
#pragma unroll
    for (int j = 0; j < 4; ++j) a2[j] += (f32x2){lo2f(mw[i][j]), hi2f(mw[i][j])} * sv;
  }
  float acc[8] = {a2[0][0], a2[0][1], a2[1][0], a2[1][1], a2[2][0], a2[2][1], a2[3][0], a2[3][1]};
#pragma unroll
  for (int j = 0; j < 4; ++j) { auto a = __builtin_amdgcn_permlane32_swap(__float_as_uint(acc[j]), __float_as_uint(acc[j + 4]), false, false); acc[j] = __uint_as_float(a[0]) + __uint_as_float(a[1]); }
#pragma unroll
  for (int j = 0; j < 2; ++j) { auto a = __builtin_amdgcn_permlane16_swap(__float_as_uint(acc[j]), __float_as_uint(acc[j + 2]), false, false); acc[j] = __uint_as_float(a[0]) + __uint_as_float(a[1]); }
#pragma unroll
  for (int j = 0; j < 2; ++j) acc[j] += __uint_as_float(__builtin_amdgcn_mov_dpp(__float_as_uint(acc[j]), 0x128, 0xf, 0xf, false));
  return (f32x2){acc[0], acc[1]};
}

constexpr int K3_B = 18432, K3_STG = 18432 + 24576;
constexpr int K4_RED = 57344;
constexpr int K5_G = 16384, K5_U = 16384 + 7168 + 16, K5_STG = 30736, K5_NS = 5;
constexpr int K6_NW = 12, K6_NA = 7, K6_A = K6_NW * 8192, K6_X = 9 * 8192;
constexpr int LDS_VEC = 158720, LDS_DUMMY = LDS_VEC + 1024, LDS_ARR = LDS_DUMMY + 256, MEGA_LDS = LDS_ARR + 64;
constexpr unsigned SPIN_CAP = 1u << 19;
#define KNOBS KB = 8, XD0 = 16, PFD = 16, FD1 = 8, FS0 = 4, PQ = 2, PQ5 = 4, PD2 = 4, PD3 = 4, PD4 = 4, PD5 = 48, PD6 = 6, PS6 = 1, XS0 = 14, XS1 = 4, TS5 = 2, VB3 = 2, SLB = 15, NDR = 1
enum { KNOBS };
constexpr unsigned long long SKIP6 = 0x7c;
static_assert(__builtin_popcountll(SKIP6) == 5, "44 stages of W are issued behind 49 barriers");

__device__ __forceinline__ void signal(unsigned* cnt, int c, int lane, unsigned v = 1) {
  if (lane < 8) __hip_atomic_fetch_add(cnt + (c * 8 + lane) * 32, v, __ATOMIC_RELAXED, __HIP_MEMORY_SCOPE_AGENT);
}
template <int D0, int D1 = 1> __device__ __forceinline__ void poll(const unsigned* cnt, unsigned want) {
  if (D0) __builtin_amdgcn_s_sleep(D0);
#pragma unroll 1
  for (unsigned spins = 0; spins < SPIN_CAP; ++spins) {
    if (__builtin_amdgcn_ballot_w64(__hip_atomic_load(cnt, __ATOMIC_RELAXED, __HIP_MEMORY_SCOPE_AGENT) < want) == 0) break;
    __builtin_amdgcn_s_sleep(D1);
  }
  __builtin_amdgcn_fence(__ATOMIC_ACQUIRE, "wavefront");
}

template <int D0, int D1, int EX> __device__ __forceinline__ void pollp(const unsigned* cnt, unsigned want, unsigned (&r)[4]) {
  unsigned n = SPIN_CAP;
#define PL1(a) "global_atomic_add %" #a ", %5, %9, off sc0\n\ts_sleep %8\n\t"
#define PL2(a, c) "global_atomic_add %" #a ", %5, %9, off sc0\n\ts_waitcnt vmcnt(3)\n\tv_cmp_gt_u32 vcc, %6, %" #c "\n\ts_cbranch_vccz 2f\n\ts_sleep %8\n\t"
  asm volatile("s_mov_b64 exec, %10\n\ts_sleep %7\n\t" PL1(0) PL1(1) PL1(2) "1:\n\t" PL2(3, 0) PL2(0, 1) PL2(1, 2) PL2(2, 3) "s_sub_u32 %4, %4, 1\n\ts_cbranch_scc0 1b\n2:\n\ts_mov_b64 exec, -1"
               : "=&v"(r[0]), "=&v"(r[1]), "=&v"(r[2]), "=&v"(r[3]), "+s"(n) : "v"(cnt), "s"(want), "n"(D0), "n"(D1), "v"(0), "n"(EX) : "memory", "vcc", "scc");
}
#define KEEP4(r) asm volatile("; keep %0 %1 %2 %3" ::"v"(r[0]), "v"(r[1]), "v"(r[2]), "v"(r[3]))
typedef float f32x32 __attribute__((ext_vector_type(32)));
typedef float f32x16 __attribute__((ext_vector_type(16)));
#define M5(i, j) "v_mfma_f32_16x16x32_bf16 v[28*" #i "+4*" #j ":28*" #i "+4*" #j "+3], v[128+4*" #j ":131+4*" #j "], v[112+4*" #i ":115+4*" #i "], v[28*" #i "+4*" #j ":28*" #i "+4*" #j "+3]\n\t"
#define RA5(i) "ds_read_b128 v[112+4*" #i ":115+4*" #i "], %[aA] offset:1024*" #i "\n\t"
#define RW5(j, h, a, o) "ds_read_b64_tr_b16 v[128+4*" #j "+2*" #h ":129+4*" #j "+2*" #h "], %[" #a "] offset:" #o "\n\t"
#define ST5(a) "v_add_u32 %[" #a "], %[dl], %[" #a "]\n\t"
#define SEL5A "s_add_u32 %[mask], %[mask], %[mask]\n\t"
#define SEL5B "s_cselect_b32 %[dl], %[wrap], 30736\n\t"
#define LG(n) "s_waitcnt lgkmcnt(" #n ")\n\t"
#define RP5(j, h) RW5(j, h, aP, 32*(j/2)+7184*(j&1)+3584*h)
#define TRIP5(X)                                                                                                                                        \
  M5(0, 0) RW5(6, 1, aM, 3584) M5(1, 0) RA5(2) ST5(aM) M5(1, 1) RA5(3) ST5(aP) M5(0, 1) ST5(aA) LG(6) M5(0, 2) M5(1, 2) SEL5A M5(1, 3) SEL5B              \
  M5(0, 3) M5(0, 4) M5(1, 4) LG(0) M5(1, 5) M5(0, 5) "s_barrier\n\t" M5(0, 6) RA5(0) M5(1, 6) RA5(1)                                                      \
  M5(2, 0) X M5(3, 0) RP5(0, 0) M5(3, 1) RP5(0, 1) M5(2, 1) RP5(1, 0) M5(2, 2) RP5(1, 1) M5(3, 2) RP5(2, 0) M5(3, 3) RP5(2, 1) M5(2, 3) RP5(3, 0)          \
  M5(2, 4) RP5(3, 1) M5(3, 4) RP5(4, 0) M5(3, 5) RP5(4, 1) M5(2, 5) RP5(5, 0) M5(2, 6) RP5(5, 1) LG(8) M5(3, 6) RW5(6, 0, aM, 0)
#define ACC5 "+{v[0:31]}"(c0), "+{v[32:63]}"(c1), "+{v[64:95]}"(c2), "+{v[96:111]}"(c3)
#define CLOB5 "memory", "scc", "v112", "v113", "v114", "v115", "v116", "v117", "v118", "v119", "v120", "v121", "v122", "v123", "v124", "v125", "v126", "v127", "v128", "v129", "v130", "v131", "v132", "v133", \
              "v134", "v135", "v136", "v137", "v138", "v139", "v140", "v141", "v142", "v143", "v144", "v145", "v146", "v147", "v148", "v149", "v150", "v151", "v152", "v153", "v154", "v155"
static_assert(K5_STG == 30736 && K5_U - K5_G == 7184, "TRIP5");

#define C3(i, j) "v[24*" #i "+4*" #j "-12*(" #i "/4):24*" #i "+4*" #j "-12*(" #i "/4)+3]"
#define M3(i, j) "v_mfma_f32_16x16x32_bf16 " C3(i, j) ", v[124+4*" #j ":127+4*" #j "], v[104+4*" #i ":107+4*" #i "], " C3(i, j) "\n\t"
#define RA3(i, u) "ds_read_b128 v[104+4*" #i ":107+4*" #i "], %[aA] offset:1024*" #i "+9216*" #u "\n\t"
#define RH3(u) "ds_read_b128 v[120:123], %[aH] offset:9216*" #u "\n\t"
#define RW3(j, h, a, u) "ds_read_b64_tr_b16 v[124+4*" #j "+2*" #h ":125+4*" #j "+2*" #h "], %[" #a "] offset:8192*(" #j "/2)+2048*" #h "+4096*" #u "\n\t"
#define ST3(a, d) "v_add_u32 %[" #a "], " #d ", %[" #a "]\n\t"
#define H2_3(u, S0, S1, S2, B) M3(0, 0) RW3(5, 1, a1, u) M3(1, 0) RA3(2, u) M3(1, 1) RA3(3, u) S0 M3(0, 1) LG(7) S1 M3(0, 2) S2 M3(1, 2) M3(1, 3) M3(0, 3) LG(2) M3(0, 4) M3(1, 4) LG(0) B
#define H1_3(n, X)                                                                                                                                      \
  M3(1, 5) RA3(1, n) M3(0, 5) RA3(0, n) M3(2, 0) X M3(3, 0) RW3(0, 0, a0, n) M3(3, 1) RW3(0, 1, a0, n) M3(2, 1) RW3(1, 0, a1, n) M3(2, 2) RW3(1, 1, a1, n) \
  M3(3, 2) RW3(2, 0, a1, n) M3(3, 3) RW3(2, 1, a1, n) M3(2, 3) M3(4, 3) RW3(3, 0, a0, n) M3(4, 4) RW3(3, 1, a0, n) M3(2, 4) RH3(n)                         \
  M3(3, 4) RW3(4, 0, a0, n) M3(3, 5) RW3(4, 1, a0, n) LG(7) M3(2, 5) RW3(5, 0, a1, n)
#define STAGE3(d, X) H2_3(0, , , , ) H1_3(1, ) H2_3(1, ST3(a0, d), ST3(aA, d), ST3(a1, d) ST3(aH, d), "s_barrier\n\t") H1_3(0, X)
#define CV3(i, j, t) "v_cvt_pk_bf16_f32 v[104+2*" #t "], v[24*" #i "+4*" #j "-12*(" #i "/4)], v[24*" #i "+4*" #j "-12*(" #i "/4)+1]\n\tv_cvt_pk_bf16_f32 v[105+2*" #t "], v[24*" #i "+4*" #j "-12*(" #i "/4)+2], v[24*" #i "+4*" #j "-12*(" #i "/4)+3]\n\t"
#define WR3(i, t, a, o) "ds_write_b64 %[" #a "], v[104+2*" #t ":105+2*" #t "] offset:2048*" #i "+" #o "\n\t"
#define CW3(i, j, t, a, o) CV3(i, j, t) WR3(i, t, a, o)
#define V8(a) "v" #a "0", "v" #a "1", "v" #a "2", "v" #a "3", "v" #a "4", "v" #a "5", "v" #a "6", "v" #a "7"
#define V10(a) V8(a), "v" #a "8", "v" #a "9"
static_assert(K3_STG == 43008, "STAGE3");
#define M6(i, j, s) "v_mfma_f32_16x16x32_bf16 v[16*" #i "+4*(" #j "):16*" #i "+4*(" #j ")+3], v[80+32*" #s "+4*(" #j "):83+32*" #s "+4*(" #j ")], v[64+32*" #s "+4*" #i ":67+32*" #s "+4*" #i "], v[16*" #i "+4*(" #j "):16*" #i "+4*(" #j ")+3]\n\t"
#define RA6(i, s) "ds_read_b128 v[64+32*" #s "+4*" #i ":67+32*" #s "+4*" #i "], %[aa] offset:1024*" #i "\n\t"
#define RW6(h, b, l, s) "ds_read_b64_tr_b16 v[80+32*" #s "+8*(" #h ")+4*" #b "+2*" #l ":81+32*" #s "+8*(" #h ")+4*" #b "+2*" #l "], %[a" #b "] offset:4096*(" #h ")+2048*" #l "\n\t"
#define SEL6(m, d, w) "s_cselect_b32 %[" #d "], %[" #w "], 8192\n\t"
#define SH6(m) "s_add_u32 %[" #m "], %[" #m "], %[" #m "]\n\t"
#define TRIP6(h, o, n, x, X)                                                                                                                            \
  M6(0, 2*h, o) RW6(1-h, 0, 0, x) M6(0, 2*h, n) RW6(1-h, 0, 1, x) SH6(mw) M6(0, 2*h+1, n) SEL6(mw, dw, ww) RA6(0, x) M6(0, 2*h+1, o) RW6(1-h, 1, 0, x) LG(8)  \
  M6(1, 2*h+1, o) RW6(1-h, 1, 1, x) M6(1, 2*h+1, n) RA6(1, x) SH6(ma) M6(1, 2*h, n) SEL6(ma, da, wa) RA6(2, x) M6(1, 2*h, o) RA6(3, x)                      \
  M6(2, 2*h, o) RW6(h, 0, 0, x) "v_add_u32 %[aa], %[da], %[aa]\n\t" M6(2, 2*h, n) RW6(h, 0, 1, x) M6(2, 2*h+1, n) RW6(h, 1, 0, x) "v_add_u32 %[a0], %[dw], %[a0]\n\t" \
  M6(2, 2*h+1, o) RW6(h, 1, 1, x) M6(3, 2*h+1, o) "v_add_u32 %[a1], %[dw], %[a1]\n\t" X M6(3, 2*h+1, n) M6(3, 2*h, n) LG(6) M6(3, 2*h, o) "s_barrier\n\t"
__device__ __forceinline__ int w2_stage(int j) {
  const int j2 = j - 16, p = (j2 * 52) >> 8, r = j2 - p * 5;
  return j < 16 ? j * 112 * 2048 : (p * 224 + 32 + r * 32 + (r > 2 ? 32 : 0)) * 2048 + (r == 2);
}
__global__ __launch_bounds__(768, 3) __attribute__((amdgpu_num_sgpr(98))) void k2_block(const u16* __restrict__ x, const u16* __restrict__ wqkv, const u16* __restrict__ wo,
                                                   const u16* __restrict__ w1, const u16* __restrict__ w3, const u16* __restrict__ w2,
                                                   const u16* __restrict__ cnd, const u16* __restrict__ wmod, u16* __restrict__ out, char* __restrict__ ws) {
  extern __shared__ __attribute__((aligned(16))) char smem_[];
  LDS_PTR(char) smem = (LDS_PTR(char))smem_;
  const int tid = threadIdx.x;
  int lane = tid & 63;
  const int wave = __builtin_amdgcn_readfirstlane(tid >> 6);
  const unsigned lds_base = (unsigned)(uintptr_t)smem;
  const int b = blockIdx.x, xc = b & 7, ii = b >> 3;
  const int tm3 = (xc & 1) * 8 + (ii & 7), tn3 = (xc >> 1) * 4 + (ii >> 3);
  const int tm5 = (xc & 1) * 4 + (ii & 3), tn5 = (xc >> 1) * 8 + (ii >> 2);
  const int kh6 = xc >> 2, tm6 = (xc & 1) * 8 + (xc & 2) * 2 + (ii & 3), tn6 = ii >> 2;
  const int tm4 = tm6, tn4 = tn6 * 2 + kh6;
  const int u1 = (b >> 4) * 8 + xc;
  unsigned* cnt = (unsigned*)(ws + WS_CTL);
  unsigned long long* fl1 = (unsigned long long*)(ws + WS_FL1);
  STAMP_AT(0 );

  if (wave >= 8) {
    const int lw = wave - 8, P = b * 4 + lw, lane = lane_id();
    const bool isA = lw < 2;
    asm volatile("" ::"s"(wqkv), "s"(wo), "s"(w1), "s"(w3), "s"(w2), "s"(ws));
    int kpf = KB + VB3;
    unsigned pr[4] = {};
    auto touch = [&]() {
      if (kpf < 28 && __builtin_amdgcn_s_getreg((3 << 11) | (8 << 6) | 7) <= 7) {
        stouch4((const char*)((P & 1) ? w3 : w1) + (kpf * 2048 + (P >> 1) * 4) * 128);
        ++kpf;
      }
    };
    const char* epo = ws + WS_EPO;
    const unsigned swz = ((lane & 7) ^ ((lane >> 3) & 6)) * 16;
    const int cg = lw * 16 + tn4;
    auto issue4 = [&](int s, unsigned slot) {
      const char* p = (const char*)wo + tn4 * 128 + (lw * 32 + s * 128) * 2048;
      unsigned v4[4];
#pragma unroll
      for (int i = 0; i < 4; ++i) v4[i] = (lane >> 3) * 2048 + swz + i * 15360;
      glds16x4<false>(p, p, v4, lds_base + slot + lw * 4096);
    };
    auto run3 = [&](auto isA_) {
      constexpr bool A_ = decltype(isA_)::value; constexpr int N = A_ ? 9 : 12;
      const char* sp = A_ ? ws + WS_H + lw * 131072 + (tm3 * 8 - 1) * 1024 : (const char*)wqkv + tn3 * 128;
      unsigned vo[3][4];
#pragma unroll
      for (int i = 0; i < 12; ++i) {
        const int tb = (lw - 2) * 12 + i;
        vo[i >> 2][i & 3] = A_ ? lane * 16 + (i < 8 ? 1024 + (i >> 2) * 4096 : tm3 ? 0 : 1024) : ((tb & 7) * 8 + (lane >> 3)) * 6144 + (tb >> 3) * 2048 - (i & 3) * 1024 + swz;
      }
      const unsigned d0 = lds_base + (A_ ? lw * 9216 : K3_B + (lw - 2) * 12288);
      auto issue = [&](unsigned st) {
        if (A_) { glds16x4<true>(sp, sp, vo[0], d0 + st + 1024); glds16s(sp, vo[2][0], d0 + st, 0); glds16x4<true>(sp, sp, vo[1], d0 + st + 5120); }
        else {
#pragma unroll
          for (int i = 0; i < 3; ++i) glds16x4<false>(sp, sp, vo[i], d0 + st + i * 4096);
        }
        sp += A_ ? 262144 : 64 * 6144;
      };
      { unsigned long long ep;
        asm volatile("s_load_dwordx2 %0, %1, 0x0\n\ts_waitcnt lgkmcnt(0)" : "=s"(ep) : "s"(epo) : "memory");
        ep += 1;
        if (lw == 3) { *(LDS_PTR(u32x4))(smem + LDS_ARR) = (u32x4){0u, 0u, 0u, 0u}; wait_lgkm0(); }
        if (lw == 1) {
#pragma unroll
          for (int k = 0; k < 3; ++k) {
            const int i = b * 158 + (k * 64 + lane < 158 ? k * 64 + lane : 0);
            st16<true>(ws, i < 32768 ? (unsigned)WS_XCH + i * 16 : (unsigned)WS_CTL + (i - 32768) * 16, i < 32768 ? (u32x4){~0u, ~0u, ~0u, ~0u} : (u32x4){0u, 0u, 0u, 0u});
          }
        }
        BAR();
        auto flag = [&](int idx) { wait_vm<0>(); if (lane < 8) __hip_atomic_store(fl1 + lane * 256 + idx, ep, __ATOMIC_RELAXED, __HIP_MEMORY_SCOPE_AGENT); };
        if (lw == 1 && (b & 8)) {
          const int kq = u1 & 3, cgp = u1 >> 2;
          float sm = 0.f;
#pragma unroll
          for (int r = 0; r < 8; ++r) sm += *(const LDS_PTR(float))(smem + 8192 + (r * 64 + lane) * 4);
          __hip_atomic_store((unsigned*)(ws + WS_MOD) + kq * 2048 + cgp * 64 + lane, __float_as_uint(sm), __ATOMIC_RELAXED, __HIP_MEMORY_SCOPE_AGENT);
          flag(((cgp >> 3) & 1) * 64 + (cgp >> 4) * 32 + (cgp & 7) * 4 + kq);
        }
        if (b & 8) BAR();
        BAR();
        if (lw == 1 && !(b & 8)) flag(128 + u1);
        auto flags = [&](auto n_, int first) {
#pragma unroll 1
          for (unsigned spins = 0; spins < SPIN_CAP; ++spins) {
            bool ok = true;
#pragma unroll
            for (int k = 0; k < decltype(n_)::value; ++k) ok &= __hip_atomic_load(fl1 + xc * 256 + (first + k) * 64 + lane, __ATOMIC_RELAXED, __HIP_MEMORY_SCOPE_AGENT) == ep;
            if (__builtin_amdgcn_ballot_w64(!ok) == 0) break;
            __builtin_amdgcn_s_sleep(FS0);
          }
        };
        if (lw == 0) flags(IC<1>(), (b >> 3) & 1);
        BAR();
        if (lw == 1) {
          __builtin_amdgcn_s_sleep(FD1);
          flags(IC<4>(), 0);
          if (b == 0 && lane == 0) __hip_atomic_store((unsigned long long*)(ws + WS_EPO), ep, __ATOMIC_RELAXED, __HIP_MEMORY_SCOPE_AGENT);
        }
      }
      BAR();
      if (lw == 0) {
        signal(cnt, C2 + (u1 >> 3), lane, (b & 8) ? 1u : 0x10000u);
        pollp<PD2, PQ, 3>(cnt + ((C2 + ((lane & 1) && tm3 ? tm3 - 1 : tm3)) * 8 + (tn3 & 7)) * 32, 0x80000u, pr);
      }
      if (!A_) { issue(0); issue(K3_STG); }
      BAR();
      if (A_) { issue(0); issue(K3_STG); }
      wait_vm<N>();
      KEEP4(pr);
      BAR();
      issue(2 * K3_STG);
      unsigned st = 0;
#pragma unroll 1
      for (int j = 0; j < 13; ++j) {
        wait_vm<N>();
        BAR();
        issue(st);
        st = st == 2 * K3_STG ? 0 : st + K3_STG;
      }
      wait_vm<N>();
      BAR();
      issue4(0, 57344); issue4(5, 57344 + 5 * 16384);
      glds4((P & 1) ? w3 : w1, ((KB + ((lane >> 2) < VB3 ? lane >> 2 : 0)) * 2048 + (P >> 1) * 4 + (lane & 3)) * 128, lds_base + LDS_DUMMY);
      wait_vm<9>();
    };
    if (isA) run3(T_()); else run3(F__());
    { BAR();
      BAR();
      issue4(1, 57344 + 16384);
      BAR();
      issue4(2, 57344 + 2 * 16384); issue4(3, 57344 + 3 * 16384); issue4(4, 57344 + 4 * 16384);
      BAR();
      if (lw == 0) signal(cnt, C3 + tm3, lane);
      issue4(6, 0); issue4(7, 16384);
      if (lw == 0) pollp<PD3, PQ, 1>(cnt + ((C3 + tm4) * 8 + tn6) * 32, 16, pr);
      wait_vm<8>();
      BAR();
      wait_vm<0>();
      KEEP4(pr);
      BAR();
      poll<0, 4>(cnt + (CM * 8 + cg) * 32, 4);
      f32x4 pv[4];
#pragma unroll
      for (int i = 0; i < 4; ++i) pv[i] = __builtin_bit_cast(f32x4, ld16<true>(ws + WS_MOD2, (((lane >> 4) * 4 + i) * 4096 + cg * 64 + (lane & 15) * 4) * 4));
      f32x4 v = (pv[0] + pv[1]) + (pv[2] + pv[3]);
#pragma unroll
      for (int e = 0; e < 4; ++e) { v[e] = rbf(xsum16_32(v[e])); if (lw == 2) v[e] = rbf(1.f + v[e]); if (lw == 1 || lw == 2) v[e] *= -1.4426950408889634f; }
      *(LDS_PTR(f32x4))(smem + LDS_VEC + lw * 256 + (lane & 15) * 16) = v;
      wait_lgkm0();
    }
    auto run5 = [&](auto isA_) {
      constexpr bool A_ = decltype(isA_)::value; constexpr int N = A_ ? 8 : 7;
      const char* sp = A_ ? ws + WS_H2 + (tm5 * 16 + lw * 8) * 1024 : (const char*)(lw == 2 ? w1 : w3) + tn5 * 224;
      unsigned vp[N];
#pragma unroll
      for (int i = 0; i < N; ++i) { const int L = i * 64 + lane, r = L / 14, c = L - r * 14; vp[i] = A_ ? L * 16 : r * (F_ * 2) + c * 16; }
      const unsigned d0 = lds_base + (A_ ? lw * 8192 : lw == 2 ? K5_G : K5_U);
      unsigned st = 3 * K5_STG;
      auto issue = [&]() {
#pragma unroll
        for (int i = 0; i < N; ++i) { if (A_) glds16s(sp, vp[i], d0 + st, i * 1024); else glds16(sp, vp[i], d0 + st, i * 1024); }
        sp += A_ ? 131072 : 32 * F_ * 2; st = st == (K5_NS - 1) * K5_STG ? 0 : st + K5_STG;
      };
      BAR();
      if (!A_) { issue(); issue(); }
      BAR();
      u32x4 xk[4];
      auto keep = [&]() {
#pragma unroll
        for (int i = 0; i < 4; ++i) { xk[i] = *(const LDS_PTR(u32x4))(smem + lw * 4096 + i * 1024 + lane * 16); asm volatile("" : "+v"(xk[i])); }
      };
      if (A_ && lw == 0) { signal(cnt, C4 + tm4, lane); keep(); pollp<PD4, PQ, 3>(cnt + ((C4 + tm5 * 2 + (lane & 1)) * 8 + (tn5 & 7)) * 32, 16, pr); }
      else { keep(); if (!A_) { issue(); issue(); issue(); } }
      BAR();
      if (A_) { issue(); issue(); }
      wait_vm<A_ ? N : 4 * N>();
      KEEP4(pr);
      BAR();
      if (A_) { issue(); issue(); issue(); }
#pragma unroll 1
      for (int j = 0; j < 27; ++j) {
        wait_vm<3 * N>();
        BAR();
        issue();
        if (j == 6) {
#pragma unroll
          for (int i = 0; i < 4; ++i) st16<true>(ws + WS_PART, (b * 4 + lw) * 4096 + i * 1024 + lane * 16, (u32x4){~0u, ~0u, ~0u, ~0u});
        }
        if (j == 12 && lw == 0 && lane == 0) __hip_atomic_store((unsigned*)(ws + WS_FLG) + b * 32, 16u, __ATOMIC_RELAXED, __HIP_MEMORY_SCOPE_AGENT);
        touch(); __builtin_amdgcn_s_sleep(TS5); touch();
      }
      const char* sb = A_ ? ws + WS_HH + (kh6 * 56 * 128 + tm6 * 8 + lw * 4) * 1024 : (const char*)w2 + kh6 * 1792 * 2048 + tn6 * 256 + ((lw ^ kh6 ^ 1) & 1) * 128;
      unsigned vo[4];
#pragma unroll
      for (int i = 0; i < 4; ++i) {
        const int r = i * 8 + (lane >> 3), k = 8 * ((r >> 2) & 3) + 4 * (r >> 4) + (r & 3);
        vo[i] = A_ ? lane * 16 : k * 2048 - i * 1024 + swz;
      }
      const unsigned d6 = lds_base + (A_ ? K6_A : 0) + (lw & 1) * 4096;
      st = 0;
      const int tab = w2_stage(lane);
      int jn = 0;
      auto issue6 = [&]() {
        if (A_) { glds16x4<true>(sb, sb, vo, d6 + st); sb += 131072; }
        else { const int o = __builtin_amdgcn_readlane(tab, jn); const char* s0 = sb + (o & ~1); glds16x4<false>(s0, s0 + (o & 1) * 65536, vo, d6 + st); ++jn; }
        st = st == ((A_ ? K6_NA : K6_NW) - 1) * 8192 ? 0 : st + 8192;
      };
      auto pre6 = [&](int n) {
        if (!A_) {
#pragma unroll 1
          for (int i = 0; i < n; ++i) issue6();
        }
      };
      constexpr int E = A_ ? 0 : 4;
      { const char* p = (const char*)w2 + (kh6 * 1792 + ii) * 2048;
        const int n = lw * 56 + (lane < 56 ? lane : 0), o = w2_stage((xc & 3) * 14 + (n >> 4));
        glds4(p, (o & ~1) + (o & (ii >> 4) & 1) * 65536 + (n & 15) * 128, lds_base + LDS_DUMMY); }
      wait_vm<3 * N + 1>(); BAR();
      wait_vm<2 * N + 1>(); BAR(); pre6(4);
      wait_vm<N + 4 * E + 1>(); BAR(); pre6(4);
      wait_vm<8 * E + 1>(); BAR(); pre6(4);
      BAR();
      if (A_) {
        volatile LDS_PTR(unsigned) go = (volatile LDS_PTR(unsigned))(smem + LDS_ARR + 4);
        if (lw == 0) { pollp<PD5, PQ5, 1>(cnt + ((C5 + (tm6 >> 1) * 4 + kh6 * 2) * 8 + tn6) * 32, 16, pr); *go = 1u; }
        else {
#pragma unroll 1
          for (unsigned spins = 0; spins < SPIN_CAP && __builtin_amdgcn_readfirstlane(*go) == 0; ++spins) __builtin_amdgcn_s_sleep(1);
          asm volatile("" ::: "memory");
        }
        STAMP_L(31);
        issue6(); issue6(); issue6();
        wait_vm<8>(); KEEP4(pr); BAR(); issue6(); issue6();
        wait_vm<12>(); BAR(); issue6(); issue6();
      } else { wait_vm<16>(); BAR(); BAR(); }
#pragma unroll 1
      for (int c = 2; c < 51; ++c) { wait_vm<16>(); BAR(); if (A_ || !((SKIP6 >> c) & 1)) issue6(); }
      wait_vm<16>(); BAR(); wait_vm<12>(); BAR(); wait_vm<8>(); BAR(); wait_vm<4>(); BAR(); wait_vm<0>(); BAR();
#pragma unroll
      for (int i = 0; i < 4; ++i) *(LDS_PTR(u32x4))(smem + K6_X + lw * 4096 + i * 1024 + lane * 16) = xk[i];
      BAR(); wait_lgkm0(); BAR();
    };
    if (isA) run5(T_()); else run5(F__());
    asm volatile("s_waitcnt lgkmcnt(0)" ::: "memory");
    return;
  }

  int fr = lane & 15, g = lane >> 4, p4 = fr & 3, row = 4 * g + (fr >> 2);
  int lsw = row * 128 + (((row >> 1) & 3) * 32) + p4 * 8;
  { const int mb = u1, hf = (b >> 3) & 1, kb0 = hf * 16 + wave * 2, cofs = (g & 1) * 16 + (g >> 1) * 8;
    u32x4 v[4];
    auto ldx = [&]() {
#pragma unroll
      for (int p = 0; p < 4; ++p) v[p] = *(const u32x4*)(x + (size_t)(mb * 16 + fr) * D_ + ((kb0 + (p & 1)) ^ ((p >> 1) * 16)) * 32 + cofs);
    };
    LDS_PTR(f32x2) sred = (LDS_PTR(f32x2))(smem + 4096);
    auto up2 = [](unsigned u) { return (f32x2){lo2f(u), hi2f(u)}; };
    auto stats = [&]() {
      f32x2 s2 = {0.f, 0.f}, q2 = {0.f, 0.f};
#pragma unroll
      for (int k = 0; k < 16; ++k) s2 += up2(v[k >> 2][k & 3]);
      const float mw = xsum16_32(s2[0] + s2[1]) * (1.f / 128.f);
#pragma unroll
      for (int k = 0; k < 16; ++k) { const f32x2 d = up2(v[k >> 2][k & 3]) - mw; q2 += d * d; }
      const float qw = xsum16_32(q2[0] + q2[1]);
      if (lane < 16) sred[wave * 16 + fr] = (f32x2){mw, qw};
      wait_lgkm0();
    };
    auto rest = [&]() {
    STAMP_AT(1 );
    float mean = 0.f, m2 = 0.f; f32x2 sw[8];
#pragma unroll
    for (int w = 0; w < 8; ++w) { sw[w] = sred[w * 16 + fr]; mean += sw[w][0]; m2 += sw[w][1]; }
    mean *= 0.125f;
#pragma unroll
    for (int w = 0; w < 8; ++w) { const float d = sw[w][0] - mean; m2 += 128.f * d * d; }
    const float rstd = ln_rstd(m2 * (1.f / 1024.f));
    mean = rbf(mean);
    unsigned nv[8];
#pragma unroll
    for (int k = 0; k < 8; ++k) {
      const f32x2 d = up2(v[k >> 2][k & 3]) - mean;
      const f32x2 n = up2(pk2(d[0], d[1])) * rstd;
      nv[k] = pk2(n[0], n[1]);
      asm volatile("" : "+v"(nv[k]));
    }
    SCHED0(); BAR(); SCHED0();
    STAMP_AT(2 );
    float mp[8];
#pragma unroll
    for (int i = 0; i < 8; ++i) mp[i] = __uint_as_float(__hip_atomic_load((unsigned*)(ws + WS_MOD) + hf * 512 + tid + i * 1024, __ATOMIC_RELAXED, __HIP_MEMORY_SCOPE_AGENT));
    *(LDS_PTR(unsigned))(smem + tid * 4) = pk2((mp[0] + mp[2]) + (mp[4] + mp[6]), 1.f + rbf((mp[1] + mp[3]) + (mp[5] + mp[7])));
    wait_lgkm0();
#pragma unroll
    for (int p = 0; p < 2; ++p) {
      const LDS_PTR(u32x4) tp = (const LDS_PTR(u32x4))(smem + ((wave * 2 + p) * 32 + cofs) * 4);
      const u32x4 t0 = tp[0], t1 = tp[1];
      unsigned o[4];
#pragma unroll
      for (int j = 0; j < 4; ++j) {
        const unsigned ta = j < 2 ? t0[2 * j] : t1[2 * j - 4], tb = j < 2 ? t0[2 * j + 1] : t1[2 * j - 3];
        const unsigned u = pk2(lo2f(nv[p * 4 + j]) * hi2f(ta), hi2f(nv[p * 4 + j]) * hi2f(tb));
        o[j] = pk2(lo2f(u) + lo2f(ta), hi2f(u) + lo2f(tb));
      }
      auto s0 = __builtin_amdgcn_permlane16_swap(o[0], o[2], false, false);
      auto s1 = __builtin_amdgcn_permlane16_swap(o[1], o[3], false, false);
      st16<true>(ws + WS_H, ((kb0 + p) * 128 + mb) * 1024 + lane * 16, (u32x4){s0[0], s1[0], s0[1], s1[1]});
    }
    STAMP_AT(3 );
    wait_vm<0>();
    STAMP_AT(4 );
    BAR();
    STAMP_AT(5 );
    };
    if (hf) {
      const int mrow = (u1 & 3) * 256 + (tid >> 3) * 4;
      const u32x2 c2 = *(const u32x2*)(cnd + mrow);
      u32x4 mw[4];
#pragma unroll
      for (int i = 0; i < 4; ++i) mw[i] = __builtin_nontemporal_load((const u32x4*)(wmod + (size_t)(mrow + i) * 6144 + (u1 >> 2) * 64 + (lane & 7) * 8));
      const unsigned mc[2] = {c2[0], c2[1]};
      *(LDS_PTR(f32x2))(smem + 8192 + (wave * 64 + (lane & 7) * 8 + (lane >> 4) * 2) * 4) = slab_sum<4>(mc, mw);
      wait_lgkm0();
      BAR();
      BAR();
      ldx();
      stats();
      BAR();
      rest();
    } else {
      __builtin_amdgcn_s_sleep(XD0);
      ldx();
      SCHED0();
      const int sl = (3 - ((unsigned)(uintptr_t)wqkv >> 7) - (xc >> 1) * 4) & 7;
      const int i2 = tid - 384, r0 = i2 < 0 ? tid / 12 : i2 / 3, c0 = i2 < 0 ? tid - r0 * 12 : (i2 - r0 * 3) * 4 + sl;
      const bool on = i2 < 0 || (sl < 4 && i2 < 96);
      __builtin_amdgcn_s_sleep(PFD);
      const unsigned pf = *(const unsigned*)((const char*)wqkv + (xc >> 1) * 512 + (on ? ((((xc & 1) ^ (i2 >= 0)) * 512 + (ii >> 1) * 32 + r0) * 48 + (c0 >> 2) * 16 + (c0 & 3)) * 128 : 0));
      SCHED0(); BAR(); SCHED0();
      stats();
      SCHED0(); BAR(); SCHED0();
      rest();
      asm volatile("" ::"v"(pf));
    }
  }
  {
    f32x4 bias[2];
    { const int kbase = tm3 * 128 + wave * 16 - 16;
#pragma unroll
      for (int t = 0; t < 4; ++t) {
        const int kk = (t >> 1) * 16 + 4 * g + (t & 1) * 2 + (fr & 1), df = 16 + fr - kk;
        bias[t >> 1][(t & 1) * 2] = bias[t >> 1][(t & 1) * 2 + 1] = (df >= 0 && df <= 14 && kbase + kk >= 0) ? 0.f : -__builtin_inff();
      }
      asm volatile("" : "+v"(bias[0]), "+v"(bias[1])); }
    u32x4 mc, mw[8];
    if (wave < 4) {
      const int wm = wave & 1, bA = wave;
      unsigned a0 = lds_base + K3_B + (lsw ^ (bA * 32)), a1 = lds_base + K3_B + (lsw ^ ((bA ^ 1) * 32)), aA = lds_base + 1024 + wm * 4096 + lane * 16, aH = lds_base + lane * 16;
      unsigned lq = lds_base + bA * 512 + lane * 8, lv = lds_base + 36864 + bA * 512 + (lane ^ ((g >> 1) * 8)) * 8;
      int n = 4;
      asm volatile(".irp r,0,8,16,24,32,40,48,56,64,72,80,88,96\n\tv_mov_b64 v[\\r:\\r+1], 0\n\tv_mov_b64 v[\\r+2:\\r+3], 0\n\tv_mov_b64 v[\\r+4:\\r+5], 0\n\tv_mov_b64 v[\\r+6:\\r+7], 0\n\t.endr\n\t"
                   "s_setprio 2\n\ts_waitcnt vmcnt(0)\n\ts_barrier\n\t"
                   "s_barrier\n\t"
                   RA3(1, 0) RA3(0, 0) RW3(0, 0, a0, 0) RW3(0, 1, a0, 0) RW3(1, 0, a1, 0) RW3(1, 1, a1, 0) RW3(2, 0, a1, 0) RW3(2, 1, a1, 0) RW3(3, 0, a0, 0) RW3(3, 1, a0, 0) RH3(0)
                   RW3(4, 0, a0, 0) RW3(4, 1, a0, 0) RW3(5, 0, a1, 0) LG(8)
                   ".p2align 5\n1:\n\t" STAGE3(43008, ) STAGE3(43008, ) STAGE3(-86016, "s_sub_u32 %[n], %[n], 1\n\t") "s_cbranch_scc0 1b\n\t"
                   H2_3(0, , , , ) H1_3(1, )
                   H2_3(1, "v_add_u32 %[a0], %[mo], %[lq]\n\t", "v_add_u32 %[aA], %[mo], %[lv]\n\t", "v_xor_b32 %[a1], 512, %[a0]\n\tv_xor_b32 %[aH], 512, %[aA]\n\t", "s_barrier\n\t")
                   M3(1, 5) M3(0, 5) M3(2, 0) CV3(0, 0, 0) M3(3, 0) WR3(0, 0, a0, 0) CV3(1, 0, 1) M3(3, 1) WR3(1, 1, a0, 0) CV3(0, 1, 2) M3(2, 1) WR3(0, 2, a1, 0) CV3(1, 1, 3)
                   M3(2, 2) WR3(1, 3, a1, 0) CV3(0, 2, 0) M3(3, 2) WR3(0, 0, a1, 18432) CV3(1, 2, 1) M3(3, 3) WR3(1, 1, a1, 18432) CV3(0, 3, 2) M3(2, 3) WR3(0, 2, a0, 18432) CV3(1, 3, 3)
                   M3(4, 3) WR3(1, 3, a0, 18432) CV3(0, 4, 0) M3(4, 4) WR3(0, 0, aA, 0) CV3(1, 4, 1) M3(2, 4) WR3(1, 1, aA, 0) CV3(0, 5, 2) M3(3, 4) WR3(0, 2, aH, 0) CV3(1, 5, 3)
                   M3(3, 5) WR3(1, 3, aH, 0) CV3(2, 0, 0) M3(2, 5) WR3(2, 0, a0, 0)
                   CW3(3, 0, 1, a0, 0) CW3(3, 1, 2, a1, 0) CW3(2, 1, 3, a1, 0) CW3(2, 2, 0, a1, 18432) CW3(3, 2, 1, a1, 18432) CW3(3, 3, 2, a0, 18432) CW3(2, 3, 3, a0, 18432)
                   CV3(4, 3, 0) WR3(0, 0, lq, 18432) CV3(4, 4, 1) WR3(0, 1, lv, 0) CW3(2, 4, 2, aA, 0) CW3(3, 4, 3, aA, 0) CW3(3, 5, 0, aH, 0) CW3(2, 5, 1, aH, 0)
                   "s_setprio 0\n\t" LG(0)
                   : [a0] "+v"(a0), [a1] "+v"(a1), [aA] "+v"(aA), [aH] "+v"(aH), [n] "+s"(n)
                   : [lq] "v"(lq), [lv] "v"(lv), [mo] "s"((wm * 4 + 1) * 2048)
                   : "memory", "scc", V10(), V10(1), V10(2), V10(3), V10(4), V10(5), V10(6), V10(7), V10(8), V10(9), V10(10), V10(11), V10(12), V10(13), V8(14));
    } else {
      wait_vm<0>();
      BAR(); BAR();
      int k = -4; const int P = b * 4 + (wave & 3);
      auto idle = [&]() {
        if (k < KB && __builtin_amdgcn_s_getreg((3 << 11) | (8 << 6) | 7) <= 7) {
          stouch4(k < 0 ? (const char*)wo + ((k + 4) * 4096 + P * 4) * 128 : (const char*)((P & 1) ? w3 : w1) + (k * 2048 + (P >> 1) * 4) * 128); ++k; }
        BAR();
      };
      {
        const unsigned* pc = cnt + ((C2 + ((lane & 1) && tm3 ? tm3 - 1 : tm3)) * 8 + (tn3 & 7)) * 32;
        unsigned seen = 0x80008u;
        idle(); idle(); idle();
        if (wave == 4) asm volatile("global_load_dword %0, %1, off sc1" : "=v"(seen) : "v"(pc) : "memory");
        idle(); idle();
        asm volatile("s_waitcnt vmcnt(0)" : "+v"(seen)::"memory");
        if (__builtin_amdgcn_ballot_w64(seen < 0x80008u) != 0) poll<0>(pc, 0x80008u);
      }
#pragma unroll 1
      for (int t = 5; t < SLB; ++t) { if (t < 14) idle(); else BAR(); }
      wait_lgkm0();
      const int mrow = ((b & 3) * 4 + (wave & 3)) * 64 + (lane >> 3) * 8;
      mc = *(const u32x4*)(cnd + mrow);
#pragma unroll
      for (int i = 0; i < 8; ++i) mw[i] = __builtin_nontemporal_load((const u32x4*)(wmod + (size_t)(mrow + i) * 6144 + 2048 + (b >> 2) * 64 + (lane & 7) * 8));
      SCHED0();
#pragma unroll
      for (int t = SLB; t < 16; ++t) BAR();
    }
    lane = lane_id(); fr = lane & 15; g = lane >> 4; p4 = fr & 3; row = 4 * g + (fr >> 2); lsw = row * 128 + (((row >> 1) & 3) * 32) + p4 * 8;
    wait_lgkm0();
    BAR();
    STAMP_AT(9 );
    const LDS_PTR(char) eb = smem + wave * 2048;
    auto ld2 = [&](int o) { return cat8(*(const LDS_PTR(bf16x4))(eb + o), *(const LDS_PTR(bf16x4))(eb + o + 512)); };
    const int vo = 36864 + ((p4 * 16 + row) ^ ((p4 >> 1) * 8)) * 8;
    bf16x8 qf[2], k0[2], k1[2], v0[2], v1[2];
#pragma unroll
    for (int hd = 0; hd < 2; ++hd) { qf[hd] = ld2(2048 + hd * 1024 + lane * 8); k0[hd] = ld2(18432 + hd * 1024 + lane * 8); k1[hd] = ld2(18432 + 2048 + hd * 1024 + lane * 8); }
#pragma unroll
    for (int hd = 0; hd < 2; ++hd) {
      v0[hd] = cat8(ld_tr4(eb + vo + hd * 1024), ld_tr4(eb + vo + hd * 1024 + 2048));
      v1[hd] = cat8(ld_tr4(eb + vo + hd * 1024 + 512), ld_tr4(eb + vo + hd * 1024 + 512 + 2048));
    }
    SCHED0();
    const f32x4 z = {0.f, 0.f, 0.f, 0.f};
    const bool odd = fr & 1;
    const unsigned sel0 = odd ? 0x01000c0cu : 0x0c0c0100u, sel1 = odd ? 0x03020c0cu : 0x0c0c0302u;
    float inf = __builtin_inff(); asm("" : "+v"(inf));
    auto mx2 = [&](float a, float c) { return __builtin_amdgcn_fmed3f(a, c, inf); };
    auto both = [](float a, float c, auto op, auto fin) {
      auto u = __builtin_amdgcn_permlane16_swap(__float_as_uint(a), __float_as_uint(c), false, false);
      float r = op(__uint_as_float(u[0]), __uint_as_float(u[1]));
      auto w = __builtin_amdgcn_permlane32_swap(__float_as_uint(r), __float_as_uint(r), false, false);
      r = fin(op(__uint_as_float(w[0]), __uint_as_float(w[1])));
      auto y = __builtin_amdgcn_permlane16_swap(__float_as_uint(r), __float_as_uint(r), false, false);
      return (f32x2){__uint_as_float(y[0]), __uint_as_float(y[1])};
    };
    f32x2 l[2][2];
#pragma unroll
    for (int hd = 0; hd < 2; ++hd) {
      const f32x4 s0 = __builtin_amdgcn_mfma_f32_16x16x32_bf16(k0[hd], qf[hd], bias[0], 0, 0, 0), s1 = __builtin_amdgcn_mfma_f32_16x16x32_bf16(k1[hd], qf[hd], bias[1], 0, 0, 0);
#pragma unroll
      for (int t = 0; t < 2; ++t) {
        const f32x4 sv = t ? s1 : s0;
        unsigned u = pk2(odd ? sv[1] : sv[0], odd ? sv[3] : sv[2]);
        u = pk2(lo2f(u) * 0.17677669529663687f, hi2f(u) * 0.17677669529663687f);
        l[hd][t] = (f32x2){lo2f(u), hi2f(u)};
      }
    }
    const f32x2 mx = both(mx2(mx2(l[0][0][0], l[0][0][1]), mx2(l[0][1][0], l[0][1][1])), mx2(mx2(l[1][0][0], l[1][0][1]), mx2(l[1][1][0], l[1][1][1])), mx2,
                          [](float r) { return r * -1.4426950408889634f; });
    float sm[2];
#pragma unroll
    for (int hd = 0; hd < 2; ++hd) {
#pragma unroll
      for (int t = 0; t < 2; ++t) { const f32x2 e = l[hd][t] * 1.4426950408889634f + mx[hd]; l[hd][t] = (f32x2){__builtin_amdgcn_exp2f(e[0]), __builtin_amdgcn_exp2f(e[1])}; }
      const f32x2 a = l[hd][0] + l[hd][1]; sm[hd] = a[0] + a[1];
    }
    const f32x2 inv = both(sm[0], sm[1], [](float a, float c) { return a + c; }, [](float r) { return __builtin_amdgcn_rcpf(r); });
#pragma unroll
    for (int hd = 0; hd < 2; ++hd) {
      u32x4 pp;
#pragma unroll
      for (int t = 0; t < 2; ++t) {
        const f32x2 p = l[hd][t] * inv[hd]; const unsigned u = pk2(p[0], p[1]);
        pp[2 * t] = __builtin_amdgcn_perm(u, u, sel0); pp[2 * t + 1] = __builtin_amdgcn_perm(u, u, sel1);
      }
      const bf16x8 pf = __builtin_bit_cast(bf16x8, pp);
      const f32x4 oa = __builtin_amdgcn_mfma_f32_16x16x32_bf16(v0[hd], pf, z, 0, 0, 0), ob = __builtin_amdgcn_mfma_f32_16x16x32_bf16(v1[hd], pf, z, 0, 0, 0);
      u32x4 ov; ov[0] = pk2(oa[0], oa[1]); ov[1] = pk2(oa[2], oa[3]); ov[2] = pk2(ob[0], ob[1]); ov[3] = pk2(ob[2], ob[3]);
      st16<true>(ws + WS_O, ((tn3 * 2 + hd) * 128 + tm3 * 8 + wave) * 1024 + lane * 16, ov);
    }
    STAMP_AT(10 );
    wait_vm<0>();
    STAMP_AT(11 );
    BAR();
    STAMP_AT(12 );
    if (wave >= 4) {
      const unsigned m4[4] = {mc[0], mc[1], mc[2], mc[3]};
      const f32x2 ps = slab_sum<8>(m4, mw);
      if (!(lane & 8))
        __hip_atomic_store((unsigned long long*)(ws + WS_MOD2) + (((b & 3) * 4 + (wave & 3)) * 4096 + (b >> 2) * 64 + (lane & 7) * 8 + (lane >> 4) * 2) / 2,
                           ((unsigned long long)__float_as_uint(ps[1]) << 32) | __float_as_uint(ps[0]), __ATOMIC_RELAXED, __HIP_MEMORY_SCOPE_AGENT);
      wait_vm<0>();
      if (lane == 0 && __hip_atomic_fetch_add((LDS_PTR(unsigned))(smem + LDS_ARR + 8), 1u, __ATOMIC_RELAXED, __HIP_MEMORY_SCOPE_WORKGROUP) == 3)
        __hip_atomic_fetch_add(cnt + (CM * 8 + (b >> 2)) * 32, 1u, __ATOMIC_RELAXED, __HIP_MEMORY_SCOPE_AGENT);
    }
  }
  { const int wm = wave & 3, ks = wave >> 2, mb = tm4 * 8 + wm * 2 + ks;
    constexpr int R = 6;
    u32x4 rx[2];
#pragma unroll
    for (int p = 0; p < 2; ++p) rx[p] = *(const u32x4*)(x + (size_t)(mb * 16 + fr) * D_ + tn4 * 64 + p * 32 + (g & 1) * 16 + (g >> 1) * 8);
    const unsigned va0 = (ks * 128 + mb) * 1024 + lane * 16, va1 = va0 ^ 1024;
    bf16x8 fa[R][2], fw[2][4];
    auto issueA = [&](int t) {
      const char* pa = ws + WS_O + t * 262144;
      fa[t % R][0] = __builtin_bit_cast(bf16x8, ld16<true>(pa, va0));
      fa[t % R][1] = __builtin_bit_cast(bf16x8, ld16<true>(pa, va1));
    };
    unsigned aw[4];
    auto rdw = [&](int t, int j) {
      const int o = (t & 3) * 8192;
      fw[t & 1][j] = cat8(ld_tr4(smem + aw[j] + o), ld_tr4(smem + aw[j] + o + 2048));
    };
    auto stepw = [&](int t) {
      const int gi = t >> 2, base = gi < 3 ? 57344 + gi * 32768 : 0;
#pragma unroll
      for (int j = 0; j < 4; ++j) { aw[j] = base + ks * 4096 + (lsw ^ (j * 32)); asm volatile("" : "+v"(aw[j])); }
    };
    auto up2 = [](unsigned u) { return (f32x2){lo2f(u), hi2f(u)}; };
    f32x2 f[8];
    unsigned slot = (mb * 256 + fr + kh6 * 32768 + 4 * g * 16) * 8, sown = (mb * 256 + fr + (g & 1) * 32768 + tn4 * 16) * 8;
    f32x4 acc[2][4];
#pragma unroll
    for (int i = 0; i < 2; ++i)
#pragma unroll
      for (int j = 0; j < 4; ++j) acc[i][j] = (f32x4){0.f, 0.f, 0.f, 0.f};
    stepw(0);
    wait_lgkm0();
    SCHED0(); BAR(); SCHED0();
    STAMP_AT(13 );
#pragma unroll
    for (int t = 0; t < R; ++t) issueA(t);
#pragma unroll
    for (int j = 0; j < 4; ++j) rdw(0, j);
    SCHED0();
#pragma unroll
    for (int t = 0; t < 16; ++t) {
      if (t < 15 && ((t + 1) & 3) == 0) stepw(t + 1);
#pragma unroll
      for (int j = 0; j < 4; ++j) {
        if (t < 15) rdw(t + 1, j);
        SCHED0();
#pragma unroll
        for (int i = 0; i < 2; ++i) acc[i][j] = __builtin_amdgcn_mfma_f32_16x16x32_bf16(fw[t & 1][j], fa[t % R][i], acc[i][j], 0, 0, 0);
        SCHED0();
      }
      if (t + R < 16) issueA(t + R);
      if (t == 2) {
#pragma unroll
        for (int p = 0; p < 2; ++p) {
          auto s0 = __builtin_amdgcn_permlane16_swap(rx[p][0], rx[p][2], false, false);
          auto s1 = __builtin_amdgcn_permlane16_swap(rx[p][1], rx[p][3], false, false);
          f[4 * p] = up2(s0[0]); f[4 * p + 1] = up2(s1[0]); f[4 * p + 2] = up2(s0[1]); f[4 * p + 3] = up2(s1[1]);
        }
#pragma unroll
        for (int j = 0; j < 8; ++j) asm volatile("" : "+v"(f[j]));
        asm volatile("" : "+v"(slot), "+v"(sown));
      }
      if (t == 5) BAR();
      SCHED0();
    }
    STAMP_AT(14 );
    LDS_PTR(f32x4) red = (LDS_PTR(f32x4))(smem + K4_RED);
#pragma unroll
    for (int j = 0; j < 4; ++j) red[((wave ^ 4) * 4 + j) * 64 + lane] = acc[1][j];
    wait_lgkm0();
    BAR();
    STAMP_AT(15 );
    const LDS_PTR(float) vec = (const LDS_PTR(float))(smem + LDS_VEC);
    unsigned o[8];
    f32x4 pr[4], ga[4];
#pragma unroll
    for (int j = 0; j < 4; ++j) { pr[j] = red[(wave * 4 + j) * 64 + lane]; ga[j] = *(const LDS_PTR(f32x4))(vec + j * 16 + 4 * g); }
    SCHED0();
#pragma unroll
    for (int j = 0; j < 4; ++j) {
      const f32x4 y = acc[0][j] + pr[j];
      const f32x2 lo = y.xy * ga[j].xy + f[2 * j], hi = y.zw * ga[j].zw + f[2 * j + 1];
      o[2 * j] = pk2(lo[0], lo[1]); o[2 * j + 1] = pk2(hi[0], hi[1]);
      f[2 * j] = up2(o[2 * j]); f[2 * j + 1] = up2(o[2 * j + 1]);
    }
    const f32x2 s2 = ((f[0] + f[1]) + (f[2] + f[3])) + ((f[4] + f[5]) + (f[6] + f[7]));
    float mt = xsum16_32(s2[0] + s2[1]) * (1.f / 64.f); asm("" : "+v"(mt));
    f32x2 q2 = {0.f, 0.f}, q3 = {0.f, 0.f};
#pragma unroll
    for (int j = 0; j < 8; j += 2) { f[j] -= mt; f[j + 1] -= mt; q2 += f[j] * f[j]; q3 += f[j + 1] * f[j + 1]; }
    q2 += q3;
    const float qq = xsum16_32(q2[0] + q2[1]);
    if (lane < 32) {
      const unsigned qb = qq == qq ? __float_as_uint(qq) : 0x7fc00000u;
      __hip_atomic_store((unsigned long long*)(ws + WS_XCH + sown), ((unsigned long long)qb << 32) | __float_as_uint(mt), __ATOMIC_RELAXED, __HIP_MEMORY_SCOPE_AGENT);
    }
    *(LDS_PTR(u32x4))(smem + (mb & 7) * 2048 + lane * 16) = (u32x4){o[0], o[1], o[2], o[3]};
    *(LDS_PTR(u32x4))(smem + (mb & 7) * 2048 + 1024 + lane * 16) = (u32x4){o[4], o[5], o[6], o[7]};
    STAMP_AT(16 );
    f32x4 ss[4], gg[4];
#pragma unroll
    for (int j = 0; j < 4; ++j) {
      ss[j] = *(const LDS_PTR(f32x4))(vec + 64 + j * 16 + 4 * g); gg[j] = *(const LDS_PTR(f32x4))(vec + 128 + j * 16 + 4 * g);
      f[2 * j] *= gg[j].xy; f[2 * j + 1] *= gg[j].zw;
      asm volatile("" : "+v"(ss[j]), "+v"(gg[j]), "+v"(f[2 * j]), "+v"(f[2 * j + 1]));
    }
    unsigned long long pv[4];
    __builtin_amdgcn_s_sleep(XS0);
    for (unsigned spins = 0;; ++spins) {
      bool ok = true;
#pragma unroll
      for (int i = 0; i < 4; ++i) { pv[i] = __hip_atomic_load((unsigned long long*)(ws + WS_XCH + slot) + i * 16, __ATOMIC_RELAXED, __HIP_MEMORY_SCOPE_AGENT); ok &= (unsigned)(pv[i] >> 32) != 0xffffffffu; }
      if (__builtin_amdgcn_ballot_w64(!ok) == 0 || spins > SPIN_CAP) break;
      __builtin_amdgcn_s_sleep(XS1);
    }
    STAMP_AT(17 );
    float ms = 0.f, m2 = 0.f;
#pragma unroll
    for (int i = 0; i < 4; ++i) { ms += __uint_as_float((unsigned)pv[i]); m2 += __uint_as_float((unsigned)(pv[i] >> 32)); }
    const float mean = xsum16_32(ms) * (1.f / 16.f);
#pragma unroll
    for (int i = 0; i < 4; ++i) { const float d = __uint_as_float((unsigned)pv[i]) - mean; m2 += 64.f * d * d; }
    const float rstd = ln_rstd(xsum16_32(m2) * (1.f / 1024.f));
    const float sh = (mt - rbf(mean)) * rstd;
#pragma unroll
    for (int p = 0; p < 2; ++p) {
      u32x4 v;
#pragma unroll
      for (int j = 0; j < 4; ++j) {
        const int jb = 2 * p + (j >> 1);
        const f32x2 h = f[4 * p + j] * rstd + (((j & 1) ? gg[jb].zw : gg[jb].xy) * sh + ((j & 1) ? ss[jb].zw : ss[jb].xy));
        v[j] = pk2(h[0], h[1]);
      }
      st16<true>(ws + WS_H2, ((tn4 * 2 + p) * 128 + mb) * 1024 + lane * 16, v);
    }
    STAMP_AT(18 );
    wait_vm<0>();
    wait_lgkm0();
    BAR();
    STAMP_AT(19 );

  }
  const int grp = wave >> 2;
  { const int wm = wave & 3;
    const LDS_PTR(char) sb = smem + 3 * K5_STG;
    const LDS_PTR(char) aP = sb + K5_G + row * 224 + grp * 96 + p4 * 8;
    const LDS_PTR(char) aM = sb + (p4 < 2 ? K5_G : K5_U) + row * 224 + (12 + grp) * 16 + (p4 & 1) * 8;
    const LDS_PTR(char) aA = sb + wm * 4096 + lane * 16;
    f32x32 c0, c1, c2; f32x16 c3;
    asm volatile(".irp r,0,8,16,24,32,40,48,56,64,72,80,88,96,104\n\tv_mov_b64 v[\\r:\\r+1], 0\n\tv_mov_b64 v[\\r+2:\\r+3], 0\n\tv_mov_b64 v[\\r+4:\\r+5], 0\n\tv_mov_b64 v[\\r+6:\\r+7], 0\n\t.endr"
                 : "={v[0:31]}"(c0), "={v[32:63]}"(c1), "={v[64:95]}"(c2), "={v[96:111]}"(c3)::"memory");
    int wrap = -(K5_NS - 1) * K5_STG, dl = K5_STG, n = 7;
    unsigned mask = wrap_mask(4, K5_NS);
    asm volatile("" : "+s"(wrap), "+v"(aP), "+v"(aM), "+v"(aA));
    if (grp) __builtin_amdgcn_s_setprio(1); else __builtin_amdgcn_s_setprio(2);
    BAR();
    STAMP_AT(20 );
    BAR();
    STAMP_G(21, 26);
    asm volatile(RA5(0) RA5(1) RP5(0, 0) RP5(0, 1) RP5(1, 0) RP5(1, 1) RP5(2, 0) RP5(2, 1) RP5(3, 0) RP5(3, 1) RP5(4, 0) RP5(4, 1) RP5(5, 0) RP5(5, 1) RW5(6, 0, aM, 0) LG(9)
                 ".p2align 5\n1:\n\t" TRIP5("") TRIP5("") TRIP5("") TRIP5("s_sub_u32 %[n], %[n], 1\n\t") "s_cbranch_scc0 1b\n\t" LG(0) "s_nop 7"
                 : ACC5, [aP] "+v"(aP), [aM] "+v"(aM), [aA] "+v"(aA), [dl] "+s"(dl), [mask] "+s"(mask), [n] "+s"(n) : [wrap] "s"(wrap) : CLOB5);
    f32x4 acc[4][7];
#pragma unroll
    for (int e = 0; e < 112; ++e) acc[e / 28][(e % 28) / 4][e & 3] = e < 32 ? c0[e & 31] : e < 64 ? c1[e & 31] : e < 96 ? c2[e & 31] : c3[e & 15];
    STAMP_G(22, 27);
    auto swi4 = [](const f32x4& gv, const f32x4& uv, unsigned& o0, unsigned& o1) {
      constexpr float c2 = 2.0813689810056077f;
      f32x2 pa = gv.xy * uv.xy, pb = gv.zw * uv.zw;
      const f32x2 ta = (f32x2){__builtin_amdgcn_exp2f(gv[0]), __builtin_amdgcn_exp2f(gv[1])} * c2 + c2, tb = (f32x2){__builtin_amdgcn_exp2f(gv[2]), __builtin_amdgcn_exp2f(gv[3])} * c2 + c2;
      pa *= (f32x2){__builtin_amdgcn_rcpf(ta[0]), __builtin_amdgcn_rcpf(ta[1])}; pb *= (f32x2){__builtin_amdgcn_rcpf(tb[0]), __builtin_amdgcn_rcpf(tb[1])};
      o0 = pk2(pa[0], pa[1]); o1 = pk2(pb[0], pb[1]);
    };
    const int lz = lane_id();
    const int gz = lz >> 4, fz = lz & 15;
    const int qb = (tn5 >> 4) * 224, ql = qb + 64 + (tn5 & 15) * 10;
    auto hof = [&](int e, int i) { return (unsigned)((e >> 2) * 131072 + (e & 3) * 256 + fz * 16 + i * 1024); };
    const unsigned ho0 = hof((grp ? ql + 2 : qb + (tn5 & 15) * 4) + 2 * (gz & 1) + (gz >> 1), 0), ho1 = hof(ql + 6 * grp + (gz >> 1), gz & 1), hom = hof(ql + 8 + grp, gz);
    char* hb = ws + WS_HH + (tm5 * 16 + wm * 4) * 1024;
    const int c5 = C5 + tm5 * 4 + (tn5 >> 4) * 2;
    auto tr16 = [](const unsigned (&o)[4]) {
      auto s0 = __builtin_amdgcn_permlane16_swap(o[0], o[2], false, false);
      auto s1 = __builtin_amdgcn_permlane16_swap(o[1], o[3], false, false);
      u32x4 v = {s0[0], s1[0], s0[1], s1[1]}; asm volatile("" : "+v"(v)); return v;
    };
    auto pair = [&](int p, int i) {
      unsigned o[4];
      swi4(acc[i][2 * p], acc[i][2 * p + 1], o[0], o[1]);
      swi4(acc[i + 1][2 * p], acc[i + 1][2 * p + 1], o[2], o[3]);
      return tr16(o);
    };
    u32x4 v[3];
#pragma unroll
    for (int i = 0; i < 4; ++i) {
      unsigned o[4];
      swi4(acc[i][0], acc[i][1], o[0], o[1]);
      swi4(acc[i][2], acc[i][3], o[2], o[3]);
      st16<true>(hb, ho0 + i * 1024, tr16(o));
      SCHED0();
    }
    STAMP_G(23, 28);
    v[0] = pair(2, 0);
    if (!grp) {
      wait_vm<0>();
      unsigned arr = 0;
      if (lz == 0) arr = __hip_atomic_fetch_add((LDS_PTR(unsigned))(smem + LDS_ARR), 1u, __ATOMIC_RELAXED, __HIP_MEMORY_SCOPE_WORKGROUP);
      if (__builtin_amdgcn_readfirstlane(arr) == 3) signal(cnt, c5, lz);
    }
    STAMP_G(24, 29);
    v[1] = pair(2, 2);
    { unsigned m[4];
#pragma unroll
      for (int i = 0; i < 4; i += 2) {
        f32x4 gv, uv;
#pragma unroll
        for (int e = 0; e < 4; ++e) {
          auto w = __builtin_amdgcn_permlane32_swap(__float_as_uint(acc[i + (e >> 1)][6][e & 1]), __float_as_uint(acc[i + (e >> 1)][6][2 + (e & 1)]), false, false);
          gv[e] = __uint_as_float(w[0]); uv[e] = __uint_as_float(w[1]);
        }
        swi4(gv, uv, m[i], m[i + 1]);
      }
      auto x0 = __builtin_amdgcn_permlane32_swap(m[0], m[2], false, false);
      auto x1 = __builtin_amdgcn_permlane32_swap(m[1], m[3], false, false);
      const unsigned o[4] = {x0[0], x0[1], x1[0], x1[1]};
      v[2] = tr16(o);
      st16<true>(hb, ho1, v[0]); st16<true>(hb, ho1 + 2048, v[1]); st16<true>(hb, hom, v[2]);
    }
    STAMP_G(25, 30);
    if (grp) {
#pragma unroll
      for (int t = 0; t < NDR; ++t) BAR();
      wait_vm<0>();
      BAR();
      if (wave == 4) signal(cnt, c5 + 1, lz);
      if (wave > 4) return;
#pragma unroll 1
      for (int t = NDR + 1; t < 9; ++t) BAR();
      unsigned seen = 16;
      const unsigned* pc = (lz & 1) ? (const unsigned*)(ws + WS_FLG) + (b ^ 4) * 32 : cnt + ((C5 + (tm6 >> 1) * 4 + kh6 * 2 + 1) * 8 + tn6) * 32;
      asm volatile("global_load_dword %0, %1, off sc1" : "=v"(seen) : "v"(pc) : "memory");
      BAR(); BAR();
      asm volatile("s_waitcnt vmcnt(0)" : "+v"(seen)::"memory");
      if (__builtin_amdgcn_ballot_w64(seen < 16) != 0) poll<0>(pc, 16);
      return;
    }
  }
  { lane = lane_id(); fr = lane & 15; g = lane >> 4; p4 = fr & 3; row = 4 * g + (fr >> 2);
    const int wm = wave & 1, wn = wave >> 1;
    const int lw0 = row * 128 + (((row >> 1) & 3) * 32) + p4 * 8;
    const int mb0 = tm6 * 8 + wm * 4, t64 = tn6 * 2 + kh6;
    unsigned a0 = lds_base + (lw0 ^ (wn * 64)), a1 = lds_base + (lw0 ^ (wn * 64 + 32)), aa = lds_base + K6_A + wm * 4096 + lane * 16;
    int dw, da, n = 4, n2 = 1;
    unsigned mw = wrap_mask(2, K6_NW), ma = wrap_mask(2, K6_NA);
    f32x32 c0, c1;
    asm volatile(".irp r,0,8,16,24,32,40,48,56\n\tv_mov_b64 v[\\r:\\r+1], 0\n\tv_mov_b64 v[\\r+2:\\r+3], 0\n\tv_mov_b64 v[\\r+4:\\r+5], 0\n\tv_mov_b64 v[\\r+6:\\r+7], 0\n\t.endr\n\t"
                 "s_waitcnt vmcnt(0)\n\ts_barrier\n\t"
                 RW6(1, 0, 0, 0) RW6(1, 0, 1, 0) RA6(0, 0) RW6(1, 1, 0, 0) RW6(1, 1, 1, 0) RA6(1, 0) RA6(2, 0) RA6(3, 0) RW6(0, 0, 0, 0) RW6(0, 0, 1, 0) RW6(0, 1, 0, 0) RW6(0, 1, 1, 0)
                 "v_add_u32 %[a0], 8192, %[a0]\n\tv_add_u32 %[a1], 8192, %[a1]\n\tv_add_u32 %[aa], 8192, %[aa]\n\t" LG(4) "s_barrier\n\t"
                 RW6(0, 0, 0, 1) RW6(0, 0, 1, 1) RA6(0, 1) RW6(0, 1, 0, 1) M6(0, 2, 0) RW6(0, 1, 1, 1) M6(0, 3, 0) RA6(1, 1) M6(1, 3, 0) RA6(2, 1) M6(1, 2, 0) RA6(3, 1)
                 M6(2, 2, 0) RW6(1, 0, 0, 1) M6(2, 3, 0) RW6(1, 0, 1, 1) M6(3, 3, 0) RW6(1, 1, 0, 1) M6(3, 2, 0) RW6(1, 1, 1, 1)
                 "v_add_u32 %[a0], 8192, %[a0]\n\tv_add_u32 %[a1], 8192, %[a1]\n\tv_add_u32 %[aa], 8192, %[aa]\n\t" LG(6) "s_barrier\n\t"
                 ".p2align 5\n1:\n\t" TRIP6(0, 0, 1, 2, ) TRIP6(1, 1, 2, 0, ) TRIP6(0, 2, 0, 1, ) TRIP6(1, 0, 1, 2, ) TRIP6(0, 1, 2, 0, ) TRIP6(1, 2, 0, 1, "s_sub_u32 %[n], %[n], 1\n\t")
                 "s_cbranch_scc0 1b\n\ts_mov_b32 %[n], 3\n\ts_mov_b32 %[mw], %[mw2]\n\ts_mov_b32 %[ma], %[ma2]\n\ts_sub_u32 %[n2], %[n2], 1\n\ts_cbranch_scc0 1b\n\t"
                 M6(0, 0, 0) M6(0, 0, 1) M6(0, 1, 1) M6(0, 1, 0) LG(0) M6(1, 1, 0) M6(1, 1, 1) M6(1, 0, 1) M6(1, 0, 0) M6(2, 0, 0) M6(2, 0, 1) M6(2, 1, 1) M6(2, 1, 0)
                 M6(3, 1, 0) M6(3, 1, 1) M6(3, 0, 1) M6(3, 0, 0) "s_barrier\n\t"
                 M6(0, 2, 1) M6(0, 3, 1) M6(1, 3, 1) M6(1, 2, 1) M6(2, 2, 1) M6(2, 3, 1) M6(3, 3, 1) M6(3, 2, 1) "s_nop 8"
                 : "={v[0:31]}"(c0), "={v[32:63]}"(c1), [a0] "+v"(a0), [a1] "+v"(a1), [aa] "+v"(aa), [dw] "=&s"(dw), [da] "=&s"(da), [mw] "+s"(mw), [ma] "+s"(ma), [n] "+s"(n), [n2] "+s"(n2)
                 : [ww] "s"(-(K6_NW - 1) * 8192), [wa] "s"(-(K6_NA - 1) * 8192), [mw2] "n"(wrap_mask(32, K6_NW)), [ma2] "n"(wrap_mask(32, K6_NA))
                 : "memory", "scc", "v64", "v65", "v66", "v67", "v68", "v69", V10(7), V10(8), V10(9), V10(10), V10(11), V10(12), V10(13), V10(14), V10(15));
    f32x4 acc[4][4];
#pragma unroll
    for (int e = 0; e < 64; ++e) acc[e >> 4][(e >> 2) & 3][e & 3] = e < 32 ? c0[e & 31] : c1[e & 31];
    STAMP_AT(34 );
    lane = lane_id(); fr = lane & 15; g = lane >> 4;
    const int slot = wm * 2 + wn, pb = (b ^ 4) * 4 + slot;
    { auto pk = [](float lo, float hi) { const unsigned u = pk2(lo, hi); return u < 0xfffffffeu ? u : 0xfffffffeu; };
#pragma unroll
      for (int i = 0; i < 4; ++i)
        st16<true>(ws + WS_PART, (b * 4 + slot) * 4096 + i * 1024 + lane * 16,
                   (u32x4){pk(acc[i][0][0], acc[i][0][1]), pk(acc[i][0][2], acc[i][0][3]), pk(acc[i][1][0], acc[i][1][1]), pk(acc[i][1][2], acc[i][1][3])});
      STAMP_AT(35 );
    }
    { const LDS_PTR(float) vec = (const LDS_PTR(float))(smem + LDS_VEC) + 192 + wn * 32 + 4 * g;
      const f32x4 gv0 = *(const LDS_PTR(f32x4))vec, gv1 = *(const LDS_PTR(f32x4))(vec + 16);
      u32x4 rv[4];
#pragma unroll
      for (int i = 0; i < 4; ++i) { rv[i] = *(const LDS_PTR(u32x4))(smem + K6_X + (wm * 4 + i) * 2048 + wn * 1024 + lane * 16); asm volatile("" : "+v"(rv[i])); }
      u32x4 z[4];
      __builtin_amdgcn_s_sleep(PD6);
#pragma unroll 1
      for (unsigned spins = 0; spins < SPIN_CAP; ++spins) {
        asm volatile("" ::: "memory");
        unsigned m = 0;
#pragma unroll
        for (int i = 0; i < 4; ++i) z[i] = ld16<true>(ws + WS_PART, pb * 4096 + i * 1024 + lane * 16);
#pragma unroll
        for (int i = 0; i < 16; ++i) m = m > z[i >> 2][i & 3] ? m : z[i >> 2][i & 3];
        if (__builtin_amdgcn_ballot_w64(m == ~0u) == 0) break;
        __builtin_amdgcn_s_sleep(PS6);
      }
      STAMP_AT(36 );
#pragma unroll
      for (int i = 0; i < 4; ++i) {
        const f32x4 v0 = (acc[i][2] + (f32x4){lo2f(z[i][0]), hi2f(z[i][0]), lo2f(z[i][1]), hi2f(z[i][1])}) * gv0;
        const f32x4 v1 = (acc[i][3] + (f32x4){lo2f(z[i][2]), hi2f(z[i][2]), lo2f(z[i][3]), hi2f(z[i][3])}) * gv1;
        const unsigned o0 = pk2(lo2f(rv[i][0]) + v0[0], hi2f(rv[i][0]) + v0[1]), o1 = pk2(lo2f(rv[i][1]) + v0[2], hi2f(rv[i][1]) + v0[3]);
        const unsigned o2 = pk2(lo2f(rv[i][2]) + v1[0], hi2f(rv[i][2]) + v1[1]), o3 = pk2(lo2f(rv[i][3]) + v1[2], hi2f(rv[i][3]) + v1[3]);
        auto s0 = __builtin_amdgcn_permlane16_swap(o0, o2, false, false);
        auto s1 = __builtin_amdgcn_permlane16_swap(o1, o3, false, false);
        st16<false>(out, (((mb0 + i) * 16 + fr) * D_ + t64 * 64 + wn * 32 + (g & 1) * 16 + (g >> 1) * 8) * 2, (u32x4){s0[0], s1[0], s0[1], s1[1]});
      }
    }
    STAMP_AT(37 );
  }
}

extern "C" void kernel_launch(void* const* d_in, const int* in_sizes, int n_in, void* d_out, int out_size, void* d_ws,
                              size_t ws_size, hipStream_t stream) {
  const u16* x = (const u16*)d_in[0];
  const u16* wqkv = (const u16*)d_in[1];
  const u16* wo = (const u16*)d_in[2];
  const u16* w1 = (const u16*)d_in[4];
  const u16* w3 = (const u16*)d_in[5];
  const u16* w2 = (const u16*)d_in[6];
  const u16* c = (const u16*)d_in[7];
  const u16* wmod = (const u16*)d_in[8];
  (void)hipFuncSetAttribute((const void*)k2_block, hipFuncAttributeMaxDynamicSharedMemorySize, MEGA_LDS);
  char* ws = (char*)d_ws;
  k2_block<<<256, 768, MEGA_LDS, stream>>>(x, wqkv, wo, w1, w3, w2, c, wmod, (u16*)d_out, ws);
}
```

```cpp
#include <hip/hip_runtime.h>
#include <stdint.h>
#include <type_traits>
#pragma clang diagnostic ignored "-Winline-asm"

typedef unsigned short u16;
typedef short bf16x8 __attribute__((ext_vector_type(8)));
typedef short bf16x4 __attribute__((ext_vector_type(4)));
typedef float f32x4 __attribute__((ext_vector_type(4)));
typedef float f32x2 __attribute__((ext_vector_type(2)));
typedef unsigned int u32x4 __attribute__((ext_vector_type(4)));
typedef unsigned int u32x2 __attribute__((ext_vector_type(2)));
#define LDS_PTR(T) __attribute__((address_space(3))) T*
#define BAR() do { asm volatile("" ::: "memory"); __builtin_amdgcn_s_barrier(); asm volatile("" ::: "memory"); } while (0)
#define SCHED0() __builtin_amdgcn_sched_barrier(0)
template <int N> using IC = std::integral_constant<int, N>;
typedef std::integral_constant<bool, true> T_;
typedef std::integral_constant<bool, false> F__;

constexpr int S_ = 2048, D_ = 1024, F_ = 3584;
#define STAMP_AT(i)
#define STAMP_G(ia, ib)
#define STAMP_L(i)

__device__ __forceinline__ unsigned pk2(float lo, float hi) {
  typedef __bf16 bf2 __attribute__((ext_vector_type(2)));
  f32x2 v = {lo, hi}; bf2 r = __builtin_convertvector(v, bf2); return __builtin_bit_cast(unsigned, r);
}
__device__ __forceinline__ float lo2f(unsigned u) { return __uint_as_float(u << 16); }
__device__ __forceinline__ float hi2f(unsigned u) { return __uint_as_float(u & 0xffff0000u); }
__device__ __forceinline__ float rbf(float v) { return lo2f(pk2(v, 0.f)); }
#define glds16(sbase, voff, lds_off, imm) \
  asm volatile("s_add_u32 m0, %2, %3\n\ts_nop 0\n\tglobal_load_lds_dwordx4 %0, %1" ::"v"(voff), "s"(sbase), "s"(lds_off), "n"(imm) : "memory", "scc")
#define glds16s(sbase, voff, lds_off, imm) \
  asm volatile("s_add_u32 m0, %2, %3\n\ts_nop 0\n\tglobal_load_lds_dwordx4 %0, %1 sc1" ::"v"(voff), "s"(sbase), "s"(lds_off), "n"(imm) : "memory", "scc")
#define GLDS4(SC) "s_mov_b32 m0, %6\n\ts_nop 0\n\tglobal_load_lds_dwordx4 %0, %4" SC "\n\tglobal_load_lds_dwordx4 %1, %5 offset:1024" SC \
                  "\n\tglobal_load_lds_dwordx4 %2, %4 offset:2048" SC "\n\tglobal_load_lds_dwordx4 %3, %5 offset:3072" SC
template <bool SC1> __device__ __forceinline__ void glds16x4(const void* s0, const void* s1, const unsigned (&v)[4], unsigned lds) {
  if (SC1) asm volatile(GLDS4(" sc1") ::"v"(v[0]), "v"(v[1]), "v"(v[2]), "v"(v[3]), "s"(s0), "s"(s1), "s"(lds) : "memory");
  else asm volatile(GLDS4("") ::"v"(v[0]), "v"(v[1]), "v"(v[2]), "v"(v[3]), "s"(s0), "s"(s1), "s"(lds) : "memory");
}
__device__ __forceinline__ void glds4(const void* sbase, unsigned voff, unsigned lds_off) {
  asm volatile("s_mov_b32 m0, %2\n\ts_nop 0\n\tglobal_load_lds_dword %0, %1" ::"v"(voff), "s"(sbase), "s"(lds_off) : "memory");
}
__device__ __forceinline__ void stouch4(const void* p) {
  asm volatile("s_load_dwordx2 s[100:101], %0, 0x3c\n\ts_load_dwordx2 s[100:101], %0, 0xbc\n\ts_load_dwordx2 s[100:101], %0, 0x13c\n\ts_load_dwordx2 s[100:101], %0, 0x1bc"
               ::"s"(p) : "memory", "s100", "s101");
}
template <bool SC1> __device__ __forceinline__ void st16(void* sbase, unsigned voff, u32x4 v) {
  __builtin_amdgcn_raw_buffer_store_b128(v, __builtin_amdgcn_make_buffer_rsrc(sbase, 0, 0x7fffffff, 0x00020000), voff, 0, SC1 ? 16 : 0);
}
template <bool SC1> __device__ __forceinline__ u32x4 ld16(const void* sbase, unsigned voff) {
  return __builtin_amdgcn_raw_buffer_load_b128(__builtin_amdgcn_make_buffer_rsrc((void*)sbase, 0, 0x7fffffff, 0x00020000), voff, 0, SC1 ? 16 : 0);
}
template <int N> __device__ __forceinline__ void wait_vm() { asm volatile("s_waitcnt vmcnt(%0)" ::"n"(N) : "memory"); }
__device__ __forceinline__ void wait_lgkm0() { asm volatile("" ::: "memory"); __builtin_amdgcn_s_waitcnt(0xC07F); asm volatile("" ::: "memory"); }
__device__ __forceinline__ bf16x4 ld_tr4(const LDS_PTR(char) p) { return __builtin_amdgcn_ds_read_tr16_b64_v4i16((LDS_PTR(bf16x4))p); }
__device__ __forceinline__ bf16x8 cat8(bf16x4 lo, bf16x4 hi) { return __builtin_shufflevector(lo, hi, 0, 1, 2, 3, 4, 5, 6, 7); }
__device__ __forceinline__ void mfma_acc(f32x4& acc, bf16x8 a, bf16x8 b) { asm volatile("v_mfma_f32_16x16x32_bf16 %0, %1, %2, %0" : "+v"(acc) : "v"(a), "v"(b)); }
template <int STEP> __device__ __forceinline__ void mfma_sel(f32x4& acc, bf16x8 a, bf16x8 b, unsigned& mask, int& dl, int wrap) {
  asm volatile("s_add_u32 %1, %1, %1\n\tv_mfma_f32_16x16x32_bf16 %0, %3, %4, %0\n\ts_cselect_b32 %2, %5, %6" : "+v"(acc), "+s"(mask), "=s"(dl) : "v"(a), "v"(b), "s"(wrap), "n"(STEP) : "scc");
}
constexpr unsigned wrap_mask(int first, int ns) { unsigned m = 0; for (int t = 0; t < 32; ++t) if ((first + t) % ns == ns - 1) m |= 0x80000000u >> t; return m; }
template <int N> __device__ __forceinline__ void wait_lgkm() { __builtin_amdgcn_s_waitcnt(0xC07F | (N << 8)); }
template <int R, int C> __device__ __forceinline__ void mfma_done(f32x4 (&acc)[R][C]) {
  asm volatile("s_nop 9");
#pragma unroll
  for (int i = 0; i < R; ++i)
#pragma unroll
    for (int j = 0; j < C; ++j) asm volatile("" : "+v"(acc[i][j]));
}
__device__ __forceinline__ int lane_id() { int l; asm volatile("v_mbcnt_lo_u32_b32 %0, -1, 0\n\tv_mbcnt_hi_u32_b32 %0, -1, %0" : "=v"(l)); return l; }
__device__ __forceinline__ float xsum16_32(float v) {
  auto a = __builtin_amdgcn_permlane16_swap(__float_as_uint(v), __float_as_uint(v), false, false); v = __uint_as_float(a[0]) + __uint_as_float(a[1]);
  auto c = __builtin_amdgcn_permlane32_swap(__float_as_uint(v), __float_as_uint(v), false, false); return __uint_as_float(c[0]) + __uint_as_float(c[1]);
}

__device__ __forceinline__ float ln_rstd(float var) { return __builtin_amdgcn_rcpf(rbf(__builtin_amdgcn_sqrtf(rbf(rbf(var) + 1e-5f)))); }

constexpr size_t WS_MOD = 0;
constexpr size_t WS_EPO = 64 << 10, WS_FL1 = 96 << 10;
constexpr size_t WS_CTL = 128 << 10;
constexpr int C2 = 0, C3 = 16, C4 = 32, C5 = 48, CM = 80;
constexpr size_t WS_FLG = WS_CTL + (88 << 10), CTL_BYTES = 120 << 10;
constexpr size_t WS_XCH = 512 << 10;
constexpr size_t WS_MOD2 = 1 << 20;
constexpr size_t WS_H = 2 << 20, WS_O = WS_H + (4 << 20), WS_H2 = WS_O + (4 << 20), WS_HH = WS_H2 + (4 << 20),
                 WS_PART = WS_HH + (size_t)S_ * F_ * 2;

template <int NL> __device__ __forceinline__ f32x2 slab_sum(const unsigned (&mc)[NL / 2], const u32x4 (&mw)[NL]) {
  f32x2 a2[4] = {};
#pragma unroll
  for (int i = 0; i < NL; ++i) {
    const float cv = (i & 1) ? hi2f(mc[i >> 1]) : lo2f(mc[i >> 1]);
    const float sv = rbf(cv * __builtin_amdgcn_rcpf(1.f + __builtin_amdgcn_exp2f(cv * -1.4426950408889634f)));
#pragma unroll
    for (int j = 0; j < 4; ++j) a2[j] += (f32x2){lo2f(mw[i][j]), hi2f(mw[i][j])} * sv;
  }
  float acc[8] = {a2[0][0], a2[0][1], a2[1][0], a2[1][1], a2[2][0], a2[2][1], a2[3][0], a2[3][1]};
#pragma unroll
  for (int j = 0; j < 4; ++j) { auto a = __builtin_amdgcn_permlane32_swap(__float_as_uint(acc[j]), __float_as_uint(acc[j + 4]), false, false); acc[j] = __uint_as_float(a[0]) + __uint_as_float(a[1]); }
#pragma unroll
  for (int j = 0; j < 2; ++j) { auto a = __builtin_amdgcn_permlane16_swap(__float_as_uint(acc[j]), __float_as_uint(acc[j + 2]), false, false); acc[j] = __uint_as_float(a[0]) + __uint_as_float(a[1]); }
#pragma unroll
  for (int j = 0; j < 2; ++j) acc[j] += __uint_as_float(__builtin_amdgcn_mov_dpp(__float_as_uint(acc[j]), 0x128, 0xf, 0xf, false));
  return (f32x2){acc[0], acc[1]};
}

constexpr int K3_B = 18432, K3_STG = 18432 + 24576;
constexpr int K4_RED = 57344;
constexpr int K5_G = 16384, K5_U = 16384 + 7168 + 16, K5_STG = 30736, K5_NS = 5;
constexpr int K6_NW = 12, K6_NA = 7, K6_A = K6_NW * 8192, K6_X = 9 * 8192;
constexpr int LDS_VEC = 158720, LDS_DUMMY = LDS_VEC + 1024, LDS_ARR = LDS_DUMMY + 256, MEGA_LDS = LDS_ARR + 64;
constexpr unsigned SPIN_CAP = 1u << 19;
#define KNOBS CP = 10, CQ = 0, KB = 8, XD0 = 16, PFD = 16, FD1 = 8, FS0 = 4, PQ = 2, PQ5 = 4, PD2 = 4, PD3 = 4, PD4 = 4, PD5 = 48, PD6 = 6, PS6 = 1, XS0 = 14, XS1 = 4, TS5 = 2, VB3 = 2, SLB = 15, NDR = 1
enum { KNOBS };
constexpr unsigned long long SKIP6 = 0x7c;
static_assert(__builtin_popcountll(SKIP6) == 5, "44 stages of W are issued behind 49 barriers");

__device__ __forceinline__ void signal(unsigned* cnt, int c, int lane, unsigned v = 1) {
  if (lane < 8) __hip_atomic_fetch_add(cnt + (c * 8 + lane) * 32, v, __ATOMIC_RELAXED, __HIP_MEMORY_SCOPE_AGENT);
}
template <int D0, int D1 = 1> __device__ __forceinline__ void poll(const unsigned* cnt, unsigned want) {
  if (D0) __builtin_amdgcn_s_sleep(D0);
#pragma unroll 1
  for (unsigned spins = 0; spins < SPIN_CAP; ++spins) {
    if (__builtin_amdgcn_ballot_w64(__hip_atomic_load(cnt, __ATOMIC_RELAXED, __HIP_MEMORY_SCOPE_AGENT) < want) == 0) break;
    __builtin_amdgcn_s_sleep(D1);
  }
  __builtin_amdgcn_fence(__ATOMIC_ACQUIRE, "wavefront");
}

template <int D0, int D1, int EX> __device__ __forceinline__ void pollp(const unsigned* cnt, unsigned want, unsigned (&r)[4]) {
  unsigned n = SPIN_CAP;
#define PL1(a) "global_atomic_add %" #a ", %5, %9, off sc0\n\ts_sleep %8\n\t"
#define PL2(a, c) "global_atomic_add %" #a ", %5, %9, off sc0\n\ts_waitcnt vmcnt(3)\n\tv_cmp_gt_u32 vcc, %6, %" #c "\n\ts_cbranch_vccz 2f\n\ts_sleep %8\n\t"
  asm volatile("s_mov_b64 exec, %10\n\ts_sleep %7\n\t" PL1(0) PL1(1) PL1(2) "1:\n\t" PL2(3, 0) PL2(0, 1) PL2(1, 2) PL2(2, 3) "s_sub_u32 %4, %4, 1\n\ts_cbranch_scc0 1b\n2:\n\ts_mov_b64 exec, -1"
               : "=&v"(r[0]), "=&v"(r[1]), "=&v"(r[2]), "=&v"(r[3]), "+s"(n) : "v"(cnt), "s"(want), "n"(D0), "n"(D1), "v"(0), "n"(EX) : "memory", "vcc", "scc");
}
#define KEEP4(r) asm volatile("; keep %0 %1 %2 %3" ::"v"(r[0]), "v"(r[1]), "v"(r[2]), "v"(r[3]))
typedef float f32x32 __attribute__((ext_vector_type(32)));
typedef float f32x16 __attribute__((ext_vector_type(16)));
#define M5(i, j) "v_mfma_f32_16x16x32_bf16 v[28*" #i "+4*" #j ":28*" #i "+4*" #j "+3], v[128+4*" #j ":131+4*" #j "], v[112+4*" #i ":115+4*" #i "], v[28*" #i "+4*" #j ":28*" #i "+4*" #j "+3]\n\t"
#define RA5(i) "ds_read_b128 v[112+4*" #i ":115+4*" #i "], %[aA] offset:1024*" #i "\n\t"
#define RW5(j, h, a, o) "ds_read_b64_tr_b16 v[128+4*" #j "+2*" #h ":129+4*" #j "+2*" #h "], %[" #a "] offset:" #o "\n\t"
#define ST5(a) "v_add_u32 %[" #a "], %[dl], %[" #a "]\n\t"
#define SEL5A "s_add_u32 %[mask], %[mask], %[mask]\n\t"
#define SEL5B "s_cselect_b32 %[dl], %[wrap], 30736\n\t"
#define LG(n) "s_waitcnt lgkmcnt(" #n ")\n\t"
#define RP5(j, h) RW5(j, h, aP, 32*(j/2)+7184*(j&1)+3584*h)
#define TRIP5(X)                                                                                                                                        \
  M5(0, 0) RW5(6, 1, aM, 3584) M5(1, 0) RA5(2) ST5(aM) M5(1, 1) RA5(3) ST5(aP) M5(0, 1) ST5(aA) LG(6) M5(0, 2) M5(1, 2) SEL5A M5(1, 3) SEL5B              \
  M5(0, 3) M5(0, 4) M5(1, 4) LG(0) M5(1, 5) M5(0, 5) "s_barrier\n\t" M5(0, 6) RA5(0) M5(1, 6) RA5(1)                                                      \
  M5(2, 0) X M5(3, 0) RP5(0, 0) M5(3, 1) RP5(0, 1) M5(2, 1) RP5(1, 0) M5(2, 2) RP5(1, 1) M5(3, 2) RP5(2, 0) M5(3, 3) RP5(2, 1) M5(2, 3) RP5(3, 0)          \
  M5(2, 4) RP5(3, 1) M5(3, 4) RP5(4, 0) M5(3, 5) RP5(4, 1) M5(2, 5) RP5(5, 0) M5(2, 6) RP5(5, 1) LG(8) M5(3, 6) RW5(6, 0, aM, 0)
#define ACC5 "+{v[0:31]}"(c0), "+{v[32:63]}"(c1), "+{v[64:95]}"(c2), "+{v[96:111]}"(c3)
#define CLOB5 "memory", "scc", "v112", "v113", "v114", "v115", "v116", "v117", "v118", "v119", "v120", "v121", "v122", "v123", "v124", "v125", "v126", "v127", "v128", "v129", "v130", "v131", "v132", "v133", \
              "v134", "v135", "v136", "v137", "v138", "v139", "v140", "v141", "v142", "v143", "v144", "v145", "v146", "v147", "v148", "v149", "v150", "v151", "v152", "v153", "v154", "v155"
static_assert(K5_STG == 30736 && K5_U - K5_G == 7184, "TRIP5");

#define C3(i, j) "v[24*" #i "+4*" #j "-12*(" #i "/4):24*" #i "+4*" #j "-12*(" #i "/4)+3]"
#define M3(i, j) "v_mfma_f32_16x16x32_bf16 " C3(i, j) ", v[124+4*" #j ":127+4*" #j "], v[104+4*" #i ":107+4*" #i "], " C3(i, j) "\n\t"
#define RA3(i, u) "ds_read_b128 v[104+4*" #i ":107+4*" #i "], %[aA] offset:1024*" #i "+9216*" #u "\n\t"
#define RH3(u) "ds_read_b128 v[120:123], %[aH] offset:9216*" #u "\n\t"
#define RW3(j, h, a, u) "ds_read_b64_tr_b16 v[124+4*" #j "+2*" #h ":125+4*" #j "+2*" #h "], %[" #a "] offset:8192*(" #j "/2)+2048*" #h "+4096*" #u "\n\t"
#define ST3(a, d) "v_add_u32 %[" #a "], " #d ", %[" #a "]\n\t"
#define H2_3(u, S0, S1, S2, B) M3(0, 0) RW3(5, 1, a1, u) M3(1, 0) RA3(2, u) M3(1, 1) RA3(3, u) S0 M3(0, 1) LG(7) S1 M3(0, 2) S2 M3(1, 2) M3(1, 3) M3(0, 3) LG(2) M3(0, 4) M3(1, 4) LG(0) B
#define H1_3(n, X)                                                                                                                                      \
  M3(1, 5) RA3(1, n) M3(0, 5) RA3(0, n) M3(2, 0) X M3(3, 0) RW3(0, 0, a0, n) M3(3, 1) RW3(0, 1, a0, n) M3(2, 1) RW3(1, 0, a1, n) M3(2, 2) RW3(1, 1, a1, n) \
  M3(3, 2) RW3(2, 0, a1, n) M3(3, 3) RW3(2, 1, a1, n) M3(2, 3) M3(4, 3) RW3(3, 0, a0, n) M3(4, 4) RW3(3, 1, a0, n) M3(2, 4) RH3(n)                         \
  M3(3, 4) RW3(4, 0, a0, n) M3(3, 5) RW3(4, 1, a0, n) LG(7) M3(2, 5) RW3(5, 0, a1, n)
#define STAGE3(d, X) H2_3(0, , , , ) H1_3(1, ) H2_3(1, ST3(a0, d), ST3(aA, d), ST3(a1, d) ST3(aH, d), "s_barrier\n\t") H1_3(0, X)
#define CV3(i, j, t) "v_cvt_pk_bf16_f32 v[104+2*" #t "], v[24*" #i "+4*" #j "-12*(" #i "/4)], v[24*" #i "+4*" #j "-12*(" #i "/4)+1]\n\tv_cvt_pk_bf16_f32 v[105+2*" #t "], v[24*" #i "+4*" #j "-12*(" #i "/4)+2], v[24*" #i "+4*" #j "-12*(" #i "/4)+3]\n\t"
#define WR3(i, t, a, o) "ds_write_b64 %[" #a "], v[104+2*" #t ":105+2*" #t "] offset:2048*" #i "+" #o "\n\t"
#define CW3(i, j, t, a, o) CV3(i, j, t) WR3(i, t, a, o)
#define V8(a) "v" #a "0", "v" #a "1", "v" #a "2", "v" #a "3", "v" #a "4", "v" #a "5", "v" #a "6", "v" #a "7"
#define V10(a) V8(a), "v" #a "8", "v" #a "9"
static_assert(K3_STG == 43008, "STAGE3");
#define M6(i, j, s) "v_mfma_f32_16x16x32_bf16 v[16*" #i "+4*(" #j "):16*" #i "+4*(" #j ")+3], v[80+32*" #s "+4*(" #j "):83+32*" #s "+4*(" #j ")], v[64+32*" #s "+4*" #i ":67+32*" #s "+4*" #i "], v[16*" #i "+4*(" #j "):16*" #i "+4*(" #j ")+3]\n\t"
#define RA6(i, s) "ds_read_b128 v[64+32*" #s "+4*" #i ":67+32*" #s "+4*" #i "], %[aa] offset:1024*" #i "\n\t"
#define RW6(h, b, l, s) "ds_read_b64_tr_b16 v[80+32*" #s "+8*(" #h ")+4*" #b "+2*" #l ":81+32*" #s "+8*(" #h ")+4*" #b "+2*" #l "], %[a" #b "] offset:4096*(" #h ")+2048*" #l "\n\t"
#define SEL6(m, d, w) "s_cselect_b32 %[" #d "], %[" #w "], 8192\n\t"
#define SH6(m) "s_add_u32 %[" #m "], %[" #m "], %[" #m "]\n\t"
#define TRIP6(h, o, n, x, X)                                                                                                                            \
  M6(0, 2*h, o) RW6(1-h, 0, 0, x) M6(0, 2*h, n) RW6(1-h, 0, 1, x) SH6(mw) M6(0, 2*h+1, n) SEL6(mw, dw, ww) RA6(0, x) M6(0, 2*h+1, o) RW6(1-h, 1, 0, x) LG(8)  \
  M6(1, 2*h+1, o) RW6(1-h, 1, 1, x) M6(1, 2*h+1, n) RA6(1, x) SH6(ma) M6(1, 2*h, n) SEL6(ma, da, wa) RA6(2, x) M6(1, 2*h, o) RA6(3, x)                      \
  M6(2, 2*h, o) RW6(h, 0, 0, x) "v_add_u32 %[aa], %[da], %[aa]\n\t" M6(2, 2*h, n) RW6(h, 0, 1, x) M6(2, 2*h+1, n) RW6(h, 1, 0, x) "v_add_u32 %[a0], %[dw], %[a0]\n\t" \
  M6(2, 2*h+1, o) RW6(h, 1, 1, x) M6(3, 2*h+1, o) "v_add_u32 %[a1], %[dw], %[a1]\n\t" X M6(3, 2*h+1, n) M6(3, 2*h, n) LG(6) M6(3, 2*h, o) "s_barrier\n\t"
__device__ __forceinline__ int w2_stage(int j) {
  const int j2 = j - 16, p = (j2 * 52) >> 8, r = j2 - p * 5;
  return j < 16 ? j * 112 * 2048 : (p * 224 + 32 + r * 32 + (r > 2 ? 32 : 0)) * 2048 + (r == 2);
}
#define CODE_SYM "_Z8k2_blockPKtS0_S0_S0_S0_S0_S0_S0_PtPc"
__global__ __launch_bounds__(768, 3) __attribute__((amdgpu_num_sgpr(98))) void k2_block(const u16* __restrict__ x, const u16* __restrict__ wqkv, const u16* __restrict__ wo,
                                                   const u16* __restrict__ w1, const u16* __restrict__ w3, const u16* __restrict__ w2,
                                                   const u16* __restrict__ cnd, const u16* __restrict__ wmod, u16* __restrict__ out, char* __restrict__ ws) {
  extern __shared__ __attribute__((aligned(16))) char smem_[];
  LDS_PTR(char) smem = (LDS_PTR(char))smem_;
  const int tid = threadIdx.x;
  int lane = tid & 63;
  const int wave = __builtin_amdgcn_readfirstlane(tid >> 6);
  const unsigned lds_base = (unsigned)(uintptr_t)smem;
  const int b = blockIdx.x, xc = b & 7, ii = b >> 3;
  const int tm3 = (xc & 1) * 8 + (ii & 7), tn3 = (xc >> 1) * 4 + (ii >> 3);
  const int tm5 = (xc & 1) * 4 + (ii & 3), tn5 = (xc >> 1) * 8 + (ii >> 2);
  const int kh6 = xc >> 2, tm6 = (xc & 1) * 8 + (xc & 2) * 2 + (ii & 3), tn6 = ii >> 2;
  const int tm4 = tm6, tn4 = tn6 * 2 + kh6;
  const int u1 = (b >> 4) * 8 + xc;
  unsigned* cnt = (unsigned*)(ws + WS_CTL);
  unsigned long long* fl1 = (unsigned long long*)(ws + WS_FL1);
  STAMP_AT(0 );
  if ((CP & 8) && wave == 11 && (CQ || !(b & 8))) {
    const char* cb; unsigned len;
    asm volatile("s_getpc_b64 %0\n1:\n\ts_mov_b32 %1, .Lk2_last-1b" : "=s"(cb), "=s"(len));
    const unsigned o = (CQ ? ii * 16 + (lane & 15) : (ii >> 1) * 32 + (lane & 31)) * 128;
    glds4(cb, o < len ? o : 0, lds_base + LDS_DUMMY);
  }

  if (wave >= 8) {
    const int lw = wave - 8, P = b * 4 + lw, lane = lane_id();
    const bool isA = lw < 2;
    asm volatile("" ::"s"(wqkv), "s"(wo), "s"(w1), "s"(w3), "s"(w2), "s"(ws));
    int kpf = KB + VB3;
    unsigned pr[4] = {};
    auto touch = [&]() {
      if (kpf < 28 && __builtin_amdgcn_s_getreg((3 << 11) | (8 << 6) | 7) <= 7) {
        stouch4((const char*)((P & 1) ? w3 : w1) + (kpf * 2048 + (P >> 1) * 4) * 128);
        ++kpf;
      }
    };
    auto code = [&]() {
      const char* cb; unsigned len;
      asm volatile("s_getpc_b64 %0\n1:\n\ts_mov_b32 %1, 1b-" CODE_SYM : "=s"(cb), "=s"(len));
      cb -= len;
      asm volatile("s_mov_b32 %0, .Lk2_last-" CODE_SYM : "=s"(len));
      const unsigned o = ((ii * 4 + lw) * 4 + (lane & 3)) * 128;
      glds4(cb, o < len ? o : 0, lds_base + LDS_DUMMY);
    };
    const char* epo = ws + WS_EPO;
    const unsigned swz = ((lane & 7) ^ ((lane >> 3) & 6)) * 16;
    const int cg = lw * 16 + tn4;
    auto issue4 = [&](int s, unsigned slot) {
      const char* p = (const char*)wo + tn4 * 128 + (lw * 32 + s * 128) * 2048;
      unsigned v4[4];
#pragma unroll
      for (int i = 0; i < 4; ++i) v4[i] = (lane >> 3) * 2048 + swz + i * 15360;
      glds16x4<false>(p, p, v4, lds_base + slot + lw * 4096);
    };
    auto run3 = [&](auto isA_) {
      constexpr bool A_ = decltype(isA_)::value; constexpr int N = A_ ? 9 : 12;
      const char* sp = A_ ? ws + WS_H + lw * 131072 + (tm3 * 8 - 1) * 1024 : (const char*)wqkv + tn3 * 128;
      unsigned vo[3][4];
#pragma unroll
      for (int i = 0; i < 12; ++i) {
        const int tb = (lw - 2) * 12 + i;
        vo[i >> 2][i & 3] = A_ ? lane * 16 + (i < 8 ? 1024 + (i >> 2) * 4096 : tm3 ? 0 : 1024) : ((tb & 7) * 8 + (lane >> 3)) * 6144 + (tb >> 3) * 2048 - (i & 3) * 1024 + swz;
      }
      const unsigned d0 = lds_base + (A_ ? lw * 9216 : K3_B + (lw - 2) * 12288);
      auto issue = [&](unsigned st) {
        if (A_) { glds16x4<true>(sp, sp, vo[0], d0 + st + 1024); glds16s(sp, vo[2][0], d0 + st, 0); glds16x4<true>(sp, sp, vo[1], d0 + st + 5120); }
        else {
#pragma unroll
          for (int i = 0; i < 3; ++i) glds16x4<false>(sp, sp, vo[i], d0 + st + i * 4096);
        }
        sp += A_ ? 262144 : 64 * 6144;
      };
      { unsigned long long ep;
        asm volatile("s_load_dwordx2 %0, %1, 0x0\n\ts_waitcnt lgkmcnt(0)" : "=s"(ep) : "s"(epo) : "memory");
        ep += 1;
        if (lw == 3) { *(LDS_PTR(u32x4))(smem + LDS_ARR) = (u32x4){0u, 0u, 0u, 0u}; wait_lgkm0(); }
        if (lw == 1) {
#pragma unroll
          for (int k = 0; k < 3; ++k) {
            const int i = b * 158 + (k * 64 + lane < 158 ? k * 64 + lane : 0);
            st16<true>(ws, i < 32768 ? (unsigned)WS_XCH + i * 16 : (unsigned)WS_CTL + (i - 32768) * 16, i < 32768 ? (u32x4){~0u, ~0u, ~0u, ~0u} : (u32x4){0u, 0u, 0u, 0u});
          }
        }
        if (CP & 1) code();
        BAR();
        auto flag = [&](int idx) { wait_vm<0>(); if (lane < 8) __hip_atomic_store(fl1 + lane * 256 + idx, ep, __ATOMIC_RELAXED, __HIP_MEMORY_SCOPE_AGENT); };
        if (lw == 1 && (b & 8)) {
          const int kq = u1 & 3, cgp = u1 >> 2;
          float sm = 0.f;
#pragma unroll
          for (int r = 0; r < 8; ++r) sm += *(const LDS_PTR(float))(smem + 8192 + (r * 64 + lane) * 4);
          __hip_atomic_store((unsigned*)(ws + WS_MOD) + kq * 2048 + cgp * 64 + lane, __float_as_uint(sm), __ATOMIC_RELAXED, __HIP_MEMORY_SCOPE_AGENT);
          flag(((cgp >> 3) & 1) * 64 + (cgp >> 4) * 32 + (cgp & 7) * 4 + kq);
        }
        if (b & 8) BAR();
        BAR();
        if (lw == 1 && !(b & 8)) flag(128 + u1);
        auto flags = [&](auto n_, int first) {
#pragma unroll 1
          for (unsigned spins = 0; spins < SPIN_CAP; ++spins) {
            bool ok = true;
#pragma unroll
            for (int k = 0; k < decltype(n_)::value; ++k) ok &= __hip_atomic_load(fl1 + xc * 256 + (first + k) * 64 + lane, __ATOMIC_RELAXED, __HIP_MEMORY_SCOPE_AGENT) == ep;
            if (__builtin_amdgcn_ballot_w64(!ok) == 0) break;
            __builtin_amdgcn_s_sleep(FS0);
          }
        };
        if (lw == 0) flags(IC<1>(), (b >> 3) & 1);
        BAR();
        if (lw == 1) {
          __builtin_amdgcn_s_sleep(FD1);
          flags(IC<4>(), 0);
          if (b == 0 && lane == 0) __hip_atomic_store((unsigned long long*)(ws + WS_EPO), ep, __ATOMIC_RELAXED, __HIP_MEMORY_SCOPE_AGENT);
        }
      }
      BAR();
      if (lw == 0) {
        signal(cnt, C2 + (u1 >> 3), lane, (b & 8) ? 1u : 0x10000u);
        pollp<PD2, PQ, 3>(cnt + ((C2 + ((lane & 1) && tm3 ? tm3 - 1 : tm3)) * 8 + (tn3 & 7)) * 32, 0x80000u, pr);
      }
      if (!A_) { issue(0); issue(K3_STG); }
      BAR();
      if (A_) { issue(0); issue(K3_STG); }
      wait_vm<N>();
      KEEP4(pr);
      BAR();
      issue(2 * K3_STG);
      unsigned st = 0;
#pragma unroll 1
      for (int j = 0; j < 13; ++j) {
        wait_vm<N>();
        BAR();
        issue(st);
        st = st == 2 * K3_STG ? 0 : st + K3_STG;
      }
      wait_vm<N>();
      BAR();
      issue4(0, 57344); issue4(5, 57344 + 5 * 16384);
      glds4((P & 1) ? w3 : w1, ((KB + ((lane >> 2) < VB3 ? lane >> 2 : 0)) * 2048 + (P >> 1) * 4 + (lane & 3)) * 128, lds_base + LDS_DUMMY);
      if (CP & 2) code();
      wait_vm<(CP & 2) ? 10 : 9>();
    };
    if (isA) run3(T_()); else run3(F__());
    { BAR();
      BAR();
      issue4(1, 57344 + 16384);
      BAR();
      issue4(2, 57344 + 2 * 16384); issue4(3, 57344 + 3 * 16384); issue4(4, 57344 + 4 * 16384);
      BAR();
      if (lw == 0) signal(cnt, C3 + tm3, lane);
      issue4(6, 0); issue4(7, 16384);
      if (lw == 0) pollp<PD3, PQ, 1>(cnt + ((C3 + tm4) * 8 + tn6) * 32, 16, pr);
      wait_vm<8>();
      BAR();
      wait_vm<0>();
      KEEP4(pr);
      BAR();
      poll<0, 4>(cnt + (CM * 8 + cg) * 32, 4);
      f32x4 pv[4];
#pragma unroll
      for (int i = 0; i < 4; ++i) pv[i] = __builtin_bit_cast(f32x4, ld16<true>(ws + WS_MOD2, (((lane >> 4) * 4 + i) * 4096 + cg * 64 + (lane & 15) * 4) * 4));
      f32x4 v = (pv[0] + pv[1]) + (pv[2] + pv[3]);
#pragma unroll
      for (int e = 0; e < 4; ++e) { v[e] = rbf(xsum16_32(v[e])); if (lw == 2) v[e] = rbf(1.f + v[e]); if (lw == 1 || lw == 2) v[e] *= -1.4426950408889634f; }
      *(LDS_PTR(f32x4))(smem + LDS_VEC + lw * 256 + (lane & 15) * 16) = v;
      wait_lgkm0();
    }
    auto run5 = [&](auto isA_) {
      constexpr bool A_ = decltype(isA_)::value; constexpr int N = A_ ? 8 : 7;
      const char* sp = A_ ? ws + WS_H2 + (tm5 * 16 + lw * 8) * 1024 : (const char*)(lw == 2 ? w1 : w3) + tn5 * 224;
      unsigned vp[N];
#pragma unroll
      for (int i = 0; i < N; ++i) { const int L = i * 64 + lane, r = L / 14, c = L - r * 14; vp[i] = A_ ? L * 16 : r * (F_ * 2) + c * 16; }
      const unsigned d0 = lds_base + (A_ ? lw * 8192 : lw == 2 ? K5_G : K5_U);
      unsigned st = 3 * K5_STG;
      auto issue = [&]() {
#pragma unroll
        for (int i = 0; i < N; ++i) { if (A_) glds16s(sp, vp[i], d0 + st, i * 1024); else glds16(sp, vp[i], d0 + st, i * 1024); }
        sp += A_ ? 131072 : 32 * F_ * 2; st = st == (K5_NS - 1) * K5_STG ? 0 : st + K5_STG;
      };
      BAR();
      if (!A_) { issue(); issue(); }
      BAR();
      u32x4 xk[4];
      auto keep = [&]() {
#pragma unroll
        for (int i = 0; i < 4; ++i) { xk[i] = *(const LDS_PTR(u32x4))(smem + lw * 4096 + i * 1024 + lane * 16); asm volatile("" : "+v"(xk[i])); }
      };
      if (A_ && lw == 0) { signal(cnt, C4 + tm4, lane); keep(); pollp<PD4, PQ, 3>(cnt + ((C4 + tm5 * 2 + (lane & 1)) * 8 + (tn5 & 7)) * 32, 16, pr); }
      else { keep(); if (!A_) { issue(); issue(); issue(); } }
      BAR();
      if (A_) { issue(); issue(); }
      wait_vm<A_ ? N : 4 * N>();
      KEEP4(pr);
      BAR();
      if (A_) { issue(); issue(); issue(); }
#pragma unroll 1
      for (int j = 0; j < 27; ++j) {
        wait_vm<3 * N>();
        BAR();
        issue();
        if (j == 6) {
#pragma unroll
          for (int i = 0; i < 4; ++i) st16<true>(ws + WS_PART, (b * 4 + lw) * 4096 + i * 1024 + lane * 16, (u32x4){~0u, ~0u, ~0u, ~0u});
        }
        if (j == 12 && lw == 0 && lane == 0) __hip_atomic_store((unsigned*)(ws + WS_FLG) + b * 32, 16u, __ATOMIC_RELAXED, __HIP_MEMORY_SCOPE_AGENT);
        touch(); __builtin_amdgcn_s_sleep(TS5); touch();
      }
      const char* sb = A_ ? ws + WS_HH + (kh6 * 56 * 128 + tm6 * 8 + lw * 4) * 1024 : (const char*)w2 + kh6 * 1792 * 2048 + tn6 * 256 + ((lw ^ kh6 ^ 1) & 1) * 128;
      unsigned vo[4];
#pragma unroll
      for (int i = 0; i < 4; ++i) {
        const int r = i * 8 + (lane >> 3), k = 8 * ((r >> 2) & 3) + 4 * (r >> 4) + (r & 3);
        vo[i] = A_ ? lane * 16 : k * 2048 - i * 1024 + swz;
      }
      const unsigned d6 = lds_base + (A_ ? K6_A : 0) + (lw & 1) * 4096;
      st = 0;
      const int tab = w2_stage(lane);
      int jn = 0;
      auto issue6 = [&]() {
        if (A_) { glds16x4<true>(sb, sb, vo, d6 + st); sb += 131072; }
        else { const int o = __builtin_amdgcn_readlane(tab, jn); const char* s0 = sb + (o & ~1); glds16x4<false>(s0, s0 + (o & 1) * 65536, vo, d6 + st); ++jn; }
        st = st == ((A_ ? K6_NA : K6_NW) - 1) * 8192 ? 0 : st + 8192;
      };
      auto pre6 = [&](int n) {
        if (!A_) {
#pragma unroll 1
          for (int i = 0; i < n; ++i) issue6();
        }
      };
      constexpr int E = A_ ? 0 : 4;
      { const char* p = (const char*)w2 + (kh6 * 1792 + ii) * 2048;
        const int n = lw * 56 + (lane < 56 ? lane : 0), o = w2_stage((xc & 3) * 14 + (n >> 4));
        glds4(p, (o & ~1) + (o & (ii >> 4) & 1) * 65536 + (n & 15) * 128, lds_base + LDS_DUMMY); }
      constexpr int X = (CP & 4) ? 2 : 1;
      if (CP & 4) code();
      wait_vm<3 * N + X>(); BAR();
      wait_vm<2 * N + X>(); BAR(); pre6(4);
      wait_vm<N + 4 * E + X>(); BAR(); pre6(4);
      wait_vm<8 * E + X>(); BAR(); pre6(4);
      BAR();
      if (A_) {
        volatile LDS_PTR(unsigned) go = (volatile LDS_PTR(unsigned))(smem + LDS_ARR + 4);
        if (lw == 0) { pollp<PD5, PQ5, 1>(cnt + ((C5 + (tm6 >> 1) * 4 + kh6 * 2) * 8 + tn6) * 32, 16, pr); *go = 1u; }
        else {
#pragma unroll 1
          for (unsigned spins = 0; spins < SPIN_CAP && __builtin_amdgcn_readfirstlane(*go) == 0; ++spins) __builtin_amdgcn_s_sleep(1);
          asm volatile("" ::: "memory");
        }
        STAMP_L(31);
        issue6(); issue6(); issue6();
        wait_vm<8>(); KEEP4(pr); BAR(); issue6(); issue6();
        wait_vm<12>(); BAR(); issue6(); issue6();
      } else { wait_vm<16>(); BAR(); BAR(); }
#pragma unroll 1
      for (int c = 2; c < 51; ++c) { wait_vm<16>(); BAR(); if (A_ || !((SKIP6 >> c) & 1)) issue6(); }
      wait_vm<16>(); BAR(); wait_vm<12>(); BAR(); wait_vm<8>(); BAR(); wait_vm<4>(); BAR(); wait_vm<0>(); BAR();
#pragma unroll
      for (int i = 0; i < 4; ++i) *(LDS_PTR(u32x4))(smem + K6_X + lw * 4096 + i * 1024 + lane * 16) = xk[i];
      BAR(); wait_lgkm0(); BAR();
    };
    if (isA) run5(T_()); else run5(F__());
    asm volatile(".Lk2_last:\n\ts_waitcnt lgkmcnt(0)" ::: "memory");
    return;
  }

  int fr = lane & 15, g = lane >> 4, p4 = fr & 3, row = 4 * g + (fr >> 2);
  int lsw = row * 128 + (((row >> 1) & 3) * 32) + p4 * 8;
  { const int mb = u1, hf = (b >> 3) & 1, kb0 = hf * 16 + wave * 2, cofs = (g & 1) * 16 + (g >> 1) * 8;
    u32x4 v[4];
    auto ldx = [&]() {
#pragma unroll
      for (int p = 0; p < 4; ++p) v[p] = *(const u32x4*)(x + (size_t)(mb * 16 + fr) * D_ + ((kb0 + (p & 1)) ^ ((p >> 1) * 16)) * 32 + cofs);
    };
    LDS_PTR(f32x2) sred = (LDS_PTR(f32x2))(smem + 4096);
    auto up2 = [](unsigned u) { return (f32x2){lo2f(u), hi2f(u)}; };
    auto stats = [&]() {
      f32x2 s2 = {0.f, 0.f}, q2 = {0.f, 0.f};
#pragma unroll
      for (int k = 0; k < 16; ++k) s2 += up2(v[k >> 2][k & 3]);
      const float mw = xsum16_32(s2[0] + s2[1]) * (1.f / 128.f);
#pragma unroll
      for (int k = 0; k < 16; ++k) { const f32x2 d = up2(v[k >> 2][k & 3]) - mw; q2 += d * d; }
      const float qw = xsum16_32(q2[0] + q2[1]);
      if (lane < 16) sred[wave * 16 + fr] = (f32x2){mw, qw};
      wait_lgkm0();
    };
    auto rest = [&]() {
    STAMP_AT(1 );
    float mean = 0.f, m2 = 0.f; f32x2 sw[8];
#pragma unroll
    for (int w = 0; w < 8; ++w) { sw[w] = sred[w * 16 + fr]; mean += sw[w][0]; m2 += sw[w][1]; }
    mean *= 0.125f;
#pragma unroll
    for (int w = 0; w < 8; ++w) { const float d = sw[w][0] - mean; m2 += 128.f * d * d; }
    const float rstd = ln_rstd(m2 * (1.f / 1024.f));
    mean = rbf(mean);
    unsigned nv[8];
#pragma unroll
    for (int k = 0; k < 8; ++k) {
      const f32x2 d = up2(v[k >> 2][k & 3]) - mean;
      const f32x2 n = up2(pk2(d[0], d[1])) * rstd;
      nv[k] = pk2(n[0], n[1]);
      asm volatile("" : "+v"(nv[k]));
    }
    SCHED0(); BAR(); SCHED0();
    STAMP_AT(2 );
    float mp[8];
#pragma unroll
    for (int i = 0; i < 8; ++i) mp[i] = __uint_as_float(__hip_atomic_load((unsigned*)(ws + WS_MOD) + hf * 512 + tid + i * 1024, __ATOMIC_RELAXED, __HIP_MEMORY_SCOPE_AGENT));
    *(LDS_PTR(unsigned))(smem + tid * 4) = pk2((mp[0] + mp[2]) + (mp[4] + mp[6]), 1.f + rbf((mp[1] + mp[3]) + (mp[5] + mp[7])));
    wait_lgkm0();
#pragma unroll
    for (int p = 0; p < 2; ++p) {
      const LDS_PTR(u32x4) tp = (const LDS_PTR(u32x4))(smem + ((wave * 2 + p) * 32 + cofs) * 4);
      const u32x4 t0 = tp[0], t1 = tp[1];
      unsigned o[4];
#pragma unroll
      for (int j = 0; j < 4; ++j) {
        const unsigned ta = j < 2 ? t0[2 * j] : t1[2 * j - 4], tb = j < 2 ? t0[2 * j + 1] : t1[2 * j - 3];
        const unsigned u = pk2(lo2f(nv[p * 4 + j]) * hi2f(ta), hi2f(nv[p * 4 + j]) * hi2f(tb));
        o[j] = pk2(lo2f(u) + lo2f(ta), hi2f(u) + lo2f(tb));
      }
      auto s0 = __builtin_amdgcn_permlane16_swap(o[0], o[2], false, false);
      auto s1 = __builtin_amdgcn_permlane16_swap(o[1], o[3], false, false);
      st16<true>(ws + WS_H, ((kb0 + p) * 128 + mb) * 1024 + lane * 16, (u32x4){s0[0], s1[0], s0[1], s1[1]});
    }
    STAMP_AT(3 );
    wait_vm<0>();
    STAMP_AT(4 );
    BAR();
    STAMP_AT(5 );
    };
    if (hf) {
      const int mrow = (u1 & 3) * 256 + (tid >> 3) * 4;
      const u32x2 c2 = *(const u32x2*)(cnd + mrow);
      u32x4 mw[4];
#pragma unroll
      for (int i = 0; i < 4; ++i) mw[i] = __builtin_nontemporal_load((const u32x4*)(wmod + (size_t)(mrow + i) * 6144 + (u1 >> 2) * 64 + (lane & 7) * 8));
      const unsigned mc[2] = {c2[0], c2[1]};
      *(LDS_PTR(f32x2))(smem + 8192 + (wave * 64 + (lane & 7) * 8 + (lane >> 4) * 2) * 4) = slab_sum<4>(mc, mw);
      wait_lgkm0();
      BAR();
      BAR();
      ldx();
      stats();
      BAR();
      rest();
    } else {
      __builtin_amdgcn_s_sleep(XD0);
      ldx();
      SCHED0();
      const int sl = (3 - ((unsigned)(uintptr_t)wqkv >> 7) - (xc >> 1) * 4) & 7;
      const int i2 = tid - 384, r0 = i2 < 0 ? tid / 12 : i2 / 3, c0 = i2 < 0 ? tid - r0 * 12 : (i2 - r0 * 3) * 4 + sl;
      const bool on = i2 < 0 || (sl < 4 && i2 < 96);
      __builtin_amdgcn_s_sleep(PFD);
      const unsigned pf = *(const unsigned*)((const char*)wqkv + (xc >> 1) * 512 + (on ? ((((xc & 1) ^ (i2 >= 0)) * 512 + (ii >> 1) * 32 + r0) * 48 + (c0 >> 2) * 16 + (c0 & 3)) * 128 : 0));
      SCHED0(); BAR(); SCHED0();
      stats();
      SCHED0(); BAR(); SCHED0();
      rest();
      asm volatile("" ::"v"(pf));
    }
  }
  {
    f32x4 bias[2];
    { const int kbase = tm3 * 128 + wave * 16 - 16;
#pragma unroll
      for (int t = 0; t < 4; ++t) {
        const int kk = (t >> 1) * 16 + 4 * g + (t & 1) * 2 + (fr & 1), df = 16 + fr - kk;
        bias[t >> 1][(t & 1) * 2] = bias[t >> 1][(t & 1) * 2 + 1] = (df >= 0 && df <= 14 && kbase + kk >= 0) ? 0.f : -__builtin_inff();
      }
      asm volatile("" : "+v"(bias[0]), "+v"(bias[1])); }
    u32x4 mc, mw[8];
    if (wave < 4) {
      const int wm = wave & 1, bA = wave;
      unsigned a0 = lds_base + K3_B + (lsw ^ (bA * 32)), a1 = lds_base + K3_B + (lsw ^ ((bA ^ 1) * 32)), aA = lds_base + 1024 + wm * 4096 + lane * 16, aH = lds_base + lane * 16;
      unsigned lq = lds_base + bA * 512 + lane * 8, lv = lds_base + 36864 + bA * 512 + (lane ^ ((g >> 1) * 8)) * 8;
      int n = 4;
      asm volatile(".irp r,0,8,16,24,32,40,48,56,64,72,80,88,96\n\tv_mov_b64 v[\\r:\\r+1], 0\n\tv_mov_b64 v[\\r+2:\\r+3], 0\n\tv_mov_b64 v[\\r+4:\\r+5], 0\n\tv_mov_b64 v[\\r+6:\\r+7], 0\n\t.endr\n\t"
                   "s_setprio 2\n\ts_waitcnt vmcnt(0)\n\ts_barrier\n\t"
                   "s_barrier\n\t"
                   RA3(1, 0) RA3(0, 0) RW3(0, 0, a0, 0) RW3(0, 1, a0, 0) RW3(1, 0, a1, 0) RW3(1, 1, a1, 0) RW3(2, 0, a1, 0) RW3(2, 1, a1, 0) RW3(3, 0, a0, 0) RW3(3, 1, a0, 0) RH3(0)
                   RW3(4, 0, a0, 0) RW3(4, 1, a0, 0) RW3(5, 0, a1, 0) LG(8)
                   ".p2align 5\n1:\n\t" STAGE3(43008, ) STAGE3(43008, ) STAGE3(-86016, "s_sub_u32 %[n], %[n], 1\n\t") "s_cbranch_scc0 1b\n\t"
                   H2_3(0, , , , ) H1_3(1, )
                   H2_3(1, "v_add_u32 %[a0], %[mo], %[lq]\n\t", "v_add_u32 %[aA], %[mo], %[lv]\n\t", "v_xor_b32 %[a1], 512, %[a0]\n\tv_xor_b32 %[aH], 512, %[aA]\n\t", "s_barrier\n\t")
                   M3(1, 5) M3(0, 5) M3(2, 0) CV3(0, 0, 0) M3(3, 0) WR3(0, 0, a0, 0) CV3(1, 0, 1) M3(3, 1) WR3(1, 1, a0, 0) CV3(0, 1, 2) M3(2, 1) WR3(0, 2, a1, 0) CV3(1, 1, 3)
                   M3(2, 2) WR3(1, 3, a1, 0) CV3(0, 2, 0) M3(3, 2) WR3(0, 0, a1, 18432) CV3(1, 2, 1) M3(3, 3) WR3(1, 1, a1, 18432) CV3(0, 3, 2) M3(2, 3) WR3(0, 2, a0, 18432) CV3(1, 3, 3)
                   M3(4, 3) WR3(1, 3, a0, 18432) CV3(0, 4, 0) M3(4, 4) WR3(0, 0, aA, 0) CV3(1, 4, 1) M3(2, 4) WR3(1, 1, aA, 0) CV3(0, 5, 2) M3(3, 4) WR3(0, 2, aH, 0) CV3(1, 5, 3)
                   M3(3, 5) WR3(1, 3, aH, 0) CV3(2, 0, 0) M3(2, 5) WR3(2, 0, a0, 0)
                   CW3(3, 0, 1, a0, 0) CW3(3, 1, 2, a1, 0) CW3(2, 1, 3, a1, 0) CW3(2, 2, 0, a1, 18432) CW3(3, 2, 1, a1, 18432) CW3(3, 3, 2, a0, 18432) CW3(2, 3, 3, a0, 18432)
                   CV3(4, 3, 0) WR3(0, 0, lq, 18432) CV3(4, 4, 1) WR3(0, 1, lv, 0) CW3(2, 4, 2, aA, 0) CW3(3, 4, 3, aA, 0) CW3(3, 5, 0, aH, 0) CW3(2, 5, 1, aH, 0)
                   "s_setprio 0\n\t" LG(0)
                   : [a0] "+v"(a0), [a1] "+v"(a1), [aA] "+v"(aA), [aH] "+v"(aH), [n] "+s"(n)
                   : [lq] "v"(lq), [lv] "v"(lv), [mo] "s"((wm * 4 + 1) * 2048)
                   : "memory", "scc", V10(), V10(1), V10(2), V10(3), V10(4), V10(5), V10(6), V10(7), V10(8), V10(9), V10(10), V10(11), V10(12), V10(13), V8(14));
    } else {
      wait_vm<0>();
      BAR(); BAR();
      int k = -4; const int P = b * 4 + (wave & 3);
      auto idle = [&]() {
        if (k < KB && __builtin_amdgcn_s_getreg((3 << 11) | (8 << 6) | 7) <= 7) {
          stouch4(k < 0 ? (const char*)wo + ((k + 4) * 4096 + P * 4) * 128 : (const char*)((P & 1) ? w3 : w1) + (k * 2048 + (P >> 1) * 4) * 128); ++k; }
        BAR();
      };
      {
        const unsigned* pc = cnt + ((C2 + ((lane & 1) && tm3 ? tm3 - 1 : tm3)) * 8 + (tn3 & 7)) * 32;
        unsigned seen = 0x80008u;
        idle(); idle(); idle();
        if (wave == 4) asm volatile("global_load_dword %0, %1, off sc1" : "=v"(seen) : "v"(pc) : "memory");
        idle(); idle();
        asm volatile("s_waitcnt vmcnt(0)" : "+v"(seen)::"memory");
        if (__builtin_amdgcn_ballot_w64(seen < 0x80008u) != 0) poll<0>(pc, 0x80008u);
      }
#pragma unroll 1
      for (int t = 5; t < SLB; ++t) { if (t < 14) idle(); else BAR(); }
      wait_lgkm0();
      const int mrow = ((b & 3) * 4 + (wave & 3)) * 64 + (lane >> 3) * 8;
      mc = *(const u32x4*)(cnd + mrow);
#pragma unroll
      for (int i = 0; i < 8; ++i) mw[i] = __builtin_nontemporal_load((const u32x4*)(wmod + (size_t)(mrow + i) * 6144 + 2048 + (b >> 2) * 64 + (lane & 7) * 8));
      SCHED0();
#pragma unroll
      for (int t = SLB; t < 16; ++t) BAR();
    }
    lane = lane_id(); fr = lane & 15; g = lane >> 4; p4 = fr & 3; row = 4 * g + (fr >> 2); lsw = row * 128 + (((row >> 1) & 3) * 32) + p4 * 8;
    wait_lgkm0();
    BAR();
    STAMP_AT(9 );
    const LDS_PTR(char) eb = smem + wave * 2048;
    auto ld2 = [&](int o) { return cat8(*(const LDS_PTR(bf16x4))(eb + o), *(const LDS_PTR(bf16x4))(eb + o + 512)); };
    const int vo = 36864 + ((p4 * 16 + row) ^ ((p4 >> 1) * 8)) * 8;
    bf16x8 qf[2], k0[2], k1[2], v0[2], v1[2];
#pragma unroll
    for (int hd = 0; hd < 2; ++hd) { qf[hd] = ld2(2048 + hd * 1024 + lane * 8); k0[hd] = ld2(18432 + hd * 1024 + lane * 8); k1[hd] = ld2(18432 + 2048 + hd * 1024 + lane * 8); }
#pragma unroll
    for (int hd = 0; hd < 2; ++hd) {
      v0[hd] = cat8(ld_tr4(eb + vo + hd * 1024), ld_tr4(eb + vo + hd * 1024 + 2048));
      v1[hd] = cat8(ld_tr4(eb + vo + hd * 1024 + 512), ld_tr4(eb + vo + hd * 1024 + 512 + 2048));
    }
    SCHED0();
    const f32x4 z = {0.f, 0.f, 0.f, 0.f};
    const bool odd = fr & 1;
    const unsigned sel0 = odd ? 0x01000c0cu : 0x0c0c0100u, sel1 = odd ? 0x03020c0cu : 0x0c0c0302u;
    float inf = __builtin_inff(); asm("" : "+v"(inf));
    auto mx2 = [&](float a, float c) { return __builtin_amdgcn_fmed3f(a, c, inf); };
    auto both = [](float a, float c, auto op, auto fin) {
      auto u = __builtin_amdgcn_permlane16_swap(__float_as_uint(a), __float_as_uint(c), false, false);
      float r = op(__uint_as_float(u[0]), __uint_as_float(u[1]));
      auto w = __builtin_amdgcn_permlane32_swap(__float_as_uint(r), __float_as_uint(r), false, false);
      r = fin(op(__uint_as_float(w[0]), __uint_as_float(w[1])));
      auto y = __builtin_amdgcn_permlane16_swap(__float_as_uint(r), __float_as_uint(r), false, false);
      return (f32x2){__uint_as_float(y[0]), __uint_as_float(y[1])};
    };
    f32x2 l[2][2];
#pragma unroll
    for (int hd = 0; hd < 2; ++hd) {
      const f32x4 s0 = __builtin_amdgcn_mfma_f32_16x16x32_bf16(k0[hd], qf[hd], bias[0], 0, 0, 0), s1 = __builtin_amdgcn_mfma_f32_16x16x32_bf16(k1[hd], qf[hd], bias[1], 0, 0, 0);
#pragma unroll
      for (int t = 0; t < 2; ++t) {
        const f32x4 sv = t ? s1 : s0;
        unsigned u = pk2(odd ? sv[1] : sv[0], odd ? sv[3] : sv[2]);
        u = pk2(lo2f(u) * 0.17677669529663687f, hi2f(u) * 0.17677669529663687f);
        l[hd][t] = (f32x2){lo2f(u), hi2f(u)};
      }
    }
    const f32x2 mx = both(mx2(mx2(l[0][0][0], l[0][0][1]), mx2(l[0][1][0], l[0][1][1])), mx2(mx2(l[1][0][0], l[1][0][1]), mx2(l[1][1][0], l[1][1][1])), mx2,
                          [](float r) { return r * -1.4426950408889634f; });
    float sm[2];
#pragma unroll
    for (int hd = 0; hd < 2; ++hd) {
#pragma unroll
      for (int t = 0; t < 2; ++t) { const f32x2 e = l[hd][t] * 1.4426950408889634f + mx[hd]; l[hd][t] = (f32x2){__builtin_amdgcn_exp2f(e[0]), __builtin_amdgcn_exp2f(e[1])}; }
      const f32x2 a = l[hd][0] + l[hd][1]; sm[hd] = a[0] + a[1];
    }
    const f32x2 inv = both(sm[0], sm[1], [](float a, float c) { return a + c; }, [](float r) { return __builtin_amdgcn_rcpf(r); });
#pragma unroll
    for (int hd = 0; hd < 2; ++hd) {
      u32x4 pp;
#pragma unroll
      for (int t = 0; t < 2; ++t) {
        const f32x2 p = l[hd][t] * inv[hd]; const unsigned u = pk2(p[0], p[1]);
        pp[2 * t] = __builtin_amdgcn_perm(u, u, sel0); pp[2 * t + 1] = __builtin_amdgcn_perm(u, u, sel1);
      }
      const bf16x8 pf = __builtin_bit_cast(bf16x8, pp);
      const f32x4 oa = __builtin_amdgcn_mfma_f32_16x16x32_bf16(v0[hd], pf, z, 0, 0, 0), ob = __builtin_amdgcn_mfma_f32_16x16x32_bf16(v1[hd], pf, z, 0, 0, 0);
      u32x4 ov; ov[0] = pk2(oa[0], oa[1]); ov[1] = pk2(oa[2], oa[3]); ov[2] = pk2(ob[0], ob[1]); ov[3] = pk2(ob[2], ob[3]);
      st16<true>(ws + WS_O, ((tn3 * 2 + hd) * 128 + tm3 * 8 + wave) * 1024 + lane * 16, ov);
    }
    STAMP_AT(10 );
    wait_vm<0>();
    STAMP_AT(11 );
    BAR();
    STAMP_AT(12 );
    if (wave >= 4) {
      const unsigned m4[4] = {mc[0], mc[1], mc[2], mc[3]};
      const f32x2 ps = slab_sum<8>(m4, mw);
      if (!(lane & 8))
        __hip_atomic_store((unsigned long long*)(ws + WS_MOD2) + (((b & 3) * 4 + (wave & 3)) * 4096 + (b >> 2) * 64 + (lane & 7) * 8 + (lane >> 4) * 2) / 2,
                           ((unsigned long long)__float_as_uint(ps[1]) << 32) | __float_as_uint(ps[0]), __ATOMIC_RELAXED, __HIP_MEMORY_SCOPE_AGENT);
      wait_vm<0>();
      if (lane == 0 && __hip_atomic_fetch_add((LDS_PTR(unsigned))(smem + LDS_ARR + 8), 1u, __ATOMIC_RELAXED, __HIP_MEMORY_SCOPE_WORKGROUP) == 3)
        __hip_atomic_fetch_add(cnt + (CM * 8 + (b >> 2)) * 32, 1u, __ATOMIC_RELAXED, __HIP_MEMORY_SCOPE_AGENT);
    }
  }
  { const int wm = wave & 3, ks = wave >> 2, mb = tm4 * 8 + wm * 2 + ks;
    constexpr int R = 6;
    u32x4 rx[2];
#pragma unroll
    for (int p = 0; p < 2; ++p) rx[p] = *(const u32x4*)(x + (size_t)(mb * 16 + fr) * D_ + tn4 * 64 + p * 32 + (g & 1) * 16 + (g >> 1) * 8);
    const unsigned va0 = (ks * 128 + mb) * 1024 + lane * 16, va1 = va0 ^ 1024;
    bf16x8 fa[R][2], fw[2][4];
    auto issueA = [&](int t) {
      const char* pa = ws + WS_O + t * 262144;
      fa[t % R][0] = __builtin_bit_cast(bf16x8, ld16<true>(pa, va0));
      fa[t % R][1] = __builtin_bit_cast(bf16x8, ld16<true>(pa, va1));
    };
    unsigned aw[4];
    auto rdw = [&](int t, int j) {
      const int o = (t & 3) * 8192;
      fw[t & 1][j] = cat8(ld_tr4(smem + aw[j] + o), ld_tr4(smem + aw[j] + o + 2048));
    };
    auto stepw = [&](int t) {
      const int gi = t >> 2, base = gi < 3 ? 57344 + gi * 32768 : 0;
#pragma unroll
      for (int j = 0; j < 4; ++j) { aw[j] = base + ks * 4096 + (lsw ^ (j * 32)); asm volatile("" : "+v"(aw[j])); }
    };
    auto up2 = [](unsigned u) { return (f32x2){lo2f(u), hi2f(u)}; };
    f32x2 f[8];
    unsigned slot = (mb * 256 + fr + kh6 * 32768 + 4 * g * 16) * 8, sown = (mb * 256 + fr + (g & 1) * 32768 + tn4 * 16) * 8;
    f32x4 acc[2][4];
#pragma unroll
    for (int i = 0; i < 2; ++i)
#pragma unroll
      for (int j = 0; j < 4; ++j) acc[i][j] = (f32x4){0.f, 0.f, 0.f, 0.f};
    stepw(0);
    wait_lgkm0();
    SCHED0(); BAR(); SCHED0();
    STAMP_AT(13 );
#pragma unroll
    for (int t = 0; t < R; ++t) issueA(t);
#pragma unroll
    for (int j = 0; j < 4; ++j) rdw(0, j);
    SCHED0();
#pragma unroll
    for (int t = 0; t < 16; ++t) {
      if (t < 15 && ((t + 1) & 3) == 0) stepw(t + 1);
#pragma unroll
      for (int j = 0; j < 4; ++j) {
        if (t < 15) rdw(t + 1, j);
        SCHED0();
#pragma unroll
        for (int i = 0; i < 2; ++i) acc[i][j] = __builtin_amdgcn_mfma_f32_16x16x32_bf16(fw[t & 1][j], fa[t % R][i], acc[i][j], 0, 0, 0);
        SCHED0();
      }
      if (t + R < 16) issueA(t + R);
      if (t == 2) {
#pragma unroll
        for (int p = 0; p < 2; ++p) {
          auto s0 = __builtin_amdgcn_permlane16_swap(rx[p][0], rx[p][2], false, false);
          auto s1 = __builtin_amdgcn_permlane16_swap(rx[p][1], rx[p][3], false, false);
          f[4 * p] = up2(s0[0]); f[4 * p + 1] = up2(s1[0]); f[4 * p + 2] = up2(s0[1]); f[4 * p + 3] = up2(s1[1]);
        }
#pragma unroll
        for (int j = 0; j < 8; ++j) asm volatile("" : "+v"(f[j]));
        asm volatile("" : "+v"(slot), "+v"(sown));
      }
      if (t == 5) BAR();
      SCHED0();
    }
    STAMP_AT(14 );
    LDS_PTR(f32x4) red = (LDS_PTR(f32x4))(smem + K4_RED);
#pragma unroll
    for (int j = 0; j < 4; ++j) red[((wave ^ 4) * 4 + j) * 64 + lane] = acc[1][j];
    wait_lgkm0();
    BAR();
    STAMP_AT(15 );
    const LDS_PTR(float) vec = (const LDS_PTR(float))(smem + LDS_VEC);
    unsigned o[8];
    f32x4 pr[4], ga[4];
#pragma unroll
    for (int j = 0; j < 4; ++j) { pr[j] = red[(wave * 4 + j) * 64 + lane]; ga[j] = *(const LDS_PTR(f32x4))(vec + j * 16 + 4 * g); }
    SCHED0();
#pragma unroll
    for (int j = 0; j < 4; ++j) {
      const f32x4 y = acc[0][j] + pr[j];
      const f32x2 lo = y.xy * ga[j].xy + f[2 * j], hi = y.zw * ga[j].zw + f[2 * j + 1];
      o[2 * j] = pk2(lo[0], lo[1]); o[2 * j + 1] = pk2(hi[0], hi[1]);
      f[2 * j] = up2(o[2 * j]); f[2 * j + 1] = up2(o[2 * j + 1]);
    }
    const f32x2 s2 = ((f[0] + f[1]) + (f[2] + f[3])) + ((f[4] + f[5]) + (f[6] + f[7]));
    float mt = xsum16_32(s2[0] + s2[1]) * (1.f / 64.f); asm("" : "+v"(mt));
    f32x2 q2 = {0.f, 0.f}, q3 = {0.f, 0.f};
#pragma unroll
    for (int j = 0; j < 8; j += 2) { f[j] -= mt; f[j + 1] -= mt; q2 += f[j] * f[j]; q3 += f[j + 1] * f[j + 1]; }
    q2 += q3;
    const float qq = xsum16_32(q2[0] + q2[1]);
    if (lane < 32) {
      const unsigned qb = qq == qq ? __float_as_uint(qq) : 0x7fc00000u;
      __hip_atomic_store((unsigned long long*)(ws + WS_XCH + sown), ((unsigned long long)qb << 32) | __float_as_uint(mt), __ATOMIC_RELAXED, __HIP_MEMORY_SCOPE_AGENT);
    }
    *(LDS_PTR(u32x4))(smem + (mb & 7) * 2048 + lane * 16) = (u32x4){o[0], o[1], o[2], o[3]};
    *(LDS_PTR(u32x4))(smem + (mb & 7) * 2048 + 1024 + lane * 16) = (u32x4){o[4], o[5], o[6], o[7]};
    STAMP_AT(16 );
    f32x4 ss[4], gg[4];
#pragma unroll
    for (int j = 0; j < 4; ++j) {
      ss[j] = *(const LDS_PTR(f32x4))(vec + 64 + j * 16 + 4 * g); gg[j] = *(const LDS_PTR(f32x4))(vec + 128 + j * 16 + 4 * g);
      f[2 * j] *= gg[j].xy; f[2 * j + 1] *= gg[j].zw;
      asm volatile("" : "+v"(ss[j]), "+v"(gg[j]), "+v"(f[2 * j]), "+v"(f[2 * j + 1]));
    }
    unsigned long long pv[4];
    __builtin_amdgcn_s_sleep(XS0);
    for (unsigned spins = 0;; ++spins) {
      bool ok = true;
#pragma unroll
      for (int i = 0; i < 4; ++i) { pv[i] = __hip_atomic_load((unsigned long long*)(ws + WS_XCH + slot) + i * 16, __ATOMIC_RELAXED, __HIP_MEMORY_SCOPE_AGENT); ok &= (unsigned)(pv[i] >> 32) != 0xffffffffu; }
      if (__builtin_amdgcn_ballot_w64(!ok) == 0 || spins > SPIN_CAP) break;
      __builtin_amdgcn_s_sleep(XS1);
    }
    STAMP_AT(17 );
    float ms = 0.f, m2 = 0.f;
#pragma unroll
    for (int i = 0; i < 4; ++i) { ms += __uint_as_float((unsigned)pv[i]); m2 += __uint_as_float((unsigned)(pv[i] >> 32)); }
    const float mean = xsum16_32(ms) * (1.f / 16.f);
#pragma unroll
    for (int i = 0; i < 4; ++i) { const float d = __uint_as_float((unsigned)pv[i]) - mean; m2 += 64.f * d * d; }
    const float rstd = ln_rstd(xsum16_32(m2) * (1.f / 1024.f));
    const float sh = (mt - rbf(mean)) * rstd;
#pragma unroll
    for (int p = 0; p < 2; ++p) {
      u32x4 v;
#pragma unroll
      for (int j = 0; j < 4; ++j) {
        const int jb = 2 * p + (j >> 1);
        const f32x2 h = f[4 * p + j] * rstd + (((j & 1) ? gg[jb].zw : gg[jb].xy) * sh + ((j & 1) ? ss[jb].zw : ss[jb].xy));
        v[j] = pk2(h[0], h[1]);
      }
      st16<true>(ws + WS_H2, ((tn4 * 2 + p) * 128 + mb) * 1024 + lane * 16, v);
    }
    STAMP_AT(18 );
    wait_vm<0>();
    wait_lgkm0();
    BAR();
    STAMP_AT(19 );

  }
  const int grp = wave >> 2;
  { const int wm = wave & 3;
    const LDS_PTR(char) sb = smem + 3 * K5_STG;
    const LDS_PTR(char) aP = sb + K5_G + row * 224 + grp * 96 + p4 * 8;
    const LDS_PTR(char) aM = sb + (p4 < 2 ? K5_G : K5_U) + row * 224 + (12 + grp) * 16 + (p4 & 1) * 8;
    const LDS_PTR(char) aA = sb + wm * 4096 + lane * 16;
    f32x32 c0, c1, c2; f32x16 c3;
    asm volatile(".irp r,0,8,16,24,32,40,48,56,64,72,80,88,96,104\n\tv_mov_b64 v[\\r:\\r+1], 0\n\tv_mov_b64 v[\\r+2:\\r+3], 0\n\tv_mov_b64 v[\\r+4:\\r+5], 0\n\tv_mov_b64 v[\\r+6:\\r+7], 0\n\t.endr"
                 : "={v[0:31]}"(c0), "={v[32:63]}"(c1), "={v[64:95]}"(c2), "={v[96:111]}"(c3)::"memory");
    int wrap = -(K5_NS - 1) * K5_STG, dl = K5_STG, n = 7;
    unsigned mask = wrap_mask(4, K5_NS);
    asm volatile("" : "+s"(wrap), "+v"(aP), "+v"(aM), "+v"(aA));
    if (grp) __builtin_amdgcn_s_setprio(1); else __builtin_amdgcn_s_setprio(2);
    BAR();
    STAMP_AT(20 );
    BAR();
    STAMP_G(21, 26);
    asm volatile(RA5(0) RA5(1) RP5(0, 0) RP5(0, 1) RP5(1, 0) RP5(1, 1) RP5(2, 0) RP5(2, 1) RP5(3, 0) RP5(3, 1) RP5(4, 0) RP5(4, 1) RP5(5, 0) RP5(5, 1) RW5(6, 0, aM, 0) LG(9)
                 ".p2align 5\n1:\n\t" TRIP5("") TRIP5("") TRIP5("") TRIP5("s_sub_u32 %[n], %[n], 1\n\t") "s_cbranch_scc0 1b\n\t" LG(0) "s_nop 7"
                 : ACC5, [aP] "+v"(aP), [aM] "+v"(aM), [aA] "+v"(aA), [dl] "+s"(dl), [mask] "+s"(mask), [n] "+s"(n) : [wrap] "s"(wrap) : CLOB5);
    f32x4 acc[4][7];
#pragma unroll
    for (int e = 0; e < 112; ++e) acc[e / 28][(e % 28) / 4][e & 3] = e < 32 ? c0[e & 31] : e < 64 ? c1[e & 31] : e < 96 ? c2[e & 31] : c3[e & 15];
    STAMP_G(22, 27);
    auto swi4 = [](const f32x4& gv, const f32x4& uv, unsigned& o0, unsigned& o1) {
      constexpr float c2 = 2.0813689810056077f;
      f32x2 pa = gv.xy * uv.xy, pb = gv.zw * uv.zw;
      const f32x2 ta = (f32x2){__builtin_amdgcn_exp2f(gv[0]), __builtin_amdgcn_exp2f(gv[1])} * c2 + c2, tb = (f32x2){__builtin_amdgcn_exp2f(gv[2]), __builtin_amdgcn_exp2f(gv[3])} * c2 + c2;
      pa *= (f32x2){__builtin_amdgcn_rcpf(ta[0]), __builtin_amdgcn_rcpf(ta[1])}; pb *= (f32x2){__builtin_amdgcn_rcpf(tb[0]), __builtin_amdgcn_rcpf(tb[1])};
      o0 = pk2(pa[0], pa[1]); o1 = pk2(pb[0], pb[1]);
    };
    const int lz = lane_id();
    const int gz = lz >> 4, fz = lz & 15;
    const int qb = (tn5 >> 4) * 224, ql = qb + 64 + (tn5 & 15) * 10;
    auto hof = [&](int e, int i) { return (unsigned)((e >> 2) * 131072 + (e & 3) * 256 + fz * 16 + i * 1024); };
    const unsigned ho0 = hof((grp ? ql + 2 : qb + (tn5 & 15) * 4) + 2 * (gz & 1) + (gz >> 1), 0), ho1 = hof(ql + 6 * grp + (gz >> 1), gz & 1), hom = hof(ql + 8 + grp, gz);
    char* hb = ws + WS_HH + (tm5 * 16 + wm * 4) * 1024;
    const int c5 = C5 + tm5 * 4 + (tn5 >> 4) * 2;
    auto tr16 = [](const unsigned (&o)[4]) {
      auto s0 = __builtin_amdgcn_permlane16_swap(o[0], o[2], false, false);
      auto s1 = __builtin_amdgcn_permlane16_swap(o[1], o[3], false, false);
      u32x4 v = {s0[0], s1[0], s0[1], s1[1]}; asm volatile("" : "+v"(v)); return v;
    };
    auto pair = [&](int p, int i) {
      unsigned o[4];
      swi4(acc[i][2 * p], acc[i][2 * p + 1], o[0], o[1]);
      swi4(acc[i + 1][2 * p], acc[i + 1][2 * p + 1], o[2], o[3]);
      return tr16(o);
    };
    u32x4 v[3];
#pragma unroll
    for (int i = 0; i < 4; ++i) {
      unsigned o[4];
      swi4(acc[i][0], acc[i][1], o[0], o[1]);
      swi4(acc[i][2], acc[i][3], o[2], o[3]);
      st16<true>(hb, ho0 + i * 1024, tr16(o));
      SCHED0();
    }
    STAMP_G(23, 28);
    v[0] = pair(2, 0);
    if (!grp) {
      wait_vm<0>();
      unsigned arr = 0;
      if (lz == 0) arr = __hip_atomic_fetch_add((LDS_PTR(unsigned))(smem + LDS_ARR), 1u, __ATOMIC_RELAXED, __HIP_MEMORY_SCOPE_WORKGROUP);
      if (__builtin_amdgcn_readfirstlane(arr) == 3) signal(cnt, c5, lz);
    }
    STAMP_G(24, 29);
    v[1] = pair(2, 2);
    { unsigned m[4];
#pragma unroll
      for (int i = 0; i < 4; i += 2) {
        f32x4 gv, uv;
#pragma unroll
        for (int e = 0; e < 4; ++e) {
          auto w = __builtin_amdgcn_permlane32_swap(__float_as_uint(acc[i + (e >> 1)][6][e & 1]), __float_as_uint(acc[i + (e >> 1)][6][2 + (e & 1)]), false, false);
          gv[e] = __uint_as_float(w[0]); uv[e] = __uint_as_float(w[1]);
        }
        swi4(gv, uv, m[i], m[i + 1]);
      }
      auto x0 = __builtin_amdgcn_permlane32_swap(m[0], m[2], false, false);
      auto x1 = __builtin_amdgcn_permlane32_swap(m[1], m[3], false, false);
      const unsigned o[4] = {x0[0], x0[1], x1[0], x1[1]};
      v[2] = tr16(o);
      st16<true>(hb, ho1, v[0]); st16<true>(hb, ho1 + 2048, v[1]); st16<true>(hb, hom, v[2]);
    }
    STAMP_G(25, 30);
    if (grp) {
#pragma unroll
      for (int t = 0; t < NDR; ++t) BAR();
      wait_vm<0>();
      BAR();
      if (wave == 4) signal(cnt, c5 + 1, lz);
      if (wave > 4) return;
#pragma unroll 1
      for (int t = NDR + 1; t < 9; ++t) BAR();
      unsigned seen = 16;
      const unsigned* pc = (lz & 1) ? (const unsigned*)(ws + WS_FLG) + (b ^ 4) * 32 : cnt + ((C5 + (tm6 >> 1) * 4 + kh6 * 2 + 1) * 8 + tn6) * 32;
      asm volatile("global_load_dword %0, %1, off sc1" : "=v"(seen) : "v"(pc) : "memory");
      BAR(); BAR();
      asm volatile("s_waitcnt vmcnt(0)" : "+v"(seen)::"memory");
      if (__builtin_amdgcn_ballot_w64(seen < 16) != 0) poll<0>(pc, 16);
      return;
    }
  }
  { lane = lane_id(); fr = lane & 15; g = lane >> 4; p4 = fr & 3; row = 4 * g + (fr >> 2);
    const int wm = wave & 1, wn = wave >> 1;
    const int lw0 = row * 128 + (((row >> 1) & 3) * 32) + p4 * 8;
    const int mb0 = tm6 * 8 + wm * 4, t64 = tn6 * 2 + kh6;
    unsigned a0 = lds_base + (lw0 ^ (wn * 64)), a1 = lds_base + (lw0 ^ (wn * 64 + 32)), aa = lds_base + K6_A + wm * 4096 + lane * 16;
    int dw, da, n = 4, n2 = 1;
    unsigned mw = wrap_mask(2, K6_NW), ma = wrap_mask(2, K6_NA);
    f32x32 c0, c1;
    asm volatile(".irp r,0,8,16,24,32,40,48,56\n\tv_mov_b64 v[\\r:\\r+1], 0\n\tv_mov_b64 v[\\r+2:\\r+3], 0\n\tv_mov_b64 v[\\r+4:\\r+5], 0\n\tv_mov_b64 v[\\r+6:\\r+7], 0\n\t.endr\n\t"
                 "s_waitcnt vmcnt(0)\n\ts_barrier\n\t"
                 RW6(1, 0, 0, 0) RW6(1, 0, 1, 0) RA6(0, 0) RW6(1, 1, 0, 0) RW6(1, 1, 1, 0) RA6(1, 0) RA6(2, 0) RA6(3, 0) RW6(0, 0, 0, 0) RW6(0, 0, 1, 0) RW6(0, 1, 0, 0) RW6(0, 1, 1, 0)
                 "v_add_u32 %[a0], 8192, %[a0]\n\tv_add_u32 %[a1], 8192, %[a1]\n\tv_add_u32 %[aa], 8192, %[aa]\n\t" LG(4) "s_barrier\n\t"
                 RW6(0, 0, 0, 1) RW6(0, 0, 1, 1) RA6(0, 1) RW6(0, 1, 0, 1) M6(0, 2, 0) RW6(0, 1, 1, 1) M6(0, 3, 0) RA6(1, 1) M6(1, 3, 0) RA6(2, 1) M6(1, 2, 0) RA6(3, 1)
                 M6(2, 2, 0) RW6(1, 0, 0, 1) M6(2, 3, 0) RW6(1, 0, 1, 1) M6(3, 3, 0) RW6(1, 1, 0, 1) M6(3, 2, 0) RW6(1, 1, 1, 1)
                 "v_add_u32 %[a0], 8192, %[a0]\n\tv_add_u32 %[a1], 8192, %[a1]\n\tv_add_u32 %[aa], 8192, %[aa]\n\t" LG(6) "s_barrier\n\t"
                 ".p2align 5\n1:\n\t" TRIP6(0, 0, 1, 2, ) TRIP6(1, 1, 2, 0, ) TRIP6(0, 2, 0, 1, ) TRIP6(1, 0, 1, 2, ) TRIP6(0, 1, 2, 0, ) TRIP6(1, 2, 0, 1, "s_sub_u32 %[n], %[n], 1\n\t")
                 "s_cbranch_scc0 1b\n\ts_mov_b32 %[n], 3\n\ts_mov_b32 %[mw], %[mw2]\n\ts_mov_b32 %[ma], %[ma2]\n\ts_sub_u32 %[n2], %[n2], 1\n\ts_cbranch_scc0 1b\n\t"
                 M6(0, 0, 0) M6(0, 0, 1) M6(0, 1, 1) M6(0, 1, 0) LG(0) M6(1, 1, 0) M6(1, 1, 1) M6(1, 0, 1) M6(1, 0, 0) M6(2, 0, 0) M6(2, 0, 1) M6(2, 1, 1) M6(2, 1, 0)
                 M6(3, 1, 0) M6(3, 1, 1) M6(3, 0, 1) M6(3, 0, 0) "s_barrier\n\t"
                 M6(0, 2, 1) M6(0, 3, 1) M6(1, 3, 1) M6(1, 2, 1) M6(2, 2, 1) M6(2, 3, 1) M6(3, 3, 1) M6(3, 2, 1) "s_nop 8"
                 : "={v[0:31]}"(c0), "={v[32:63]}"(c1), [a0] "+v"(a0), [a1] "+v"(a1), [aa] "+v"(aa), [dw] "=&s"(dw), [da] "=&s"(da), [mw] "+s"(mw), [ma] "+s"(ma), [n] "+s"(n), [n2] "+s"(n2)
                 : [ww] "s"(-(K6_NW - 1) * 8192), [wa] "s"(-(K6_NA - 1) * 8192), [mw2] "n"(wrap_mask(32, K6_NW)), [ma2] "n"(wrap_mask(32, K6_NA))
                 : "memory", "scc", "v64", "v65", "v66", "v67", "v68", "v69", V10(7), V10(8), V10(9), V10(10), V10(11), V10(12), V10(13), V10(14), V10(15));
    f32x4 acc[4][4];
#pragma unroll
    for (int e = 0; e < 64; ++e) acc[e >> 4][(e >> 2) & 3][e & 3] = e < 32 ? c0[e & 31] : c1[e & 31];
    STAMP_AT(34 );
    lane = lane_id(); fr = lane & 15; g = lane >> 4;
    const int slot = wm * 2 + wn, pb = (b ^ 4) * 4 + slot;
    { auto pk = [](float lo, float hi) { const unsigned u = pk2(lo, hi); return u < 0xfffffffeu ? u : 0xfffffffeu; };
#pragma unroll
      for (int i = 0; i < 4; ++i)
        st16<true>(ws + WS_PART, (b * 4 + slot) * 4096 + i * 1024 + lane * 16,
                   (u32x4){pk(acc[i][0][0], acc[i][0][1]), pk(acc[i][0][2], acc[i][0][3]), pk(acc[i][1][0], acc[i][1][1]), pk(acc[i][1][2], acc[i][1][3])});
      STAMP_AT(35 );
    }
    { const LDS_PTR(float) vec = (const LDS_PTR(float))(smem + LDS_VEC) + 192 + wn * 32 + 4 * g;
      const f32x4 gv0 = *(const LDS_PTR(f32x4))vec, gv1 = *(const LDS_PTR(f32x4))(vec + 16);
      u32x4 rv[4];
#pragma unroll
      for (int i = 0; i < 4; ++i) { rv[i] = *(const LDS_PTR(u32x4))(smem + K6_X + (wm * 4 + i) * 2048 + wn * 1024 + lane * 16); asm volatile("" : "+v"(rv[i])); }
      u32x4 z[4];
      __builtin_amdgcn_s_sleep(PD6);
#pragma unroll 1
      for (unsigned spins = 0; spins < SPIN_CAP; ++spins) {
        asm volatile("" ::: "memory");
        unsigned m = 0;
#pragma unroll
        for (int i = 0; i < 4; ++i) z[i] = ld16<true>(ws + WS_PART, pb * 4096 + i * 1024 + lane * 16);
#pragma unroll
        for (int i = 0; i < 16; ++i) m = m > z[i >> 2][i & 3] ? m : z[i >> 2][i & 3];
        if (__builtin_amdgcn_ballot_w64(m == ~0u) == 0) break;
        __builtin_amdgcn_s_sleep(PS6);
      }
      STAMP_AT(36 );
#pragma unroll
      for (int i = 0; i < 4; ++i) {
        const f32x4 v0 = (acc[i][2] + (f32x4){lo2f(z[i][0]), hi2f(z[i][0]), lo2f(z[i][1]), hi2f(z[i][1])}) * gv0;
        const f32x4 v1 = (acc[i][3] + (f32x4){lo2f(z[i][2]), hi2f(z[i][2]), lo2f(z[i][3]), hi2f(z[i][3])}) * gv1;
        const unsigned o0 = pk2(lo2f(rv[i][0]) + v0[0], hi2f(rv[i][0]) + v0[1]), o1 = pk2(lo2f(rv[i][1]) + v0[2], hi2f(rv[i][1]) + v0[3]);
        const unsigned o2 = pk2(lo2f(rv[i][2]) + v1[0], hi2f(rv[i][2]) + v1[1]), o3 = pk2(lo2f(rv[i][3]) + v1[2], hi2f(rv[i][3]) + v1[3]);
        auto s0 = __builtin_amdgcn_permlane16_swap(o0, o2, false, false);
        auto s1 = __builtin_amdgcn_permlane16_swap(o1, o3, false, false);
        st16<false>(out, (((mb0 + i) * 16 + fr) * D_ + t64 * 64 + wn * 32 + (g & 1) * 16 + (g >> 1) * 8) * 2, (u32x4){s0[0], s1[0], s0[1], s1[1]});
      }
    }
    STAMP_AT(37 );
  }
}

extern "C" void kernel_launch(void* const* d_in, const int* in_sizes, int n_in, void* d_out, int out_size, void* d_ws,
                              size_t ws_size, hipStream_t stream) {
  const u16* x = (const u16*)d_in[0];
  const u16* wqkv = (const u16*)d_in[1];
  const u16* wo = (const u16*)d_in[2];
  const u16* w1 = (const u16*)d_in[4];
  const u16* w3 = (const u16*)d_in[5];
  const u16* w2 = (const u16*)d_in[6];
  const u16* c = (const u16*)d_in[7];
  const u16* wmod = (const u16*)d_in[8];
  (void)hipFuncSetAttribute((const void*)k2_block, hipFuncAttributeMaxDynamicSharedMemorySize, MEGA_LDS);
  char* ws = (char*)d_ws;
  k2_block<<<256, 768, MEGA_LDS, stream>>>(x, wqkv, wo, w1, w3, w2, c, wmod, (u16*)d_out, ws);
}
```

```cpp
#include <hip/hip_runtime.h>
#include <stdint.h>
#include <type_traits>
#pragma clang diagnostic ignored "-Winline-asm"

typedef unsigned short u16;
typedef short bf16x8 __attribute__((ext_vector_type(8)));
typedef short bf16x4 __attribute__((ext_vector_type(4)));
typedef float f32x4 __attribute__((ext_vector_type(4)));
typedef float f32x2 __attribute__((ext_vector_type(2)));
typedef unsigned int u32x4 __attribute__((ext_vector_type(4)));
typedef unsigned int u32x2 __attribute__((ext_vector_type(2)));
#define LDS_PTR(T) __attribute__((address_space(3))) T*
#define BAR() do { asm volatile("" ::: "memory"); __builtin_amdgcn_s_barrier(); asm volatile("" ::: "memory"); } while (0)
#define SCHED0() __builtin_amdgcn_sched_barrier(0)
template <int N> using IC = std::integral_constant<int, N>;
typedef std::integral_constant<bool, true> T_;
typedef std::integral_constant<bool, false> F__;

constexpr int S_ = 2048, D_ = 1024, F_ = 3584;
#define STAMP_AT(i)
#define STAMP_G(ia, ib)
#define STAMP_L(i)

__device__ __forceinline__ unsigned pk2(float lo, float hi) {
  typedef __bf16 bf2 __attribute__((ext_vector_type(2)));
  f32x2 v = {lo, hi}; bf2 r = __builtin_convertvector(v, bf2); return __builtin_bit_cast(unsigned, r);
}
__device__ __forceinline__ float lo2f(unsigned u) { return __uint_as_float(u << 16); }
__device__ __forceinline__ float hi2f(unsigned u) { return __uint_as_float(u & 0xffff0000u); }
__device__ __forceinline__ float rbf(float v) { return lo2f(pk2(v, 0.f)); }
#define glds16(sbase, voff, lds_off, imm) \
  asm volatile("s_add_u32 m0, %2, %3\n\ts_nop 0\n\tglobal_load_lds_dwordx4 %0, %1" ::"v"(voff), "s"(sbase), "s"(lds_off), "n"(imm) : "memory", "scc")
#define glds16s(sbase, voff, lds_off, imm) \
  asm volatile("s_add_u32 m0, %2, %3\n\ts_nop 0\n\tglobal_load_lds_dwordx4 %0, %1 sc1" ::"v"(voff), "s"(sbase), "s"(lds_off), "n"(imm) : "memory", "scc")
#define GLDS4(SC) "s_mov_b32 m0, %6\n\ts_nop 0\n\tglobal_load_lds_dwordx4 %0, %4" SC "\n\tglobal_load_lds_dwordx4 %1, %5 offset:1024" SC \
                  "\n\tglobal_load_lds_dwordx4 %2, %4 offset:2048" SC "\n\tglobal_load_lds_dwordx4 %3, %5 offset:3072" SC
template <bool SC1> __device__ __forceinline__ void glds16x4(const void* s0, const void* s1, const unsigned (&v)[4], unsigned lds) {
  if (SC1) asm volatile(GLDS4(" sc1") ::"v"(v[0]), "v"(v[1]), "v"(v[2]), "v"(v[3]), "s"(s0), "s"(s1), "s"(lds) : "memory");
  else asm volatile(GLDS4("") ::"v"(v[0]), "v"(v[1]), "v"(v[2]), "v"(v[3]), "s"(s0), "s"(s1), "s"(lds) : "memory");
}
__device__ __forceinline__ void glds4(const void* sbase, unsigned voff, unsigned lds_off) {
  asm volatile("s_mov_b32 m0, %2\n\ts_nop 0\n\tglobal_load_lds_dword %0, %1" ::"v"(voff), "s"(sbase), "s"(lds_off) : "memory");
}
__device__ __forceinline__ void stouch4(const void* p) {
  asm volatile("s_load_dwordx2 s[100:101], %0, 0x3c\n\ts_load_dwordx2 s[100:101], %0, 0xbc\n\ts_load_dwordx2 s[100:101], %0, 0x13c\n\ts_load_dwordx2 s[100:101], %0, 0x1bc"
               ::"s"(p) : "memory", "s100", "s101");
}
template <bool SC1> __device__ __forceinline__ void st16(void* sbase, unsigned voff, u32x4 v) {
  __builtin_amdgcn_raw_buffer_store_b128(v, __builtin_amdgcn_make_buffer_rsrc(sbase, 0, 0x7fffffff, 0x00020000), voff, 0, SC1 ? 16 : 0);
}
template <bool SC1> __device__ __forceinline__ u32x4 ld16(const void* sbase, unsigned voff) {
  return __builtin_amdgcn_raw_buffer_load_b128(__builtin_amdgcn_make_buffer_rsrc((void*)sbase, 0, 0x7fffffff, 0x00020000), voff, 0, SC1 ? 16 : 0);
}
template <int N> __device__ __forceinline__ void wait_vm() { asm volatile("s_waitcnt vmcnt(%0)" ::"n"(N) : "memory"); }
__device__ __forceinline__ void wait_lgkm0() { asm volatile("" ::: "memory"); __builtin_amdgcn_s_waitcnt(0xC07F); asm volatile("" ::: "memory"); }
__device__ __forceinline__ bf16x4 ld_tr4(const LDS_PTR(char) p) { return __builtin_amdgcn_ds_read_tr16_b64_v4i16((LDS_PTR(bf16x4))p); }
__device__ __forceinline__ bf16x8 cat8(bf16x4 lo, bf16x4 hi) { return __builtin_shufflevector(lo, hi, 0, 1, 2, 3, 4, 5, 6, 7); }
__device__ __forceinline__ void mfma_acc(f32x4& acc, bf16x8 a, bf16x8 b) { asm volatile("v_mfma_f32_16x16x32_bf16 %0, %1, %2, %0" : "+v"(acc) : "v"(a), "v"(b)); }
template <int STEP> __device__ __forceinline__ void mfma_sel(f32x4& acc, bf16x8 a, bf16x8 b, unsigned& mask, int& dl, int wrap) {
  asm volatile("s_add_u32 %1, %1, %1\n\tv_mfma_f32_16x16x32_bf16 %0, %3, %4, %0\n\ts_cselect_b32 %2, %5, %6" : "+v"(acc), "+s"(mask), "=s"(dl) : "v"(a), "v"(b), "s"(wrap), "n"(STEP) : "scc");
}
constexpr unsigned wrap_mask(int first, int ns) { unsigned m = 0; for (int t = 0; t < 32; ++t) if ((first + t) % ns == ns - 1) m |= 0x80000000u >> t; return m; }
template <int N> __device__ __forceinline__ void wait_lgkm() { __builtin_amdgcn_s_waitcnt(0xC07F | (N << 8)); }
template <int R, int C> __device__ __forceinline__ void mfma_done(f32x4 (&acc)[R][C]) {
  asm volatile("s_nop 9");
#pragma unroll
  for (int i = 0; i < R; ++i)
#pragma unroll
    for (int j = 0; j < C; ++j) asm volatile("" : "+v"(acc[i][j]));
}
__device__ __forceinline__ int lane_id() { int l; asm volatile("v_mbcnt_lo_u32_b32 %0, -1, 0\n\tv_mbcnt_hi_u32_b32 %0, -1, %0" : "=v"(l)); return l; }
__device__ __forceinline__ float xsum16_32(float v) {
  auto a = __builtin_amdgcn_permlane16_swap(__float_as_uint(v), __float_as_uint(v), false, false); v = __uint_as_float(a[0]) + __uint_as_float(a[1]);
  auto c = __builtin_amdgcn_permlane32_swap(__float_as_uint(v), __float_as_uint(v), false, false); return __uint_as_float(c[0]) + __uint_as_float(c[1]);
}

__device__ __forceinline__ float ln_rstd(float var) { return __builtin_amdgcn_rcpf(rbf(__builtin_amdgcn_sqrtf(rbf(rbf(var) + 1e-5f)))); }

constexpr size_t WS_MOD = 0;
constexpr size_t WS_EPO = 64 << 10, WS_FL1 = 96 << 10;
constexpr size_t WS_CTL = 128 << 10;
constexpr int C2 = 0, C3 = 16, C4 = 32, C5 = 48, CM = 80;
constexpr size_t WS_FLG = WS_CTL + (88 << 10), CTL_BYTES = 120 << 10;
constexpr size_t WS_XCH = 512 << 10;
constexpr size_t WS_MOD2 = 1 << 20;
constexpr size_t WS_H = 2 << 20, WS_O = WS_H + (4 << 20), WS_H2 = WS_O + (4 << 20), WS_HH = WS_H2 + (4 << 20),
                 WS_PART = WS_HH + (size_t)S_ * F_ * 2;

template <int NL> __device__ __forceinline__ f32x2 slab_sum(const unsigned (&mc)[NL / 2], const u32x4 (&mw)[NL]) {
  f32x2 a2[4] = {};
#pragma unroll
  for (int i = 0; i < NL; ++i) {
    const float cv = (i & 1) ? hi2f(mc[i >> 1]) : lo2f(mc[i >> 1]);
    const float sv = rbf(cv * __builtin_amdgcn_rcpf(1.f + __builtin_amdgcn_exp2f(cv * -1.4426950408889634f)));
#pragma unroll
    for (int j = 0; j < 4; ++j) a2[j] += (f32x2){lo2f(mw[i][j]), hi2f(mw[i][j])} * sv;
  }
  float acc[8] = {a2[0][0], a2[0][1], a2[1][0], a2[1][1], a2[2][0], a2[2][1], a2[3][0], a2[3][1]};
#pragma unroll
  for (int j = 0; j < 4; ++j) { auto a = __builtin_amdgcn_permlane32_swap(__float_as_uint(acc[j]), __float_as_uint(acc[j + 4]), false, false); acc[j] = __uint_as_float(a[0]) + __uint_as_float(a[1]); }
#pragma unroll
  for (int j = 0; j < 2; ++j) { auto a = __builtin_amdgcn_permlane16_swap(__float_as_uint(acc[j]), __float_as_uint(acc[j + 2]), false, false); acc[j] = __uint_as_float(a[0]) + __uint_as_float(a[1]); }
#pragma unroll
  for (int j = 0; j < 2; ++j) acc[j] += __uint_as_float(__builtin_amdgcn_mov_dpp(__float_as_uint(acc[j]), 0x128, 0xf, 0xf, false));
  return (f32x2){acc[0], acc[1]};
}

constexpr int K3_B = 18432, K3_STG = 18432 + 24576;
constexpr int K4_RED = 57344;
constexpr int K5_G = 16384, K5_U = 16384 + 7168 + 16, K5_STG = 30736, K5_NS = 5;
constexpr int K6_NW = 12, K6_NA = 7, K6_A = K6_NW * 8192, K6_X = 9 * 8192;
constexpr int LDS_VEC = 158720, LDS_DUMMY = LDS_VEC + 1024, LDS_ARR = LDS_DUMMY + 256, MEGA_LDS = LDS_ARR + 64;
constexpr unsigned SPIN_CAP = 1u << 19;
#define KNOBS PA6 = 5, PB6 = 5, KB = 8, XD0 = 16, PFD = 16, FD1 = 8, FS0 = 4, PQ = 2, PQ5 = 4, PD2 = 4, PD3 = 4, PD4 = 4, PD5 = 48, PD6 = 6, PS6 = 1, XS0 = 14, XS1 = 4, TS5 = 2, VB3 = 2, SLB = 15, NDR = 1
enum { KNOBS };
constexpr unsigned long long SKIP6 = 0x7c;
static_assert(__builtin_popcountll(SKIP6) == 5, "44 stages of W are issued behind 49 barriers");

__device__ __forceinline__ void signal(unsigned* cnt, int c, int lane, unsigned v = 1) {
  if (lane < 8) __hip_atomic_fetch_add(cnt + (c * 8 + lane) * 32, v, __ATOMIC_RELAXED, __HIP_MEMORY_SCOPE_AGENT);
}
template <int D0, int D1 = 1> __device__ __forceinline__ void poll(const unsigned* cnt, unsigned want) {
  if (D0) __builtin_amdgcn_s_sleep(D0);
#pragma unroll 1
  for (unsigned spins = 0; spins < SPIN_CAP; ++spins) {
    if (__builtin_amdgcn_ballot_w64(__hip_atomic_load(cnt, __ATOMIC_RELAXED, __HIP_MEMORY_SCOPE_AGENT) < want) == 0) break;
    __builtin_amdgcn_s_sleep(D1);
  }
  __builtin_amdgcn_fence(__ATOMIC_ACQUIRE, "wavefront");
}

template <int D0, int D1, int EX> __device__ __forceinline__ void pollp(const unsigned* cnt, unsigned want, unsigned (&r)[4]) {
  unsigned n = SPIN_CAP;
#define PL1(a) "global_atomic_add %" #a ", %5, %9, off sc0\n\ts_sleep %8\n\t"
#define PL2(a, c) "global_atomic_add %" #a ", %5, %9, off sc0\n\ts_waitcnt vmcnt(3)\n\tv_cmp_gt_u32 vcc, %6, %" #c "\n\ts_cbranch_vccz 2f\n\ts_sleep %8\n\t"
  asm volatile("s_mov_b64 exec, %10\n\ts_sleep %7\n\t" PL1(0) PL1(1) PL1(2) "1:\n\t" PL2(3, 0) PL2(0, 1) PL2(1, 2) PL2(2, 3) "s_sub_u32 %4, %4, 1\n\ts_cbranch_scc0 1b\n2:\n\ts_mov_b64 exec, -1"
               : "=&v"(r[0]), "=&v"(r[1]), "=&v"(r[2]), "=&v"(r[3]), "+s"(n) : "v"(cnt), "s"(want), "n"(D0), "n"(D1), "v"(0), "n"(EX) : "memory", "vcc", "scc");
}
#define KEEP4(r) asm volatile("; keep %0 %1 %2 %3" ::"v"(r[0]), "v"(r[1]), "v"(r[2]), "v"(r[3]))
typedef float f32x32 __attribute__((ext_vector_type(32)));
typedef float f32x16 __attribute__((ext_vector_type(16)));
#define M5(i, j, s, w) "v_mfma_f32_16x16x32_bf16 v[28*" #i "+4*" #j ":28*" #i "+4*" #j "+3], v[144+4*(" #w "):147+4*(" #w ")], v[112+16*(" #s ")+4*" #i ":115+16*(" #s ")+4*" #i "], v[28*" #i "+4*" #j ":28*" #i "+4*" #j "+3]\n\t"
#define RA5(i, s) "ds_read_b128 v[112+16*" #s "+4*" #i ":115+16*" #s "+4*" #i "], %[aA] offset:1024*" #i "\n\t"
#define RW5(j, h, a, w) "ds_read_b64_tr_b16 v[144+4*(" #w ")+2*" #h ":145+4*(" #w ")+2*" #h "], %[" #a "] offset:32*(" #j "/2)+7184*(" #j "&1)+3584*" #h "\n\t"
#define RM5(h, w) RW5(0, h, aM, w)
#define AD5(d, a) "v_add_u32 %[" #d "], %[dl], %[" #a "]\n\t"
#define SEL5A "s_add_u32 %[mask], %[mask], %[mask]\n\t"
#define SEL5B "s_cselect_b32 %[dl], %[wrap], 30736\n\t"
#define LG(n) "s_waitcnt lgkmcnt(" #n ")\n\t"
#define SB "s_barrier\n\t"
#define GN5(j, o, b, g0, g1, g2, g3, g4, g5, g6, g7) \
  M5(0, j, o, 2*(b)) g0 M5(0, j, 1-o, 2*(b)+1) g1 M5(1, j, 1-o, 2*(b)+1) g2 M5(1, j, o, 2*(b)) g3 M5(2, j, o, 2*(b)) g4 M5(2, j, 1-o, 2*(b)+1) g5 M5(3, j, o, 2*(b)) g6 M5(3, j, 1-o, 2*(b)+1) g7
#define GL5(j, o, b, g0, g1, g5, g7) \
  M5(0, j, o, 2*(b)) g0 M5(0, j, 1-o, 2*(b)+1) g1 M5(1, j, o, 2*(b)) RA5(0, o) M5(1, j, 1-o, 2*(b)+1) RA5(1, o) M5(2, j, o, 2*(b)) RA5(2, o) M5(2, j, 1-o, 2*(b)+1) g5 M5(3, j, o, 2*(b)) RA5(3, o) M5(3, j, 1-o, 2*(b)+1) g7 LG(4)
#define TRIPA(b, Z)                                                                                                                                     \
  GN5(0, 0, b, RW5(1, 1, X, 2-2*b), RW5(1, 0, Y, 3-2*b) LG(5), RW5(1, 1, Y, 3-2*b), , LG(4), , RW5(2, 0, X, 2*b), RW5(2, 1, X, 2*b) LG(2))                \
  GN5(1, 0, 1-b, RW5(2, 0, Y, 2*b+1), RW5(2, 1, Y, 2*b+1), , , , , RW5(3, 0, X, 2-2*b), RW5(3, 1, X, 2-2*b) LG(2))                                       \
  GN5(2, 0, b, RW5(3, 0, Y, 3-2*b), RW5(3, 1, Y, 3-2*b) SEL5A, SEL5B, AD5(X, Y) AD5(aA, aA), LG(2), SB, RW5(4, 0, Y, 2*b), RW5(4, 1, Y, 2*b) LG(2))        \
  GL5(3, 0, 1-b, RW5(4, 0, X, 2*b+1), RW5(4, 1, X, 2*b+1), Z, RW5(5, 0, Y, 2-2*b))
#define TRIPB(b, Z)                                                                                                                                     \
  GN5(4, 1, b, RW5(5, 1, Y, 2-2*b), RW5(5, 0, X, 3-2*b) LG(5), RW5(5, 1, X, 3-2*b), , LG(4), , RM5(0, 2*b), RM5(1, 2*b) LG(2))                            \
  GN5(5, 1, 1-b, AD5(aM, aM) RM5(0, 2*b+1), RM5(1, 2*b+1) SEL5A, SEL5B, AD5(Y, X) AD5(aA, aA), LG(2), SB, RW5(0, 0, X, 2-2*b), RW5(0, 1, X, 2-2*b) LG(2)) \
  GL5(6, 1, b, RW5(0, 0, Y, 3-2*b), RW5(0, 1, Y, 3-2*b), AD5(aM, aM) Z, RW5(1, 0, X, 2*b))
#define Q5(j, s, w) M5(0, j, s, w) M5(1, j, s, w) M5(2, j, s, w) M5(3, j, s, w)
#define W5(j, a, w) RW5(j, 0, a, w) RW5(j, 1, a, w)
#define A5(s) RA5(0, s) RA5(1, s) RA5(2, s) RA5(3, s)
static_assert(K5_STG == 30736 && K5_U - K5_G == 7184, "TRIP5");

#define C3(i, j) "v[24*" #i "+4*" #j "-12*(" #i "/4):24*" #i "+4*" #j "-12*(" #i "/4)+3]"
#define M3(i, j) "v_mfma_f32_16x16x32_bf16 " C3(i, j) ", v[124+4*" #j ":127+4*" #j "], v[104+4*" #i ":107+4*" #i "], " C3(i, j) "\n\t"
#define RA3(i, u) "ds_read_b128 v[104+4*" #i ":107+4*" #i "], %[aA] offset:1024*" #i "+9216*" #u "\n\t"
#define RH3(u) "ds_read_b128 v[120:123], %[aH] offset:9216*" #u "\n\t"
#define RW3(j, h, a, u) "ds_read_b64_tr_b16 v[124+4*" #j "+2*" #h ":125+4*" #j "+2*" #h "], %[" #a "] offset:8192*(" #j "/2)+2048*" #h "+4096*" #u "\n\t"
#define ST3(a, d) "v_add_u32 %[" #a "], " #d ", %[" #a "]\n\t"
#define H2_3(u, S0, S1, S2, B) M3(0, 0) RW3(5, 1, a1, u) M3(1, 0) RA3(2, u) M3(1, 1) RA3(3, u) S0 M3(0, 1) LG(7) S1 M3(0, 2) S2 M3(1, 2) M3(1, 3) M3(0, 3) LG(2) M3(0, 4) M3(1, 4) LG(0) B
#define H1_3(n, X)                                                                                                                                      \
  M3(1, 5) RA3(1, n) M3(0, 5) RA3(0, n) M3(2, 0) X M3(3, 0) RW3(0, 0, a0, n) M3(3, 1) RW3(0, 1, a0, n) M3(2, 1) RW3(1, 0, a1, n) M3(2, 2) RW3(1, 1, a1, n) \
  M3(3, 2) RW3(2, 0, a1, n) M3(3, 3) RW3(2, 1, a1, n) M3(2, 3) M3(4, 3) RW3(3, 0, a0, n) M3(4, 4) RW3(3, 1, a0, n) M3(2, 4) RH3(n)                         \
  M3(3, 4) RW3(4, 0, a0, n) M3(3, 5) RW3(4, 1, a0, n) LG(7) M3(2, 5) RW3(5, 0, a1, n)
#define STAGE3(d, X) H2_3(0, , , , ) H1_3(1, ) H2_3(1, ST3(a0, d), ST3(aA, d), ST3(a1, d) ST3(aH, d), "s_barrier\n\t") H1_3(0, X)
#define CV3(i, j, t) "v_cvt_pk_bf16_f32 v[104+2*" #t "], v[24*" #i "+4*" #j "-12*(" #i "/4)], v[24*" #i "+4*" #j "-12*(" #i "/4)+1]\n\tv_cvt_pk_bf16_f32 v[105+2*" #t "], v[24*" #i "+4*" #j "-12*(" #i "/4)+2], v[24*" #i "+4*" #j "-12*(" #i "/4)+3]\n\t"
#define WR3(i, t, a, o) "ds_write_b64 %[" #a "], v[104+2*" #t ":105+2*" #t "] offset:2048*" #i "+" #o "\n\t"
#define CW3(i, j, t, a, o) CV3(i, j, t) WR3(i, t, a, o)
#define V8(a) "v" #a "0", "v" #a "1", "v" #a "2", "v" #a "3", "v" #a "4", "v" #a "5", "v" #a "6", "v" #a "7"
#define V10(a) V8(a), "v" #a "8", "v" #a "9"
static_assert(K3_STG == 43008, "STAGE3");
#define M6(i, j, s) "v_mfma_f32_16x16x32_bf16 v[16*" #i "+4*(" #j "):16*" #i "+4*(" #j ")+3], v[80+32*" #s "+4*(" #j "):83+32*" #s "+4*(" #j ")], v[64+32*" #s "+4*" #i ":67+32*" #s "+4*" #i "], v[16*" #i "+4*(" #j "):16*" #i "+4*(" #j ")+3]\n\t"
#define RA6(i, s) "ds_read_b128 v[64+32*" #s "+4*" #i ":67+32*" #s "+4*" #i "], %[aa] offset:1024*" #i "\n\t"
#define RW6(h, b, l, s) "ds_read_b64_tr_b16 v[80+32*" #s "+8*(" #h ")+4*" #b "+2*" #l ":81+32*" #s "+8*(" #h ")+4*" #b "+2*" #l "], %[a" #b "] offset:4096*(" #h ")+2048*" #l "\n\t"
#define SEL6(m, d, w) "s_cselect_b32 %[" #d "], %[" #w "], 8192\n\t"
#define SH6(m) "s_add_u32 %[" #m "], %[" #m "], %[" #m "]\n\t"
#define TRIP6(h, o, n, x, X)                                                                                                                            \
  M6(0, 2*h, o) RW6(1-h, 0, 0, x) M6(0, 2*h, n) RW6(1-h, 0, 1, x) SH6(mw) M6(0, 2*h+1, n) SEL6(mw, dw, ww) RA6(0, x) M6(0, 2*h+1, o) RW6(1-h, 1, 0, x) LG(8)  \
  M6(1, 2*h+1, o) RW6(1-h, 1, 1, x) M6(1, 2*h+1, n) RA6(1, x) SH6(ma) M6(1, 2*h, n) SEL6(ma, da, wa) RA6(2, x) M6(1, 2*h, o) RA6(3, x)                      \
  M6(2, 2*h, o) RW6(h, 0, 0, x) "v_add_u32 %[aa], %[da], %[aa]\n\t" M6(2, 2*h, n) RW6(h, 0, 1, x) M6(2, 2*h+1, n) RW6(h, 1, 0, x) "v_add_u32 %[a0], %[dw], %[a0]\n\t" \
  M6(2, 2*h+1, o) RW6(h, 1, 1, x) M6(3, 2*h+1, o) "v_add_u32 %[a1], %[dw], %[a1]\n\t" X M6(3, 2*h+1, n) M6(3, 2*h, n) LG(6) M6(3, 2*h, o) "s_barrier\n\t"
__device__ __forceinline__ int w2_stage(int j) {
  const int j2 = j - 16, p = (j2 * 52) >> 8, r = j2 - p * 5;
  return j < 16 ? j * 112 * 2048 : (p * 224 + 32 + r * 32 + (r > 2 ? 32 : 0)) * 2048 + (r == 2);
}
__global__ __launch_bounds__(768, 3) __attribute__((amdgpu_num_sgpr(98))) void k2_block(const u16* __restrict__ x, const u16* __restrict__ wqkv, const u16* __restrict__ wo,
                                                   const u16* __restrict__ w1, const u16* __restrict__ w3, const u16* __restrict__ w2,
                                                   const u16* __restrict__ cnd, const u16* __restrict__ wmod, u16* __restrict__ out, char* __restrict__ ws) {
  extern __shared__ __attribute__((aligned(16))) char smem_[];
  LDS_PTR(char) smem = (LDS_PTR(char))smem_;
  const int tid = threadIdx.x;
  int lane = tid & 63;
  const int wave = __builtin_amdgcn_readfirstlane(tid >> 6);
  const unsigned lds_base = (unsigned)(uintptr_t)smem;
  const int b = blockIdx.x, xc = b & 7, ii = b >> 3;
  const int tm3 = (xc & 1) * 8 + (ii & 7), tn3 = (xc >> 1) * 4 + (ii >> 3);
  const int tm5 = (xc & 1) * 4 + (ii & 3), tn5 = (xc >> 1) * 8 + (ii >> 2);
  const int kh6 = xc >> 2, tm6 = (xc & 1) * 8 + (xc & 2) * 2 + (ii & 3), tn6 = ii >> 2;
  const int tm4 = tm6, tn4 = tn6 * 2 + kh6;
  const int u1 = (b >> 4) * 8 + xc;
  unsigned* cnt = (unsigned*)(ws + WS_CTL);
  unsigned long long* fl1 = (unsigned long long*)(ws + WS_FL1);
  STAMP_AT(0 );

  if (wave >= 8) {
    const int lw = wave - 8, P = b * 4 + lw, lane = lane_id();
    const bool isA = lw < 2;
    asm volatile("" ::"s"(wqkv), "s"(wo), "s"(w1), "s"(w3), "s"(w2), "s"(ws));
    int kpf = KB + VB3;
    unsigned pr[4] = {};
    auto touch = [&]() {
      if (kpf < 28 && __builtin_amdgcn_s_getreg((3 << 11) | (8 << 6) | 7) <= 7) {
        stouch4((const char*)((P & 1) ? w3 : w1) + (kpf * 2048 + (P >> 1) * 4) * 128);
        ++kpf;
      }
    };
    const char* epo = ws + WS_EPO;
    const unsigned swz = ((lane & 7) ^ ((lane >> 3) & 6)) * 16;
    const int cg = lw * 16 + tn4;
    auto issue4 = [&](int s, unsigned slot) {
      const char* p = (const char*)wo + tn4 * 128 + (lw * 32 + s * 128) * 2048;
      unsigned v4[4];
#pragma unroll
      for (int i = 0; i < 4; ++i) v4[i] = (lane >> 3) * 2048 + swz + i * 15360;
      glds16x4<false>(p, p, v4, lds_base + slot + lw * 4096);
    };
    auto run3 = [&](auto isA_) {
      constexpr bool A_ = decltype(isA_)::value; constexpr int N = A_ ? 9 : 12;
      const char* sp = A_ ? ws + WS_H + lw * 131072 + (tm3 * 8 - 1) * 1024 : (const char*)wqkv + tn3 * 128;
      unsigned vo[3][4];
#pragma unroll
      for (int i = 0; i < 12; ++i) {
        const int tb = (lw - 2) * 12 + i;
        vo[i >> 2][i & 3] = A_ ? lane * 16 + (i < 8 ? 1024 + (i >> 2) * 4096 : tm3 ? 0 : 1024) : ((tb & 7) * 8 + (lane >> 3)) * 6144 + (tb >> 3) * 2048 - (i & 3) * 1024 + swz;
      }
      const unsigned d0 = lds_base + (A_ ? lw * 9216 : K3_B + (lw - 2) * 12288);
      auto issue = [&](unsigned st) {
        if (A_) { glds16x4<true>(sp, sp, vo[0], d0 + st + 1024); glds16s(sp, vo[2][0], d0 + st, 0); glds16x4<true>(sp, sp, vo[1], d0 + st + 5120); }
        else {
#pragma unroll
          for (int i = 0; i < 3; ++i) glds16x4<false>(sp, sp, vo[i], d0 + st + i * 4096);
        }
        sp += A_ ? 262144 : 64 * 6144;
      };
      { unsigned long long ep;
        asm volatile("s_load_dwordx2 %0, %1, 0x0\n\ts_waitcnt lgkmcnt(0)" : "=s"(ep) : "s"(epo) : "memory");
        ep += 1;
        if (lw == 3) { *(LDS_PTR(u32x4))(smem + LDS_ARR) = (u32x4){0u, 0u, 0u, 0u}; wait_lgkm0(); }
        if (lw == 1) {
#pragma unroll
          for (int k = 0; k < 3; ++k) {
            const int i = b * 158 + (k * 64 + lane < 158 ? k * 64 + lane : 0);
            st16<true>(ws, i < 32768 ? (unsigned)WS_XCH + i * 16 : (unsigned)WS_CTL + (i - 32768) * 16, i < 32768 ? (u32x4){~0u, ~0u, ~0u, ~0u} : (u32x4){0u, 0u, 0u, 0u});
          }
        }
        BAR();
        auto flag = [&](int idx) { wait_vm<0>(); if (lane < 8) __hip_atomic_store(fl1 + lane * 256 + idx, ep, __ATOMIC_RELAXED, __HIP_MEMORY_SCOPE_AGENT); };
        if (lw == 1 && (b & 8)) {
          const int kq = u1 & 3, cgp = u1 >> 2;
          float sm = 0.f;
#pragma unroll
          for (int r = 0; r < 8; ++r) sm += *(const LDS_PTR(float))(smem + 8192 + (r * 64 + lane) * 4);
          __hip_atomic_store((unsigned*)(ws + WS_MOD) + kq * 2048 + cgp * 64 + lane, __float_as_uint(sm), __ATOMIC_RELAXED, __HIP_MEMORY_SCOPE_AGENT);
          flag(((cgp >> 3) & 1) * 64 + (cgp >> 4) * 32 + (cgp & 7) * 4 + kq);
        }
        if (b & 8) BAR();
        BAR();
        if (lw == 1 && !(b & 8)) flag(128 + u1);
        auto flags = [&](auto n_, int first) {
#pragma unroll 1
          for (unsigned spins = 0; spins < SPIN_CAP; ++spins) {
            bool ok = true;
#pragma unroll
            for (int k = 0; k < decltype(n_)::value; ++k) ok &= __hip_atomic_load(fl1 + xc * 256 + (first + k) * 64 + lane, __ATOMIC_RELAXED, __HIP_MEMORY_SCOPE_AGENT) == ep;
            if (__builtin_amdgcn_ballot_w64(!ok) == 0) break;
            __builtin_amdgcn_s_sleep(FS0);
          }
        };
        if (lw == 0) flags(IC<1>(), (b >> 3) & 1);
        BAR();
        if (lw == 1) {
          __builtin_amdgcn_s_sleep(FD1);
          flags(IC<4>(), 0);
          if (b == 0 && lane == 0) __hip_atomic_store((unsigned long long*)(ws + WS_EPO), ep, __ATOMIC_RELAXED, __HIP_MEMORY_SCOPE_AGENT);
        }
      }
      BAR();
      if (lw == 0) {
        signal(cnt, C2 + (u1 >> 3), lane, (b & 8) ? 1u : 0x10000u);
        pollp<PD2, PQ, 3>(cnt + ((C2 + ((lane & 1) && tm3 ? tm3 - 1 : tm3)) * 8 + (tn3 & 7)) * 32, 0x80000u, pr);
      }
      if (!A_) { issue(0); issue(K3_STG); }
      BAR();
      if (A_) { issue(0); issue(K3_STG); }
      wait_vm<N>();
      KEEP4(pr);
      BAR();
      issue(2 * K3_STG);
      unsigned st = 0;
#pragma unroll 1
      for (int j = 0; j < 13; ++j) {
        wait_vm<N>();
        BAR();
        issue(st);
        st = st == 2 * K3_STG ? 0 : st + K3_STG;
      }
      wait_vm<N>();
      BAR();
      issue4(0, 57344); issue4(5, 57344 + 5 * 16384);
      glds4((P & 1) ? w3 : w1, ((KB + ((lane >> 2) < VB3 ? lane >> 2 : 0)) * 2048 + (P >> 1) * 4 + (lane & 3)) * 128, lds_base + LDS_DUMMY);
      wait_vm<9>();
    };
    if (isA) run3(T_()); else run3(F__());
    { BAR();
      BAR();
      issue4(1, 57344 + 16384);
      BAR();
      issue4(2, 57344 + 2 * 16384); issue4(3, 57344 + 3 * 16384); issue4(4, 57344 + 4 * 16384);
      BAR();
      if (lw == 0) signal(cnt, C3 + tm3, lane);
      issue4(6, 0); issue4(7, 16384);
      if (lw == 0) pollp<PD3, PQ, 1>(cnt + ((C3 + tm4) * 8 + tn6) * 32, 16, pr);
      wait_vm<8>();
      BAR();
      wait_vm<0>();
      KEEP4(pr);
      BAR();
      poll<0, 4>(cnt + (CM * 8 + cg) * 32, 4);
      f32x4 pv[4];
#pragma unroll
      for (int i = 0; i < 4; ++i) pv[i] = __builtin_bit_cast(f32x4, ld16<true>(ws + WS_MOD2, (((lane >> 4) * 4 + i) * 4096 + cg * 64 + (lane & 15) * 4) * 4));
      f32x4 v = (pv[0] + pv[1]) + (pv[2] + pv[3]);
#pragma unroll
      for (int e = 0; e < 4; ++e) { v[e] = rbf(xsum16_32(v[e])); if (lw == 2) v[e] = rbf(1.f + v[e]); if (lw == 1 || lw == 2) v[e] *= -1.4426950408889634f; }
      *(LDS_PTR(f32x4))(smem + LDS_VEC + lw * 256 + (lane & 15) * 16) = v;
      wait_lgkm0();
    }
    auto run5 = [&](auto isA_) {
      constexpr bool A_ = decltype(isA_)::value; constexpr int N = A_ ? 8 : 7;
      const char* sp = A_ ? ws + WS_H2 + (tm5 * 16 + lw * 8) * 1024 : (const char*)(lw == 2 ? w1 : w3) + tn5 * 224;
      unsigned vp[N];
#pragma unroll
      for (int i = 0; i < N; ++i) { const int L = i * 64 + lane, r = L / 14, c = L - r * 14; vp[i] = A_ ? L * 16 : r * (F_ * 2) + c * 16; }
      const unsigned d0 = lds_base + (A_ ? lw * 8192 : lw == 2 ? K5_G : K5_U);
      unsigned st = 3 * K5_STG;
      auto issue = [&]() {
#pragma unroll
        for (int i = 0; i < N; ++i) { if (A_) glds16s(sp, vp[i], d0 + st, i * 1024); else glds16(sp, vp[i], d0 + st, i * 1024); }
        sp += A_ ? 131072 : 32 * F_ * 2; st = st == (K5_NS - 1) * K5_STG ? 0 : st + K5_STG;
      };
      BAR();
      if (!A_) { issue(); issue(); }
      BAR();
      u32x4 xk[4];
      auto keep = [&]() {
#pragma unroll
        for (int i = 0; i < 4; ++i) { xk[i] = *(const LDS_PTR(u32x4))(smem + lw * 4096 + i * 1024 + lane * 16); asm volatile("" : "+v"(xk[i])); }
      };
      if (A_ && lw == 0) { signal(cnt, C4 + tm4, lane); keep(); pollp<PD4, PQ, 3>(cnt + ((C4 + tm5 * 2 + (lane & 1)) * 8 + (tn5 & 7)) * 32, 16, pr); }
      else { keep(); if (!A_) { issue(); issue(); issue(); } }
      BAR();
      if (A_) { issue(); issue(); }
      wait_vm<A_ ? N : 4 * N>();
      KEEP4(pr);
      BAR();
      if (A_) issue();
      wait_vm<A_ ? N : 3 * N>();
      BAR();
      if (A_) { issue(); issue(); }
#pragma unroll 1
      for (int j = 0; j < 27; ++j) {
        wait_vm<2 * N>();
        BAR();
        issue();
        if (j == 6) {
#pragma unroll
          for (int i = 0; i < 4; ++i) st16<true>(ws + WS_PART, (b * 4 + lw) * 4096 + i * 1024 + lane * 16, (u32x4){~0u, ~0u, ~0u, ~0u});
        }
        if (j == 12 && lw == 0 && lane == 0) __hip_atomic_store((unsigned*)(ws + WS_FLG) + b * 32, 16u, __ATOMIC_RELAXED, __HIP_MEMORY_SCOPE_AGENT);
        touch(); __builtin_amdgcn_s_sleep(TS5); touch();
      }
      const char* sb = A_ ? ws + WS_HH + (kh6 * 56 * 128 + tm6 * 8 + lw * 4) * 1024 : (const char*)w2 + kh6 * 1792 * 2048 + tn6 * 256 + ((lw ^ kh6 ^ 1) & 1) * 128;
      unsigned vo[4];
#pragma unroll
      for (int i = 0; i < 4; ++i) {
        const int r = i * 8 + (lane >> 3), k = 8 * ((r >> 2) & 3) + 4 * (r >> 4) + (r & 3);
        vo[i] = A_ ? lane * 16 : k * 2048 - i * 1024 + swz;
      }
      const unsigned d6 = lds_base + (A_ ? K6_A : 0) + (lw & 1) * 4096;
      st = 0;
      const int tab = w2_stage(lane);
      int jn = 0;
      auto issue6 = [&]() {
        if (A_) { glds16x4<true>(sb, sb, vo, d6 + st); sb += 131072; }
        else { const int o = __builtin_amdgcn_readlane(tab, jn); const char* s0 = sb + (o & ~1); glds16x4<false>(s0, s0 + (o & 1) * 65536, vo, d6 + st); ++jn; }
        st = st == ((A_ ? K6_NA : K6_NW) - 1) * 8192 ? 0 : st + 8192;
      };
      auto pre6 = [&](int n) {
        if (!A_) {
#pragma unroll 1
          for (int i = 0; i < n; ++i) issue6();
        }
      };
      constexpr int E = A_ ? 0 : 4;
      { const char* p = (const char*)w2 + (kh6 * 1792 + ii) * 2048;
        const int n = lw * 56 + (lane < 56 ? lane : 0), o = w2_stage((xc & 3) * 14 + (n >> 4));
        glds4(p, (o & ~1) + (o & (ii >> 4) & 1) * 65536 + (n & 15) * 128, lds_base + LDS_DUMMY); }
      wait_vm<2 * N + 1>(); BAR();
      wait_vm<N + 1>(); BAR(); pre6(PA6);
      wait_vm<PA6 * E + 1>(); BAR(); pre6(PB6);
      BAR(); pre6(12 - PA6 - PB6);
      BAR();
      if (A_) {
        volatile LDS_PTR(unsigned) go = (volatile LDS_PTR(unsigned))(smem + LDS_ARR + 4);
        if (lw == 0) { pollp<PD5, PQ5, 1>(cnt + ((C5 + (tm6 >> 1) * 4 + kh6 * 2) * 8 + tn6) * 32, 16, pr); *go = 1u; }
        else {
#pragma unroll 1
          for (unsigned spins = 0; spins < SPIN_CAP && __builtin_amdgcn_readfirstlane(*go) == 0; ++spins) __builtin_amdgcn_s_sleep(1);
          asm volatile("" ::: "memory");
        }
        STAMP_L(31);
        issue6(); issue6(); issue6();
        wait_vm<8>(); KEEP4(pr); BAR(); issue6(); issue6();
        wait_vm<12>(); BAR(); issue6(); issue6();
      } else { wait_vm<16>(); BAR(); BAR(); }
#pragma unroll 1
      for (int c = 2; c < 51; ++c) { wait_vm<16>(); BAR(); if (A_ || !((SKIP6 >> c) & 1)) issue6(); }
      wait_vm<16>(); BAR(); wait_vm<12>(); BAR(); wait_vm<8>(); BAR(); wait_vm<4>(); BAR(); wait_vm<0>(); BAR();
#pragma unroll
      for (int i = 0; i < 4; ++i) *(LDS_PTR(u32x4))(smem + K6_X + lw * 4096 + i * 1024 + lane * 16) = xk[i];
      BAR(); wait_lgkm0(); BAR();
    };
    if (isA) run5(T_()); else run5(F__());
    asm volatile("s_waitcnt lgkmcnt(0)" ::: "memory");
    return;
  }

  int fr = lane & 15, g = lane >> 4, p4 = fr & 3, row = 4 * g + (fr >> 2);
  int lsw = row * 128 + (((row >> 1) & 3) * 32) + p4 * 8;
  { const int mb = u1, hf = (b >> 3) & 1, kb0 = hf * 16 + wave * 2, cofs = (g & 1) * 16 + (g >> 1) * 8;
    u32x4 v[4];
    auto ldx = [&]() {
#pragma unroll
      for (int p = 0; p < 4; ++p) v[p] = *(const u32x4*)(x + (size_t)(mb * 16 + fr) * D_ + ((kb0 + (p & 1)) ^ ((p >> 1) * 16)) * 32 + cofs);
    };
    LDS_PTR(f32x2) sred = (LDS_PTR(f32x2))(smem + 4096);
    auto up2 = [](unsigned u) { return (f32x2){lo2f(u), hi2f(u)}; };
    auto stats = [&]() {
      f32x2 s2 = {0.f, 0.f}, q2 = {0.f, 0.f};
#pragma unroll
      for (int k = 0; k < 16; ++k) s2 += up2(v[k >> 2][k & 3]);
      const float mw = xsum16_32(s2[0] + s2[1]) * (1.f / 128.f);
#pragma unroll
      for (int k = 0; k < 16; ++k) { const f32x2 d = up2(v[k >> 2][k & 3]) - mw; q2 += d * d; }
      const float qw = xsum16_32(q2[0] + q2[1]);
      if (lane < 16) sred[wave * 16 + fr] = (f32x2){mw, qw};
      wait_lgkm0();
    };
    auto rest = [&]() {
    STAMP_AT(1 );
    float mean = 0.f, m2 = 0.f; f32x2 sw[8];
#pragma unroll
    for (int w = 0; w < 8; ++w) { sw[w] = sred[w * 16 + fr]; mean += sw[w][0]; m2 += sw[w][1]; }
    mean *= 0.125f;
#pragma unroll
    for (int w = 0; w < 8; ++w) { const float d = sw[w][0] - mean; m2 += 128.f * d * d; }
    const float rstd = ln_rstd(m2 * (1.f / 1024.f));
    mean = rbf(mean);
    unsigned nv[8];
#pragma unroll
    for (int k = 0; k < 8; ++k) {
      const f32x2 d = up2(v[k >> 2][k & 3]) - mean;
      const f32x2 n = up2(pk2(d[0], d[1])) * rstd;
      nv[k] = pk2(n[0], n[1]);
      asm volatile("" : "+v"(nv[k]));
    }
    SCHED0(); BAR(); SCHED0();
    STAMP_AT(2 );
    float mp[8];
#pragma unroll
    for (int i = 0; i < 8; ++i) mp[i] = __uint_as_float(__hip_atomic_load((unsigned*)(ws + WS_MOD) + hf * 512 + tid + i * 1024, __ATOMIC_RELAXED, __HIP_MEMORY_SCOPE_AGENT));
    *(LDS_PTR(unsigned))(smem + tid * 4) = pk2((mp[0] + mp[2]) + (mp[4] + mp[6]), 1.f + rbf((mp[1] + mp[3]) + (mp[5] + mp[7])));
    wait_lgkm0();
#pragma unroll
    for (int p = 0; p < 2; ++p) {
      const LDS_PTR(u32x4) tp = (const LDS_PTR(u32x4))(smem + ((wave * 2 + p) * 32 + cofs) * 4);
      const u32x4 t0 = tp[0], t1 = tp[1];
      unsigned o[4];
#pragma unroll
      for (int j = 0; j < 4; ++j) {
        const unsigned ta = j < 2 ? t0[2 * j] : t1[2 * j - 4], tb = j < 2 ? t0[2 * j + 1] : t1[2 * j - 3];
        const unsigned u = pk2(lo2f(nv[p * 4 + j]) * hi2f(ta), hi2f(nv[p * 4 + j]) * hi2f(tb));
        o[j] = pk2(lo2f(u) + lo2f(ta), hi2f(u) + lo2f(tb));
      }
      auto s0 = __builtin_amdgcn_permlane16_swap(o[0], o[2], false, false);
      auto s1 = __builtin_amdgcn_permlane16_swap(o[1], o[3], false, false);
      st16<true>(ws + WS_H, ((kb0 + p) * 128 + mb) * 1024 + lane * 16, (u32x4){s0[0], s1[0], s0[1], s1[1]});
    }
    STAMP_AT(3 );
    wait_vm<0>();
    STAMP_AT(4 );
    BAR();
    STAMP_AT(5 );
    };
    if (hf) {
      const int mrow = (u1 & 3) * 256 + (tid >> 3) * 4;
      const u32x2 c2 = *(const u32x2*)(cnd + mrow);
      u32x4 mw[4];
#pragma unroll
      for (int i = 0; i < 4; ++i) mw[i] = __builtin_nontemporal_load((const u32x4*)(wmod + (size_t)(mrow + i) * 6144 + (u1 >> 2) * 64 + (lane & 7) * 8));
      const unsigned mc[2] = {c2[0], c2[1]};
      *(LDS_PTR(f32x2))(smem + 8192 + (wave * 64 + (lane & 7) * 8 + (lane >> 4) * 2) * 4) = slab_sum<4>(mc, mw);
      wait_lgkm0();
      BAR();
      BAR();
      ldx();
      stats();
      BAR();
      rest();
    } else {
      __builtin_amdgcn_s_sleep(XD0);
      ldx();
      SCHED0();
      const int sl = (3 - ((unsigned)(uintptr_t)wqkv >> 7) - (xc >> 1) * 4) & 7;
      const int i2 = tid - 384, r0 = i2 < 0 ? tid / 12 : i2 / 3, c0 = i2 < 0 ? tid - r0 * 12 : (i2 - r0 * 3) * 4 + sl;
      const bool on = i2 < 0 || (sl < 4 && i2 < 96);
      __builtin_amdgcn_s_sleep(PFD);
      const unsigned pf = *(const unsigned*)((const char*)wqkv + (xc >> 1) * 512 + (on ? ((((xc & 1) ^ (i2 >= 0)) * 512 + (ii >> 1) * 32 + r0) * 48 + (c0 >> 2) * 16 + (c0 & 3)) * 128 : 0));
      SCHED0(); BAR(); SCHED0();
      stats();
      SCHED0(); BAR(); SCHED0();
      rest();
      asm volatile("" ::"v"(pf));
    }
  }
  {
    f32x4 bias[2];
    { const int kbase = tm3 * 128 + wave * 16 - 16;
#pragma unroll
      for (int t = 0; t < 4; ++t) {
        const int kk = (t >> 1) * 16 + 4 * g + (t & 1) * 2 + (fr & 1), df = 16 + fr - kk;
        bias[t >> 1][(t & 1) * 2] = bias[t >> 1][(t & 1) * 2 + 1] = (df >= 0 && df <= 14 && kbase + kk >= 0) ? 0.f : -__builtin_inff();
      }
      asm volatile("" : "+v"(bias[0]), "+v"(bias[1])); }
    u32x4 mc, mw[8];
    if (wave < 4) {
      const int wm = wave & 1, bA = wave;
      unsigned a0 = lds_base + K3_B + (lsw ^ (bA * 32)), a1 = lds_base + K3_B + (lsw ^ ((bA ^ 1) * 32)), aA = lds_base + 1024 + wm * 4096 + lane * 16, aH = lds_base + lane * 16;
      unsigned lq = lds_base + bA * 512 + lane * 8, lv = lds_base + 36864 + bA * 512 + (lane ^ ((g >> 1) * 8)) * 8;
      int n = 4;
      asm volatile(".irp r,0,8,16,24,32,40,48,56,64,72,80,88,96\n\tv_mov_b64 v[\\r:\\r+1], 0\n\tv_mov_b64 v[\\r+2:\\r+3], 0\n\tv_mov_b64 v[\\r+4:\\r+5], 0\n\tv_mov_b64 v[\\r+6:\\r+7], 0\n\t.endr\n\t"
                   "s_setprio 2\n\ts_waitcnt vmcnt(0)\n\ts_barrier\n\t"
                   "s_barrier\n\t"
                   RA3(1, 0) RA3(0, 0) RW3(0, 0, a0, 0) RW3(0, 1, a0, 0) RW3(1, 0, a1, 0) RW3(1, 1, a1, 0) RW3(2, 0, a1, 0) RW3(2, 1, a1, 0) RW3(3, 0, a0, 0) RW3(3, 1, a0, 0) RH3(0)
                   RW3(4, 0, a0, 0) RW3(4, 1, a0, 0) RW3(5, 0, a1, 0) LG(8)
                   ".p2align 5\n1:\n\t" STAGE3(43008, ) STAGE3(43008, ) STAGE3(-86016, "s_sub_u32 %[n], %[n], 1\n\t") "s_cbranch_scc0 1b\n\t"
                   H2_3(0, , , , ) H1_3(1, )
                   H2_3(1, "v_add_u32 %[a0], %[mo], %[lq]\n\t", "v_add_u32 %[aA], %[mo], %[lv]\n\t", "v_xor_b32 %[a1], 512, %[a0]\n\tv_xor_b32 %[aH], 512, %[aA]\n\t", "s_barrier\n\t")
                   M3(1, 5) M3(0, 5) M3(2, 0) CV3(0, 0, 0) M3(3, 0) WR3(0, 0, a0, 0) CV3(1, 0, 1) M3(3, 1) WR3(1, 1, a0, 0) CV3(0, 1, 2) M3(2, 1) WR3(0, 2, a1, 0) CV3(1, 1, 3)
                   M3(2, 2) WR3(1, 3, a1, 0) CV3(0, 2, 0) M3(3, 2) WR3(0, 0, a1, 18432) CV3(1, 2, 1) M3(3, 3) WR3(1, 1, a1, 18432) CV3(0, 3, 2) M3(2, 3) WR3(0, 2, a0, 18432) CV3(1, 3, 3)
                   M3(4, 3) WR3(1, 3, a0, 18432) CV3(0, 4, 0) M3(4, 4) WR3(0, 0, aA, 0) CV3(1, 4, 1) M3(2, 4) WR3(1, 1, aA, 0) CV3(0, 5, 2) M3(3, 4) WR3(0, 2, aH, 0) CV3(1, 5, 3)
                   M3(3, 5) WR3(1, 3, aH, 0) CV3(2, 0, 0) M3(2, 5) WR3(2, 0, a0, 0)
                   CW3(3, 0, 1, a0, 0) CW3(3, 1, 2, a1, 0) CW3(2, 1, 3, a1, 0) CW3(2, 2, 0, a1, 18432) CW3(3, 2, 1, a1, 18432) CW3(3, 3, 2, a0, 18432) CW3(2, 3, 3, a0, 18432)
                   CV3(4, 3, 0) WR3(0, 0, lq, 18432) CV3(4, 4, 1) WR3(0, 1, lv, 0) CW3(2, 4, 2, aA, 0) CW3(3, 4, 3, aA, 0) CW3(3, 5, 0, aH, 0) CW3(2, 5, 1, aH, 0)
                   "s_setprio 0\n\t" LG(0)
                   : [a0] "+v"(a0), [a1] "+v"(a1), [aA] "+v"(aA), [aH] "+v"(aH), [n] "+s"(n)
                   : [lq] "v"(lq), [lv] "v"(lv), [mo] "s"((wm * 4 + 1) * 2048)
                   : "memory", "scc", V10(), V10(1), V10(2), V10(3), V10(4), V10(5), V10(6), V10(7), V10(8), V10(9), V10(10), V10(11), V10(12), V10(13), V8(14));
    } else {
      wait_vm<0>();
      BAR(); BAR();
      int k = -4; const int P = b * 4 + (wave & 3);
      auto idle = [&]() {
        if (k < KB && __builtin_amdgcn_s_getreg((3 << 11) | (8 << 6) | 7) <= 7) {
          stouch4(k < 0 ? (const char*)wo + ((k + 4) * 4096 + P * 4) * 128 : (const char*)((P & 1) ? w3 : w1) + (k * 2048 + (P >> 1) * 4) * 128); ++k; }
        BAR();
      };
      {
        const unsigned* pc = cnt + ((C2 + ((lane & 1) && tm3 ? tm3 - 1 : tm3)) * 8 + (tn3 & 7)) * 32;
        unsigned seen = 0x80008u;
        idle(); idle(); idle();
        if (wave == 4) asm volatile("global_load_dword %0, %1, off sc1" : "=v"(seen) : "v"(pc) : "memory");
        idle(); idle();
        asm volatile("s_waitcnt vmcnt(0)" : "+v"(seen)::"memory");
        if (__builtin_amdgcn_ballot_w64(seen < 0x80008u) != 0) poll<0>(pc, 0x80008u);
      }
#pragma unroll 1
      for (int t = 5; t < SLB; ++t) { if (t < 14) idle(); else BAR(); }
      wait_lgkm0();
      const int mrow = ((b & 3) * 4 + (wave & 3)) * 64 + (lane >> 3) * 8;
      mc = *(const u32x4*)(cnd + mrow);
#pragma unroll
      for (int i = 0; i < 8; ++i) mw[i] = __builtin_nontemporal_load((const u32x4*)(wmod + (size_t)(mrow + i) * 6144 + 2048 + (b >> 2) * 64 + (lane & 7) * 8));
      SCHED0();
#pragma unroll
      for (int t = SLB; t < 16; ++t) BAR();
    }
    lane = lane_id(); fr = lane & 15; g = lane >> 4; p4 = fr & 3; row = 4 * g + (fr >> 2); lsw = row * 128 + (((row >> 1) & 3) * 32) + p4 * 8;
    wait_lgkm0();
    BAR();
    STAMP_AT(9 );
    const LDS_PTR(char) eb = smem + wave * 2048;
    auto ld2 = [&](int o) { return cat8(*(const LDS_PTR(bf16x4))(eb + o), *(const LDS_PTR(bf16x4))(eb + o + 512)); };
    const int vo = 36864 + ((p4 * 16 + row) ^ ((p4 >> 1) * 8)) * 8;
    bf16x8 qf[2], k0[2], k1[2], v0[2], v1[2];
#pragma unroll
    for (int hd = 0; hd < 2; ++hd) { qf[hd] = ld2(2048 + hd * 1024 + lane * 8); k0[hd] = ld2(18432 + hd * 1024 + lane * 8); k1[hd] = ld2(18432 + 2048 + hd * 1024 + lane * 8); }
#pragma unroll
    for (int hd = 0; hd < 2; ++hd) {
      v0[hd] = cat8(ld_tr4(eb + vo + hd * 1024), ld_tr4(eb + vo + hd * 1024 + 2048));
      v1[hd] = cat8(ld_tr4(eb + vo + hd * 1024 + 512), ld_tr4(eb + vo + hd * 1024 + 512 + 2048));
    }
    SCHED0();
    const f32x4 z = {0.f, 0.f, 0.f, 0.f};
    const bool odd = fr & 1;
    const unsigned sel0 = odd ? 0x01000c0cu : 0x0c0c0100u, sel1 = odd ? 0x03020c0cu : 0x0c0c0302u;
    float inf = __builtin_inff(); asm("" : "+v"(inf));
    auto mx2 = [&](float a, float c) { return __builtin_amdgcn_fmed3f(a, c, inf); };
    auto both = [](float a, float c, auto op, auto fin) {
      auto u = __builtin_amdgcn_permlane16_swap(__float_as_uint(a), __float_as_uint(c), false, false);
      float r = op(__uint_as_float(u[0]), __uint_as_float(u[1]));
      auto w = __builtin_amdgcn_permlane32_swap(__float_as_uint(r), __float_as_uint(r), false, false);
      r = fin(op(__uint_as_float(w[0]), __uint_as_float(w[1])));
      auto y = __builtin_amdgcn_permlane16_swap(__float_as_uint(r), __float_as_uint(r), false, false);
      return (f32x2){__uint_as_float(y[0]), __uint_as_float(y[1])};
    };
    f32x2 l[2][2];
#pragma unroll
    for (int hd = 0; hd < 2; ++hd) {
      const f32x4 s0 = __builtin_amdgcn_mfma_f32_16x16x32_bf16(k0[hd], qf[hd], bias[0], 0, 0, 0), s1 = __builtin_amdgcn_mfma_f32_16x16x32_bf16(k1[hd], qf[hd], bias[1], 0, 0, 0);
#pragma unroll
      for (int t = 0; t < 2; ++t) {
        const f32x4 sv = t ? s1 : s0;
        unsigned u = pk2(odd ? sv[1] : sv[0], odd ? sv[3] : sv[2]);
        u = pk2(lo2f(u) * 0.17677669529663687f, hi2f(u) * 0.17677669529663687f);
        l[hd][t] = (f32x2){lo2f(u), hi2f(u)};
      }
    }
    const f32x2 mx = both(mx2(mx2(l[0][0][0], l[0][0][1]), mx2(l[0][1][0], l[0][1][1])), mx2(mx2(l[1][0][0], l[1][0][1]), mx2(l[1][1][0], l[1][1][1])), mx2,
                          [](float r) { return r * -1.4426950408889634f; });
    float sm[2];
#pragma unroll
    for (int hd = 0; hd < 2; ++hd) {
#pragma unroll
      for (int t = 0; t < 2; ++t) { const f32x2 e = l[hd][t] * 1.4426950408889634f + mx[hd]; l[hd][t] = (f32x2){__builtin_amdgcn_exp2f(e[0]), __builtin_amdgcn_exp2f(e[1])}; }
      const f32x2 a = l[hd][0] + l[hd][1]; sm[hd] = a[0] + a[1];
    }
    const f32x2 inv = both(sm[0], sm[1], [](float a, float c) { return a + c; }, [](float r) { return __builtin_amdgcn_rcpf(r); });
#pragma unroll
    for (int hd = 0; hd < 2; ++hd) {
      u32x4 pp;
#pragma unroll
      for (int t = 0; t < 2; ++t) {
        const f32x2 p = l[hd][t] * inv[hd]; const unsigned u = pk2(p[0], p[1]);
        pp[2 * t] = __builtin_amdgcn_perm(u, u, sel0); pp[2 * t + 1] = __builtin_amdgcn_perm(u, u, sel1);
      }
      const bf16x8 pf = __builtin_bit_cast(bf16x8, pp);
      const f32x4 oa = __builtin_amdgcn_mfma_f32_16x16x32_bf16(v0[hd], pf, z, 0, 0, 0), ob = __builtin_amdgcn_mfma_f32_16x16x32_bf16(v1[hd], pf, z, 0, 0, 0);
      u32x4 ov; ov[0] = pk2(oa[0], oa[1]); ov[1] = pk2(oa[2], oa[3]); ov[2] = pk2(ob[0], ob[1]); ov[3] = pk2(ob[2], ob[3]);
      st16<true>(ws + WS_O, ((tn3 * 2 + hd) * 128 + tm3 * 8 + wave) * 1024 + lane * 16, ov);
    }
    STAMP_AT(10 );
    wait_vm<0>();
    STAMP_AT(11 );
    BAR();
    STAMP_AT(12 );
    if (wave >= 4) {
      const unsigned m4[4] = {mc[0], mc[1], mc[2], mc[3]};
      const f32x2 ps = slab_sum<8>(m4, mw);
      if (!(lane & 8))
        __hip_atomic_store((unsigned long long*)(ws + WS_MOD2) + (((b & 3) * 4 + (wave & 3)) * 4096 + (b >> 2) * 64 + (lane & 7) * 8 + (lane >> 4) * 2) / 2,
                           ((unsigned long long)__float_as_uint(ps[1]) << 32) | __float_as_uint(ps[0]), __ATOMIC_RELAXED, __HIP_MEMORY_SCOPE_AGENT);
      wait_vm<0>();
      if (lane == 0 && __hip_atomic_fetch_add((LDS_PTR(unsigned))(smem + LDS_ARR + 8), 1u, __ATOMIC_RELAXED, __HIP_MEMORY_SCOPE_WORKGROUP) == 3)
        __hip_atomic_fetch_add(cnt + (CM * 8 + (b >> 2)) * 32, 1u, __ATOMIC_RELAXED, __HIP_MEMORY_SCOPE_AGENT);
    }
  }
  { const int wm = wave & 3, ks = wave >> 2, mb = tm4 * 8 + wm * 2 + ks;
    constexpr int R = 6;
    u32x4 rx[2];
#pragma unroll
    for (int p = 0; p < 2; ++p) rx[p] = *(const u32x4*)(x + (size_t)(mb * 16 + fr) * D_ + tn4 * 64 + p * 32 + (g & 1) * 16 + (g >> 1) * 8);
    const unsigned va0 = (ks * 128 + mb) * 1024 + lane * 16, va1 = va0 ^ 1024;
    bf16x8 fa[R][2], fw[2][4];
    auto issueA = [&](int t) {
      const char* pa = ws + WS_O + t * 262144;
      fa[t % R][0] = __builtin_bit_cast(bf16x8, ld16<true>(pa, va0));
      fa[t % R][1] = __builtin_bit_cast(bf16x8, ld16<true>(pa, va1));
    };
    unsigned aw[4];
    auto rdw = [&](int t, int j) {
      const int o = (t & 3) * 8192;
      fw[t & 1][j] = cat8(ld_tr4(smem + aw[j] + o), ld_tr4(smem + aw[j] + o + 2048));
    };
    auto stepw = [&](int t) {
      const int gi = t >> 2, base = gi < 3 ? 57344 + gi * 32768 : 0;
#pragma unroll
      for (int j = 0; j < 4; ++j) { aw[j] = base + ks * 4096 + (lsw ^ (j * 32)); asm volatile("" : "+v"(aw[j])); }
    };
    auto up2 = [](unsigned u) { return (f32x2){lo2f(u), hi2f(u)}; };
    f32x2 f[8];
    unsigned slot = (mb * 256 + fr + kh6 * 32768 + 4 * g * 16) * 8, sown = (mb * 256 + fr + (g & 1) * 32768 + tn4 * 16) * 8;
    f32x4 acc[2][4];
#pragma unroll
    for (int i = 0; i < 2; ++i)
#pragma unroll
      for (int j = 0; j < 4; ++j) acc[i][j] = (f32x4){0.f, 0.f, 0.f, 0.f};
    stepw(0);
    wait_lgkm0();
    SCHED0(); BAR(); SCHED0();
    STAMP_AT(13 );
#pragma unroll
    for (int t = 0; t < R; ++t) issueA(t);
#pragma unroll
    for (int j = 0; j < 4; ++j) rdw(0, j);
    SCHED0();
#pragma unroll
    for (int t = 0; t < 16; ++t) {
      if (t < 15 && ((t + 1) & 3) == 0) stepw(t + 1);
#pragma unroll
      for (int j = 0; j < 4; ++j) {
        if (t < 15) rdw(t + 1, j);
        SCHED0();
#pragma unroll
        for (int i = 0; i < 2; ++i) acc[i][j] = __builtin_amdgcn_mfma_f32_16x16x32_bf16(fw[t & 1][j], fa[t % R][i], acc[i][j], 0, 0, 0);
        SCHED0();
      }
      if (t + R < 16) issueA(t + R);
      if (t == 2) {
#pragma unroll
        for (int p = 0; p < 2; ++p) {
          auto s0 = __builtin_amdgcn_permlane16_swap(rx[p][0], rx[p][2], false, false);
          auto s1 = __builtin_amdgcn_permlane16_swap(rx[p][1], rx[p][3], false, false);
          f[4 * p] = up2(s0[0]); f[4 * p + 1] = up2(s1[0]); f[4 * p + 2] = up2(s0[1]); f[4 * p + 3] = up2(s1[1]);
        }
#pragma unroll
        for (int j = 0; j < 8; ++j) asm volatile("" : "+v"(f[j]));
        asm volatile("" : "+v"(slot), "+v"(sown));
      }
      if (t == 5) BAR();
      SCHED0();
    }
    STAMP_AT(14 );
    LDS_PTR(f32x4) red = (LDS_PTR(f32x4))(smem + K4_RED);
#pragma unroll
    for (int j = 0; j < 4; ++j) red[((wave ^ 4) * 4 + j) * 64 + lane] = acc[1][j];
    wait_lgkm0();
    BAR();
    STAMP_AT(15 );
    const LDS_PTR(float) vec = (const LDS_PTR(float))(smem + LDS_VEC);
    unsigned o[8];
    f32x4 pr[4], ga[4];
#pragma unroll
    for (int j = 0; j < 4; ++j) { pr[j] = red[(wave * 4 + j) * 64 + lane]; ga[j] = *(const LDS_PTR(f32x4))(vec + j * 16 + 4 * g); }
    SCHED0();
#pragma unroll
    for (int j = 0; j < 4; ++j) {
      const f32x4 y = acc[0][j] + pr[j];
      const f32x2 lo = y.xy * ga[j].xy + f[2 * j], hi = y.zw * ga[j].zw + f[2 * j + 1];
      o[2 * j] = pk2(lo[0], lo[1]); o[2 * j + 1] = pk2(hi[0], hi[1]);
      f[2 * j] = up2(o[2 * j]); f[2 * j + 1] = up2(o[2 * j + 1]);
    }
    const f32x2 s2 = ((f[0] + f[1]) + (f[2] + f[3])) + ((f[4] + f[5]) + (f[6] + f[7]));
    float mt = xsum16_32(s2[0] + s2[1]) * (1.f / 64.f); asm("" : "+v"(mt));
    f32x2 q2 = {0.f, 0.f}, q3 = {0.f, 0.f};
#pragma unroll
    for (int j = 0; j < 8; j += 2) { f[j] -= mt; f[j + 1] -= mt; q2 += f[j] * f[j]; q3 += f[j + 1] * f[j + 1]; }
    q2 += q3;
    const float qq = xsum16_32(q2[0] + q2[1]);
    if (lane < 32) {
      const unsigned qb = qq == qq ? __float_as_uint(qq) : 0x7fc00000u;
      __hip_atomic_store((unsigned long long*)(ws + WS_XCH + sown), ((unsigned long long)qb << 32) | __float_as_uint(mt), __ATOMIC_RELAXED, __HIP_MEMORY_SCOPE_AGENT);
    }
    *(LDS_PTR(u32x4))(smem + (mb & 7) * 2048 + lane * 16) = (u32x4){o[0], o[1], o[2], o[3]};
    *(LDS_PTR(u32x4))(smem + (mb & 7) * 2048 + 1024 + lane * 16) = (u32x4){o[4], o[5], o[6], o[7]};
    STAMP_AT(16 );
    f32x4 ss[4], gg[4];
#pragma unroll
    for (int j = 0; j < 4; ++j) {
      ss[j] = *(const LDS_PTR(f32x4))(vec + 64 + j * 16 + 4 * g); gg[j] = *(const LDS_PTR(f32x4))(vec + 128 + j * 16 + 4 * g);
      f[2 * j] *= gg[j].xy; f[2 * j + 1] *= gg[j].zw;
      asm volatile("" : "+v"(ss[j]), "+v"(gg[j]), "+v"(f[2 * j]), "+v"(f[2 * j + 1]));
    }
    unsigned long long pv[4];
    __builtin_amdgcn_s_sleep(XS0);
    for (unsigned spins = 0;; ++spins) {
      bool ok = true;
#pragma unroll
      for (int i = 0; i < 4; ++i) { pv[i] = __hip_atomic_load((unsigned long long*)(ws + WS_XCH + slot) + i * 16, __ATOMIC_RELAXED, __HIP_MEMORY_SCOPE_AGENT); ok &= (unsigned)(pv[i] >> 32) != 0xffffffffu; }
      if (__builtin_amdgcn_ballot_w64(!ok) == 0 || spins > SPIN_CAP) break;
      __builtin_amdgcn_s_sleep(XS1);
    }
    STAMP_AT(17 );
    float ms = 0.f, m2 = 0.f;
#pragma unroll
    for (int i = 0; i < 4; ++i) { ms += __uint_as_float((unsigned)pv[i]); m2 += __uint_as_float((unsigned)(pv[i] >> 32)); }
    const float mean = xsum16_32(ms) * (1.f / 16.f);
#pragma unroll
    for (int i = 0; i < 4; ++i) { const float d = __uint_as_float((unsigned)pv[i]) - mean; m2 += 64.f * d * d; }
    const float rstd = ln_rstd(xsum16_32(m2) * (1.f / 1024.f));
    const float sh = (mt - rbf(mean)) * rstd;
#pragma unroll
    for (int p = 0; p < 2; ++p) {
      u32x4 v;
#pragma unroll
      for (int j = 0; j < 4; ++j) {
        const int jb = 2 * p + (j >> 1);
        const f32x2 h = f[4 * p + j] * rstd + (((j & 1) ? gg[jb].zw : gg[jb].xy) * sh + ((j & 1) ? ss[jb].zw : ss[jb].xy));
        v[j] = pk2(h[0], h[1]);
      }
      st16<true>(ws + WS_H2, ((tn4 * 2 + p) * 128 + mb) * 1024 + lane * 16, v);
    }
    STAMP_AT(18 );
    wait_vm<0>();
    wait_lgkm0();
    BAR();
    STAMP_AT(19 );

  }
  const int grp = wave >> 2;
  { const int wm = wave & 3;
    const LDS_PTR(char) sb = smem + 3 * K5_STG;
    const LDS_PTR(char) X = sb + K5_G + row * 224 + grp * 96 + p4 * 8;
    const LDS_PTR(char) Y = X + (grp ? 0 : K5_STG); X += grp ? K5_STG : 0;
    const LDS_PTR(char) aM = sb + (p4 < 2 ? K5_G : K5_U) + row * 224 + (12 + grp) * 16 + (p4 & 1) * 8;
    const LDS_PTR(char) aA = sb + wm * 4096 + lane * 16;
    f32x32 c0, c1, c2; f32x16 c3;
    asm volatile(".irp r,0,8,16,24,32,40,48,56,64,72,80,88,96,104\n\tv_mov_b64 v[\\r:\\r+1], 0\n\tv_mov_b64 v[\\r+2:\\r+3], 0\n\tv_mov_b64 v[\\r+4:\\r+5], 0\n\tv_mov_b64 v[\\r+6:\\r+7], 0\n\t.endr"
                 : "={v[0:31]}"(c0), "={v[32:63]}"(c1), "={v[64:95]}"(c2), "={v[96:111]}"(c3)::"memory");
    int wrap = -(K5_NS - 1) * K5_STG, dl = K5_STG, na = grp ? 99 : 7, nb = grp ? 7 : 99;
    unsigned mask = wrap_mask(4, K5_NS);
    asm volatile("" : "+s"(wrap), "+v"(X), "+v"(Y), "+v"(aM), "+v"(aA));
    if (grp) __builtin_amdgcn_s_setprio(1); else __builtin_amdgcn_s_setprio(2);
    BAR();
    STAMP_AT(20 );
    BAR();
    STAMP_G(21, 26);
    asm volatile("s_cmp_eq_u32 %[na], 7\n\ts_cbranch_scc1 0f\n\t"
                 W5(0, Y, 0) A5(1) W5(1, Y, 1) W5(2, Y, 2) W5(3, Y, 3) AD5(aA, aA) LG(6) Q5(0, 1, 0) W5(4, Y, 0) LG(6) Q5(1, 1, 1) SB
                 A5(0) W5(4, X, 1) LG(8) Q5(2, 1, 2) RW5(5, 0, Y, 2) Q5(3, 1, 3) LG(0) "s_branch 2f\n"
                 "0:\n\t" W5(0, aM, 2) A5(0) W5(4, X, 1) W5(5, X, 3) W5(0, X, 0) AD5(aA, aA) AD5(aM, aM) LG(6) Q5(6, 0, 2) RW5(1, 0, X, 2) LG(5) Q5(4, 0, 1) SB
                 A5(1) W5(0, Y, 1) LG(9) Q5(5, 0, 3) LG(0)
                 ".p2align 5\n1:\n\t" TRIPA(0, ) "2:\n\t" TRIPB(0, ) TRIPA(1, "s_sub_u32 %[na], %[na], 1\n\t") "s_cbranch_scc1 3f\n\t" TRIPB(1, "s_sub_u32 %[nb], %[nb], 1\n\t") "s_cbranch_scc0 1b\n\t"
                 RW5(1, 1, X, 2) W5(2, X, 3) W5(3, X, 1) Q5(0, 0, 0) LG(0) SB Q5(1, 0, 2) Q5(2, 0, 3) Q5(3, 0, 1) "s_branch 4f\n"
                 "3:\n\t" RW5(5, 1, Y, 0) W5(0, aM, 1) Q5(4, 1, 2) LG(0) SB Q5(5, 1, 0) Q5(6, 1, 1)
                 "4:\n\ts_nop 8"
                 : "+{v[0:31]}"(c0), "+{v[32:63]}"(c1), "+{v[64:95]}"(c2), "+{v[96:111]}"(c3), [X] "+v"(X), [Y] "+v"(Y), [aM] "+v"(aM), [aA] "+v"(aA), [dl] "+s"(dl), [mask] "+s"(mask),
                   [na] "+s"(na), [nb] "+s"(nb)
                 : [wrap] "s"(wrap) : "memory", "scc", "v112", "v113", "v114", "v115", "v116", "v117", "v118", "v119", V10(12), V10(13), V10(14), V10(15));
    f32x4 acc[4][7];
#pragma unroll
    for (int e = 0; e < 112; ++e) acc[e / 28][(e % 28) / 4][e & 3] = e < 32 ? c0[e & 31] : e < 64 ? c1[e & 31] : e < 96 ? c2[e & 31] : c3[e & 15];
    STAMP_G(22, 27);
    auto swi4 = [](const f32x4& gv, const f32x4& uv, unsigned& o0, unsigned& o1) {
      constexpr float c2 = 2.0813689810056077f;
      f32x2 pa = gv.xy * uv.xy, pb = gv.zw * uv.zw;
      const f32x2 ta = (f32x2){__builtin_amdgcn_exp2f(gv[0]), __builtin_amdgcn_exp2f(gv[1])} * c2 + c2, tb = (f32x2){__builtin_amdgcn_exp2f(gv[2]), __builtin_amdgcn_exp2f(gv[3])} * c2 + c2;
      pa *= (f32x2){__builtin_amdgcn_rcpf(ta[0]), __builtin_amdgcn_rcpf(ta[1])}; pb *= (f32x2){__builtin_amdgcn_rcpf(tb[0]), __builtin_amdgcn_rcpf(tb[1])};
      o0 = pk2(pa[0], pa[1]); o1 = pk2(pb[0], pb[1]);
    };
    const int lz = lane_id();
    const int gz = lz >> 4, fz = lz & 15;
    const int qb = (tn5 >> 4) * 224, ql = qb + 64 + (tn5 & 15) * 10;
    auto hof = [&](int e, int i) { return (unsigned)((e >> 2) * 131072 + (e & 3) * 256 + fz * 16 + i * 1024); };
    const unsigned ho0 = hof((grp ? ql + 2 : qb + (tn5 & 15) * 4) + 2 * (gz & 1) + (gz >> 1), 0), ho1 = hof(ql + 6 * grp + (gz >> 1), gz & 1), hom = hof(ql + 8 + grp, gz);
    char* hb = ws + WS_HH + (tm5 * 16 + wm * 4) * 1024;
    const int c5 = C5 + tm5 * 4 + (tn5 >> 4) * 2;
    auto tr16 = [](const unsigned (&o)[4]) {
      auto s0 = __builtin_amdgcn_permlane16_swap(o[0], o[2], false, false);
      auto s1 = __builtin_amdgcn_permlane16_swap(o[1], o[3], false, false);
      u32x4 v = {s0[0], s1[0], s0[1], s1[1]}; asm volatile("" : "+v"(v)); return v;
    };
    auto pair = [&](int p, int i) {
      unsigned o[4];
      swi4(acc[i][2 * p], acc[i][2 * p + 1], o[0], o[1]);
      swi4(acc[i + 1][2 * p], acc[i + 1][2 * p + 1], o[2], o[3]);
      return tr16(o);
    };
    u32x4 v[3];
#pragma unroll
    for (int i = 0; i < 4; ++i) {
      unsigned o[4];
      swi4(acc[i][0], acc[i][1], o[0], o[1]);
      swi4(acc[i][2], acc[i][3], o[2], o[3]);
      st16<true>(hb, ho0 + i * 1024, tr16(o));
      SCHED0();
    }
    STAMP_G(23, 28);
    v[0] = pair(2, 0);
    if (!grp) {
      wait_vm<0>();
      unsigned arr = 0;
      if (lz == 0) arr = __hip_atomic_fetch_add((LDS_PTR(unsigned))(smem + LDS_ARR), 1u, __ATOMIC_RELAXED, __HIP_MEMORY_SCOPE_WORKGROUP);
      if (__builtin_amdgcn_readfirstlane(arr) == 3) signal(cnt, c5, lz);
    }
    STAMP_G(24, 29);
    v[1] = pair(2, 2);
    { unsigned m[4];
#pragma unroll
      for (int i = 0; i < 4; i += 2) {
        f32x4 gv, uv;
#pragma unroll
        for (int e = 0; e < 4; ++e) {
          auto w = __builtin_amdgcn_permlane32_swap(__float_as_uint(acc[i + (e >> 1)][6][e & 1]), __float_as_uint(acc[i + (e >> 1)][6][2 + (e & 1)]), false, false);
          gv[e] = __uint_as_float(w[0]); uv[e] = __uint_as_float(w[1]);
        }
        swi4(gv, uv, m[i], m[i + 1]);
      }
      auto x0 = __builtin_amdgcn_permlane32_swap(m[0], m[2], false, false);
      auto x1 = __builtin_amdgcn_permlane32_swap(m[1], m[3], false, false);
      const unsigned o[4] = {x0[0], x0[1], x1[0], x1[1]};
      v[2] = tr16(o);
      st16<true>(hb, ho1, v[0]); st16<true>(hb, ho1 + 2048, v[1]); st16<true>(hb, hom, v[2]);
    }
    STAMP_G(25, 30);
    if (grp) {
#pragma unroll
      for (int t = 0; t < NDR; ++t) BAR();
      wait_vm<0>();
      BAR();
      if (wave == 4) signal(cnt, c5 + 1, lz);
      if (wave > 4) return;
#pragma unroll 1
      for (int t = NDR + 1; t < 9; ++t) BAR();
      unsigned seen = 16;
      const unsigned* pc = (lz & 1) ? (const unsigned*)(ws + WS_FLG) + (b ^ 4) * 32 : cnt + ((C5 + (tm6 >> 1) * 4 + kh6 * 2 + 1) * 8 + tn6) * 32;
      asm volatile("global_load_dword %0, %1, off sc1" : "=v"(seen) : "v"(pc) : "memory");
      BAR(); BAR();
      asm volatile("s_waitcnt vmcnt(0)" : "+v"(seen)::"memory");
      if (__builtin_amdgcn_ballot_w64(seen < 16) != 0) poll<0>(pc, 16);
      return;
    }
  }
  { lane = lane_id(); fr = lane & 15; g = lane >> 4; p4 = fr & 3; row = 4 * g + (fr >> 2);
    const int wm = wave & 1, wn = wave >> 1;
    const int lw0 = row * 128 + (((row >> 1) & 3) * 32) + p4 * 8;
    const int mb0 = tm6 * 8 + wm * 4, t64 = tn6 * 2 + kh6;
    unsigned a0 = lds_base + (lw0 ^ (wn * 64)), a1 = lds_base + (lw0 ^ (wn * 64 + 32)), aa = lds_base + K6_A + wm * 4096 + lane * 16;
    int dw, da, n = 4, n2 = 1;
    unsigned mw = wrap_mask(2, K6_NW), ma = wrap_mask(2, K6_NA);
    f32x32 c0, c1;
    asm volatile(".irp r,0,8,16,24,32,40,48,56\n\tv_mov_b64 v[\\r:\\r+1], 0\n\tv_mov_b64 v[\\r+2:\\r+3], 0\n\tv_mov_b64 v[\\r+4:\\r+5], 0\n\tv_mov_b64 v[\\r+6:\\r+7], 0\n\t.endr\n\t"
                 "s_waitcnt vmcnt(0)\n\ts_barrier\n\t"
                 RW6(1, 0, 0, 0) RW6(1, 0, 1, 0) RA6(0, 0) RW6(1, 1, 0, 0) RW6(1, 1, 1, 0) RA6(1, 0) RA6(2, 0) RA6(3, 0) RW6(0, 0, 0, 0) RW6(0, 0, 1, 0) RW6(0, 1, 0, 0) RW6(0, 1, 1, 0)
                 "v_add_u32 %[a0], 8192, %[a0]\n\tv_add_u32 %[a1], 8192, %[a1]\n\tv_add_u32 %[aa], 8192, %[aa]\n\t" LG(4) "s_barrier\n\t"
                 RW6(0, 0, 0, 1) RW6(0, 0, 1, 1) RA6(0, 1) RW6(0, 1, 0, 1) M6(0, 2, 0) RW6(0, 1, 1, 1) M6(0, 3, 0) RA6(1, 1) M6(1, 3, 0) RA6(2, 1) M6(1, 2, 0) RA6(3, 1)
                 M6(2, 2, 0) RW6(1, 0, 0, 1) M6(2, 3, 0) RW6(1, 0, 1, 1) M6(3, 3, 0) RW6(1, 1, 0, 1) M6(3, 2, 0) RW6(1, 1, 1, 1)
                 "v_add_u32 %[a0], 8192, %[a0]\n\tv_add_u32 %[a1], 8192, %[a1]\n\tv_add_u32 %[aa], 8192, %[aa]\n\t" LG(6) "s_barrier\n\t"
                 ".p2align 5\n1:\n\t" TRIP6(0, 0, 1, 2, ) TRIP6(1, 1, 2, 0, ) TRIP6(0, 2, 0, 1, ) TRIP6(1, 0, 1, 2, ) TRIP6(0, 1, 2, 0, ) TRIP6(1, 2, 0, 1, "s_sub_u32 %[n], %[n], 1\n\t")
                 "s_cbranch_scc0 1b\n\ts_mov_b32 %[n], 3\n\ts_mov_b32 %[mw], %[mw2]\n\ts_mov_b32 %[ma], %[ma2]\n\ts_sub_u32 %[n2], %[n2], 1\n\ts_cbranch_scc0 1b\n\t"
                 M6(0, 0, 0) M6(0, 0, 1) M6(0, 1, 1) M6(0, 1, 0) LG(0) M6(1, 1, 0) M6(1, 1, 1) M6(1, 0, 1) M6(1, 0, 0) M6(2, 0, 0) M6(2, 0, 1) M6(2, 1, 1) M6(2, 1, 0)
                 M6(3, 1, 0) M6(3, 1, 1) M6(3, 0, 1) M6(3, 0, 0) "s_barrier\n\t"
                 M6(0, 2, 1) M6(0, 3, 1) M6(1, 3, 1) M6(1, 2, 1) M6(2, 2, 1) M6(2, 3, 1) M6(3, 3, 1) M6(3, 2, 1) "s_nop 8"
                 : "={v[0:31]}"(c0), "={v[32:63]}"(c1), [a0] "+v"(a0), [a1] "+v"(a1), [aa] "+v"(aa), [dw] "=&s"(dw), [da] "=&s"(da), [mw] "+s"(mw), [ma] "+s"(ma), [n] "+s"(n), [n2] "+s"(n2)
                 : [ww] "s"(-(K6_NW - 1) * 8192), [wa] "s"(-(K6_NA - 1) * 8192), [mw2] "n"(wrap_mask(32, K6_NW)), [ma2] "n"(wrap_mask(32, K6_NA))
                 : "memory", "scc", "v64", "v65", "v66", "v67", "v68", "v69", V10(7), V10(8), V10(9), V10(10), V10(11), V10(12), V10(13), V10(14), V10(15));
    f32x4 acc[4][4];
#pragma unroll
    for (int e = 0; e < 64; ++e) acc[e >> 4][(e >> 2) & 3][e & 3] = e < 32 ? c0[e & 31] : c1[e & 31];
    STAMP_AT(34 );
    lane = lane_id(); fr = lane & 15; g = lane >> 4;
    const int slot = wm * 2 + wn, pb = (b ^ 4) * 4 + slot;
    { auto pk = [](float lo, float hi) { const unsigned u = pk2(lo, hi); return u < 0xfffffffeu ? u : 0xfffffffeu; };
#pragma unroll
      for (int i = 0; i < 4; ++i)
        st16<true>(ws + WS_PART, (b * 4 + slot) * 4096 + i * 1024 + lane * 16,
                   (u32x4){pk(acc[i][0][0], acc[i][0][1]), pk(acc[i][0][2], acc[i][0][3]), pk(acc[i][1][0], acc[i][1][1]), pk(acc[i][1][2], acc[i][1][3])});
      STAMP_AT(35 );
    }
    { const LDS_PTR(float) vec = (const LDS_PTR(float))(smem + LDS_VEC) + 192 + wn * 32 + 4 * g;
      const f32x4 gv0 = *(const LDS_PTR(f32x4))vec, gv1 = *(const LDS_PTR(f32x4))(vec + 16);
      u32x4 rv[4];
#pragma unroll
      for (int i = 0; i < 4; ++i) { rv[i] = *(const LDS_PTR(u32x4))(smem + K6_X + (wm * 4 + i) * 2048 + wn * 1024 + lane * 16); asm volatile("" : "+v"(rv[i])); }
      u32x4 z[4];
      __builtin_amdgcn_s_sleep(PD6);
#pragma unroll 1
      for (unsigned spins = 0; spins < SPIN_CAP; ++spins) {
        asm volatile("" ::: "memory");
        unsigned m = 0;
#pragma unroll
        for (int i = 0; i < 4; ++i) z[i] = ld16<true>(ws + WS_PART, pb * 4096 + i * 1024 + lane * 16);
#pragma unroll
        for (int i = 0; i < 16; ++i) m = m > z[i >> 2][i & 3] ? m : z[i >> 2][i & 3];
        if (__builtin_amdgcn_ballot_w64(m == ~0u) == 0) break;
        __builtin_amdgcn_s_sleep(PS6);
      }
      STAMP_AT(36 );
#pragma unroll
      for (int i = 0; i < 4; ++i) {
        const f32x4 v0 = (acc[i][2] + (f32x4){lo2f(z[i][0]), hi2f(z[i][0]), lo2f(z[i][1]), hi2f(z[i][1])}) * gv0;
        const f32x4 v1 = (acc[i][3] + (f32x4){lo2f(z[i][2]), hi2f(z[i][2]), lo2f(z[i][3]), hi2f(z[i][3])}) * gv1;
        const unsigned o0 = pk2(lo2f(rv[i][0]) + v0[0], hi2f(rv[i][0]) + v0[1]), o1 = pk2(lo2f(rv[i][1]) + v0[2], hi2f(rv[i][1]) + v0[3]);
        const unsigned o2 = pk2(lo2f(rv[i][2]) + v1[0], hi2f(rv[i][2]) + v1[1]), o3 = pk2(lo2f(rv[i][3]) + v1[2], hi2f(rv[i][3]) + v1[3]);
        auto s0 = __builtin_amdgcn_permlane16_swap(o0, o2, false, false);
        auto s1 = __builtin_amdgcn_permlane16_swap(o1, o3, false, false);
        st16<false>(out, (((mb0 + i) * 16 + fr) * D_ + t64 * 64 + wn * 32 + (g & 1) * 16 + (g >> 1) * 8) * 2, (u32x4){s0[0], s1[0], s0[1], s1[1]});
      }
    }
    STAMP_AT(37 );
  }
}

extern "C" void kernel_launch(void* const* d_in, const int* in_sizes, int n_in, void* d_out, int out_size, void* d_ws,
                              size_t ws_size, hipStream_t stream) {
  const u16* x = (const u16*)d_in[0];
  const u16* wqkv = (const u16*)d_in[1];
  const u16* wo = (const u16*)d_in[2];
  const u16* w1 = (const u16*)d_in[4];
  const u16* w3 = (const u16*)d_in[5];
  const u16* w2 = (const u16*)d_in[6];
  const u16* c = (const u16*)d_in[7];
  const u16* wmod = (const u16*)d_in[8];
  (void)hipFuncSetAttribute((const void*)k2_block, hipFuncAttributeMaxDynamicSharedMemorySize, MEGA_LDS);
  char* ws = (char*)d_ws;
  k2_block<<<256, 768, MEGA_LDS, stream>>>(x, wqkv, wo, w1, w3, w2, c, wmod, (u16*)d_out, ws);
}
```

```cpp
#include <hip/hip_runtime.h>
#include <stdint.h>
#include <type_traits>
#pragma clang diagnostic ignored "-Winline-asm"

typedef unsigned short u16;
typedef short bf16x8 __attribute__((ext_vector_type(8)));
typedef short bf16x4 __attribute__((ext_vector_type(4)));
typedef float f32x4 __attribute__((ext_vector_type(4)));
typedef float f32x2 __attribute__((ext_vector_type(2)));
typedef unsigned int u32x4 __attribute__((ext_vector_type(4)));
typedef unsigned int u32x2 __attribute__((ext_vector_type(2)));
#define LDS_PTR(T) __attribute__((address_space(3))) T*
#define BAR() do { asm volatile("" ::: "memory"); __builtin_amdgcn_s_barrier(); asm volatile("" ::: "memory"); } while (0)
#define SCHED0() __builtin_amdgcn_sched_barrier(0)
template <int N> using IC = std::integral_constant<int, N>;
typedef std::integral_constant<bool, true> T_;
typedef std::integral_constant<bool, false> F__;

constexpr int S_ = 2048, D_ = 1024, F_ = 3584;
#define STAMP_AT(i)
#define STAMP_G(ia, ib)
#define STAMP_L(i)

__device__ __forceinline__ unsigned pk2(float lo, float hi) {
  typedef __bf16 bf2 __attribute__((ext_vector_type(2)));
  f32x2 v = {lo, hi}; bf2 r = __builtin_convertvector(v, bf2); return __builtin_bit_cast(unsigned, r);
}
__device__ __forceinline__ float lo2f(unsigned u) { return __uint_as_float(u << 16); }
__device__ __forceinline__ float hi2f(unsigned u) { return __uint_as_float(u & 0xffff0000u); }
__device__ __forceinline__ float rbf(float v) { return lo2f(pk2(v, 0.f)); }
#define glds16(sbase, voff, lds_off, imm) \
  asm volatile("s_add_u32 m0, %2, %3\n\ts_nop 0\n\tglobal_load_lds_dwordx4 %0, %1" ::"v"(voff), "s"(sbase), "s"(lds_off), "n"(imm) : "memory", "scc")
#define glds16s(sbase, voff, lds_off, imm) \
  asm volatile("s_add_u32 m0, %2, %3\n\ts_nop 0\n\tglobal_load_lds_dwordx4 %0, %1 sc1" ::"v"(voff), "s"(sbase), "s"(lds_off), "n"(imm) : "memory", "scc")
#define GLDS4(SC) "s_mov_b32 m0, %6\n\ts_nop 0\n\tglobal_load_lds_dwordx4 %0, %4" SC "\n\tglobal_load_lds_dwordx4 %1, %5 offset:1024" SC \
                  "\n\tglobal_load_lds_dwordx4 %2, %4 offset:2048" SC "\n\tglobal_load_lds_dwordx4 %3, %5 offset:3072" SC
template <bool SC1> __device__ __forceinline__ void glds16x4(const void* s0, const void* s1, const unsigned (&v)[4], unsigned lds) {
  if (SC1) asm volatile(GLDS4(" sc1") ::"v"(v[0]), "v"(v[1]), "v"(v[2]), "v"(v[3]), "s"(s0), "s"(s1), "s"(lds) : "memory");
  else asm volatile(GLDS4("") ::"v"(v[0]), "v"(v[1]), "v"(v[2]), "v"(v[3]), "s"(s0), "s"(s1), "s"(lds) : "memory");
}
__device__ __forceinline__ void glds4(const void* sbase, unsigned voff, unsigned lds_off) {
  asm volatile("s_mov_b32 m0, %2\n\ts_nop 0\n\tglobal_load_lds_dword %0, %1" ::"v"(voff), "s"(sbase), "s"(lds_off) : "memory");
}
__device__ __forceinline__ void stouch4(const void* p) {
  asm volatile("s_load_dwordx2 s[100:101], %0, 0x3c\n\ts_load_dwordx2 s[100:101], %0, 0xbc\n\ts_load_dwordx2 s[100:101], %0, 0x13c\n\ts_load_dwordx2 s[100:101], %0, 0x1bc"
               ::"s"(p) : "memory", "s100", "s101");
}
template <bool SC1> __device__ __forceinline__ void st16(void* sbase, unsigned voff, u32x4 v) {
  __builtin_amdgcn_raw_buffer_store_b128(v, __builtin_amdgcn_make_buffer_rsrc(sbase, 0, 0x7fffffff, 0x00020000), voff, 0, SC1 ? 16 : 0);
}
template <bool SC1> __device__ __forceinline__ u32x4 ld16(const void* sbase, unsigned voff) {
  return __builtin_amdgcn_raw_buffer_load_b128(__builtin_amdgcn_make_buffer_rsrc((void*)sbase, 0, 0x7fffffff, 0x00020000), voff, 0, SC1 ? 16 : 0);
}
template <int N> __device__ __forceinline__ void wait_vm() { asm volatile("s_waitcnt vmcnt(%0)" ::"n"(N) : "memory"); }
__device__ __forceinline__ void wait_lgkm0() { asm volatile("" ::: "memory"); __builtin_amdgcn_s_waitcnt(0xC07F); asm volatile("" ::: "memory"); }
__device__ __forceinline__ bf16x4 ld_tr4(const LDS_PTR(char) p) { return __builtin_amdgcn_ds_read_tr16_b64_v4i16((LDS_PTR(bf16x4))p); }
__device__ __forceinline__ bf16x8 cat8(bf16x4 lo, bf16x4 hi) { return __builtin_shufflevector(lo, hi, 0, 1, 2, 3, 4, 5, 6, 7); }
__device__ __forceinline__ void mfma_acc(f32x4& acc, bf16x8 a, bf16x8 b) { asm volatile("v_mfma_f32_16x16x32_bf16 %0, %1, %2, %0" : "+v"(acc) : "v"(a), "v"(b)); }
template <int STEP> __device__ __forceinline__ void mfma_sel(f32x4& acc, bf16x8 a, bf16x8 b, unsigned& mask, int& dl, int wrap) {
  asm volatile("s_add_u32 %1, %1, %1\n\tv_mfma_f32_16x16x32_bf16 %0, %3, %4, %0\n\ts_cselect_b32 %2, %5, %6" : "+v"(acc), "+s"(mask), "=s"(dl) : "v"(a), "v"(b), "s"(wrap), "n"(STEP) : "scc");
}
constexpr unsigned wrap_mask(int first, int ns) { unsigned m = 0; for (int t = 0; t < 32; ++t) if ((first + t) % ns == ns - 1) m |= 0x80000000u >> t; return m; }
template <int N> __device__ __forceinline__ void wait_lgkm() { __builtin_amdgcn_s_waitcnt(0xC07F | (N << 8)); }
template <int R, int C> __device__ __forceinline__ void mfma_done(f32x4 (&acc)[R][C]) {
  asm volatile("s_nop 9");
#pragma unroll
  for (int i = 0; i < R; ++i)
#pragma unroll
    for (int j = 0; j < C; ++j) asm volatile("" : "+v"(acc[i][j]));
}
__device__ __forceinline__ int lane_id() { int l; asm volatile("v_mbcnt_lo_u32_b32 %0, -1, 0\n\tv_mbcnt_hi_u32_b32 %0, -1, %0" : "=v"(l)); return l; }
__device__ __forceinline__ float xsum16_32(float v) {
  auto a = __builtin_amdgcn_permlane16_swap(__float_as_uint(v), __float_as_uint(v), false, false); v = __uint_as_float(a[0]) + __uint_as_float(a[1]);
  auto c = __builtin_amdgcn_permlane32_swap(__float_as_uint(v), __float_as_uint(v), false, false); return __uint_as_float(c[0]) + __uint_as_float(c[1]);
}

__device__ __forceinline__ float ln_rstd(float var) { return __builtin_amdgcn_rcpf(rbf(__builtin_amdgcn_sqrtf(rbf(rbf(var) + 1e-5f)))); }

constexpr size_t WS_MOD = 0;
constexpr size_t WS_EPO = 64 << 10, WS_FL1 = 96 << 10;
constexpr size_t WS_CTL = 128 << 10;
constexpr int C2 = 0, C3 = 16, C4 = 32, C5 = 48, CM = 80;
constexpr size_t WS_FLG = WS_CTL + (88 << 10), CTL_BYTES = 120 << 10;
constexpr size_t WS_XCH = 512 << 10;
constexpr size_t WS_MOD2 = 1 << 20;
constexpr size_t WS_H = 2 << 20, WS_O = WS_H + (4 << 20), WS_H2 = WS_O + (4 << 20), WS_HH = WS_H2 + (4 << 20),
                 WS_PART = WS_HH + (size_t)S_ * F_ * 2;

template <int NL> __device__ __forceinline__ f32x2 slab_sum(const unsigned (&mc)[NL / 2], const u32x4 (&mw)[NL]) {
  f32x2 a2[4] = {};
#pragma unroll
  for (int i = 0; i < NL; ++i) {
    const float cv = (i & 1) ? hi2f(mc[i >> 1]) : lo2f(mc[i >> 1]);
    const float sv = rbf(cv * __builtin_amdgcn_rcpf(1.f + __builtin_amdgcn_exp2f(cv * -1.4426950408889634f)));
#pragma unroll
    for (int j = 0; j < 4; ++j) a2[j] += (f32x2){lo2f(mw[i][j]), hi2f(mw[i][j])} * sv;
  }
  float acc[8] = {a2[0][0], a2[0][1], a2[1][0], a2[1][1], a2[2][0], a2[2][1], a2[3][0], a2[3][1]};
#pragma unroll
  for (int j = 0; j < 4; ++j) { auto a = __builtin_amdgcn_permlane32_swap(__float_as_uint(acc[j]), __float_as_uint(acc[j + 4]), false, false); acc[j] = __uint_as_float(a[0]) + __uint_as_float(a[1]); }
#pragma unroll
  for (int j = 0; j < 2; ++j) { auto a = __builtin_amdgcn_permlane16_swap(__float_as_uint(acc[j]), __float_as_uint(acc[j + 2]), false, false); acc[j] = __uint_as_float(a[0]) + __uint_as_float(a[1]); }
#pragma unroll
  for (int j = 0; j < 2; ++j) acc[j] += __uint_as_float(__builtin_amdgcn_mov_dpp(__float_as_uint(acc[j]), 0x128, 0xf, 0xf, false));
  return (f32x2){acc[0], acc[1]};
}

constexpr int K3_B = 9216, K3_STG = 9216 + 12288, K3_NS = 7;
constexpr int K4_RED = 57344;
constexpr int K5_G = 16384, K5_U = 16384 + 7168 + 16, K5_STG = 30736, K5_NS = 5;
constexpr int K6_NW = 12, K6_NA = 7, K6_A = K6_NW * 8192, K6_X = 9 * 8192;
constexpr int LDS_VEC = 158720, LDS_DUMMY = LDS_VEC + 1024, LDS_ARR = LDS_DUMMY + 256, MEGA_LDS = LDS_ARR + 64;
constexpr unsigned SPIN_CAP = 1u << 19;
#define KNOBS PA6 = 5, PB6 = 5, KB = 8, XD0 = 22, PFD = 16, FD1 = 8, FS0 = 4, PQ = 2, PQ5 = 4, PD2 = 4, PD3 = 4, PD4 = 4, PD5 = 48, PD6 = 6, PS6 = 1, XS0 = 14, XS1 = 4, TS5 = 2, VB3 = 2, SLB = 29, WE = 1, IA0 = 3, IA1 = 5, IW0 = 6, IW1 = 6, NDR = 1
enum { KNOBS };
constexpr unsigned long long SKIP6 = 0x7c;
static_assert(__builtin_popcountll(SKIP6) == 5, "44 stages of W are issued behind 49 barriers");

__device__ __forceinline__ void signal(unsigned* cnt, int c, int lane, unsigned v = 1) {
  if (lane < 8) __hip_atomic_fetch_add(cnt + (c * 8 + lane) * 32, v, __ATOMIC_RELAXED, __HIP_MEMORY_SCOPE_AGENT);
}
template <int D0, int D1 = 1> __device__ __forceinline__ void poll(const unsigned* cnt, unsigned want) {
  if (D0) __builtin_amdgcn_s_sleep(D0);
#pragma unroll 1
  for (unsigned spins = 0; spins < SPIN_CAP; ++spins) {
    if (__builtin_amdgcn_ballot_w64(__hip_atomic_load(cnt, __ATOMIC_RELAXED, __HIP_MEMORY_SCOPE_AGENT) < want) == 0) break;
    __builtin_amdgcn_s_sleep(D1);
  }
  __builtin_amdgcn_fence(__ATOMIC_ACQUIRE, "wavefront");
}

template <int D0, int D1, int EX> __device__ __forceinline__ void pollp(const unsigned* cnt, unsigned want, unsigned (&r)[4]) {
  unsigned n = SPIN_CAP;
#define PL1(a) "global_atomic_add %" #a ", %5, %9, off sc0\n\ts_sleep %8\n\t"
#define PL2(a, c) "global_atomic_add %" #a ", %5, %9, off sc0\n\ts_waitcnt vmcnt(3)\n\tv_cmp_gt_u32 vcc, %6, %" #c "\n\ts_cbranch_vccz 2f\n\ts_sleep %8\n\t"
  asm volatile("s_mov_b64 exec, %10\n\ts_sleep %7\n\t" PL1(0) PL1(1) PL1(2) "1:\n\t" PL2(3, 0) PL2(0, 1) PL2(1, 2) PL2(2, 3) "s_sub_u32 %4, %4, 1\n\ts_cbranch_scc0 1b\n2:\n\ts_mov_b64 exec, -1"
               : "=&v"(r[0]), "=&v"(r[1]), "=&v"(r[2]), "=&v"(r[3]), "+s"(n) : "v"(cnt), "s"(want), "n"(D0), "n"(D1), "v"(0), "n"(EX) : "memory", "vcc", "scc");
}
#define KEEP4(r) asm volatile("; keep %0 %1 %2 %3" ::"v"(r[0]), "v"(r[1]), "v"(r[2]), "v"(r[3]))
typedef float f32x32 __attribute__((ext_vector_type(32)));
typedef float f32x16 __attribute__((ext_vector_type(16)));
#define M5(i, j, s, w) "v_mfma_f32_16x16x32_bf16 v[28*" #i "+4*" #j ":28*" #i "+4*" #j "+3], v[144+4*(" #w "):147+4*(" #w ")], v[112+16*(" #s ")+4*" #i ":115+16*(" #s ")+4*" #i "], v[28*" #i "+4*" #j ":28*" #i "+4*" #j "+3]\n\t"
#define RA5(i, s) "ds_read_b128 v[112+16*" #s "+4*" #i ":115+16*" #s "+4*" #i "], %[aA] offset:1024*" #i "\n\t"
#define RW5(j, h, a, w) "ds_read_b64_tr_b16 v[144+4*(" #w ")+2*" #h ":145+4*(" #w ")+2*" #h "], %[" #a "] offset:32*(" #j "/2)+7184*(" #j "&1)+3584*" #h "\n\t"
#define RM5(h, w) RW5(0, h, aM, w)
#define AD5(d, a) "v_add_u32 %[" #d "], %[dl], %[" #a "]\n\t"
#define SEL5A "s_add_u32 %[mask], %[mask], %[mask]\n\t"
#define SEL5B "s_cselect_b32 %[dl], %[wrap], 30736\n\t"
#define LG(n) "s_waitcnt lgkmcnt(" #n ")\n\t"
#define SB "s_barrier\n\t"
#define GN5(j, o, b, g0, g1, g2, g3, g4, g5, g6, g7) \
  M5(0, j, o, 2*(b)) g0 M5(0, j, 1-o, 2*(b)+1) g1 M5(1, j, 1-o, 2*(b)+1) g2 M5(1, j, o, 2*(b)) g3 M5(2, j, o, 2*(b)) g4 M5(2, j, 1-o, 2*(b)+1) g5 M5(3, j, o, 2*(b)) g6 M5(3, j, 1-o, 2*(b)+1) g7
#define GL5(j, o, b, g0, g1, g5, g7) \
  M5(0, j, o, 2*(b)) g0 M5(0, j, 1-o, 2*(b)+1) g1 M5(1, j, o, 2*(b)) RA5(0, o) M5(1, j, 1-o, 2*(b)+1) RA5(1, o) M5(2, j, o, 2*(b)) RA5(2, o) M5(2, j, 1-o, 2*(b)+1) g5 M5(3, j, o, 2*(b)) RA5(3, o) M5(3, j, 1-o, 2*(b)+1) g7 LG(4)
#define TRIPA(b, Z)                                                                                                                                     \
  GN5(0, 0, b, RW5(1, 1, X, 2-2*b), RW5(1, 0, Y, 3-2*b) LG(5), RW5(1, 1, Y, 3-2*b), , LG(4), , RW5(2, 0, X, 2*b), RW5(2, 1, X, 2*b) LG(2))                \
  GN5(1, 0, 1-b, RW5(2, 0, Y, 2*b+1), RW5(2, 1, Y, 2*b+1), , , , , RW5(3, 0, X, 2-2*b), RW5(3, 1, X, 2-2*b) LG(2))                                       \
  GN5(2, 0, b, RW5(3, 0, Y, 3-2*b), RW5(3, 1, Y, 3-2*b) SEL5A, SEL5B, AD5(X, Y) AD5(aA, aA), LG(2), SB, RW5(4, 0, Y, 2*b), RW5(4, 1, Y, 2*b) LG(2))        \
  GL5(3, 0, 1-b, RW5(4, 0, X, 2*b+1), RW5(4, 1, X, 2*b+1), Z, RW5(5, 0, Y, 2-2*b))
#define TRIPB(b, Z)                                                                                                                                     \
  GN5(4, 1, b, RW5(5, 1, Y, 2-2*b), RW5(5, 0, X, 3-2*b) LG(5), RW5(5, 1, X, 3-2*b), , LG(4), , RM5(0, 2*b), RM5(1, 2*b) LG(2))                            \
  GN5(5, 1, 1-b, AD5(aM, aM) RM5(0, 2*b+1), RM5(1, 2*b+1) SEL5A, SEL5B, AD5(Y, X) AD5(aA, aA), LG(2), SB, RW5(0, 0, X, 2-2*b), RW5(0, 1, X, 2-2*b) LG(2)) \
  GL5(6, 1, b, RW5(0, 0, Y, 3-2*b), RW5(0, 1, Y, 3-2*b), AD5(aM, aM) Z, RW5(1, 0, X, 2*b))
#define Q5(j, s, w) M5(0, j, s, w) M5(1, j, s, w) M5(2, j, s, w) M5(3, j, s, w)
#define W5(j, a, w) RW5(j, 0, a, w) RW5(j, 1, a, w)
#define A5(s) RA5(0, s) RA5(1, s) RA5(2, s) RA5(3, s)
static_assert(K5_STG == 30736 && K5_U - K5_G == 7184, "TRIP5");

#define N3(i, j) "24*" #i "+4*(" #j ")-(" #i "/4)*(4*(" #j ")-4*((" #j ")/5))"
#define M3(i, j, s, w) "v_mfma_f32_16x16x32_bf16 v[" N3(i, j) ":" N3(i, j) "+3], v[144+4*(" #w "):147+4*(" #w ")], v[104+20*(" #s ")+4*" #i ":107+20*(" #s ")+4*" #i "], v[" N3(i, j) ":" N3(i, j) "+3]\n\t"
#define RA3(i, s) "ds_read_b128 v[104+20*" #s "+4*" #i ":107+20*" #s "+4*" #i "], %[aA] offset:1024*" #i "\n\t"
#define RH3(s) "ds_read_b128 v[120+20*" #s ":123+20*" #s "], %[aH]\n\t"
#define RW3(p, h, a, w) "ds_read_b64_tr_b16 v[144+4*(" #w ")+2*" #h ":145+4*(" #w ")+2*" #h "], %[" #a "] offset:4096*" #p "+2048*" #h "\n\t"
#define W3(p, a, w) RW3(p, 0, a, w) RW3(p, 1, a, w)
#define A3(s) RA3(0, s) RA3(1, s) RA3(2, s) RA3(3, s) RH3(s)
#define Q3(j, s, w) M3(0, j, s, w) M3(1, j, s, w) M3(2, j, s, w) M3(3, j, s, w)
#define SEL3B "s_cselect_b32 %[dl], %[wrap], -21504\n\t"
#define GN3(j, o, b, g0, g1, g2, g3, g4, g5, g6, g7) \
  M3(0, j, o, 2*(b)) g0 M3(0, j, 1-o, 2*(b)+1) g1 M3(1, j, 1-o, 2*(b)+1) g2 M3(1, j, o, 2*(b)) g3 M3(2, j, o, 2*(b)) g4 M3(2, j, 1-o, 2*(b)+1) g5 M3(3, j, o, 2*(b)) g6 M3(3, j, 1-o, 2*(b)+1) g7
#define GL3(j, o, g0, g1, g2, g3, g4, g5, g6, g7, g8, g9) \
  M3(0, j, o, 2*o) g0 M3(0, j, 1-o, 2*o+1) g1 M3(1, j, o, 2*o) g2 M3(1, j, 1-o, 2*o+1) g3 M3(2, j, o, 2*o) g4 M3(2, j, 1-o, 2*o+1) g5 M3(3, j, o, 2*o) g6 M3(3, j, 1-o, 2*o+1) g7 M3(4, j, o, 2*o) g8 M3(4, j, 1-o, 2*o+1) g9
#define HEAD3(o, p1, p2, U, V)                                                                                                                          \
  GN3(3*o, o, o, RW3(p1, 1, U, 2-2*o), RW3(p1, 0, V, 3-2*o), RW3(p1, 1, V, 3-2*o), , LG(5), , RW3(p2, 0, U, 2*o), RW3(p2, 1, U, 2*o) LG(2))               \
  GN3(3*o+1, o, 1-o, RW3(p2, 0, V, 2*o+1), RW3(p2, 1, V, 2*o+1) SEL5A, SEL3B "v_xor_b32 %[" #V "], 32, %[" #V "]\n\t", AD5(U, V) AD5(aA, aA), LG(2) AD5(aH, aH), SB, \
      RW3(0, 0, V, 2-2*o), RW3(0, 1, V, 2-2*o) LG(2))
#define TRIP3(o, p1, p2, U, V, Z) HEAD3(o, p1, p2, U, V) GL3(3*o+2, o, RW3(0, 0, U, 3-2*o), RW3(0, 1, U, 3-2*o), RA3(0, o), RA3(1, o), RA3(2, o), Z, RA3(3, o), , RH3(o), RW3(p2, 0, V, 2*o) LG(4))
#define PAIR3(Z) TRIP3(0, 2, 1, X, Y, ) TRIP3(1, 1, 2, Y, X, Z)
#define CV3(i, j, t) "v_cvt_pk_bf16_f32 v[108+2*" #t "], v[" N3(i, j) "], v[" N3(i, j) "+1]\n\tv_cvt_pk_bf16_f32 v[109+2*" #t "], v[" N3(i, j) "+2], v[" N3(i, j) "+3]\n\t"
#define WR3(i, t, a, o) "ds_write_b64 %[" #a "], v[108+2*" #t ":109+2*" #t "] offset:2048*" #i "+" #o "\n\t"
#define CW3(i, j, t, a, o) CV3(i, j, t) WR3(i, t, a, o)
#define V8(a) "v" #a "0", "v" #a "1", "v" #a "2", "v" #a "3", "v" #a "4", "v" #a "5", "v" #a "6", "v" #a "7"
#define V10(a) V8(a), "v" #a "8", "v" #a "9"
static_assert(K3_STG == 21504, "TRIP3");
#define M6(i, j, s) "v_mfma_f32_16x16x32_bf16 v[16*" #i "+4*(" #j "):16*" #i "+4*(" #j ")+3], v[80+32*" #s "+4*(" #j "):83+32*" #s "+4*(" #j ")], v[64+32*" #s "+4*" #i ":67+32*" #s "+4*" #i "], v[16*" #i "+4*(" #j "):16*" #i "+4*(" #j ")+3]\n\t"
#define RA6(i, s) "ds_read_b128 v[64+32*" #s "+4*" #i ":67+32*" #s "+4*" #i "], %[aa] offset:1024*" #i "\n\t"
#define RW6(h, b, l, s) "ds_read_b64_tr_b16 v[80+32*" #s "+8*(" #h ")+4*" #b "+2*" #l ":81+32*" #s "+8*(" #h ")+4*" #b "+2*" #l "], %[a" #b "] offset:4096*(" #h ")+2048*" #l "\n\t"
#define SEL6(m, d, w) "s_cselect_b32 %[" #d "], %[" #w "], 8192\n\t"
#define SH6(m) "s_add_u32 %[" #m "], %[" #m "], %[" #m "]\n\t"
#define TRIP6(h, o, n, x, X)                                                                                                                            \
  M6(0, 2*h, o) RW6(1-h, 0, 0, x) M6(0, 2*h, n) RW6(1-h, 0, 1, x) SH6(mw) M6(0, 2*h+1, n) SEL6(mw, dw, ww) RA6(0, x) M6(0, 2*h+1, o) RW6(1-h, 1, 0, x) LG(8)  \
  M6(1, 2*h+1, o) RW6(1-h, 1, 1, x) M6(1, 2*h+1, n) RA6(1, x) SH6(ma) M6(1, 2*h, n) SEL6(ma, da, wa) RA6(2, x) M6(1, 2*h, o) RA6(3, x)                      \
  M6(2, 2*h, o) RW6(h, 0, 0, x) "v_add_u32 %[aa], %[da], %[aa]\n\t" M6(2, 2*h, n) RW6(h, 0, 1, x) M6(2, 2*h+1, n) RW6(h, 1, 0, x) "v_add_u32 %[a0], %[dw], %[a0]\n\t" \
  M6(2, 2*h+1, o) RW6(h, 1, 1, x) M6(3, 2*h+1, o) "v_add_u32 %[a1], %[dw], %[a1]\n\t" X M6(3, 2*h+1, n) M6(3, 2*h, n) LG(6) M6(3, 2*h, o) "s_barrier\n\t"
__device__ __forceinline__ int w2_stage(int j) {
  const int j2 = j - 16, p = (j2 * 52) >> 8, r = j2 - p * 5;
  return j < 16 ? j * 112 * 2048 : (p * 224 + 32 + r * 32 + (r > 2 ? 32 : 0)) * 2048 + (r == 2);
}
__global__ __launch_bounds__(768, 3) __attribute__((amdgpu_num_sgpr(98))) void k2_block(const u16* __restrict__ x, const u16* __restrict__ wqkv, const u16* __restrict__ wo,
                                                   const u16* __restrict__ w1, const u16* __restrict__ w3, const u16* __restrict__ w2,
                                                   const u16* __restrict__ cnd, const u16* __restrict__ wmod, u16* __restrict__ out, char* __restrict__ ws) {
  extern __shared__ __attribute__((aligned(16))) char smem_[];
  LDS_PTR(char) smem = (LDS_PTR(char))smem_;
  const int tid = threadIdx.x;
  int lane = tid & 63;
  const int wave = __builtin_amdgcn_readfirstlane(tid >> 6);
  const unsigned lds_base = (unsigned)(uintptr_t)smem;
  const int b = blockIdx.x, xc = b & 7, ii = b >> 3;
  const int tm3 = (xc & 1) * 8 + (ii & 7), tn3 = (xc >> 1) * 4 + (ii >> 3);
  const int tm5 = (xc & 1) * 4 + (ii & 3), tn5 = (xc >> 1) * 8 + (ii >> 2);
  const int kh6 = xc >> 2, tm6 = (xc & 1) * 8 + (xc & 2) * 2 + (ii & 3), tn6 = ii >> 2;
  const int tm4 = tm6, tn4 = tn6 * 2 + kh6;
  const int u1 = (b >> 4) * 8 + xc;
  unsigned* cnt = (unsigned*)(ws + WS_CTL);
  unsigned long long* fl1 = (unsigned long long*)(ws + WS_FL1);
  STAMP_AT(0 );

  if (wave >= 8) {
    const int lw = wave - 8, P = b * 4 + lw, lane = lane_id();
    const bool isA = lw < 2;
    asm volatile("" ::"s"(wqkv), "s"(wo), "s"(w1), "s"(w3), "s"(w2), "s"(ws));
    int kpf = KB + VB3;
    unsigned pr[4] = {};
    auto touch = [&]() {
      if (kpf < 28 && __builtin_amdgcn_s_getreg((3 << 11) | (8 << 6) | 7) <= 7) {
        stouch4((const char*)((P & 1) ? w3 : w1) + (kpf * 2048 + (P >> 1) * 4) * 128);
        ++kpf;
      }
    };
    const char* epo = ws + WS_EPO;
    const unsigned swz = ((lane & 7) ^ ((lane >> 3) & 6)) * 16;
    const int cg = lw * 16 + tn4;
    auto issue4 = [&](int s, unsigned slot) {
      const char* p = (const char*)wo + tn4 * 128 + (lw * 32 + s * 128) * 2048;
      unsigned v4[4];
#pragma unroll
      for (int i = 0; i < 4; ++i) v4[i] = (lane >> 3) * 2048 + swz + i * 15360;
      glds16x4<false>(p, p, v4, lds_base + slot + lw * 4096);
    };
    auto run3 = [&](auto isA_) {
      constexpr bool A_ = decltype(isA_)::value; constexpr int N = A_ ? 4 : 6;
      const char* sp = A_ ? ws + WS_H + (tm3 * 8 - 1) * 1024 : (const char*)wqkv + tn3 * 128;
      unsigned vo[4], vx[2];
#pragma unroll
      for (int i = 0; i < 6; ++i) {
        const int tb = (lw - 2) * 6 + i;
        const unsigned v = A_ ? lane * 16 + (i < 4 ? 1024 + lw * 4096 : tm3 ? 0 : 1024) : ((tb & 3) * 8 + (lane >> 3)) * 6144 + (tb >> 2) * 2048 + 4096 - (i < 4 ? i * 1024 : 0) + swz;
        if (i < 4) vo[i] = v; else vx[i - 4] = v;
      }
      if (!A_) sp -= 4096;
      const unsigned d0 = lds_base + (A_ ? 1024 + lw * 4096 : K3_B + (lw - 2) * 6144);
      unsigned st = 3 * K3_STG;
      auto issue = [&]() {
        glds16x4<A_>(sp, sp, vo, d0 + st);
        if (A_) { if (lw == 0) glds16s(sp, vx[0], lds_base + st, 0); }
        else { glds16(sp, vx[0], d0 + st, 4096); glds16(sp, vx[1], d0 + st, 5120); }
        sp += A_ ? 131072 : 32 * 6144; st = st == 0 ? (K3_NS - 1) * K3_STG : st - K3_STG;
      };
      { unsigned long long ep;
        asm volatile("s_load_dwordx2 %0, %1, 0x0\n\ts_waitcnt lgkmcnt(0)" : "=s"(ep) : "s"(epo) : "memory");
        ep += 1;
        if (lw == 3) { *(LDS_PTR(u32x4))(smem + LDS_ARR) = (u32x4){0u, 0u, 0u, 0u}; wait_lgkm0(); }
        if (lw == 1) {
#pragma unroll
          for (int k = 0; k < 3; ++k) {
            const int i = b * 158 + (k * 64 + lane < 158 ? k * 64 + lane : 0);
            st16<true>(ws, i < 32768 ? (unsigned)WS_XCH + i * 16 : (unsigned)WS_CTL + (i - 32768) * 16, i < 32768 ? (u32x4){~0u, ~0u, ~0u, ~0u} : (u32x4){0u, 0u, 0u, 0u});
          }
        }
        BAR();
        auto flag = [&](int idx) { wait_vm<0>(); if (lane < 8) __hip_atomic_store(fl1 + lane * 256 + idx, ep, __ATOMIC_RELAXED, __HIP_MEMORY_SCOPE_AGENT); };
        if (lw == 1 && (b & 8)) {
          const int kq = u1 & 3, cgp = u1 >> 2;
          float sm = 0.f;
#pragma unroll
          for (int r = 0; r < 8; ++r) sm += *(const LDS_PTR(float))(smem + 8192 + (r * 64 + lane) * 4);
          __hip_atomic_store((unsigned*)(ws + WS_MOD) + kq * 2048 + cgp * 64 + lane, __float_as_uint(sm), __ATOMIC_RELAXED, __HIP_MEMORY_SCOPE_AGENT);
          flag(((cgp >> 3) & 1) * 64 + (cgp >> 4) * 32 + (cgp & 7) * 4 + kq);
        }
        if (b & 8) BAR();
        BAR();
        if (lw == 1 && !(b & 8)) flag(128 + u1);
        auto flags = [&](auto n_, int first) {
#pragma unroll 1
          for (unsigned spins = 0; spins < SPIN_CAP; ++spins) {
            bool ok = true;
#pragma unroll
            for (int k = 0; k < decltype(n_)::value; ++k) ok &= __hip_atomic_load(fl1 + xc * 256 + (first + k) * 64 + lane, __ATOMIC_RELAXED, __HIP_MEMORY_SCOPE_AGENT) == ep;
            if (__builtin_amdgcn_ballot_w64(!ok) == 0) break;
            __builtin_amdgcn_s_sleep(FS0);
          }
        };
        if (lw == 0) flags(IC<1>(), (b >> 3) & 1);
        BAR();
        if (lw == 1) {
          __builtin_amdgcn_s_sleep(FD1);
          flags(IC<4>(), 0);
          if (b == 0 && lane == 0) __hip_atomic_store((unsigned long long*)(ws + WS_EPO), ep, __ATOMIC_RELAXED, __HIP_MEMORY_SCOPE_AGENT);
        }
      }
      BAR();
      if (lw == 0) {
        signal(cnt, C2 + (u1 >> 3), lane, (b & 8) ? 1u : 0x10000u);
        pollp<PD2, PQ, 3>(cnt + ((C2 + ((lane & 1) && tm3 ? tm3 - 1 : tm3)) * 8 + (tn3 & 7)) * 32, 0x80000u, pr);
      }
      constexpr int I0 = A_ ? IA0 : IW0, I1 = A_ ? IA1 : IW1;
      if (!A_) for (int i = 0; i < I0; ++i) issue();
      BAR();
      if (A_) for (int i = 0; i < I0; ++i) issue();
      wait_vm<(I0 - 1) * N>();
      KEEP4(pr);
      BAR();
      for (int i = I0; i < I1; ++i) issue();
      wait_vm<(I1 - 2) * N>(); BAR();
      for (int i = I1; i < 7; ++i) issue();
#pragma unroll 1
      for (int t = 2; t < 27; ++t) { wait_vm<4 * N>(); BAR(); issue(); }
      wait_vm<4 * N>(); BAR(); issue4(5, 57344 + 5 * 16384);
      wait_vm<3 * N + 4>(); BAR(); issue4(4, 57344 + 4 * 16384);
      glds4((P & 1) ? w3 : w1, ((KB + ((lane >> 2) < VB3 ? lane >> 2 : 0)) * 2048 + (P >> 1) * 4 + (lane & 3)) * 128, lds_base + LDS_DUMMY);
      wait_vm<2 * N + 9>(); BAR(); if (WE & 1) issue4(3, 57344 + 3 * 16384);
      wait_vm<N + 9 + (WE & 1) * 4>(); BAR(); if (WE & 2) issue4(2, 57344 + 2 * 16384);
      wait_vm<9 + (WE & 1) * 4 + (WE & 2) * 2>(); BAR();
    };
    if (isA) run3(T_()); else run3(F__());
    { BAR();
      BAR();
      issue4(0, 57344);
      BAR();
      issue4(1, 57344 + 16384); if (!(WE & 2)) issue4(2, 57344 + 2 * 16384); if (!(WE & 1)) issue4(3, 57344 + 3 * 16384);
      BAR();
      if (lw == 0) signal(cnt, C3 + tm3, lane);
      issue4(6, 0); issue4(7, 16384);
      if (lw == 0) pollp<PD3, PQ, 1>(cnt + ((C3 + tm4) * 8 + tn6) * 32, 16, pr);
      wait_vm<8>();
      BAR();
      wait_vm<0>();
      KEEP4(pr);
      BAR();
      poll<0, 4>(cnt + (CM * 8 + cg) * 32, 4);
      f32x4 pv[4];
#pragma unroll
      for (int i = 0; i < 4; ++i) pv[i] = __builtin_bit_cast(f32x4, ld16<true>(ws + WS_MOD2, (((lane >> 4) * 4 + i) * 4096 + cg * 64 + (lane & 15) * 4) * 4));
      f32x4 v = (pv[0] + pv[1]) + (pv[2] + pv[3]);
#pragma unroll
      for (int e = 0; e < 4; ++e) { v[e] = rbf(xsum16_32(v[e])); if (lw == 2) v[e] = rbf(1.f + v[e]); if (lw == 1 || lw == 2) v[e] *= -1.4426950408889634f; }
      *(LDS_PTR(f32x4))(smem + LDS_VEC + lw * 256 + (lane & 15) * 16) = v;
      wait_lgkm0();
    }
    auto run5 = [&](auto isA_) {
      constexpr bool A_ = decltype(isA_)::value; constexpr int N = A_ ? 8 : 7;
      const char* sp = A_ ? ws + WS_H2 + (tm5 * 16 + lw * 8) * 1024 : (const char*)(lw == 2 ? w1 : w3) + tn5 * 224;
      unsigned vp[N];
#pragma unroll
      for (int i = 0; i < N; ++i) { const int L = i * 64 + lane, r = L / 14, c = L - r * 14; vp[i] = A_ ? L * 16 : r * (F_ * 2) + c * 16; }
      const unsigned d0 = lds_base + (A_ ? lw * 8192 : lw == 2 ? K5_G : K5_U);
      unsigned st = 3 * K5_STG;
      auto issue = [&]() {
#pragma unroll
        for (int i = 0; i < N; ++i) { if (A_) glds16s(sp, vp[i], d0 + st, i * 1024); else glds16(sp, vp[i], d0 + st, i * 1024); }
        sp += A_ ? 131072 : 32 * F_ * 2; st = st == (K5_NS - 1) * K5_STG ? 0 : st + K5_STG;
      };
      BAR();
      if (!A_) { issue(); issue(); }
      BAR();
      u32x4 xk[4];
      auto keep = [&]() {
#pragma unroll
        for (int i = 0; i < 4; ++i) { xk[i] = *(const LDS_PTR(u32x4))(smem + lw * 4096 + i * 1024 + lane * 16); asm volatile("" : "+v"(xk[i])); }
      };
      if (A_ && lw == 0) { signal(cnt, C4 + tm4, lane); keep(); pollp<PD4, PQ, 3>(cnt + ((C4 + tm5 * 2 + (lane & 1)) * 8 + (tn5 & 7)) * 32, 16, pr); }
      else { keep(); if (!A_) { issue(); issue(); issue(); } }
      BAR();
      if (A_) { issue(); issue(); }
      wait_vm<A_ ? N : 4 * N>();
      KEEP4(pr);
      BAR();
      if (A_) issue();
      wait_vm<A_ ? N : 3 * N>();
      BAR();
      if (A_) { issue(); issue(); }
#pragma unroll 1
      for (int j = 0; j < 27; ++j) {
        wait_vm<2 * N>();
        BAR();
        issue();
        if (j == 6) {
#pragma unroll
          for (int i = 0; i < 4; ++i) st16<true>(ws + WS_PART, (b * 4 + lw) * 4096 + i * 1024 + lane * 16, (u32x4){~0u, ~0u, ~0u, ~0u});
        }
        if (j == 12 && lw == 0 && lane == 0) __hip_atomic_store((unsigned*)(ws + WS_FLG) + b * 32, 16u, __ATOMIC_RELAXED, __HIP_MEMORY_SCOPE_AGENT);
        touch(); __builtin_amdgcn_s_sleep(TS5); touch();
      }
      const char* sb = A_ ? ws + WS_HH + (kh6 * 56 * 128 + tm6 * 8 + lw * 4) * 1024 : (const char*)w2 + kh6 * 1792 * 2048 + tn6 * 256 + ((lw ^ kh6 ^ 1) & 1) * 128;
      unsigned vo[4];
#pragma unroll
      for (int i = 0; i < 4; ++i) {
        const int r = i * 8 + (lane >> 3), k = 8 * ((r >> 2) & 3) + 4 * (r >> 4) + (r & 3);
        vo[i] = A_ ? lane * 16 : k * 2048 - i * 1024 + swz;
      }
      const unsigned d6 = lds_base + (A_ ? K6_A : 0) + (lw & 1) * 4096;
      st = 0;
      const int tab = w2_stage(lane);
      int jn = 0;
      auto issue6 = [&]() {
        if (A_) { glds16x4<true>(sb, sb, vo, d6 + st); sb += 131072; }
        else { const int o = __builtin_amdgcn_readlane(tab, jn); const char* s0 = sb + (o & ~1); glds16x4<false>(s0, s0 + (o & 1) * 65536, vo, d6 + st); ++jn; }
        st = st == ((A_ ? K6_NA : K6_NW) - 1) * 8192 ? 0 : st + 8192;
      };
      auto pre6 = [&](int n) {
        if (!A_) {
#pragma unroll 1
          for (int i = 0; i < n; ++i) issue6();
        }
      };
      constexpr int E = A_ ? 0 : 4;
      { const char* p = (const char*)w2 + (kh6 * 1792 + ii) * 2048;
        const int n = lw * 56 + (lane < 56 ? lane : 0), o = w2_stage((xc & 3) * 14 + (n >> 4));
        glds4(p, (o & ~1) + (o & (ii >> 4) & 1) * 65536 + (n & 15) * 128, lds_base + LDS_DUMMY); }
      wait_vm<2 * N + 1>(); BAR();
      wait_vm<N + 1>(); BAR(); pre6(PA6);
      wait_vm<PA6 * E + 1>(); BAR(); pre6(PB6);
      BAR(); pre6(12 - PA6 - PB6);
      BAR();
      if (A_) {
        volatile LDS_PTR(unsigned) go = (volatile LDS_PTR(unsigned))(smem + LDS_ARR + 4);
        if (lw == 0) { pollp<PD5, PQ5, 1>(cnt + ((C5 + (tm6 >> 1) * 4 + kh6 * 2) * 8 + tn6) * 32, 16, pr); *go = 1u; }
        else {
#pragma unroll 1
          for (unsigned spins = 0; spins < SPIN_CAP && __builtin_amdgcn_readfirstlane(*go) == 0; ++spins) __builtin_amdgcn_s_sleep(1);
          asm volatile("" ::: "memory");
        }
        STAMP_L(31);
        issue6(); issue6(); issue6();
        wait_vm<8>(); KEEP4(pr); BAR(); issue6(); issue6();
        wait_vm<12>(); BAR(); issue6(); issue6();
      } else { wait_vm<16>(); BAR(); BAR(); }
#pragma unroll 1
      for (int c = 2; c < 51; ++c) { wait_vm<16>(); BAR(); if (A_ || !((SKIP6 >> c) & 1)) issue6(); }
      wait_vm<16>(); BAR(); wait_vm<12>(); BAR(); wait_vm<8>(); BAR(); wait_vm<4>(); BAR(); wait_vm<0>(); BAR();
#pragma unroll
      for (int i = 0; i < 4; ++i) *(LDS_PTR(u32x4))(smem + K6_X + lw * 4096 + i * 1024 + lane * 16) = xk[i];
      BAR(); wait_lgkm0(); BAR();
    };
    if (isA) run5(T_()); else run5(F__());
    asm volatile("s_waitcnt lgkmcnt(0)" ::: "memory");
    return;
  }

  int fr = lane & 15, g = lane >> 4, p4 = fr & 3, row = 4 * g + (fr >> 2);
  int lsw = row * 128 + (((row >> 1) & 3) * 32) + p4 * 8;
  { const int mb = u1, hf = (b >> 3) & 1, kb0 = hf * 16 + wave * 2, cofs = (g & 1) * 16 + (g >> 1) * 8;
    u32x4 v[4];
    auto ldx = [&]() {
#pragma unroll
      for (int p = 0; p < 4; ++p) v[p] = *(const u32x4*)(x + (size_t)(mb * 16 + fr) * D_ + ((kb0 + (p & 1)) ^ ((p >> 1) * 16)) * 32 + cofs);
    };
    LDS_PTR(f32x2) sred = (LDS_PTR(f32x2))(smem + 4096);
    auto up2 = [](unsigned u) { return (f32x2){lo2f(u), hi2f(u)}; };
    auto stats = [&]() {
      f32x2 s2 = {0.f, 0.f}, q2 = {0.f, 0.f};
#pragma unroll
      for (int k = 0; k < 16; ++k) s2 += up2(v[k >> 2][k & 3]);
      const float mw = xsum16_32(s2[0] + s2[1]) * (1.f / 128.f);
#pragma unroll
      for (int k = 0; k < 16; ++k) { const f32x2 d = up2(v[k >> 2][k & 3]) - mw; q2 += d * d; }
      const float qw = xsum16_32(q2[0] + q2[1]);
      if (lane < 16) sred[wave * 16 + fr] = (f32x2){mw, qw};
      wait_lgkm0();
    };
    auto rest = [&]() {
    STAMP_AT(1 );
    float mean = 0.f, m2 = 0.f; f32x2 sw[8];
#pragma unroll
    for (int w = 0; w < 8; ++w) { sw[w] = sred[w * 16 + fr]; mean += sw[w][0]; m2 += sw[w][1]; }
    mean *= 0.125f;
#pragma unroll
    for (int w = 0; w < 8; ++w) { const float d = sw[w][0] - mean; m2 += 128.f * d * d; }
    const float rstd = ln_rstd(m2 * (1.f / 1024.f));
    mean = rbf(mean);
    unsigned nv[8];
#pragma unroll
    for (int k = 0; k < 8; ++k) {
      const f32x2 d = up2(v[k >> 2][k & 3]) - mean;
      const f32x2 n = up2(pk2(d[0], d[1])) * rstd;
      nv[k] = pk2(n[0], n[1]);
      asm volatile("" : "+v"(nv[k]));
    }
    SCHED0(); BAR(); SCHED0();
    STAMP_AT(2 );
    float mp[8];
#pragma unroll
    for (int i = 0; i < 8; ++i) mp[i] = __uint_as_float(__hip_atomic_load((unsigned*)(ws + WS_MOD) + hf * 512 + tid + i * 1024, __ATOMIC_RELAXED, __HIP_MEMORY_SCOPE_AGENT));
    *(LDS_PTR(unsigned))(smem + tid * 4) = pk2((mp[0] + mp[2]) + (mp[4] + mp[6]), 1.f + rbf((mp[1] + mp[3]) + (mp[5] + mp[7])));
    wait_lgkm0();
#pragma unroll
    for (int p = 0; p < 2; ++p) {
      const LDS_PTR(u32x4) tp = (const LDS_PTR(u32x4))(smem + ((wave * 2 + p) * 32 + cofs) * 4);
      const u32x4 t0 = tp[0], t1 = tp[1];
      unsigned o[4];
#pragma unroll
      for (int j = 0; j < 4; ++j) {
        const unsigned ta = j < 2 ? t0[2 * j] : t1[2 * j - 4], tb = j < 2 ? t0[2 * j + 1] : t1[2 * j - 3];
        const unsigned u = pk2(lo2f(nv[p * 4 + j]) * hi2f(ta), hi2f(nv[p * 4 + j]) * hi2f(tb));
        o[j] = pk2(lo2f(u) + lo2f(ta), hi2f(u) + lo2f(tb));
      }
      auto s0 = __builtin_amdgcn_permlane16_swap(o[0], o[2], false, false);
      auto s1 = __builtin_amdgcn_permlane16_swap(o[1], o[3], false, false);
      st16<true>(ws + WS_H, ((kb0 + p) * 128 + mb) * 1024 + lane * 16, (u32x4){s0[0], s1[0], s0[1], s1[1]});
    }
    STAMP_AT(3 );
    wait_vm<0>();
    STAMP_AT(4 );
    BAR();
    STAMP_AT(5 );
    };
    if (hf) {
      const int mrow = (u1 & 3) * 256 + (tid >> 3) * 4;
      const u32x2 c2 = *(const u32x2*)(cnd + mrow);
      u32x4 mw[4];
#pragma unroll
      for (int i = 0; i < 4; ++i) mw[i] = __builtin_nontemporal_load((const u32x4*)(wmod + (size_t)(mrow + i) * 6144 + (u1 >> 2) * 64 + (lane & 7) * 8));
      const unsigned mc[2] = {c2[0], c2[1]};
      *(LDS_PTR(f32x2))(smem + 8192 + (wave * 64 + (lane & 7) * 8 + (lane >> 4) * 2) * 4) = slab_sum<4>(mc, mw);
      wait_lgkm0();
      BAR();
      BAR();
      ldx();
      stats();
      BAR();
      rest();
    } else {
      __builtin_amdgcn_s_sleep(XD0);
      ldx();
      SCHED0();
      const int sl = (3 - ((unsigned)(uintptr_t)wqkv >> 7) - (xc >> 1) * 4) & 7;
      const int i2 = tid - 384, r0 = i2 < 0 ? tid / 12 : i2 / 3, c0 = i2 < 0 ? tid - r0 * 12 : (i2 - r0 * 3) * 4 + sl;
      const bool on = i2 < 0 || (sl < 4 && i2 < 96);
      __builtin_amdgcn_s_sleep(PFD);
      const unsigned pf = *(const unsigned*)((const char*)wqkv + (xc >> 1) * 512 + (on ? ((((xc & 1) ^ (i2 >= 0)) * 512 + (ii >> 1) * 32 + r0) * 48 + (c0 >> 2) * 16 + (c0 & 3)) * 128 : 0));
      SCHED0(); BAR(); SCHED0();
      stats();
      SCHED0(); BAR(); SCHED0();
      rest();
      asm volatile("" ::"v"(pf));
    }
  }
  {
    unsigned long long vis[4];
    { const int kbase = tm3 * 128 + wave * 16 - 16;
#pragma unroll
      for (int t = 0; t < 4; ++t) {
        const int kk = (t >> 1) * 16 + 4 * g + (t & 1) * 2 + (fr & 1), df = 16 + fr - kk;
        vis[t] = __builtin_amdgcn_ballot_w64(df >= 0 && df <= 14 && kbase + kk >= 0);
        asm volatile("" : "+s"(vis[t]));
      } }
    u32x4 mc, mw[8];
    if (wave < 4) {
      const int wm = wave & 1, bA = wave;
      const unsigned s0 = lds_base + 3 * K3_STG;
      unsigned X = s0 + K3_B + (lsw ^ (bA * 32)), Y = X ^ 32, aA = s0 + 1024 + wm * 4096 + lane * 16, aH = s0 + lane * 16;
      unsigned lq = lds_base + bA * 512 + lane * 8, lv = lds_base + 36864 + (bA ^ 1) * 512 + (lane ^ ((g >> 1) * 8)) * 8;
      int n = 4, dl = -K3_STG; unsigned mask = wrap_mask(4, K3_NS);
      asm volatile(".irp r,0,8,16,24,32,40,48,56,64,72,80,88,96\n\tv_mov_b64 v[\\r:\\r+1], 0\n\tv_mov_b64 v[\\r+2:\\r+3], 0\n\tv_mov_b64 v[\\r+4:\\r+5], 0\n\tv_mov_b64 v[\\r+6:\\r+7], 0\n\t.endr\n\t"
                   "s_setprio 2\n\ts_waitcnt vmcnt(0)\n\ts_barrier\n\t"
                   SB W3(1, Y, 2) A3(0) W3(0, Y, 1) W3(2, Y, 3) W3(0, X, 0) AD5(Y, X) AD5(aA, aA) AD5(aH, aH) LG(7) Q3(4, 0, 2) RW3(2, 0, X, 2) LG(5) Q3(3, 0, 1) SB
                   W3(0, Y, 1) A3(1) LG(10) Q3(5, 0, 3) M3(4, 5, 0, 3) LG(0)
                   ".p2align 5\n1:\n\t" PAIR3() PAIR3() PAIR3("s_sub_u32 %[n], %[n], 1\n\t") "s_cbranch_scc0 1b\n\t"
                   HEAD3(0, 2, 1, X, Y)
                   GL3(2, 0, RW3(1, 0, Y, 3), RW3(1, 1, Y, 3), RW3(2, 0, Y, -10), RW3(2, 1, Y, -10), "v_add_u32 %[X], %[mo], %[lq]\n\tv_add_u32 %[aH], %[mo], %[lv]\n\t",
                       "v_xor_b32 %[Y], 512, %[X]\n\tv_xor_b32 %[aA], 512, %[aH]\n\t", CV3(0, 0, 0), CV3(1, 0, 1), LG(0) SB, WR3(0, 0, X, 0) WR3(1, 1, X, 0))
                   M3(0, 3, 1, 2) CV3(2, 0, 2) M3(1, 3, 1, 2) WR3(2, 2, X, 0) CV3(3, 0, 3) M3(2, 3, 1, 2) WR3(3, 3, X, 0) CV3(0, 1, 0) M3(3, 3, 1, 2) WR3(0, 0, aA, 0) CV3(1, 1, 1)
                   M3(3, 4, 1, 3) WR3(1, 1, aA, 0) CV3(2, 1, 2) M3(2, 4, 1, 3) WR3(2, 2, aA, 0) CV3(3, 1, 3) M3(1, 4, 1, 3) WR3(3, 3, aA, 0) CV3(0, 2, 0) M3(0, 4, 1, 3) WR3(0, 0, X, 18432) CV3(1, 2, 1)
                   M3(0, 5, 1, -10) WR3(1, 1, X, 18432) CV3(2, 2, 2) M3(1, 5, 1, -10) WR3(2, 2, X, 18432) CV3(3, 2, 3) M3(2, 5, 1, -10) WR3(3, 3, X, 18432) CV3(4, 2, 0)
                   M3(3, 5, 1, -10) WR3(0, 0, lq, 18432) CV3(0, 3, 1) M3(4, 5, 1, -10) WR3(0, 1, Y, 0)
                   CW3(1, 3, 2, Y, 0) CW3(2, 3, 3, Y, 0) CW3(3, 3, 0, Y, 0) CW3(3, 4, 1, Y, 18432) CW3(2, 4, 2, Y, 18432) CW3(1, 4, 3, Y, 18432) CW3(0, 4, 0, Y, 18432)
                   CW3(0, 5, 1, aH, 0) CW3(1, 5, 2, aH, 0) CW3(2, 5, 3, aH, 0) CW3(3, 5, 0, aH, 0) CV3(4, 5, 1) WR3(0, 1, lv, 0)
                   "s_setprio 0"
                   : [X] "+v"(X), [Y] "+v"(Y), [aA] "+v"(aA), [aH] "+v"(aH), [n] "+s"(n), [dl] "+s"(dl), [mask] "+s"(mask)
                   : [lq] "v"(lq), [lv] "v"(lv), [mo] "s"((wm * 4 + 1) * 2048), [wrap] "s"((K3_NS - 1) * K3_STG)
                   : "memory", "scc", V10(), V10(1), V10(2), V10(3), V10(4), V10(5), V10(6), V10(7), V10(8), V10(9), V10(10), V10(11), V10(12), V10(13), V10(14), V10(15));
    } else {
      wait_vm<0>();
      BAR(); BAR();
      int k = -4; const int P = b * 4 + (wave & 3);
      auto idle = [&]() {
        if (k < KB && __builtin_amdgcn_s_getreg((3 << 11) | (8 << 6) | 7) <= 7) {
          stouch4(k < 0 ? (const char*)wo + ((k + 4) * 4096 + P * 4) * 128 : (const char*)((P & 1) ? w3 : w1) + (k * 2048 + (P >> 1) * 4) * 128); ++k; }
        BAR(); BAR();
      };
      {
        const unsigned* pc = cnt + ((C2 + ((lane & 1) && tm3 ? tm3 - 1 : tm3)) * 8 + (tn3 & 7)) * 32;
        unsigned seen = 0x80008u;
        idle(); idle(); idle();
        if (wave == 4) asm volatile("global_load_dword %0, %1, off sc1" : "=v"(seen) : "v"(pc) : "memory");
        idle(); idle();
        asm volatile("s_waitcnt vmcnt(0)" : "+v"(seen)::"memory");
        if (__builtin_amdgcn_ballot_w64(seen < 0x80008u) != 0) poll<0>(pc, 0x80008u);
      }
#pragma unroll 1
      for (int t = 5; t < 14; ++t) idle();
#pragma unroll
      for (int t = 28; t < SLB; ++t) BAR();
      wait_lgkm0();
      const int mrow = ((b & 3) * 4 + (wave & 3)) * 64 + (lane >> 3) * 8;
      mc = *(const u32x4*)(cnd + mrow);
#pragma unroll
      for (int i = 0; i < 8; ++i) mw[i] = __builtin_nontemporal_load((const u32x4*)(wmod + (size_t)(mrow + i) * 6144 + 2048 + (b >> 2) * 64 + (lane & 7) * 8));
      SCHED0();
#pragma unroll
      for (int t = SLB; t < 33; ++t) BAR();
    }
    lane = lane_id(); fr = lane & 15; g = lane >> 4; p4 = fr & 3; row = 4 * g + (fr >> 2); lsw = row * 128 + (((row >> 1) & 3) * 32) + p4 * 8;
    f32x4 bias[2];
#pragma unroll
    for (int t = 0; t < 8; ++t) bias[t >> 2][t & 3] = __builtin_amdgcn_inverse_ballot_w64(vis[t >> 1]) ? 0.f : -__builtin_inff();
    asm volatile("" : "+v"(bias[0]), "+v"(bias[1]));
    wait_lgkm0();
    BAR();
    STAMP_AT(9 );
    const LDS_PTR(char) eb = smem + wave * 2048;
    auto ld2 = [&](int o) { return cat8(*(const LDS_PTR(bf16x4))(eb + o), *(const LDS_PTR(bf16x4))(eb + o + 512)); };
    const int vo = 36864 + ((p4 * 16 + row) ^ ((p4 >> 1) * 8)) * 8;
    bf16x8 qf[2], k0[2], k1[2], v0[2], v1[2];
#pragma unroll
    for (int hd = 0; hd < 2; ++hd) { qf[hd] = ld2(2048 + hd * 1024 + lane * 8); k0[hd] = ld2(18432 + hd * 1024 + lane * 8); k1[hd] = ld2(18432 + 2048 + hd * 1024 + lane * 8); }
#pragma unroll
    for (int hd = 0; hd < 2; ++hd) {
      v0[hd] = cat8(ld_tr4(eb + vo + hd * 1024), ld_tr4(eb + vo + hd * 1024 + 2048));
      v1[hd] = cat8(ld_tr4(eb + vo + hd * 1024 + 512), ld_tr4(eb + vo + hd * 1024 + 512 + 2048));
    }
    SCHED0();
    const f32x4 z = {0.f, 0.f, 0.f, 0.f};
    const bool odd = fr & 1;
    const unsigned sel0 = odd ? 0x01000c0cu : 0x0c0c0100u, sel1 = odd ? 0x03020c0cu : 0x0c0c0302u;
    float inf = __builtin_inff(); asm("" : "+v"(inf));
    auto mx2 = [&](float a, float c) { return __builtin_amdgcn_fmed3f(a, c, inf); };
    auto both = [](float a, float c, auto op, auto fin) {
      auto u = __builtin_amdgcn_permlane16_swap(__float_as_uint(a), __float_as_uint(c), false, false);
      float r = op(__uint_as_float(u[0]), __uint_as_float(u[1]));
      auto w = __builtin_amdgcn_permlane32_swap(__float_as_uint(r), __float_as_uint(r), false, false);
      r = fin(op(__uint_as_float(w[0]), __uint_as_float(w[1])));
      auto y = __builtin_amdgcn_permlane16_swap(__float_as_uint(r), __float_as_uint(r), false, false);
      return (f32x2){__uint_as_float(y[0]), __uint_as_float(y[1])};
    };
    f32x2 l[2][2];
#pragma unroll
    for (int hd = 0; hd < 2; ++hd) {
      const f32x4 s0 = __builtin_amdgcn_mfma_f32_16x16x32_bf16(k0[hd], qf[hd], bias[0], 0, 0, 0), s1 = __builtin_amdgcn_mfma_f32_16x16x32_bf16(k1[hd], qf[hd], bias[1], 0, 0, 0);
#pragma unroll
      for (int t = 0; t < 2; ++t) {
        const f32x4 sv = t ? s1 : s0;
        unsigned u = pk2(odd ? sv[1] : sv[0], odd ? sv[3] : sv[2]);
        u = pk2(lo2f(u) * 0.17677669529663687f, hi2f(u) * 0.17677669529663687f);
        l[hd][t] = (f32x2){lo2f(u), hi2f(u)};
      }
    }
    const f32x2 mx = both(mx2(mx2(l[0][0][0], l[0][0][1]), mx2(l[0][1][0], l[0][1][1])), mx2(mx2(l[1][0][0], l[1][0][1]), mx2(l[1][1][0], l[1][1][1])), mx2,
                          [](float r) { return r * -1.4426950408889634f; });
    float sm[2];
#pragma unroll
    for (int hd = 0; hd < 2; ++hd) {
#pragma unroll
      for (int t = 0; t < 2; ++t) { const f32x2 e = l[hd][t] * 1.4426950408889634f + mx[hd]; l[hd][t] = (f32x2){__builtin_amdgcn_exp2f(e[0]), __builtin_amdgcn_exp2f(e[1])}; }
      const f32x2 a = l[hd][0] + l[hd][1]; sm[hd] = a[0] + a[1];
    }
    const f32x2 inv = both(sm[0], sm[1], [](float a, float c) { return a + c; }, [](float r) { return __builtin_amdgcn_rcpf(r); });
#pragma unroll
    for (int hd = 0; hd < 2; ++hd) {
      u32x4 pp;
#pragma unroll
      for (int t = 0; t < 2; ++t) {
        const f32x2 p = l[hd][t] * inv[hd]; const unsigned u = pk2(p[0], p[1]);
        pp[2 * t] = __builtin_amdgcn_perm(u, u, sel0); pp[2 * t + 1] = __builtin_amdgcn_perm(u, u, sel1);
      }
      const bf16x8 pf = __builtin_bit_cast(bf16x8, pp);
      const f32x4 oa = __builtin_amdgcn_mfma_f32_16x16x32_bf16(v0[hd], pf, z, 0, 0, 0), ob = __builtin_amdgcn_mfma_f32_16x16x32_bf16(v1[hd], pf, z, 0, 0, 0);
      u32x4 ov; ov[0] = pk2(oa[0], oa[1]); ov[1] = pk2(oa[2], oa[3]); ov[2] = pk2(ob[0], ob[1]); ov[3] = pk2(ob[2], ob[3]);
      st16<true>(ws + WS_O, ((tn3 * 2 + hd) * 128 + tm3 * 8 + wave) * 1024 + lane * 16, ov);
    }
    STAMP_AT(10 );
    wait_vm<0>();
    STAMP_AT(11 );
    BAR();
    STAMP_AT(12 );
    if (wave >= 4) {
      const unsigned m4[4] = {mc[0], mc[1], mc[2], mc[3]};
      const f32x2 ps = slab_sum<8>(m4, mw);
      if (!(lane & 8))
        __hip_atomic_store((unsigned long long*)(ws + WS_MOD2) + (((b & 3) * 4 + (wave & 3)) * 4096 + (b >> 2) * 64 + (lane & 7) * 8 + (lane >> 4) * 2) / 2,
                           ((unsigned long long)__float_as_uint(ps[1]) << 32) | __float_as_uint(ps[0]), __ATOMIC_RELAXED, __HIP_MEMORY_SCOPE_AGENT);
      wait_vm<0>();
      if (lane == 0 && __hip_atomic_fetch_add((LDS_PTR(unsigned))(smem + LDS_ARR + 8), 1u, __ATOMIC_RELAXED, __HIP_MEMORY_SCOPE_WORKGROUP) == 3)
        __hip_atomic_fetch_add(cnt + (CM * 8 + (b >> 2)) * 32, 1u, __ATOMIC_RELAXED, __HIP_MEMORY_SCOPE_AGENT);
    }
  }
  { const int wm = wave & 3, ks = wave >> 2, mb = tm4 * 8 + wm * 2 + ks;
    constexpr int R = 6;
    u32x4 rx[2];
#pragma unroll
    for (int p = 0; p < 2; ++p) rx[p] = *(const u32x4*)(x + (size_t)(mb * 16 + fr) * D_ + tn4 * 64 + p * 32 + (g & 1) * 16 + (g >> 1) * 8);
    const unsigned va0 = (ks * 128 + mb) * 1024 + lane * 16, va1 = va0 ^ 1024;
    bf16x8 fa[R][2], fw[2][4];
    auto issueA = [&](int t) {
      const char* pa = ws + WS_O + t * 262144;
      fa[t % R][0] = __builtin_bit_cast(bf16x8, ld16<true>(pa, va0));
      fa[t % R][1] = __builtin_bit_cast(bf16x8, ld16<true>(pa, va1));
    };
    unsigned aw[4];
    auto rdw = [&](int t, int j) {
      const int o = (t & 3) * 8192;
      fw[t & 1][j] = cat8(ld_tr4(smem + aw[j] + o), ld_tr4(smem + aw[j] + o + 2048));
    };
    auto stepw = [&](int t) {
      const int gi = t >> 2, base = gi < 3 ? 57344 + gi * 32768 : 0;
#pragma unroll
      for (int j = 0; j < 4; ++j) { aw[j] = base + ks * 4096 + (lsw ^ (j * 32)); asm volatile("" : "+v"(aw[j])); }
    };
    auto up2 = [](unsigned u) { return (f32x2){lo2f(u), hi2f(u)}; };
    f32x2 f[8];
    unsigned slot = (mb * 256 + fr + kh6 * 32768 + 4 * g * 16) * 8, sown = (mb * 256 + fr + (g & 1) * 32768 + tn4 * 16) * 8;
    f32x4 acc[2][4];
#pragma unroll
    for (int i = 0; i < 2; ++i)
#pragma unroll
      for (int j = 0; j < 4; ++j) acc[i][j] = (f32x4){0.f, 0.f, 0.f, 0.f};
    stepw(0);
    wait_lgkm0();
    SCHED0(); BAR(); SCHED0();
    STAMP_AT(13 );
#pragma unroll
    for (int t = 0; t < R; ++t) issueA(t);
#pragma unroll
    for (int j = 0; j < 4; ++j) rdw(0, j);
    SCHED0();
#pragma unroll
    for (int t = 0; t < 16; ++t) {
      if (t < 15 && ((t + 1) & 3) == 0) stepw(t + 1);
#pragma unroll
      for (int j = 0; j < 4; ++j) {
        if (t < 15) rdw(t + 1, j);
        SCHED0();
#pragma unroll
        for (int i = 0; i < 2; ++i) acc[i][j] = __builtin_amdgcn_mfma_f32_16x16x32_bf16(fw[t & 1][j], fa[t % R][i], acc[i][j], 0, 0, 0);
        SCHED0();
      }
      if (t + R < 16) issueA(t + R);
      if (t == 2) {
#pragma unroll
        for (int p = 0; p < 2; ++p) {
          auto s0 = __builtin_amdgcn_permlane16_swap(rx[p][0], rx[p][2], false, false);
          auto s1 = __builtin_amdgcn_permlane16_swap(rx[p][1], rx[p][3], false, false);
          f[4 * p] = up2(s0[0]); f[4 * p + 1] = up2(s1[0]); f[4 * p + 2] = up2(s0[1]); f[4 * p + 3] = up2(s1[1]);
        }
#pragma unroll
        for (int j = 0; j < 8; ++j) asm volatile("" : "+v"(f[j]));
        asm volatile("" : "+v"(slot), "+v"(sown));
      }
      if (t == 5) BAR();
      SCHED0();
    }
    STAMP_AT(14 );
    LDS_PTR(f32x4) red = (LDS_PTR(f32x4))(smem + K4_RED);
#pragma unroll
    for (int j = 0; j < 4; ++j) red[((wave ^ 4) * 4 + j) * 64 + lane] = acc[1][j];
    wait_lgkm0();
    BAR();
    STAMP_AT(15 );
    const LDS_PTR(float) vec = (const LDS_PTR(float))(smem + LDS_VEC);
    unsigned o[8];
    f32x4 pr[4], ga[4];
#pragma unroll
    for (int j = 0; j < 4; ++j) { pr[j] = red[(wave * 4 + j) * 64 + lane]; ga[j] = *(const LDS_PTR(f32x4))(vec + j * 16 + 4 * g); }
    SCHED0();
#pragma unroll
    for (int j = 0; j < 4; ++j) {
      const f32x4 y = acc[0][j] + pr[j];
      const f32x2 lo = y.xy * ga[j].xy + f[2 * j], hi = y.zw * ga[j].zw + f[2 * j + 1];
      o[2 * j] = pk2(lo[0], lo[1]); o[2 * j + 1] = pk2(hi[0], hi[1]);
      f[2 * j] = up2(o[2 * j]); f[2 * j + 1] = up2(o[2 * j + 1]);
    }
    const f32x2 s2 = ((f[0] + f[1]) + (f[2] + f[3])) + ((f[4] + f[5]) + (f[6] + f[7]));
    float mt = xsum16_32(s2[0] + s2[1]) * (1.f / 64.f); asm("" : "+v"(mt));
    f32x2 q2 = {0.f, 0.f}, q3 = {0.f, 0.f};
#pragma unroll
    for (int j = 0; j < 8; j += 2) { f[j] -= mt; f[j + 1] -= mt; q2 += f[j] * f[j]; q3 += f[j + 1] * f[j + 1]; }
    q2 += q3;
    const float qq = xsum16_32(q2[0] + q2[1]);
    if (lane < 32) {
      const unsigned qb = qq == qq ? __float_as_uint(qq) : 0x7fc00000u;
      __hip_atomic_store((unsigned long long*)(ws + WS_XCH + sown), ((unsigned long long)qb << 32) | __float_as_uint(mt), __ATOMIC_RELAXED, __HIP_MEMORY_SCOPE_AGENT);
    }
    *(LDS_PTR(u32x4))(smem + (mb & 7) * 2048 + lane * 16) = (u32x4){o[0], o[1], o[2], o[3]};
    *(LDS_PTR(u32x4))(smem + (mb & 7) * 2048 + 1024 + lane * 16) = (u32x4){o[4], o[5], o[6], o[7]};
    STAMP_AT(16 );
    f32x4 ss[4], gg[4];
#pragma unroll
    for (int j = 0; j < 4; ++j) {
      ss[j] = *(const LDS_PTR(f32x4))(vec + 64 + j * 16 + 4 * g); gg[j] = *(const LDS_PTR(f32x4))(vec + 128 + j * 16 + 4 * g);
      f[2 * j] *= gg[j].xy; f[2 * j + 1] *= gg[j].zw;
      asm volatile("" : "+v"(ss[j]), "+v"(gg[j]), "+v"(f[2 * j]), "+v"(f[2 * j + 1]));
    }
    unsigned long long pv[4];
    __builtin_amdgcn_s_sleep(XS0);
    for (unsigned spins = 0;; ++spins) {
      bool ok = true;
#pragma unroll
      for (int i = 0; i < 4; ++i) { pv[i] = __hip_atomic_load((unsigned long long*)(ws + WS_XCH + slot) + i * 16, __ATOMIC_RELAXED, __HIP_MEMORY_SCOPE_AGENT); ok &= (unsigned)(pv[i] >> 32) != 0xffffffffu; }
      if (__builtin_amdgcn_ballot_w64(!ok) == 0 || spins > SPIN_CAP) break;
      __builtin_amdgcn_s_sleep(XS1);
    }
    STAMP_AT(17 );
    float ms = 0.f, m2 = 0.f;
#pragma unroll
    for (int i = 0; i < 4; ++i) { ms += __uint_as_float((unsigned)pv[i]); m2 += __uint_as_float((unsigned)(pv[i] >> 32)); }
    const float mean = xsum16_32(ms) * (1.f / 16.f);
#pragma unroll
    for (int i = 0; i < 4; ++i) { const float d = __uint_as_float((unsigned)pv[i]) - mean; m2 += 64.f * d * d; }
    const float rstd = ln_rstd(xsum16_32(m2) * (1.f / 1024.f));
    const float sh = (mt - rbf(mean)) * rstd;
#pragma unroll
    for (int p = 0; p < 2; ++p) {
      u32x4 v;
#pragma unroll
      for (int j = 0; j < 4; ++j) {
        const int jb = 2 * p + (j >> 1);
        const f32x2 h = f[4 * p + j] * rstd + (((j & 1) ? gg[jb].zw : gg[jb].xy) * sh + ((j & 1) ? ss[jb].zw : ss[jb].xy));
        v[j] = pk2(h[0], h[1]);
      }
      st16<true>(ws + WS_H2, ((tn4 * 2 + p) * 128 + mb) * 1024 + lane * 16, v);
    }
    STAMP_AT(18 );
    wait_vm<0>();
    wait_lgkm0();
    BAR();
    STAMP_AT(19 );

  }
  const int grp = wave >> 2;
  { const int wm = wave & 3;
    const LDS_PTR(char) sb = smem + 3 * K5_STG;
    const LDS_PTR(char) X = sb + K5_G + row * 224 + grp * 96 + p4 * 8;
    const LDS_PTR(char) Y = X + (grp ? 0 : K5_STG); X += grp ? K5_STG : 0;
    const LDS_PTR(char) aM = sb + (p4 < 2 ? K5_G : K5_U) + row * 224 + (12 + grp) * 16 + (p4 & 1) * 8;
    const LDS_PTR(char) aA = sb + wm * 4096 + lane * 16;
    f32x32 c0, c1, c2; f32x16 c3;
    asm volatile(".irp r,0,8,16,24,32,40,48,56,64,72,80,88,96,104\n\tv_mov_b64 v[\\r:\\r+1], 0\n\tv_mov_b64 v[\\r+2:\\r+3], 0\n\tv_mov_b64 v[\\r+4:\\r+5], 0\n\tv_mov_b64 v[\\r+6:\\r+7], 0\n\t.endr"
                 : "={v[0:31]}"(c0), "={v[32:63]}"(c1), "={v[64:95]}"(c2), "={v[96:111]}"(c3)::"memory");
    int wrap = -(K5_NS - 1) * K5_STG, dl = K5_STG, na = grp ? 99 : 7, nb = grp ? 7 : 99;
    unsigned mask = wrap_mask(4, K5_NS);
    asm volatile("" : "+s"(wrap), "+v"(X), "+v"(Y), "+v"(aM), "+v"(aA));
    if (grp) __builtin_amdgcn_s_setprio(1); else __builtin_amdgcn_s_setprio(2);
    BAR();
    STAMP_AT(20 );
    BAR();
    STAMP_G(21, 26);
    asm volatile("s_cmp_eq_u32 %[na], 7\n\ts_cbranch_scc1 0f\n\t"
                 W5(0, Y, 0) A5(1) W5(1, Y, 1) W5(2, Y, 2) W5(3, Y, 3) AD5(aA, aA) LG(6) Q5(0, 1, 0) W5(4, Y, 0) LG(6) Q5(1, 1, 1) SB
                 A5(0) W5(4, X, 1) LG(8) Q5(2, 1, 2) RW5(5, 0, Y, 2) Q5(3, 1, 3) LG(0) "s_branch 2f\n"
                 "0:\n\t" W5(0, aM, 2) A5(0) W5(4, X, 1) W5(5, X, 3) W5(0, X, 0) AD5(aA, aA) AD5(aM, aM) LG(6) Q5(6, 0, 2) RW5(1, 0, X, 2) LG(5) Q5(4, 0, 1) SB
                 A5(1) W5(0, Y, 1) LG(9) Q5(5, 0, 3) LG(0)
                 ".p2align 5\n1:\n\t" TRIPA(0, ) "2:\n\t" TRIPB(0, ) TRIPA(1, "s_sub_u32 %[na], %[na], 1\n\t") "s_cbranch_scc1 3f\n\t" TRIPB(1, "s_sub_u32 %[nb], %[nb], 1\n\t") "s_cbranch_scc0 1b\n\t"
                 RW5(1, 1, X, 2) W5(2, X, 3) W5(3, X, 1) Q5(0, 0, 0) LG(0) SB Q5(1, 0, 2) Q5(2, 0, 3) Q5(3, 0, 1) "s_branch 4f\n"
                 "3:\n\t" RW5(5, 1, Y, 0) W5(0, aM, 1) Q5(4, 1, 2) LG(0) SB Q5(5, 1, 0) Q5(6, 1, 1)
                 "4:\n\ts_nop 8"
                 : "+{v[0:31]}"(c0), "+{v[32:63]}"(c1), "+{v[64:95]}"(c2), "+{v[96:111]}"(c3), [X] "+v"(X), [Y] "+v"(Y), [aM] "+v"(aM), [aA] "+v"(aA), [dl] "+s"(dl), [mask] "+s"(mask),
                   [na] "+s"(na), [nb] "+s"(nb)
                 : [wrap] "s"(wrap) : "memory", "scc", "v112", "v113", "v114", "v115", "v116", "v117", "v118", "v119", V10(12), V10(13), V10(14), V10(15));
    f32x4 acc[4][7];
#pragma unroll
    for (int e = 0; e < 112; ++e) acc[e / 28][(e % 28) / 4][e & 3] = e < 32 ? c0[e & 31] : e < 64 ? c1[e & 31] : e < 96 ? c2[e & 31] : c3[e & 15];
    STAMP_G(22, 27);
    auto swi4 = [](const f32x4& gv, const f32x4& uv, unsigned& o0, unsigned& o1) {
      constexpr float c2 = 2.0813689810056077f;
      f32x2 pa = gv.xy * uv.xy, pb = gv.zw * uv.zw;
      const f32x2 ta = (f32x2){__builtin_amdgcn_exp2f(gv[0]), __builtin_amdgcn_exp2f(gv[1])} * c2 + c2, tb = (f32x2){__builtin_amdgcn_exp2f(gv[2]), __builtin_amdgcn_exp2f(gv[3])} * c2 + c2;
      pa *= (f32x2){__builtin_amdgcn_rcpf(ta[0]), __builtin_amdgcn_rcpf(ta[1])}; pb *= (f32x2){__builtin_amdgcn_rcpf(tb[0]), __builtin_amdgcn_rcpf(tb[1])};
      o0 = pk2(pa[0], pa[1]); o1 = pk2(pb[0], pb[1]);
    };
    const int lz = lane_id();
    const int gz = lz >> 4, fz = lz & 15;
    const int qb = (tn5 >> 4) * 224, ql = qb + 64 + (tn5 & 15) * 10;
    auto hof = [&](int e, int i) { return (unsigned)((e >> 2) * 131072 + (e & 3) * 256 + fz * 16 + i * 1024); };
    const unsigned ho0 = hof((grp ? ql + 2 : qb + (tn5 & 15) * 4) + 2 * (gz & 1) + (gz >> 1), 0), ho1 = hof(ql + 6 * grp + (gz >> 1), gz & 1), hom = hof(ql + 8 + grp, gz);
    char* hb = ws + WS_HH + (tm5 * 16 + wm * 4) * 1024;
    const int c5 = C5 + tm5 * 4 + (tn5 >> 4) * 2;
    auto tr16 = [](const unsigned (&o)[4]) {
      auto s0 = __builtin_amdgcn_permlane16_swap(o[0], o[2], false, false);
      auto s1 = __builtin_amdgcn_permlane16_swap(o[1], o[3], false, false);
      u32x4 v = {s0[0], s1[0], s0[1], s1[1]}; asm volatile("" : "+v"(v)); return v;
    };
    auto pair = [&](int p, int i) {
      unsigned o[4];
      swi4(acc[i][2 * p], acc[i][2 * p + 1], o[0], o[1]);
      swi4(acc[i + 1][2 * p], acc[i + 1][2 * p + 1], o[2], o[3]);
      return tr16(o);
    };
    u32x4 v[3];
#pragma unroll
    for (int i = 0; i < 4; ++i) {
      unsigned o[4];
      swi4(acc[i][0], acc[i][1], o[0], o[1]);
      swi4(acc[i][2], acc[i][3], o[2], o[3]);
      st16<true>(hb, ho0 + i * 1024, tr16(o));
      SCHED0();
    }
    STAMP_G(23, 28);
    v[0] = pair(2, 0);
    if (!grp) {
      wait_vm<0>();
      unsigned arr = 0;
      if (lz == 0) arr = __hip_atomic_fetch_add((LDS_PTR(unsigned))(smem + LDS_ARR), 1u, __ATOMIC_RELAXED, __HIP_MEMORY_SCOPE_WORKGROUP);
      if (__builtin_amdgcn_readfirstlane(arr) == 3) signal(cnt, c5, lz);
    }
    STAMP_G(24, 29);
    v[1] = pair(2, 2);
    { unsigned m[4];
#pragma unroll
      for (int i = 0; i < 4; i += 2) {
        f32x4 gv, uv;
#pragma unroll
        for (int e = 0; e < 4; ++e) {
          auto w = __builtin_amdgcn_permlane32_swap(__float_as_uint(acc[i + (e >> 1)][6][e & 1]), __float_as_uint(acc[i + (e >> 1)][6][2 + (e & 1)]), false, false);
          gv[e] = __uint_as_float(w[0]); uv[e] = __uint_as_float(w[1]);
        }
        swi4(gv, uv, m[i], m[i + 1]);
      }
      auto x0 = __builtin_amdgcn_permlane32_swap(m[0], m[2], false, false);
      auto x1 = __builtin_amdgcn_permlane32_swap(m[1], m[3], false, false);
      const unsigned o[4] = {x0[0], x0[1], x1[0], x1[1]};
      v[2] = tr16(o);
      st16<true>(hb, ho1, v[0]); st16<true>(hb, ho1 + 2048, v[1]); st16<true>(hb, hom, v[2]);
    }
    STAMP_G(25, 30);
    if (grp) {
#pragma unroll
      for (int t = 0; t < NDR; ++t) BAR();
      wait_vm<0>();
      BAR();
      if (wave == 4) signal(cnt, c5 + 1, lz);
      if (wave > 4) return;
#pragma unroll 1
      for (int t = NDR + 1; t < 9; ++t) BAR();
      unsigned seen = 16;
      const unsigned* pc = (lz & 1) ? (const unsigned*)(ws + WS_FLG) + (b ^ 4) * 32 : cnt + ((C5 + (tm6 >> 1) * 4 + kh6 * 2 + 1) * 8 + tn6) * 32;
      asm volatile("global_load_dword %0, %1, off sc1" : "=v"(seen) : "v"(pc) : "memory");
      BAR(); BAR();
      asm volatile("s_waitcnt vmcnt(0)" : "+v"(seen)::"memory");
      if (__builtin_amdgcn_ballot_w64(seen < 16) != 0) poll<0>(pc, 16);
      return;
    }
  }
  { lane = lane_id(); fr = lane & 15; g = lane >> 4; p4 = fr & 3; row = 4 * g + (fr >> 2);
    const int wm = wave & 1, wn = wave >> 1;
    const int lw0 = row * 128 + (((row >> 1) & 3) * 32) + p4 * 8;
    const int mb0 = tm6 * 8 + wm * 4, t64 = tn6 * 2 + kh6;
    unsigned a0 = lds_base + (lw0 ^ (wn * 64)), a1 = lds_base + (lw0 ^ (wn * 64 + 32)), aa = lds_base + K6_A + wm * 4096 + lane * 16;
    int dw, da, n = 4, n2 = 1;
    unsigned mw = wrap_mask(2, K6_NW), ma = wrap_mask(2, K6_NA);
    f32x32 c0, c1;
    asm volatile(".irp r,0,8,16,24,32,40,48,56\n\tv_mov_b64 v[\\r:\\r+1], 0\n\tv_mov_b64 v[\\r+2:\\r+3], 0\n\tv_mov_b64 v[\\r+4:\\r+5], 0\n\tv_mov_b64 v[\\r+6:\\r+7], 0\n\t.endr\n\t"
                 "s_waitcnt vmcnt(0)\n\ts_barrier\n\t"
                 RW6(1, 0, 0, 0) RW6(1, 0, 1, 0) RA6(0, 0) RW6(1, 1, 0, 0) RW6(1, 1, 1, 0) RA6(1, 0) RA6(2, 0) RA6(3, 0) RW6(0, 0, 0, 0) RW6(0, 0, 1, 0) RW6(0, 1, 0, 0) RW6(0, 1, 1, 0)
                 "v_add_u32 %[a0], 8192, %[a0]\n\tv_add_u32 %[a1], 8192, %[a1]\n\tv_add_u32 %[aa], 8192, %[aa]\n\t" LG(4) "s_barrier\n\t"
                 RW6(0, 0, 0, 1) RW6(0, 0, 1, 1) RA6(0, 1) RW6(0, 1, 0, 1) M6(0, 2, 0) RW6(0, 1, 1, 1) M6(0, 3, 0) RA6(1, 1) M6(1, 3, 0) RA6(2, 1) M6(1, 2, 0) RA6(3, 1)
                 M6(2, 2, 0) RW6(1, 0, 0, 1) M6(2, 3, 0) RW6(1, 0, 1, 1) M6(3, 3, 0) RW6(1, 1, 0, 1) M6(3, 2, 0) RW6(1, 1, 1, 1)
                 "v_add_u32 %[a0], 8192, %[a0]\n\tv_add_u32 %[a1], 8192, %[a1]\n\tv_add_u32 %[aa], 8192, %[aa]\n\t" LG(6) "s_barrier\n\t"
                 ".p2align 5\n1:\n\t" TRIP6(0, 0, 1, 2, ) TRIP6(1, 1, 2, 0, ) TRIP6(0, 2, 0, 1, ) TRIP6(1, 0, 1, 2, ) TRIP6(0, 1, 2, 0, ) TRIP6(1, 2, 0, 1, "s_sub_u32 %[n], %[n], 1\n\t")
                 "s_cbranch_scc0 1b\n\ts_mov_b32 %[n], 3\n\ts_mov_b32 %[mw], %[mw2]\n\ts_mov_b32 %[ma], %[ma2]\n\ts_sub_u32 %[n2], %[n2], 1\n\ts_cbranch_scc0 1b\n\t"
                 M6(0, 0, 0) M6(0, 0, 1) M6(0, 1, 1) M6(0, 1, 0) LG(0) M6(1, 1, 0) M6(1, 1, 1) M6(1, 0, 1) M6(1, 0, 0) M6(2, 0, 0) M6(2, 0, 1) M6(2, 1, 1) M6(2, 1, 0)
                 M6(3, 1, 0) M6(3, 1, 1) M6(3, 0, 1) M6(3, 0, 0) "s_barrier\n\t"
                 M6(0, 2, 1) M6(0, 3, 1) M6(1, 3, 1) M6(1, 2, 1) M6(2, 2, 1) M6(2, 3, 1) M6(3, 3, 1) M6(3, 2, 1) "s_nop 8"
                 : "={v[0:31]}"(c0), "={v[32:63]}"(c1), [a0] "+v"(a0), [a1] "+v"(a1), [aa] "+v"(aa), [dw] "=&s"(dw), [da] "=&s"(da), [mw] "+s"(mw), [ma] "+s"(ma), [n] "+s"(n), [n2] "+s"(n2)
                 : [ww] "s"(-(K6_NW - 1) * 8192), [wa] "s"(-(K6_NA - 1) * 8192), [mw2] "n"(wrap_mask(32, K6_NW)), [ma2] "n"(wrap_mask(32, K6_NA))
                 : "memory", "scc", "v64", "v65", "v66", "v67", "v68", "v69", V10(7), V10(8), V10(9), V10(10), V10(11), V10(12), V10(13), V10(14), V10(15));
    f32x4 acc[4][4];
#pragma unroll
    for (int e = 0; e < 64; ++e) acc[e >> 4][(e >> 2) & 3][e & 3] = e < 32 ? c0[e & 31] : c1[e & 31];
    STAMP_AT(34 );
    lane = lane_id(); fr = lane & 15; g = lane >> 4;
    const int slot = wm * 2 + wn, pb = (b ^ 4) * 4 + slot;
    { auto pk = [](float lo, float hi) { const unsigned u = pk2(lo, hi); return u < 0xfffffffeu ? u : 0xfffffffeu; };
#pragma unroll
      for (int i = 0; i < 4; ++i)
        st16<true>(ws + WS_PART, (b * 4 + slot) * 4096 + i * 1024 + lane * 16,
                   (u32x4){pk(acc[i][0][0], acc[i][0][1]), pk(acc[i][0][2], acc[i][0][3]), pk(acc[i][1][0], acc[i][1][1]), pk(acc[i][1][2], acc[i][1][3])});
      STAMP_AT(35 );
    }
    { const LDS_PTR(float) vec = (const LDS_PTR(float))(smem + LDS_VEC) + 192 + wn * 32 + 4 * g;
      const f32x4 gv0 = *(const LDS_PTR(f32x4))vec, gv1 = *(const LDS_PTR(f32x4))(vec + 16);
      u32x4 rv[4];
#pragma unroll
      for (int i = 0; i < 4; ++i) { rv[i] = *(const LDS_PTR(u32x4))(smem + K6_X + (wm * 4 + i) * 2048 + wn * 1024 + lane * 16); asm volatile("" : "+v"(rv[i])); }
      u32x4 z[4];
      __builtin_amdgcn_s_sleep(PD6);
#pragma unroll 1
      for (unsigned spins = 0; spins < SPIN_CAP; ++spins) {
        asm volatile("" ::: "memory");
        unsigned m = 0;
#pragma unroll
        for (int i = 0; i < 4; ++i) z[i] = ld16<true>(ws + WS_PART, pb * 4096 + i * 1024 + lane * 16);
#pragma unroll
        for (int i = 0; i < 16; ++i) m = m > z[i >> 2][i & 3] ? m : z[i >> 2][i & 3];
        if (__builtin_amdgcn_ballot_w64(m == ~0u) == 0) break;
        __builtin_amdgcn_s_sleep(PS6);
      }
      STAMP_AT(36 );
#pragma unroll
      for (int i = 0; i < 4; ++i) {
        const f32x4 v0 = (acc[i][2] + (f32x4){lo2f(z[i][0]), hi2f(z[i][0]), lo2f(z[i][1]), hi2f(z[i][1])}) * gv0;
        const f32x4 v1 = (acc[i][3] + (f32x4){lo2f(z[i][2]), hi2f(z[i][2]), lo2f(z[i][3]), hi2f(z[i][3])}) * gv1;
        const unsigned o0 = pk2(lo2f(rv[i][0]) + v0[0], hi2f(rv[i][0]) + v0[1]), o1 = pk2(lo2f(rv[i][1]) + v0[2], hi2f(rv[i][1]) + v0[3]);
        const unsigned o2 = pk2(lo2f(rv[i][2]) + v1[0], hi2f(rv[i][2]) + v1[1]), o3 = pk2(lo2f(rv[i][3]) + v1[2], hi2f(rv[i][3]) + v1[3]);
        auto s0 = __builtin_amdgcn_permlane16_swap(o0, o2, false, false);
        auto s1 = __builtin_amdgcn_permlane16_swap(o1, o3, false, false);
        st16<false>(out, (((mb0 + i) * 16 + fr) * D_ + t64 * 64 + wn * 32 + (g & 1) * 16 + (g >> 1) * 8) * 2, (u32x4){s0[0], s1[0], s0[1], s1[1]});
      }
    }
    STAMP_AT(37 );
  }
}

extern "C" void kernel_launch(void* const* d_in, const int* in_sizes, int n_in, void* d_out, int out_size, void* d_ws,
                              size_t ws_size, hipStream_t stream) {
  const u16* x = (const u16*)d_in[0];
  const u16* wqkv = (const u16*)d_in[1];
  const u16* wo = (const u16*)d_in[2];
  const u16* w1 = (const u16*)d_in[4];
  const u16* w3 = (const u16*)d_in[5];
  const u16* w2 = (const u16*)d_in[6];
  const u16* c = (const u16*)d_in[7];
  const u16* wmod = (const u16*)d_in[8];
  (void)hipFuncSetAttribute((const void*)k2_block, hipFuncAttributeMaxDynamicSharedMemorySize, MEGA_LDS);
  char* ws = (char*)d_ws;
  k2_block<<<256, 768, MEGA_LDS, stream>>>(x, wqkv, wo, w1, w3, w2, c, wmod, (u16*)d_out, ws);
}
```

```cpp
#include <hip/hip_runtime.h>
#include <stdint.h>
#include <type_traits>
#pragma clang diagnostic ignored "-Winline-asm"

typedef unsigned short u16;
typedef short bf16x8 __attribute__((ext_vector_type(8)));
typedef short bf16x4 __attribute__((ext_vector_type(4)));
typedef float f32x4 __attribute__((ext_vector_type(4)));
typedef float f32x2 __attribute__((ext_vector_type(2)));
typedef unsigned int u32x4 __attribute__((ext_vector_type(4)));
typedef unsigned int u32x2 __attribute__((ext_vector_type(2)));
#define LDS_PTR(T) __attribute__((address_space(3))) T*
#define BAR() do { asm volatile("" ::: "memory"); __builtin_amdgcn_s_barrier(); asm volatile("" ::: "memory"); } while (0)
#define SCHED0() __builtin_amdgcn_sched_barrier(0)
template <int N> using IC = std::integral_constant<int, N>;
typedef std::integral_constant<bool, true> T_;
typedef std::integral_constant<bool, false> F__;

constexpr int S_ = 2048, D_ = 1024, F_ = 3584;
#define STAMP_AT(i)
#define STAMP_G(ia, ib)
#define STAMP_L(i)

__device__ __forceinline__ unsigned pk2(float lo, float hi) {
  typedef __bf16 bf2 __attribute__((ext_vector_type(2)));
  f32x2 v = {lo, hi}; bf2 r = __builtin_convertvector(v, bf2); return __builtin_bit_cast(unsigned, r);
}
__device__ __forceinline__ float lo2f(unsigned u) { return __uint_as_float(u << 16); }
__device__ __forceinline__ float hi2f(unsigned u) { return __uint_as_float(u & 0xffff0000u); }
__device__ __forceinline__ float rbf(float v) { return lo2f(pk2(v, 0.f)); }
#define glds16(sbase, voff, lds_off, imm) \
  asm volatile("s_add_u32 m0, %2, %3\n\ts_nop 0\n\tglobal_load_lds_dwordx4 %0, %1" ::"v"(voff), "s"(sbase), "s"(lds_off), "n"(imm) : "memory", "scc")
#define glds16s(sbase, voff, lds_off, imm) \
  asm volatile("s_add_u32 m0, %2, %3\n\ts_nop 0\n\tglobal_load_lds_dwordx4 %0, %1 sc1" ::"v"(voff), "s"(sbase), "s"(lds_off), "n"(imm) : "memory", "scc")
#define GLDS4(SC) "s_mov_b32 m0, %6\n\ts_nop 0\n\tglobal_load_lds_dwordx4 %0, %4" SC "\n\tglobal_load_lds_dwordx4 %1, %5 offset:1024" SC \
                  "\n\tglobal_load_lds_dwordx4 %2, %4 offset:2048" SC "\n\tglobal_load_lds_dwordx4 %3, %5 offset:3072" SC
template <bool SC1> __device__ __forceinline__ void glds16x4(const void* s0, const void* s1, const unsigned (&v)[4], unsigned lds) {
  if (SC1) asm volatile(GLDS4(" sc1") ::"v"(v[0]), "v"(v[1]), "v"(v[2]), "v"(v[3]), "s"(s0), "s"(s1), "s"(lds) : "memory");
  else asm volatile(GLDS4("") ::"v"(v[0]), "v"(v[1]), "v"(v[2]), "v"(v[3]), "s"(s0), "s"(s1), "s"(lds) : "memory");
}
__device__ __forceinline__ void glds4(const void* sbase, unsigned voff, unsigned lds_off) {
  asm volatile("s_mov_b32 m0, %2\n\ts_nop 0\n\tglobal_load_lds_dword %0, %1" ::"v"(voff), "s"(sbase), "s"(lds_off) : "memory");
}
__device__ __forceinline__ void stouch4(const void* p) {
  asm volatile("s_load_dwordx2 s[100:101], %0, 0x3c\n\ts_load_dwordx2 s[100:101], %0, 0xbc\n\ts_load_dwordx2 s[100:101], %0, 0x13c\n\ts_load_dwordx2 s[100:101], %0, 0x1bc"
               ::"s"(p) : "memory", "s100", "s101");
}
template <bool SC1> __device__ __forceinline__ void st16(void* sbase, unsigned voff, u32x4 v) {
  __builtin_amdgcn_raw_buffer_store_b128(v, __builtin_amdgcn_make_buffer_rsrc(sbase, 0, 0x7fffffff, 0x00020000), voff, 0, SC1 ? 16 : 0);
}
template <bool SC1> __device__ __forceinline__ u32x4 ld16(const void* sbase, unsigned voff) {
  return __builtin_amdgcn_raw_buffer_load_b128(__builtin_amdgcn_make_buffer_rsrc((void*)sbase, 0, 0x7fffffff, 0x00020000), voff, 0, SC1 ? 16 : 0);
}
template <int N> __device__ __forceinline__ void wait_vm() { asm volatile("s_waitcnt vmcnt(%0)" ::"n"(N) : "memory"); }
__device__ __forceinline__ void wait_lgkm0() { asm volatile("" ::: "memory"); __builtin_amdgcn_s_waitcnt(0xC07F); asm volatile("" ::: "memory"); }
__device__ __forceinline__ bf16x4 ld_tr4(const LDS_PTR(char) p) { return __builtin_amdgcn_ds_read_tr16_b64_v4i16((LDS_PTR(bf16x4))p); }
__device__ __forceinline__ bf16x8 cat8(bf16x4 lo, bf16x4 hi) { return __builtin_shufflevector(lo, hi, 0, 1, 2, 3, 4, 5, 6, 7); }
constexpr unsigned wrap_mask(int first, int ns) { unsigned m = 0; for (int t = 0; t < 32; ++t) if ((first + t) % ns == ns - 1) m |= 0x80000000u >> t; return m; }
__device__ __forceinline__ int lane_id() { int l; asm volatile("v_mbcnt_lo_u32_b32 %0, -1, 0\n\tv_mbcnt_hi_u32_b32 %0, -1, %0" : "=v"(l)); return l; }
__device__ __forceinline__ float xsum16_32(float v) {
  auto a = __builtin_amdgcn_permlane16_swap(__float_as_uint(v), __float_as_uint(v), false, false); v = __uint_as_float(a[0]) + __uint_as_float(a[1]);
  auto c = __builtin_amdgcn_permlane32_swap(__float_as_uint(v), __float_as_uint(v), false, false); return __uint_as_float(c[0]) + __uint_as_float(c[1]);
}

__device__ __forceinline__ float ln_rstd(float var) { return __builtin_amdgcn_rcpf(rbf(__builtin_amdgcn_sqrtf(rbf(rbf(var) + 1e-5f)))); }

constexpr size_t WS_MOD = 0;
constexpr size_t WS_EPO = 64 << 10, WS_FL1 = 96 << 10;
constexpr size_t WS_CTL = 128 << 10;
constexpr int C2 = 0, C3 = 16, C4 = 32, C5 = 48, CM = 80;
constexpr size_t WS_FLG = WS_CTL + (88 << 10), CTL_BYTES = 120 << 10;
static_assert((512 << 10) + CTL_BYTES == 256 * 158 * 16, "reset shares");
constexpr size_t WS_XCH = 512 << 10;
constexpr size_t WS_MOD2 = 1 << 20;
constexpr size_t WS_H = 2 << 20, WS_O = WS_H + (4 << 20), WS_H2 = WS_O + (4 << 20), WS_HH = WS_H2 + (4 << 20),
                 WS_PART = WS_HH + (size_t)S_ * F_ * 2;

template <int NL> __device__ __forceinline__ f32x2 slab_sum(const unsigned (&mc)[NL / 2], const u32x4 (&mw)[NL]) {
  f32x2 a2[4] = {};
#pragma unroll
  for (int i = 0; i < NL; ++i) {
    const float cv = (i & 1) ? hi2f(mc[i >> 1]) : lo2f(mc[i >> 1]);
    const float sv = rbf(cv * __builtin_amdgcn_rcpf(1.f + __builtin_amdgcn_exp2f(cv * -1.4426950408889634f)));
#pragma unroll
    for (int j = 0; j < 4; ++j) a2[j] += (f32x2){lo2f(mw[i][j]), hi2f(mw[i][j])} * sv;
  }
  float acc[8] = {a2[0][0], a2[0][1], a2[1][0], a2[1][1], a2[2][0], a2[2][1], a2[3][0], a2[3][1]};
#pragma unroll
  for (int j = 0; j < 4; ++j) { auto a = __builtin_amdgcn_permlane32_swap(__float_as_uint(acc[j]), __float_as_uint(acc[j + 4]), false, false); acc[j] = __uint_as_float(a[0]) + __uint_as_float(a[1]); }
#pragma unroll
  for (int j = 0; j < 2; ++j) { auto a = __builtin_amdgcn_permlane16_swap(__float_as_uint(acc[j]), __float_as_uint(acc[j + 2]), false, false); acc[j] = __uint_as_float(a[0]) + __uint_as_float(a[1]); }
#pragma unroll
  for (int j = 0; j < 2; ++j) acc[j] += __uint_as_float(__builtin_amdgcn_mov_dpp(__float_as_uint(acc[j]), 0x128, 0xf, 0xf, false));
  return (f32x2){acc[0], acc[1]};
}

constexpr int K3_B = 9216, K3_STG = 9216 + 12288, K3_NS = 7;
constexpr int K4_RED = 57344;
constexpr int K5_G = 16384, K5_U = 16384 + 7168 + 16, K5_STG = 30736, K5_NS = 5;
constexpr int K6_NW = 12, K6_NA = 7, K6_A = K6_NW * 8192, K6_X = 9 * 8192;
constexpr int LDS_VEC = 158720, LDS_DUMMY = LDS_VEC + 1024, LDS_ARR = LDS_DUMMY + 256, MEGA_LDS = LDS_ARR + 64;
constexpr unsigned SPIN_CAP = 1u << 19;
#define KNOBS PA6 = 5, PB6 = 5, KB = 8, XD0 = 22, PFD = 16, FD1 = 8, FS0 = 4, PQ = 2, PQ5 = 4, PD2 = 4, PD3 = 4, PD4 = 4, PD5 = 48, PD6 = 6, PS6 = 1, XS0 = 14, XS1 = 4, VB3 = 2, SLB = 29, IA0 = 3, IA1 = 5, IW0 = 6, IW1 = 6, NDR = 1
enum { KNOBS };
constexpr unsigned long long SKIP6 = 0x7c;
static_assert(__builtin_popcountll(SKIP6) == 5, "44 stages of W are issued behind 49 barriers");

__device__ __forceinline__ void signal(unsigned* cnt, int c, int lane, unsigned v = 1) {
  if (lane < 8) __hip_atomic_fetch_add(cnt + (c * 8 + lane) * 32, v, __ATOMIC_RELAXED, __HIP_MEMORY_SCOPE_AGENT);
}
template <int D0, int D1 = 1> __device__ __forceinline__ void poll(const unsigned* cnt, unsigned want) {
  if (D0) __builtin_amdgcn_s_sleep(D0);
#pragma unroll 1
  for (unsigned spins = 0; spins < SPIN_CAP; ++spins) {
    if (__builtin_amdgcn_ballot_w64(__hip_atomic_load(cnt, __ATOMIC_RELAXED, __HIP_MEMORY_SCOPE_AGENT) < want) == 0) break;
    __builtin_amdgcn_s_sleep(D1);
  }
  __builtin_amdgcn_fence(__ATOMIC_ACQUIRE, "wavefront");
}

template <int D0, int D1, int EX> __device__ __forceinline__ void pollp(const unsigned* cnt, unsigned want, unsigned (&r)[4]) {
  unsigned n = SPIN_CAP;
#define PL1(a) "global_atomic_add %" #a ", %5, %9, off sc0\n\ts_sleep %8\n\t"
#define PL2(a, c) "global_atomic_add %" #a ", %5, %9, off sc0\n\ts_waitcnt vmcnt(3)\n\tv_cmp_gt_u32 vcc, %6, %" #c "\n\ts_cbranch_vccz 2f\n\ts_sleep %8\n\t"
  asm volatile("s_mov_b64 exec, %10\n\ts_sleep %7\n\t" PL1(0) PL1(1) PL1(2) "1:\n\t" PL2(3, 0) PL2(0, 1) PL2(1, 2) PL2(2, 3) "s_sub_u32 %4, %4, 1\n\ts_cbranch_scc0 1b\n2:\n\ts_mov_b64 exec, -1"
               : "=&v"(r[0]), "=&v"(r[1]), "=&v"(r[2]), "=&v"(r[3]), "+s"(n) : "v"(cnt), "s"(want), "n"(D0), "n"(D1), "v"(0), "n"(EX) : "memory", "vcc", "scc");
}
#define KEEP4(r) asm volatile("; keep %0 %1 %2 %3" ::"v"(r[0]), "v"(r[1]), "v"(r[2]), "v"(r[3]))
typedef float f32x32 __attribute__((ext_vector_type(32)));
typedef float f32x16 __attribute__((ext_vector_type(16)));
#define M5(i, j, s, w) "v_mfma_f32_16x16x32_bf16 v[28*" #i "+4*" #j ":28*" #i "+4*" #j "+3], v[144+4*(" #w "):147+4*(" #w ")], v[112+16*(" #s ")+4*" #i ":115+16*(" #s ")+4*" #i "], v[28*" #i "+4*" #j ":28*" #i "+4*" #j "+3]\n\t"
#define RA5(i, s) "ds_read_b128 v[112+16*" #s "+4*" #i ":115+16*" #s "+4*" #i "], %[aA] offset:1024*" #i "\n\t"
#define RW5(j, h, a, w) "ds_read_b64_tr_b16 v[144+4*(" #w ")+2*" #h ":145+4*(" #w ")+2*" #h "], %[" #a "] offset:32*(" #j "/2)+7184*(" #j "&1)+3584*" #h "\n\t"
#define RM5(h, w) RW5(0, h, aM, w)
#define AD5(d, a) "v_add_u32 %[" #d "], %[dl], %[" #a "]\n\t"
#define SEL5A "s_add_u32 %[mask], %[mask], %[mask]\n\t"
#define SEL5B "s_cselect_b32 %[dl], %[wrap], 30736\n\t"
#define LG(n) "s_waitcnt lgkmcnt(" #n ")\n\t"
#define SB "s_barrier\n\t"
#define GN5(j, o, b, g0, g1, g2, g3, g4, g5, g6, g7) \
  M5(0, j, o, 2*(b)) g0 M5(0, j, 1-o, 2*(b)+1) g1 M5(1, j, 1-o, 2*(b)+1) g2 M5(1, j, o, 2*(b)) g3 M5(2, j, o, 2*(b)) g4 M5(2, j, 1-o, 2*(b)+1) g5 M5(3, j, o, 2*(b)) g6 M5(3, j, 1-o, 2*(b)+1) g7
#define GL5(j, o, b, g0, g1, g5, g7) \
  M5(0, j, o, 2*(b)) g0 M5(0, j, 1-o, 2*(b)+1) g1 M5(1, j, o, 2*(b)) RA5(0, o) M5(1, j, 1-o, 2*(b)+1) RA5(1, o) M5(2, j, o, 2*(b)) RA5(2, o) M5(2, j, 1-o, 2*(b)+1) g5 M5(3, j, o, 2*(b)) RA5(3, o) M5(3, j, 1-o, 2*(b)+1) g7 LG(4)
#define TRIPA(b, Z)                                                                                                                                     \
  GN5(0, 0, b, RW5(1, 1, X, 2-2*b), RW5(1, 0, Y, 3-2*b) LG(5), RW5(1, 1, Y, 3-2*b), , LG(4), , RW5(2, 0, X, 2*b), RW5(2, 1, X, 2*b) LG(2))                \
  GN5(1, 0, 1-b, RW5(2, 0, Y, 2*b+1), RW5(2, 1, Y, 2*b+1), , , , , RW5(3, 0, X, 2-2*b), RW5(3, 1, X, 2-2*b) LG(2))                                       \
  GN5(2, 0, b, RW5(3, 0, Y, 3-2*b), RW5(3, 1, Y, 3-2*b) SEL5A, SEL5B, AD5(X, Y) AD5(aA, aA), LG(2), SB, RW5(4, 0, Y, 2*b), RW5(4, 1, Y, 2*b) LG(2))        \
  GL5(3, 0, 1-b, RW5(4, 0, X, 2*b+1), RW5(4, 1, X, 2*b+1), Z, RW5(5, 0, Y, 2-2*b))
#define TRIPB(b, Z)                                                                                                                                     \
  GN5(4, 1, b, RW5(5, 1, Y, 2-2*b), RW5(5, 0, X, 3-2*b) LG(5), RW5(5, 1, X, 3-2*b), , LG(4), , RM5(0, 2*b), RM5(1, 2*b) LG(2))                            \
  GN5(5, 1, 1-b, AD5(aM, aM) RM5(0, 2*b+1), RM5(1, 2*b+1) SEL5A, SEL5B, AD5(Y, X) AD5(aA, aA), LG(2), SB, RW5(0, 0, X, 2-2*b), RW5(0, 1, X, 2-2*b) LG(2)) \
  GL5(6, 1, b, RW5(0, 0, Y, 3-2*b), RW5(0, 1, Y, 3-2*b), AD5(aM, aM) Z, RW5(1, 0, X, 2*b))
#define Q5(j, s, w) M5(0, j, s, w) M5(1, j, s, w) M5(2, j, s, w) M5(3, j, s, w)
#define W5(j, a, w) RW5(j, 0, a, w) RW5(j, 1, a, w)
#define A5(s) RA5(0, s) RA5(1, s) RA5(2, s) RA5(3, s)
static_assert(K5_STG == 30736 && K5_U - K5_G == 7184, "TRIP5");

#define N3(i, j) "24*" #i "+4*(" #j ")-(" #i "/4)*(4*(" #j ")-4*((" #j ")/5))"
#define M3(i, j, s, w) "v_mfma_f32_16x16x32_bf16 v[" N3(i, j) ":" N3(i, j) "+3], v[144+4*(" #w "):147+4*(" #w ")], v[104+20*(" #s ")+4*" #i ":107+20*(" #s ")+4*" #i "], v[" N3(i, j) ":" N3(i, j) "+3]\n\t"
#define RA3(i, s) "ds_read_b128 v[104+20*" #s "+4*" #i ":107+20*" #s "+4*" #i "], %[aA] offset:1024*" #i "\n\t"
#define RH3(s) "ds_read_b128 v[120+20*" #s ":123+20*" #s "], %[aH]\n\t"
#define RW3(p, h, a, w) "ds_read_b64_tr_b16 v[144+4*(" #w ")+2*" #h ":145+4*(" #w ")+2*" #h "], %[" #a "] offset:4096*" #p "+2048*" #h "\n\t"
#define W3(p, a, w) RW3(p, 0, a, w) RW3(p, 1, a, w)
#define A3(s) RA3(0, s) RA3(1, s) RA3(2, s) RA3(3, s) RH3(s)
#define Q3(j, s, w) M3(0, j, s, w) M3(1, j, s, w) M3(2, j, s, w) M3(3, j, s, w)
#define SEL3B "s_cselect_b32 %[dl], %[wrap], -21504\n\t"
#define GN3(j, o, b, g0, g1, g2, g3, g4, g5, g6, g7) \
  M3(0, j, o, 2*(b)) g0 M3(0, j, 1-o, 2*(b)+1) g1 M3(1, j, 1-o, 2*(b)+1) g2 M3(1, j, o, 2*(b)) g3 M3(2, j, o, 2*(b)) g4 M3(2, j, 1-o, 2*(b)+1) g5 M3(3, j, o, 2*(b)) g6 M3(3, j, 1-o, 2*(b)+1) g7
#define GL3(j, o, g0, g1, g2, g3, g4, g5, g6, g7, g8, g9) \
  M3(0, j, o, 2*o) g0 M3(0, j, 1-o, 2*o+1) g1 M3(1, j, o, 2*o) g2 M3(1, j, 1-o, 2*o+1) g3 M3(2, j, o, 2*o) g4 M3(2, j, 1-o, 2*o+1) g5 M3(3, j, o, 2*o) g6 M3(3, j, 1-o, 2*o+1) g7 M3(4, j, o, 2*o) g8 M3(4, j, 1-o, 2*o+1) g9
#define HEAD3(o, p1, p2, U, V)                                                                                                                          \
  GN3(3*o, o, o, RW3(p1, 1, U, 2-2*o), RW3(p1, 0, V, 3-2*o), RW3(p1, 1, V, 3-2*o), , LG(5), , RW3(p2, 0, U, 2*o), RW3(p2, 1, U, 2*o) LG(2))               \
  GN3(3*o+1, o, 1-o, RW3(p2, 0, V, 2*o+1), RW3(p2, 1, V, 2*o+1) SEL5A, SEL3B "v_xor_b32 %[" #V "], 32, %[" #V "]\n\t", AD5(U, V) AD5(aA, aA), LG(2) AD5(aH, aH), SB, \
      RW3(0, 0, V, 2-2*o), RW3(0, 1, V, 2-2*o) LG(2))
#define TRIP3(o, p1, p2, U, V, Z) HEAD3(o, p1, p2, U, V) GL3(3*o+2, o, RW3(0, 0, U, 3-2*o), RW3(0, 1, U, 3-2*o), RA3(0, o), RA3(1, o), RA3(2, o), Z, RA3(3, o), , RH3(o), RW3(p2, 0, V, 2*o) LG(4))
#define PAIR3(Z) TRIP3(0, 2, 1, X, Y, ) TRIP3(1, 1, 2, Y, X, Z)
#define CV3(i, j, t) "v_cvt_pk_bf16_f32 v[108+2*" #t "], v[" N3(i, j) "], v[" N3(i, j) "+1]\n\tv_cvt_pk_bf16_f32 v[109+2*" #t "], v[" N3(i, j) "+2], v[" N3(i, j) "+3]\n\t"
#define WR3(i, t, a, o) "ds_write_b64 %[" #a "], v[108+2*" #t ":109+2*" #t "] offset:2048*" #i "+" #o "\n\t"
#define CW3(i, j, t, a, o) CV3(i, j, t) WR3(i, t, a, o)
#define V8(a) "v" #a "0", "v" #a "1", "v" #a "2", "v" #a "3", "v" #a "4", "v" #a "5", "v" #a "6", "v" #a "7"
#define V10(a) V8(a), "v" #a "8", "v" #a "9"
static_assert(K3_STG == 21504, "TRIP3");
#define M6(i, j, s) "v_mfma_f32_16x16x32_bf16 v[16*" #i "+4*(" #j "):16*" #i "+4*(" #j ")+3], v[80+32*" #s "+4*(" #j "):83+32*" #s "+4*(" #j ")], v[64+32*" #s "+4*" #i ":67+32*" #s "+4*" #i "], v[16*" #i "+4*(" #j "):16*" #i "+4*(" #j ")+3]\n\t"
#define RA6(i, s) "ds_read_b128 v[64+32*" #s "+4*" #i ":67+32*" #s "+4*" #i "], %[aa] offset:1024*" #i "\n\t"
#define RW6(h, b, l, s) "ds_read_b64_tr_b16 v[80+32*" #s "+8*(" #h ")+4*" #b "+2*" #l ":81+32*" #s "+8*(" #h ")+4*" #b "+2*" #l "], %[a" #b "] offset:4096*(" #h ")+2048*" #l "\n\t"
#define SEL6(d, w) "s_cselect_b32 %[" #d "], %[" #w "], 8192\n\t"
#define SH6(m) "s_add_u32 %[" #m "], %[" #m "], %[" #m "]\n\t"
#define TRIP6(h, o, n, x, X)                                                                                                                            \
  M6(0, 2*h, o) RW6(1-h, 0, 0, x) M6(0, 2*h, n) RW6(1-h, 0, 1, x) SH6(mw) M6(0, 2*h+1, n) SEL6(dw, ww) RA6(0, x) M6(0, 2*h+1, o) RW6(1-h, 1, 0, x) LG(8)  \
  M6(1, 2*h+1, o) RW6(1-h, 1, 1, x) M6(1, 2*h+1, n) RA6(1, x) SH6(ma) M6(1, 2*h, n) SEL6(da, wa) RA6(2, x) M6(1, 2*h, o) RA6(3, x)                      \
  M6(2, 2*h, o) RW6(h, 0, 0, x) "v_add_u32 %[aa], %[da], %[aa]\n\t" M6(2, 2*h, n) RW6(h, 0, 1, x) M6(2, 2*h+1, n) RW6(h, 1, 0, x) "v_add_u32 %[a0], %[dw], %[a0]\n\t" \
  M6(2, 2*h+1, o) RW6(h, 1, 1, x) M6(3, 2*h+1, o) "v_add_u32 %[a1], %[dw], %[a1]\n\t" X M6(3, 2*h+1, n) M6(3, 2*h, n) LG(6) M6(3, 2*h, o) "s_barrier\n\t"
__device__ __forceinline__ int w2_stage(int j) {
  const int j2 = j - 16, p = (j2 * 52) >> 8, r = j2 - p * 5;
  return j < 16 ? j * 112 * 2048 : (p * 224 + 32 + r * 32 + (r > 2 ? 32 : 0)) * 2048 + (r == 2);
}
__global__ __launch_bounds__(768, 3) __attribute__((amdgpu_num_sgpr(98))) void k2_block(const u16* __restrict__ x, const u16* __restrict__ wqkv, const u16* __restrict__ wo,
                                                   const u16* __restrict__ w1, const u16* __restrict__ w3, const u16* __restrict__ w2,
                                                   const u16* __restrict__ cnd, const u16* __restrict__ wmod, u16* __restrict__ out, char* __restrict__ ws) {
  extern __shared__ __attribute__((aligned(16))) char smem_[];
  LDS_PTR(char) smem = (LDS_PTR(char))smem_;
  const int tid = threadIdx.x;
  int lane = tid & 63;
  const int wave = __builtin_amdgcn_readfirstlane(tid >> 6);
  const unsigned lds_base = (unsigned)(uintptr_t)smem;
  const int b = blockIdx.x, xc = b & 7, ii = b >> 3;
  const int tm3 = (xc & 1) * 8 + (ii & 7), tn3 = (xc >> 1) * 4 + (ii >> 3);
  const int tm5 = (xc & 1) * 4 + (ii & 3), tn5 = (xc >> 1) * 8 + (ii >> 2);
  const int kh6 = xc >> 2, tm6 = (xc & 1) * 8 + (xc & 2) * 2 + (ii & 3), tn6 = ii >> 2;
  const int tm4 = tm6, tn4 = tn6 * 2 + kh6;
  const int u1 = (b >> 4) * 8 + xc;
  unsigned* cnt = (unsigned*)(ws + WS_CTL);
  unsigned long long* fl1 = (unsigned long long*)(ws + WS_FL1);
  STAMP_AT(0 );

  if (wave >= 8) {
    const int lw = wave - 8, P = b * 4 + lw, lane = lane_id();
    const bool isA = lw < 2;
    asm volatile("" ::"s"(wqkv), "s"(wo), "s"(w1), "s"(w3), "s"(w2), "s"(ws));
    int kpf = KB + VB3;
    const char* tp = (const char*)((P & 1) ? w3 : w1) + (kpf * 2048 + (P >> 1) * 4) * 128;
    unsigned pr[4] = {};
    auto touch = [&]() {
      if (kpf < 28 && __builtin_amdgcn_s_getreg((3 << 11) | (8 << 6) | 7) <= 7) {
        stouch4(tp);
        ++kpf; tp += 262144;
      }
    };
    const char* epo = ws + WS_EPO;
    const unsigned swz = ((lane & 7) ^ ((lane >> 3) & 6)) * 16;
    const int cg = lw * 16 + tn4;
    auto issue4 = [&](int s, unsigned slot) {
      const char* p = (const char*)wo + tn4 * 128 + (lw * 32 + s * 128) * 2048;
      unsigned v4[4];
#pragma unroll
      for (int i = 0; i < 4; ++i) v4[i] = (lane >> 3) * 2048 + swz + i * 15360;
      glds16x4<false>(p, p, v4, lds_base + slot + lw * 4096);
    };
    auto run3 = [&](auto isA_) {
      constexpr bool A_ = decltype(isA_)::value; constexpr int N = A_ ? 4 : 6;
      const char* sp = A_ ? ws + WS_H + (tm3 * 8 - 1) * 1024 : (const char*)wqkv + tn3 * 128;
      unsigned vo[4], vx[2];
#pragma unroll
      for (int i = 0; i < 6; ++i) {
        const int tb = (lw - 2) * 6 + i;
        const unsigned v = A_ ? lane * 16 + (i < 4 ? 1024 + lw * 4096 : tm3 ? 0 : 1024) : ((tb & 3) * 8 + (lane >> 3)) * 6144 + (tb >> 2) * 2048 + 4096 - (i < 4 ? i * 1024 : 0) + swz;
        if (i < 4) vo[i] = v; else vx[i - 4] = v;
      }
      if (!A_) sp -= 4096;
      const unsigned d0 = lds_base + (A_ ? 1024 + lw * 4096 : K3_B + (lw - 2) * 6144);
      unsigned st = 3 * K3_STG;
      auto issue = [&]() {
        glds16x4<A_>(sp, sp, vo, d0 + st);
        if (A_) { if (lw == 0) glds16s(sp, vx[0], lds_base + st, 0); }
        else { glds16(sp, vx[0], d0 + st, 4096); glds16(sp, vx[1], d0 + st, 5120); }
        sp += A_ ? 131072 : 32 * 6144; st = st == 0 ? (K3_NS - 1) * K3_STG : st - K3_STG;
      };
      { unsigned long long ep;
        asm volatile("s_load_dwordx2 %0, %1, 0x0\n\ts_waitcnt lgkmcnt(0)" : "=s"(ep) : "s"(epo) : "memory");
        ep += 1;
        if (lw == 3) { *(LDS_PTR(u32x4))(smem + LDS_ARR) = (u32x4){0u, 0u, 0u, 0u}; wait_lgkm0(); }
        if (lw == 1) {
#pragma unroll
          for (int k = 0; k < 3; ++k) {
            const int i = b * 158 + (k * 64 + lane < 158 ? k * 64 + lane : 0);
            st16<true>(ws, i < 32768 ? (unsigned)WS_XCH + i * 16 : (unsigned)WS_CTL + (i - 32768) * 16, i < 32768 ? (u32x4){~0u, ~0u, ~0u, ~0u} : (u32x4){0u, 0u, 0u, 0u});
          }
        }
        BAR();
        auto flag = [&](int idx) { wait_vm<0>(); if (lane < 8) __hip_atomic_store(fl1 + lane * 256 + idx, ep, __ATOMIC_RELAXED, __HIP_MEMORY_SCOPE_AGENT); };
        if (lw == 1 && (b & 8)) {
          const int kq = u1 & 3, cgp = u1 >> 2;
          float sm = 0.f;
#pragma unroll
          for (int r = 0; r < 8; ++r) sm += *(const LDS_PTR(float))(smem + 8192 + (r * 64 + lane) * 4);
          __hip_atomic_store((unsigned*)(ws + WS_MOD) + kq * 2048 + cgp * 64 + lane, __float_as_uint(sm), __ATOMIC_RELAXED, __HIP_MEMORY_SCOPE_AGENT);
          flag(((cgp >> 3) & 1) * 64 + (cgp >> 4) * 32 + (cgp & 7) * 4 + kq);
        }
        if (b & 8) BAR();
        BAR();
        if (lw == 1 && !(b & 8)) flag(128 + u1);
        auto flags = [&](auto n_, int first) {
#pragma unroll 1
          for (unsigned spins = 0; spins < SPIN_CAP; ++spins) {
            bool ok = true;
#pragma unroll
            for (int k = 0; k < decltype(n_)::value; ++k) ok &= __hip_atomic_load(fl1 + xc * 256 + (first + k) * 64 + lane, __ATOMIC_RELAXED, __HIP_MEMORY_SCOPE_AGENT) == ep;
            if (__builtin_amdgcn_ballot_w64(!ok) == 0) break;
            __builtin_amdgcn_s_sleep(FS0);
          }
        };
        if (lw == 0) flags(IC<1>(), (b >> 3) & 1);
        BAR();
        if (lw == 1) {
          __builtin_amdgcn_s_sleep(FD1);
          flags(IC<4>(), 0);
          if (b == 0 && lane == 0) __hip_atomic_store((unsigned long long*)(ws + WS_EPO), ep, __ATOMIC_RELAXED, __HIP_MEMORY_SCOPE_AGENT);
        }
      }
      BAR();
      if (lw == 0) {
        signal(cnt, C2 + (u1 >> 3), lane, (b & 8) ? 1u : 0x10000u);
        pollp<PD2, PQ, 3>(cnt + ((C2 + ((lane & 1) && tm3 ? tm3 - 1 : tm3)) * 8 + (tn3 & 7)) * 32, 0x80000u, pr);
      }
      constexpr int I0 = A_ ? IA0 : IW0, I1 = A_ ? IA1 : IW1;
      if (!A_) for (int i = 0; i < I0; ++i) issue();
      BAR();
      if (A_) for (int i = 0; i < I0; ++i) issue();
      wait_vm<(I0 - 1) * N>();
      KEEP4(pr);
      BAR();
      for (int i = I0; i < I1; ++i) issue();
      wait_vm<(I1 - 2) * N>(); BAR();
      for (int i = I1; i < 7; ++i) issue();
#pragma unroll 1
      for (int t = 2; t < 27; ++t) { wait_vm<4 * N>(); BAR(); issue(); }
      wait_vm<4 * N>(); BAR(); issue4(5, 57344 + 5 * 16384);
      wait_vm<3 * N + 4>(); BAR(); issue4(4, 57344 + 4 * 16384);
      glds4((P & 1) ? w3 : w1, ((KB + ((lane >> 2) < VB3 ? lane >> 2 : 0)) * 2048 + (P >> 1) * 4 + (lane & 3)) * 128, lds_base + LDS_DUMMY);
      wait_vm<2 * N + 9>(); BAR(); issue4(3, 57344 + 3 * 16384);
      wait_vm<N + 13>(); BAR();
      wait_vm<13>(); BAR();
    };
    if (isA) run3(T_()); else run3(F__());
    { BAR();
      BAR();
      issue4(0, 57344);
      BAR();
      issue4(1, 57344 + 16384); issue4(2, 57344 + 2 * 16384);
      BAR();
      if (lw == 0) signal(cnt, C3 + tm3, lane);
      issue4(6, 0); issue4(7, 16384);
      if (lw == 0) pollp<PD3, PQ, 1>(cnt + ((C3 + tm4) * 8 + tn6) * 32, 16, pr);
      wait_vm<8>();
      BAR();
      wait_vm<0>();
      KEEP4(pr);
      BAR();
      poll<0, 4>(cnt + (CM * 8 + cg) * 32, 4);
      f32x4 pv[4];
#pragma unroll
      for (int i = 0; i < 4; ++i) pv[i] = __builtin_bit_cast(f32x4, ld16<true>(ws + WS_MOD2, (((lane >> 4) * 4 + i) * 4096 + cg * 64 + (lane & 15) * 4) * 4));
      f32x4 v = (pv[0] + pv[1]) + (pv[2] + pv[3]);
#pragma unroll
      for (int e = 0; e < 4; ++e) { v[e] = rbf(xsum16_32(v[e])); if (lw == 2) v[e] = rbf(1.f + v[e]); if (lw == 1 || lw == 2) v[e] *= -1.4426950408889634f; }
      *(LDS_PTR(f32x4))(smem + LDS_VEC + lw * 256 + (lane & 15) * 16) = v;
      wait_lgkm0();
    }
    auto run5 = [&](auto isA_) {
      constexpr bool A_ = decltype(isA_)::value; constexpr int N = A_ ? 8 : 7;
      const char* sp = A_ ? ws + WS_H2 + (tm5 * 16 + lw * 8) * 1024 : (const char*)(lw == 2 ? w1 : w3) + tn5 * 224;
      unsigned vp[N];
#pragma unroll
      for (int i = 0; i < N; ++i) { const int L = i * 64 + lane, r = L / 14, c = L - r * 14; vp[i] = A_ ? L * 16 : r * (F_ * 2) + c * 16; }
      const unsigned d0 = lds_base + (A_ ? lw * 8192 : lw == 2 ? K5_G : K5_U);
      unsigned st = 3 * K5_STG;
      auto issue = [&](bool t = false) {
#pragma unroll
        for (int i = 0; i < N; ++i) {
          if (t && i == 4) touch();
          if (A_) glds16s(sp, vp[i], d0 + st, i * 1024); else glds16(sp, vp[i], d0 + st, i * 1024);
        }
        sp += A_ ? 131072 : 32 * F_ * 2; st = st == (K5_NS - 1) * K5_STG ? 0 : st + K5_STG;
      };
      BAR();
      if (!A_) { issue(); issue(); }
      BAR();
      u32x4 xk[4];
      auto keep = [&]() {
#pragma unroll
        for (int i = 0; i < 4; ++i) { xk[i] = *(const LDS_PTR(u32x4))(smem + lw * 4096 + i * 1024 + lane * 16); asm volatile("" : "+v"(xk[i])); }
      };
      if (A_ && lw == 0) { signal(cnt, C4 + tm4, lane); keep(); pollp<PD4, PQ, 3>(cnt + ((C4 + tm5 * 2 + (lane & 1)) * 8 + (tn5 & 7)) * 32, 16, pr); }
      else { keep(); if (!A_) { issue(); issue(); issue(); } }
      BAR();
      if (A_) { issue(); issue(); }
      wait_vm<A_ ? N : 4 * N>();
      KEEP4(pr);
      BAR();
      if (A_) issue();
      wait_vm<A_ ? N : 3 * N>();
      BAR();
      if (A_) { issue(); issue(); }
#pragma unroll 1
      for (int j = 0; j < 27; ++j) {
        wait_vm<2 * N>();
        BAR();
        issue(true);
        if (j == 6) {
#pragma unroll
          for (int i = 0; i < 4; ++i) st16<true>(ws + WS_PART, (b * 4 + lw) * 4096 + i * 1024 + lane * 16, (u32x4){~0u, ~0u, ~0u, ~0u});
        }
        if (j == 12 && lw == 0 && lane == 0) __hip_atomic_store((unsigned*)(ws + WS_FLG) + b * 32, 16u, __ATOMIC_RELAXED, __HIP_MEMORY_SCOPE_AGENT);
      }
      const char* sb = A_ ? ws + WS_HH + (kh6 * 56 * 128 + tm6 * 8 + lw * 4) * 1024 : (const char*)w2 + kh6 * 1792 * 2048 + tn6 * 256 + ((lw ^ kh6 ^ 1) & 1) * 128;
      unsigned vo[4];
#pragma unroll
      for (int i = 0; i < 4; ++i) {
        const int r = i * 8 + (lane >> 3), k = 8 * ((r >> 2) & 3) + 4 * (r >> 4) + (r & 3);
        vo[i] = A_ ? lane * 16 : k * 2048 - i * 1024 + swz;
      }
      const unsigned d6 = lds_base + (A_ ? K6_A : 0) + (lw & 1) * 4096;
      st = 0;
      const int tab = w2_stage(lane);
      int jn = 0;
      auto issue6 = [&]() {
        if (A_) { glds16x4<true>(sb, sb, vo, d6 + st); sb += 131072; }
        else { const int o = __builtin_amdgcn_readlane(tab, jn); const char* s0 = sb + (o & ~1); glds16x4<false>(s0, s0 + (o & 1) * 65536, vo, d6 + st); ++jn; }
        st = st == ((A_ ? K6_NA : K6_NW) - 1) * 8192 ? 0 : st + 8192;
      };
      auto pre6 = [&](int n) {
        if (!A_) {
#pragma unroll 1
          for (int i = 0; i < n; ++i) issue6();
        }
      };
      constexpr int E = A_ ? 0 : 4;
      { const char* p = (const char*)w2 + (kh6 * 1792 + ii) * 2048;
        const int n = lw * 56 + (lane < 56 ? lane : 0), o = w2_stage((xc & 3) * 14 + (n >> 4));
        glds4(p, (o & ~1) + (o & (ii >> 4) & 1) * 65536 + (n & 15) * 128, lds_base + LDS_DUMMY); }
      wait_vm<2 * N + 1>(); BAR();
      wait_vm<N + 1>(); BAR(); pre6(PA6);
      wait_vm<PA6 * E + 1>(); BAR(); pre6(PB6);
      BAR(); pre6(12 - PA6 - PB6);
      BAR();
      if (A_) {
        volatile LDS_PTR(unsigned) go = (volatile LDS_PTR(unsigned))(smem + LDS_ARR + 4);
        if (lw == 0) { pollp<PD5, PQ5, 1>(cnt + ((C5 + (tm6 >> 1) * 4 + kh6 * 2) * 8 + tn6) * 32, 16, pr); *go = 1u; }
        else {
#pragma unroll 1
          for (unsigned spins = 0; spins < SPIN_CAP && __builtin_amdgcn_readfirstlane(*go) == 0; ++spins) __builtin_amdgcn_s_sleep(1);
          asm volatile("" ::: "memory");
        }
        STAMP_L(31);
        issue6(); issue6(); issue6();
        wait_vm<8>(); KEEP4(pr); BAR(); issue6(); issue6();
        wait_vm<12>(); BAR(); issue6(); issue6();
      } else { wait_vm<16>(); BAR(); BAR(); }
#pragma unroll 1
      for (int c = 2; c < 51; ++c) { wait_vm<16>(); BAR(); if (A_ || !((SKIP6 >> c) & 1)) issue6(); }
      wait_vm<16>(); BAR(); wait_vm<12>(); BAR(); wait_vm<8>(); BAR(); wait_vm<4>(); BAR(); wait_vm<0>(); BAR();
#pragma unroll
      for (int i = 0; i < 4; ++i) *(LDS_PTR(u32x4))(smem + K6_X + lw * 4096 + i * 1024 + lane * 16) = xk[i];
      BAR(); wait_lgkm0(); BAR();
    };
    if (isA) run5(T_()); else run5(F__());
    asm volatile("s_waitcnt lgkmcnt(0)" ::: "memory");
    return;
  }

  int fr = lane & 15, g = lane >> 4, p4 = fr & 3, row = 4 * g + (fr >> 2);
  int lsw = row * 128 + (((row >> 1) & 3) * 32) + p4 * 8;
  { const int mb = u1, hf = (b >> 3) & 1, kb0 = hf * 16 + wave * 2, cofs = (g & 1) * 16 + (g >> 1) * 8;
    u32x4 v[4];
    auto ldx = [&]() {
#pragma unroll
      for (int p = 0; p < 4; ++p) v[p] = *(const u32x4*)(x + (size_t)(mb * 16 + fr) * D_ + ((kb0 + (p & 1)) ^ ((p >> 1) * 16)) * 32 + cofs);
    };
    LDS_PTR(f32x2) sred = (LDS_PTR(f32x2))(smem + 4096);
    auto up2 = [](unsigned u) { return (f32x2){lo2f(u), hi2f(u)}; };
    auto stats = [&]() {
      f32x2 s2 = {0.f, 0.f}, q2 = {0.f, 0.f};
#pragma unroll
      for (int k = 0; k < 16; ++k) s2 += up2(v[k >> 2][k & 3]);
      const float mw = xsum16_32(s2[0] + s2[1]) * (1.f / 128.f);
#pragma unroll
      for (int k = 0; k < 16; ++k) { const f32x2 d = up2(v[k >> 2][k & 3]) - mw; q2 += d * d; }
      const float qw = xsum16_32(q2[0] + q2[1]);
      if (lane < 16) sred[wave * 16 + fr] = (f32x2){mw, qw};
      wait_lgkm0();
    };
    auto rest = [&]() {
    STAMP_AT(1 );
    float mean = 0.f, m2 = 0.f; f32x2 sw[8];
#pragma unroll
    for (int w = 0; w < 8; ++w) { sw[w] = sred[w * 16 + fr]; mean += sw[w][0]; m2 += sw[w][1]; }
    mean *= 0.125f;
#pragma unroll
    for (int w = 0; w < 8; ++w) { const float d = sw[w][0] - mean; m2 += 128.f * d * d; }
    const float rstd = ln_rstd(m2 * (1.f / 1024.f));
    mean = rbf(mean);
    unsigned nv[8];
#pragma unroll
    for (int k = 0; k < 8; ++k) {
      const f32x2 d = up2(v[k >> 2][k & 3]) - mean;
      const f32x2 n = up2(pk2(d[0], d[1])) * rstd;
      nv[k] = pk2(n[0], n[1]);
      asm volatile("" : "+v"(nv[k]));
    }
    SCHED0(); BAR(); SCHED0();
    STAMP_AT(2 );
    float mp[8];
#pragma unroll
    for (int i = 0; i < 8; ++i) mp[i] = __uint_as_float(__hip_atomic_load((unsigned*)(ws + WS_MOD) + hf * 512 + tid + i * 1024, __ATOMIC_RELAXED, __HIP_MEMORY_SCOPE_AGENT));
    *(LDS_PTR(unsigned))(smem + tid * 4) = pk2((mp[0] + mp[2]) + (mp[4] + mp[6]), 1.f + rbf((mp[1] + mp[3]) + (mp[5] + mp[7])));
    wait_lgkm0();
#pragma unroll
    for (int p = 0; p < 2; ++p) {
      const LDS_PTR(u32x4) tp = (const LDS_PTR(u32x4))(smem + ((wave * 2 + p) * 32 + cofs) * 4);
      const u32x4 t0 = tp[0], t1 = tp[1];
      unsigned o[4];
#pragma unroll
      for (int j = 0; j < 4; ++j) {
        const unsigned ta = j < 2 ? t0[2 * j] : t1[2 * j - 4], tb = j < 2 ? t0[2 * j + 1] : t1[2 * j - 3];
        const unsigned u = pk2(lo2f(nv[p * 4 + j]) * hi2f(ta), hi2f(nv[p * 4 + j]) * hi2f(tb));
        o[j] = pk2(lo2f(u) + lo2f(ta), hi2f(u) + lo2f(tb));
      }
      auto s0 = __builtin_amdgcn_permlane16_swap(o[0], o[2], false, false);
      auto s1 = __builtin_amdgcn_permlane16_swap(o[1], o[3], false, false);
      st16<true>(ws + WS_H, ((kb0 + p) * 128 + mb) * 1024 + lane * 16, (u32x4){s0[0], s1[0], s0[1], s1[1]});
    }
    STAMP_AT(3 );
    wait_vm<0>();
    STAMP_AT(4 );
    BAR();
    STAMP_AT(5 );
    };
    if (hf) {
      const int mrow = (u1 & 3) * 256 + (tid >> 3) * 4;
      const u32x2 c2 = *(const u32x2*)(cnd + mrow);
      u32x4 mw[4];
#pragma unroll
      for (int i = 0; i < 4; ++i) mw[i] = __builtin_nontemporal_load((const u32x4*)(wmod + (size_t)(mrow + i) * 6144 + (u1 >> 2) * 64 + (lane & 7) * 8));
      const unsigned mc[2] = {c2[0], c2[1]};
      *(LDS_PTR(f32x2))(smem + 8192 + (wave * 64 + (lane & 7) * 8 + (lane >> 4) * 2) * 4) = slab_sum<4>(mc, mw);
      wait_lgkm0();
      BAR();
      BAR();
      ldx();
      stats();
      BAR();
      rest();
    } else {
      __builtin_amdgcn_s_sleep(XD0);
      ldx();
      SCHED0();
      const int sl = (3 - ((unsigned)(uintptr_t)wqkv >> 7) - (xc >> 1) * 4) & 7;
      const int i2 = tid - 384, r0 = i2 < 0 ? tid / 12 : i2 / 3, c0 = i2 < 0 ? tid - r0 * 12 : (i2 - r0 * 3) * 4 + sl;
      const bool on = i2 < 0 || (sl < 4 && i2 < 96);
      __builtin_amdgcn_s_sleep(PFD);
      const unsigned pf = *(const unsigned*)((const char*)wqkv + (xc >> 1) * 512 + (on ? ((((xc & 1) ^ (i2 >= 0)) * 512 + (ii >> 1) * 32 + r0) * 48 + (c0 >> 2) * 16 + (c0 & 3)) * 128 : 0));
      SCHED0(); BAR(); SCHED0();
      stats();
      SCHED0(); BAR(); SCHED0();
      rest();
      asm volatile("" ::"v"(pf));
    }
  }
  {
    unsigned long long vis[4];
    { const int kbase = tm3 * 128 + wave * 16 - 16;
#pragma unroll
      for (int t = 0; t < 4; ++t) {
        const int kk = (t >> 1) * 16 + 4 * g + (t & 1) * 2 + (fr & 1), df = 16 + fr - kk;
        vis[t] = __builtin_amdgcn_ballot_w64(df >= 0 && df <= 14 && kbase + kk >= 0);
        asm volatile("" : "+s"(vis[t]));
      } }
    u32x4 mc, mw[8];
    if (wave < 4) {
      const int wm = wave & 1, bA = wave;
      const unsigned s0 = lds_base + 3 * K3_STG;
      unsigned X = s0 + K3_B + (lsw ^ (bA * 32)), Y = X ^ 32, aA = s0 + 1024 + wm * 4096 + lane * 16, aH = s0 + lane * 16;
      unsigned lq = lds_base + bA * 512 + lane * 8, lv = lds_base + 36864 + (bA ^ 1) * 512 + (lane ^ ((g >> 1) * 8)) * 8;
      int n = 4, dl = -K3_STG; unsigned mask = wrap_mask(4, K3_NS);
      asm volatile(".irp r,0,8,16,24,32,40,48,56,64,72,80,88,96\n\tv_mov_b64 v[\\r:\\r+1], 0\n\tv_mov_b64 v[\\r+2:\\r+3], 0\n\tv_mov_b64 v[\\r+4:\\r+5], 0\n\tv_mov_b64 v[\\r+6:\\r+7], 0\n\t.endr\n\t"
                   "s_setprio 2\n\ts_waitcnt vmcnt(0)\n\ts_barrier\n\t"
                   SB W3(1, Y, 2) A3(0) W3(0, Y, 1) W3(2, Y, 3) W3(0, X, 0) AD5(Y, X) AD5(aA, aA) AD5(aH, aH) LG(7) Q3(4, 0, 2) RW3(2, 0, X, 2) LG(5) Q3(3, 0, 1) SB
                   W3(0, Y, 1) A3(1) LG(10) Q3(5, 0, 3) M3(4, 5, 0, 3) LG(0)
                   ".p2align 5\n1:\n\t" PAIR3() PAIR3() PAIR3("s_sub_u32 %[n], %[n], 1\n\t") "s_cbranch_scc0 1b\n\t"
                   HEAD3(0, 2, 1, X, Y)
                   GL3(2, 0, RW3(1, 0, Y, 3), RW3(1, 1, Y, 3), RW3(2, 0, Y, -10), RW3(2, 1, Y, -10), "v_add_u32 %[X], %[mo], %[lq]\n\tv_add_u32 %[aH], %[mo], %[lv]\n\t",
                       "v_xor_b32 %[Y], 512, %[X]\n\tv_xor_b32 %[aA], 512, %[aH]\n\t", CV3(0, 0, 0), CV3(1, 0, 1), LG(0) SB, WR3(0, 0, X, 0) WR3(1, 1, X, 0))
                   M3(0, 3, 1, 2) CV3(2, 0, 2) M3(1, 3, 1, 2) WR3(2, 2, X, 0) CV3(3, 0, 3) M3(2, 3, 1, 2) WR3(3, 3, X, 0) CV3(0, 1, 0) M3(3, 3, 1, 2) WR3(0, 0, aA, 0) CV3(1, 1, 1)
                   M3(3, 4, 1, 3) WR3(1, 1, aA, 0) CV3(2, 1, 2) M3(2, 4, 1, 3) WR3(2, 2, aA, 0) CV3(3, 1, 3) M3(1, 4, 1, 3) WR3(3, 3, aA, 0) CV3(0, 2, 0) M3(0, 4, 1, 3) WR3(0, 0, X, 18432) CV3(1, 2, 1)
                   M3(0, 5, 1, -10) WR3(1, 1, X, 18432) CV3(2, 2, 2) M3(1, 5, 1, -10) WR3(2, 2, X, 18432) CV3(3, 2, 3) M3(2, 5, 1, -10) WR3(3, 3, X, 18432) CV3(4, 2, 0)
                   M3(3, 5, 1, -10) WR3(0, 0, lq, 18432) CV3(0, 3, 1) M3(4, 5, 1, -10) WR3(0, 1, Y, 0)
                   CW3(1, 3, 2, Y, 0) CW3(2, 3, 3, Y, 0) CW3(3, 3, 0, Y, 0) CW3(3, 4, 1, Y, 18432) CW3(2, 4, 2, Y, 18432) CW3(1, 4, 3, Y, 18432) CW3(0, 4, 0, Y, 18432)
                   CW3(0, 5, 1, aH, 0) CW3(1, 5, 2, aH, 0) CW3(2, 5, 3, aH, 0) CW3(3, 5, 0, aH, 0) CV3(4, 5, 1) WR3(0, 1, lv, 0)
                   "s_setprio 0"
                   : [X] "+v"(X), [Y] "+v"(Y), [aA] "+v"(aA), [aH] "+v"(aH), [n] "+s"(n), [dl] "+s"(dl), [mask] "+s"(mask)
                   : [lq] "v"(lq), [lv] "v"(lv), [mo] "s"((wm * 4 + 1) * 2048), [wrap] "s"((K3_NS - 1) * K3_STG)
                   : "memory", "scc", V10(), V10(1), V10(2), V10(3), V10(4), V10(5), V10(6), V10(7), V10(8), V10(9), V10(10), V10(11), V10(12), V10(13), V10(14), V10(15));
    } else {
      wait_vm<0>();
      BAR(); BAR();
      int k = -4; const int P = b * 4 + (wave & 3);
      auto idle = [&]() {
        if (k < KB && __builtin_amdgcn_s_getreg((3 << 11) | (8 << 6) | 7) <= 7) {
          stouch4(k < 0 ? (const char*)wo + ((k + 4) * 4096 + P * 4) * 128 : (const char*)((P & 1) ? w3 : w1) + (k * 2048 + (P >> 1) * 4) * 128); ++k; }
        BAR(); BAR();
      };
      {
        const unsigned* pc = cnt + ((C2 + ((lane & 1) && tm3 ? tm3 - 1 : tm3)) * 8 + (tn3 & 7)) * 32;
        unsigned seen = 0x80008u;
        idle(); idle(); idle();
        if (wave == 4) asm volatile("global_load_dword %0, %1, off sc1" : "=v"(seen) : "v"(pc) : "memory");
        idle(); idle();
        asm volatile("s_waitcnt vmcnt(0)" : "+v"(seen)::"memory");
        if (__builtin_amdgcn_ballot_w64(seen < 0x80008u) != 0) poll<0>(pc, 0x80008u);
      }
#pragma unroll 1
      for (int t = 5; t < 14; ++t) idle();
#pragma unroll
      for (int t = 28; t < SLB; ++t) BAR();
      wait_lgkm0();
      const int mrow = ((b & 3) * 4 + (wave & 3)) * 64 + (lane >> 3) * 8;
      mc = *(const u32x4*)(cnd + mrow);
#pragma unroll
      for (int i = 0; i < 8; ++i) mw[i] = __builtin_nontemporal_load((const u32x4*)(wmod + (size_t)(mrow + i) * 6144 + 2048 + (b >> 2) * 64 + (lane & 7) * 8));
      SCHED0();
#pragma unroll
      for (int t = SLB; t < 33; ++t) BAR();
    }
    lane = lane_id(); fr = lane & 15; g = lane >> 4; p4 = fr & 3; row = 4 * g + (fr >> 2); lsw = row * 128 + (((row >> 1) & 3) * 32) + p4 * 8;
    f32x4 bias[2];
#pragma unroll
    for (int t = 0; t < 8; ++t) bias[t >> 2][t & 3] = __builtin_amdgcn_inverse_ballot_w64(vis[t >> 1]) ? 0.f : -__builtin_inff();
    asm volatile("" : "+v"(bias[0]), "+v"(bias[1]));
    wait_lgkm0();
    BAR();
    STAMP_AT(9 );
    const LDS_PTR(char) eb = smem + wave * 2048;
    auto ld2 = [&](int o) { return cat8(*(const LDS_PTR(bf16x4))(eb + o), *(const LDS_PTR(bf16x4))(eb + o + 512)); };
    const int vo = 36864 + ((p4 * 16 + row) ^ ((p4 >> 1) * 8)) * 8;
    bf16x8 qf[2], k0[2], k1[2], v0[2], v1[2];
#pragma unroll
    for (int hd = 0; hd < 2; ++hd) { qf[hd] = ld2(2048 + hd * 1024 + lane * 8); k0[hd] = ld2(18432 + hd * 1024 + lane * 8); k1[hd] = ld2(18432 + 2048 + hd * 1024 + lane * 8); }
#pragma unroll
    for (int hd = 0; hd < 2; ++hd) {
      v0[hd] = cat8(ld_tr4(eb + vo + hd * 1024), ld_tr4(eb + vo + hd * 1024 + 2048));
      v1[hd] = cat8(ld_tr4(eb + vo + hd * 1024 + 512), ld_tr4(eb + vo + hd * 1024 + 512 + 2048));
    }
    SCHED0();
    const f32x4 z = {0.f, 0.f, 0.f, 0.f};
    const bool odd = fr & 1;
    const unsigned sel0 = odd ? 0x01000c0cu : 0x0c0c0100u, sel1 = odd ? 0x03020c0cu : 0x0c0c0302u;
    float inf = __builtin_inff(); asm("" : "+v"(inf));
    auto mx2 = [&](float a, float c) { return __builtin_amdgcn_fmed3f(a, c, inf); };
    auto both = [](float a, float c, auto op, auto fin) {
      auto u = __builtin_amdgcn_permlane16_swap(__float_as_uint(a), __float_as_uint(c), false, false);
      float r = op(__uint_as_float(u[0]), __uint_as_float(u[1]));
      auto w = __builtin_amdgcn_permlane32_swap(__float_as_uint(r), __float_as_uint(r), false, false);
      r = fin(op(__uint_as_float(w[0]), __uint_as_float(w[1])));
      auto y = __builtin_amdgcn_permlane16_swap(__float_as_uint(r), __float_as_uint(r), false, false);
      return (f32x2){__uint_as_float(y[0]), __uint_as_float(y[1])};
    };
    f32x2 l[2][2];
#pragma unroll
    for (int hd = 0; hd < 2; ++hd) {
      const f32x4 s0 = __builtin_amdgcn_mfma_f32_16x16x32_bf16(k0[hd], qf[hd], bias[0], 0, 0, 0), s1 = __builtin_amdgcn_mfma_f32_16x16x32_bf16(k1[hd], qf[hd], bias[1], 0, 0, 0);
#pragma unroll
      for (int t = 0; t < 2; ++t) {
        const f32x4 sv = t ? s1 : s0;
        unsigned u = pk2(odd ? sv[1] : sv[0], odd ? sv[3] : sv[2]);
        u = pk2(lo2f(u) * 0.17677669529663687f, hi2f(u) * 0.17677669529663687f);
        l[hd][t] = (f32x2){lo2f(u), hi2f(u)};
      }
    }
    const f32x2 mx = both(mx2(mx2(l[0][0][0], l[0][0][1]), mx2(l[0][1][0], l[0][1][1])), mx2(mx2(l[1][0][0], l[1][0][1]), mx2(l[1][1][0], l[1][1][1])), mx2,
                          [](float r) { return r * -1.4426950408889634f; });
    float sm[2];
#pragma unroll
    for (int hd = 0; hd < 2; ++hd) {
#pragma unroll
      for (int t = 0; t < 2; ++t) { const f32x2 e = l[hd][t] * 1.4426950408889634f + mx[hd]; l[hd][t] = (f32x2){__builtin_amdgcn_exp2f(e[0]), __builtin_amdgcn_exp2f(e[1])}; }
      const f32x2 a = l[hd][0] + l[hd][1]; sm[hd] = a[0] + a[1];
    }
    const f32x2 inv = both(sm[0], sm[1], [](float a, float c) { return a + c; }, [](float r) { return __builtin_amdgcn_rcpf(r); });
#pragma unroll
    for (int hd = 0; hd < 2; ++hd) {
      u32x4 pp;
#pragma unroll
      for (int t = 0; t < 2; ++t) {
        const f32x2 p = l[hd][t] * inv[hd]; const unsigned u = pk2(p[0], p[1]);
        pp[2 * t] = __builtin_amdgcn_perm(u, u, sel0); pp[2 * t + 1] = __builtin_amdgcn_perm(u, u, sel1);
      }
      const bf16x8 pf = __builtin_bit_cast(bf16x8, pp);
      const f32x4 oa = __builtin_amdgcn_mfma_f32_16x16x32_bf16(v0[hd], pf, z, 0, 0, 0), ob = __builtin_amdgcn_mfma_f32_16x16x32_bf16(v1[hd], pf, z, 0, 0, 0);
      u32x4 ov; ov[0] = pk2(oa[0], oa[1]); ov[1] = pk2(oa[2], oa[3]); ov[2] = pk2(ob[0], ob[1]); ov[3] = pk2(ob[2], ob[3]);
      st16<true>(ws + WS_O, ((tn3 * 2 + hd) * 128 + tm3 * 8 + wave) * 1024 + lane * 16, ov);
    }
    STAMP_AT(10 );
    wait_vm<0>();
    STAMP_AT(11 );
    BAR();
    STAMP_AT(12 );
    if (wave >= 4) {
      const unsigned m4[4] = {mc[0], mc[1], mc[2], mc[3]};
      const f32x2 ps = slab_sum<8>(m4, mw);
      if (!(lane & 8))
        __hip_atomic_store((unsigned long long*)(ws + WS_MOD2) + (((b & 3) * 4 + (wave & 3)) * 4096 + (b >> 2) * 64 + (lane & 7) * 8 + (lane >> 4) * 2) / 2,
                           ((unsigned long long)__float_as_uint(ps[1]) << 32) | __float_as_uint(ps[0]), __ATOMIC_RELAXED, __HIP_MEMORY_SCOPE_AGENT);
      wait_vm<0>();
      if (lane == 0 && __hip_atomic_fetch_add((LDS_PTR(unsigned))(smem + LDS_ARR + 8), 1u, __ATOMIC_RELAXED, __HIP_MEMORY_SCOPE_WORKGROUP) == 3)
        __hip_atomic_fetch_add(cnt + (CM * 8 + (b >> 2)) * 32, 1u, __ATOMIC_RELAXED, __HIP_MEMORY_SCOPE_AGENT);
    }
  }
  { const int wm = wave & 3, ks = wave >> 2, mb = tm4 * 8 + wm * 2 + ks;
    constexpr int R = 6;
    u32x4 rx[2];
#pragma unroll
    for (int p = 0; p < 2; ++p) rx[p] = *(const u32x4*)(x + (size_t)(mb * 16 + fr) * D_ + tn4 * 64 + p * 32 + (g & 1) * 16 + (g >> 1) * 8);
    const unsigned va0 = (ks * 128 + mb) * 1024 + lane * 16, va1 = va0 ^ 1024;
    bf16x8 fa[R][2], fw[2][4];
    auto issueA = [&](int t) {
      const char* pa = ws + WS_O + t * 262144;
      fa[t % R][0] = __builtin_bit_cast(bf16x8, ld16<true>(pa, va0));
      fa[t % R][1] = __builtin_bit_cast(bf16x8, ld16<true>(pa, va1));
    };
    unsigned aw[4];
    auto rdw = [&](int t, int j) {
      const int o = (t & 3) * 8192;
      fw[t & 1][j] = cat8(ld_tr4(smem + aw[j] + o), ld_tr4(smem + aw[j] + o + 2048));
    };
    auto stepw = [&](int t) {
      const int gi = t >> 2, base = gi < 3 ? 57344 + gi * 32768 : 0;
#pragma unroll
      for (int j = 0; j < 4; ++j) { aw[j] = base + ks * 4096 + (lsw ^ (j * 32)); asm volatile("" : "+v"(aw[j])); }
    };
    auto up2 = [](unsigned u) { return (f32x2){lo2f(u), hi2f(u)}; };
    f32x2 f[8];
    unsigned slot = (mb * 256 + fr + kh6 * 32768 + 4 * g * 16) * 8, sown = (mb * 256 + fr + (g & 1) * 32768 + tn4 * 16) * 8;
    f32x4 acc[2][4];
#pragma unroll
    for (int i = 0; i < 2; ++i)
#pragma unroll
      for (int j = 0; j < 4; ++j) acc[i][j] = (f32x4){0.f, 0.f, 0.f, 0.f};
    stepw(0);
    wait_lgkm0();
    SCHED0(); BAR(); SCHED0();
    STAMP_AT(13 );
#pragma unroll
    for (int t = 0; t < R; ++t) issueA(t);
#pragma unroll
    for (int j = 0; j < 4; ++j) rdw(0, j);
    SCHED0();
#pragma unroll
    for (int t = 0; t < 16; ++t) {
      if (t < 15 && ((t + 1) & 3) == 0) stepw(t + 1);
#pragma unroll
      for (int j = 0; j < 4; ++j) {
        if (t < 15) rdw(t + 1, j);
        SCHED0();
#pragma unroll
        for (int i = 0; i < 2; ++i) acc[i][j] = __builtin_amdgcn_mfma_f32_16x16x32_bf16(fw[t & 1][j], fa[t % R][i], acc[i][j], 0, 0, 0);
        SCHED0();
      }
      if (t + R < 16) issueA(t + R);
      if (t == 2) {
#pragma unroll
        for (int p = 0; p < 2; ++p) {
          auto s0 = __builtin_amdgcn_permlane16_swap(rx[p][0], rx[p][2], false, false);
          auto s1 = __builtin_amdgcn_permlane16_swap(rx[p][1], rx[p][3], false, false);
          f[4 * p] = up2(s0[0]); f[4 * p + 1] = up2(s1[0]); f[4 * p + 2] = up2(s0[1]); f[4 * p + 3] = up2(s1[1]);
        }
#pragma unroll
        for (int j = 0; j < 8; ++j) asm volatile("" : "+v"(f[j]));
        asm volatile("" : "+v"(slot), "+v"(sown));
      }
      if (t == 5) BAR();
      SCHED0();
    }
    STAMP_AT(14 );
    LDS_PTR(f32x4) red = (LDS_PTR(f32x4))(smem + K4_RED);
#pragma unroll
    for (int j = 0; j < 4; ++j) red[((wave ^ 4) * 4 + j) * 64 + lane] = acc[1][j];
    wait_lgkm0();
    BAR();
    STAMP_AT(15 );
    const LDS_PTR(float) vec = (const LDS_PTR(float))(smem + LDS_VEC);
    unsigned o[8];
    f32x4 pr[4], ga[4];
#pragma unroll
    for (int j = 0; j < 4; ++j) { pr[j] = red[(wave * 4 + j) * 64 + lane]; ga[j] = *(const LDS_PTR(f32x4))(vec + j * 16 + 4 * g); }
    SCHED0();
#pragma unroll
    for (int j = 0; j < 4; ++j) {
      const f32x4 y = acc[0][j] + pr[j];
      const f32x2 lo = y.xy * ga[j].xy + f[2 * j], hi = y.zw * ga[j].zw + f[2 * j + 1];
      o[2 * j] = pk2(lo[0], lo[1]); o[2 * j + 1] = pk2(hi[0], hi[1]);
      f[2 * j] = up2(o[2 * j]); f[2 * j + 1] = up2(o[2 * j + 1]);
    }
    const f32x2 s2 = ((f[0] + f[1]) + (f[2] + f[3])) + ((f[4] + f[5]) + (f[6] + f[7]));
    float mt = xsum16_32(s2[0] + s2[1]) * (1.f / 64.f); asm("" : "+v"(mt));
    f32x2 q2 = {0.f, 0.f}, q3 = {0.f, 0.f};
#pragma unroll
    for (int j = 0; j < 8; j += 2) { f[j] -= mt; f[j + 1] -= mt; q2 += f[j] * f[j]; q3 += f[j + 1] * f[j + 1]; }
    q2 += q3;
    const float qq = xsum16_32(q2[0] + q2[1]);
    if (lane < 32) {
      const unsigned qb = qq == qq ? __float_as_uint(qq) : 0x7fc00000u;
      __hip_atomic_store((unsigned long long*)(ws + WS_XCH + sown), ((unsigned long long)qb << 32) | __float_as_uint(mt), __ATOMIC_RELAXED, __HIP_MEMORY_SCOPE_AGENT);
    }
    *(LDS_PTR(u32x4))(smem + (mb & 7) * 2048 + lane * 16) = (u32x4){o[0], o[1], o[2], o[3]};
    *(LDS_PTR(u32x4))(smem + (mb & 7) * 2048 + 1024 + lane * 16) = (u32x4){o[4], o[5], o[6], o[7]};
    STAMP_AT(16 );
    f32x4 ss[4], gg[4];
#pragma unroll
    for (int j = 0; j < 4; ++j) {
      ss[j] = *(const LDS_PTR(f32x4))(vec + 64 + j * 16 + 4 * g); gg[j] = *(const LDS_PTR(f32x4))(vec + 128 + j * 16 + 4 * g);
      f[2 * j] *= gg[j].xy; f[2 * j + 1] *= gg[j].zw;
      asm volatile("" : "+v"(ss[j]), "+v"(gg[j]), "+v"(f[2 * j]), "+v"(f[2 * j + 1]));
    }
    unsigned long long pv[4];
    __builtin_amdgcn_s_sleep(XS0);
    for (unsigned spins = 0;; ++spins) {
      bool ok = true;
#pragma unroll
      for (int i = 0; i < 4; ++i) { pv[i] = __hip_atomic_load((unsigned long long*)(ws + WS_XCH + slot) + i * 16, __ATOMIC_RELAXED, __HIP_MEMORY_SCOPE_AGENT); ok &= (unsigned)(pv[i] >> 32) != 0xffffffffu; }
      if (__builtin_amdgcn_ballot_w64(!ok) == 0 || spins > SPIN_CAP) break;
      __builtin_amdgcn_s_sleep(XS1);
    }
    STAMP_AT(17 );
    float ms = 0.f, m2 = 0.f;
#pragma unroll
    for (int i = 0; i < 4; ++i) { ms += __uint_as_float((unsigned)pv[i]); m2 += __uint_as_float((unsigned)(pv[i] >> 32)); }
    const float mean = xsum16_32(ms) * (1.f / 16.f);
#pragma unroll
    for (int i = 0; i < 4; ++i) { const float d = __uint_as_float((unsigned)pv[i]) - mean; m2 += 64.f * d * d; }
    const float rstd = ln_rstd(xsum16_32(m2) * (1.f / 1024.f));
    const float sh = (mt - rbf(mean)) * rstd;
#pragma unroll
    for (int p = 0; p < 2; ++p) {
      u32x4 v;
#pragma unroll
      for (int j = 0; j < 4; ++j) {
        const int jb = 2 * p + (j >> 1);
        const f32x2 h = f[4 * p + j] * rstd + (((j & 1) ? gg[jb].zw : gg[jb].xy) * sh + ((j & 1) ? ss[jb].zw : ss[jb].xy));
        v[j] = pk2(h[0], h[1]);
      }
      st16<true>(ws + WS_H2, ((tn4 * 2 + p) * 128 + mb) * 1024 + lane * 16, v);
    }
    STAMP_AT(18 );
    wait_vm<0>();
    wait_lgkm0();
    BAR();
    STAMP_AT(19 );

  }
  const int grp = wave >> 2;
  { const int wm = wave & 3;
    const LDS_PTR(char) sb = smem + 3 * K5_STG;
    const LDS_PTR(char) X = sb + K5_G + row * 224 + grp * 96 + p4 * 8;
    const LDS_PTR(char) Y = X + (grp ? 0 : K5_STG); X += grp ? K5_STG : 0;
    const LDS_PTR(char) aM = sb + (p4 < 2 ? K5_G : K5_U) + row * 224 + (12 + grp) * 16 + (p4 & 1) * 8;
    const LDS_PTR(char) aA = sb + wm * 4096 + lane * 16;
    f32x32 c0, c1, c2; f32x16 c3;
    asm volatile(".irp r,0,8,16,24,32,40,48,56,64,72,80,88,96,104\n\tv_mov_b64 v[\\r:\\r+1], 0\n\tv_mov_b64 v[\\r+2:\\r+3], 0\n\tv_mov_b64 v[\\r+4:\\r+5], 0\n\tv_mov_b64 v[\\r+6:\\r+7], 0\n\t.endr"
                 : "={v[0:31]}"(c0), "={v[32:63]}"(c1), "={v[64:95]}"(c2), "={v[96:111]}"(c3)::"memory");
    int wrap = -(K5_NS - 1) * K5_STG, dl = K5_STG, na = grp ? 99 : 7, nb = grp ? 7 : 99;
    unsigned mask = wrap_mask(4, K5_NS);
    asm volatile("" : "+s"(wrap), "+v"(X), "+v"(Y), "+v"(aM), "+v"(aA));
    if (grp) __builtin_amdgcn_s_setprio(1); else __builtin_amdgcn_s_setprio(2);
    BAR();
    STAMP_AT(20 );
    BAR();
    STAMP_G(21, 26);
    asm volatile("s_cmp_eq_u32 %[na], 7\n\ts_cbranch_scc1 0f\n\t"
                 W5(0, Y, 0) A5(1) W5(1, Y, 1) W5(2, Y, 2) W5(3, Y, 3) AD5(aA, aA) LG(6) Q5(0, 1, 0) W5(4, Y, 0) LG(6) Q5(1, 1, 1) SB
                 A5(0) W5(4, X, 1) LG(8) Q5(2, 1, 2) RW5(5, 0, Y, 2) Q5(3, 1, 3) LG(0) "s_branch 2f\n"
                 "0:\n\t" W5(0, aM, 2) A5(0) W5(4, X, 1) W5(5, X, 3) W5(0, X, 0) AD5(aA, aA) AD5(aM, aM) LG(6) Q5(6, 0, 2) RW5(1, 0, X, 2) LG(5) Q5(4, 0, 1) SB
                 A5(1) W5(0, Y, 1) LG(9) Q5(5, 0, 3) LG(0)
                 ".p2align 5\n1:\n\t" TRIPA(0, ) "2:\n\t" TRIPB(0, ) TRIPA(1, "s_sub_u32 %[na], %[na], 1\n\t") "s_cbranch_scc1 3f\n\t" TRIPB(1, "s_sub_u32 %[nb], %[nb], 1\n\t") "s_cbranch_scc0 1b\n\t"
                 RW5(1, 1, X, 2) W5(2, X, 3) W5(3, X, 1) Q5(0, 0, 0) LG(0) SB Q5(1, 0, 2) Q5(2, 0, 3) Q5(3, 0, 1) "s_branch 4f\n"
                 "3:\n\t" RW5(5, 1, Y, 0) W5(0, aM, 1) Q5(4, 1, 2) LG(0) SB Q5(5, 1, 0) Q5(6, 1, 1)
                 "4:\n\ts_nop 8"
                 : "+{v[0:31]}"(c0), "+{v[32:63]}"(c1), "+{v[64:95]}"(c2), "+{v[96:111]}"(c3), [X] "+v"(X), [Y] "+v"(Y), [aM] "+v"(aM), [aA] "+v"(aA), [dl] "+s"(dl), [mask] "+s"(mask),
                   [na] "+s"(na), [nb] "+s"(nb)
                 : [wrap] "s"(wrap) : "memory", "scc", "v112", "v113", "v114", "v115", "v116", "v117", "v118", "v119", V10(12), V10(13), V10(14), V10(15));
    f32x4 acc[4][7];
#pragma unroll
    for (int e = 0; e < 112; ++e) acc[e / 28][(e % 28) / 4][e & 3] = e < 32 ? c0[e & 31] : e < 64 ? c1[e & 31] : e < 96 ? c2[e & 31] : c3[e & 15];
    STAMP_G(22, 27);
    auto swi4 = [](const f32x4& gv, const f32x4& uv, unsigned& o0, unsigned& o1) {
      constexpr float c2 = 2.0813689810056077f;
      f32x2 pa = gv.xy * uv.xy, pb = gv.zw * uv.zw;
      const f32x2 ta = (f32x2){__builtin_amdgcn_exp2f(gv[0]), __builtin_amdgcn_exp2f(gv[1])} * c2 + c2, tb = (f32x2){__builtin_amdgcn_exp2f(gv[2]), __builtin_amdgcn_exp2f(gv[3])} * c2 + c2;
      pa *= (f32x2){__builtin_amdgcn_rcpf(ta[0]), __builtin_amdgcn_rcpf(ta[1])}; pb *= (f32x2){__builtin_amdgcn_rcpf(tb[0]), __builtin_amdgcn_rcpf(tb[1])};
      o0 = pk2(pa[0], pa[1]); o1 = pk2(pb[0], pb[1]);
    };
    const int lz = lane_id();
    const int gz = lz >> 4, fz = lz & 15;
    const int qb = (tn5 >> 4) * 224, ql = qb + 64 + (tn5 & 15) * 10;
    auto hof = [&](int e, int i) { return (unsigned)((e >> 2) * 131072 + (e & 3) * 256 + fz * 16 + i * 1024); };
    const unsigned ho0 = hof((grp ? ql + 2 : qb + (tn5 & 15) * 4) + 2 * (gz & 1) + (gz >> 1), 0), ho1 = hof(ql + 6 * grp + (gz >> 1), gz & 1), hom = hof(ql + 8 + grp, gz);
    char* hb = ws + WS_HH + (tm5 * 16 + wm * 4) * 1024;
    const int c5 = C5 + tm5 * 4 + (tn5 >> 4) * 2;
    auto tr16 = [](const unsigned (&o)[4]) {
      auto s0 = __builtin_amdgcn_permlane16_swap(o[0], o[2], false, false);
      auto s1 = __builtin_amdgcn_permlane16_swap(o[1], o[3], false, false);
      u32x4 v = {s0[0], s1[0], s0[1], s1[1]}; asm volatile("" : "+v"(v)); return v;
    };
    auto pair = [&](int p, int i) {
      unsigned o[4];
      swi4(acc[i][2 * p], acc[i][2 * p + 1], o[0], o[1]);
      swi4(acc[i + 1][2 * p], acc[i + 1][2 * p + 1], o[2], o[3]);
      return tr16(o);
    };
    u32x4 v[3];
#pragma unroll
    for (int i = 0; i < 4; ++i) {
      unsigned o[4];
      swi4(acc[i][0], acc[i][1], o[0], o[1]);
      swi4(acc[i][2], acc[i][3], o[2], o[3]);
      st16<true>(hb, ho0 + i * 1024, tr16(o));
      SCHED0();
    }
    STAMP_G(23, 28);
    v[0] = pair(2, 0);
    if (!grp) {
      wait_vm<0>();
      unsigned arr = 0;
      if (lz == 0) arr = __hip_atomic_fetch_add((LDS_PTR(unsigned))(smem + LDS_ARR), 1u, __ATOMIC_RELAXED, __HIP_MEMORY_SCOPE_WORKGROUP);
      if (__builtin_amdgcn_readfirstlane(arr) == 3) signal(cnt, c5, lz);
    }
    STAMP_G(24, 29);
    v[1] = pair(2, 2);
    { unsigned m[4];
#pragma unroll
      for (int i = 0; i < 4; i += 2) {
        f32x4 gv, uv;
#pragma unroll
        for (int e = 0; e < 4; ++e) {
          auto w = __builtin_amdgcn_permlane32_swap(__float_as_uint(acc[i + (e >> 1)][6][e & 1]), __float_as_uint(acc[i + (e >> 1)][6][2 + (e & 1)]), false, false);
          gv[e] = __uint_as_float(w[0]); uv[e] = __uint_as_float(w[1]);
        }
        swi4(gv, uv, m[i], m[i + 1]);
      }
      auto x0 = __builtin_amdgcn_permlane32_swap(m[0], m[2], false, false);
      auto x1 = __builtin_amdgcn_permlane32_swap(m[1], m[3], false, false);
      const unsigned o[4] = {x0[0], x0[1], x1[0], x1[1]};
      v[2] = tr16(o);
      st16<true>(hb, ho1, v[0]); st16<true>(hb, ho1 + 2048, v[1]); st16<true>(hb, hom, v[2]);
    }
    STAMP_G(25, 30);
    if (grp) {
#pragma unroll
      for (int t = 0; t < NDR; ++t) BAR();
      wait_vm<0>();
      BAR();
      if (wave == 4) signal(cnt, c5 + 1, lz);
      if (wave > 4) return;
#pragma unroll 1
      for (int t = NDR + 1; t < 9; ++t) BAR();
      unsigned seen = 16;
      const unsigned* pc = (lz & 1) ? (const unsigned*)(ws + WS_FLG) + (b ^ 4) * 32 : cnt + ((C5 + (tm6 >> 1) * 4 + kh6 * 2 + 1) * 8 + tn6) * 32;
      asm volatile("global_load_dword %0, %1, off sc1" : "=v"(seen) : "v"(pc) : "memory");
      BAR(); BAR();
      asm volatile("s_waitcnt vmcnt(0)" : "+v"(seen)::"memory");
      if (__builtin_amdgcn_ballot_w64(seen < 16) != 0) poll<0>(pc, 16);
      return;
    }
  }
  { lane = lane_id(); fr = lane & 15; g = lane >> 4; p4 = fr & 3; row = 4 * g + (fr >> 2);
    const int wm = wave & 1, wn = wave >> 1;
    const int lw0 = row * 128 + (((row >> 1) & 3) * 32) + p4 * 8;
    const int mb0 = tm6 * 8 + wm * 4, t64 = tn6 * 2 + kh6;
    unsigned a0 = lds_base + (lw0 ^ (wn * 64)), a1 = lds_base + (lw0 ^ (wn * 64 + 32)), aa = lds_base + K6_A + wm * 4096 + lane * 16;
    int dw, da, n = 4, n2 = 1;
    unsigned mw = wrap_mask(2, K6_NW), ma = wrap_mask(2, K6_NA);
    f32x32 c0, c1;
    asm volatile(".irp r,0,8,16,24,32,40,48,56\n\tv_mov_b64 v[\\r:\\r+1], 0\n\tv_mov_b64 v[\\r+2:\\r+3], 0\n\tv_mov_b64 v[\\r+4:\\r+5], 0\n\tv_mov_b64 v[\\r+6:\\r+7], 0\n\t.endr\n\t"
                 "s_waitcnt vmcnt(0)\n\ts_barrier\n\t"
                 RW6(1, 0, 0, 0) RW6(1, 0, 1, 0) RA6(0, 0) RW6(1, 1, 0, 0) RW6(1, 1, 1, 0) RA6(1, 0) RA6(2, 0) RA6(3, 0) RW6(0, 0, 0, 0) RW6(0, 0, 1, 0) RW6(0, 1, 0, 0) RW6(0, 1, 1, 0)
                 "v_add_u32 %[a0], 8192, %[a0]\n\tv_add_u32 %[a1], 8192, %[a1]\n\tv_add_u32 %[aa], 8192, %[aa]\n\t" LG(4) "s_barrier\n\t"
                 RW6(0, 0, 0, 1) RW6(0, 0, 1, 1) RA6(0, 1) RW6(0, 1, 0, 1) M6(0, 2, 0) RW6(0, 1, 1, 1) M6(0, 3, 0) RA6(1, 1) M6(1, 3, 0) RA6(2, 1) M6(1, 2, 0) RA6(3, 1)
                 M6(2, 2, 0) RW6(1, 0, 0, 1) M6(2, 3, 0) RW6(1, 0, 1, 1) M6(3, 3, 0) RW6(1, 1, 0, 1) M6(3, 2, 0) RW6(1, 1, 1, 1)
                 "v_add_u32 %[a0], 8192, %[a0]\n\tv_add_u32 %[a1], 8192, %[a1]\n\tv_add_u32 %[aa], 8192, %[aa]\n\t" LG(6) "s_barrier\n\t"
                 ".p2align 5\n1:\n\t" TRIP6(0, 0, 1, 2, ) TRIP6(1, 1, 2, 0, ) TRIP6(0, 2, 0, 1, ) TRIP6(1, 0, 1, 2, ) TRIP6(0, 1, 2, 0, ) TRIP6(1, 2, 0, 1, "s_sub_u32 %[n], %[n], 1\n\t")
                 "s_cbranch_scc0 1b\n\ts_mov_b32 %[n], 3\n\ts_mov_b32 %[mw], %[mw2]\n\ts_mov_b32 %[ma], %[ma2]\n\ts_sub_u32 %[n2], %[n2], 1\n\ts_cbranch_scc0 1b\n\t"
                 M6(0, 0, 0) M6(0, 0, 1) M6(0, 1, 1) M6(0, 1, 0) LG(0) M6(1, 1, 0) M6(1, 1, 1) M6(1, 0, 1) M6(1, 0, 0) M6(2, 0, 0) M6(2, 0, 1) M6(2, 1, 1) M6(2, 1, 0)
                 M6(3, 1, 0) M6(3, 1, 1) M6(3, 0, 1) M6(3, 0, 0) "s_barrier\n\t"
                 M6(0, 2, 1) M6(0, 3, 1) M6(1, 3, 1) M6(1, 2, 1) M6(2, 2, 1) M6(2, 3, 1) M6(3, 3, 1) M6(3, 2, 1) "s_nop 8"
                 : "={v[0:31]}"(c0), "={v[32:63]}"(c1), [a0] "+v"(a0), [a1] "+v"(a1), [aa] "+v"(aa), [dw] "=&s"(dw), [da] "=&s"(da), [mw] "+s"(mw), [ma] "+s"(ma), [n] "+s"(n), [n2] "+s"(n2)
                 : [ww] "s"(-(K6_NW - 1) * 8192), [wa] "s"(-(K6_NA - 1) * 8192), [mw2] "n"(wrap_mask(32, K6_NW)), [ma2] "n"(wrap_mask(32, K6_NA))
                 : "memory", "scc", "v64", "v65", "v66", "v67", "v68", "v69", V10(7), V10(8), V10(9), V10(10), V10(11), V10(12), V10(13), V10(14), V10(15));
    f32x4 acc[4][4];
#pragma unroll
    for (int e = 0; e < 64; ++e) acc[e >> 4][(e >> 2) & 3][e & 3] = e < 32 ? c0[e & 31] : c1[e & 31];
    STAMP_AT(34 );
    lane = lane_id(); fr = lane & 15; g = lane >> 4;
    const int slot = wm * 2 + wn, pb = (b ^ 4) * 4 + slot;
    { auto pk = [](float lo, float hi) { const unsigned u = pk2(lo, hi); return u < 0xfffffffeu ? u : 0xfffffffeu; };
#pragma unroll
      for (int i = 0; i < 4; ++i)
        st16<true>(ws + WS_PART, (b * 4 + slot) * 4096 + i * 1024 + lane * 16,
                   (u32x4){pk(acc[i][0][0], acc[i][0][1]), pk(acc[i][0][2], acc[i][0][3]), pk(acc[i][1][0], acc[i][1][1]), pk(acc[i][1][2], acc[i][1][3])});
      STAMP_AT(35 );
    }
    { const LDS_PTR(float) vec = (const LDS_PTR(float))(smem + LDS_VEC) + 192 + wn * 32 + 4 * g;
      const f32x4 gv0 = *(const LDS_PTR(f32x4))vec, gv1 = *(const LDS_PTR(f32x4))(vec + 16);
      u32x4 rv[4];
#pragma unroll
      for (int i = 0; i < 4; ++i) { rv[i] = *(const LDS_PTR(u32x4))(smem + K6_X + (wm * 4 + i) * 2048 + wn * 1024 + lane * 16); asm volatile("" : "+v"(rv[i])); }
      u32x4 z[4];
      __builtin_amdgcn_s_sleep(PD6);
#pragma unroll 1
      for (unsigned spins = 0; spins < SPIN_CAP; ++spins) {
        asm volatile("" ::: "memory");
        unsigned m = 0;
#pragma unroll
        for (int i = 0; i < 4; ++i) z[i] = ld16<true>(ws + WS_PART, pb * 4096 + i * 1024 + lane * 16);
#pragma unroll
        for (int i = 0; i < 16; ++i) m = m > z[i >> 2][i & 3] ? m : z[i >> 2][i & 3];
        if (__builtin_amdgcn_ballot_w64(m == ~0u) == 0) break;
        __builtin_amdgcn_s_sleep(PS6);
      }
      STAMP_AT(36 );
#pragma unroll
      for (int i = 0; i < 4; ++i) {
        const f32x4 v0 = (acc[i][2] + (f32x4){lo2f(z[i][0]), hi2f(z[i][0]), lo2f(z[i][1]), hi2f(z[i][1])}) * gv0;
        const f32x4 v1 = (acc[i][3] + (f32x4){lo2f(z[i][2]), hi2f(z[i][2]), lo2f(z[i][3]), hi2f(z[i][3])}) * gv1;
        const unsigned o0 = pk2(lo2f(rv[i][0]) + v0[0], hi2f(rv[i][0]) + v0[1]), o1 = pk2(lo2f(rv[i][1]) + v0[2], hi2f(rv[i][1]) + v0[3]);
        const unsigned o2 = pk2(lo2f(rv[i][2]) + v1[0], hi2f(rv[i][2]) + v1[1]), o3 = pk2(lo2f(rv[i][3]) + v1[2], hi2f(rv[i][3]) + v1[3]);
        auto s0 = __builtin_amdgcn_permlane16_swap(o0, o2, false, false);
        auto s1 = __builtin_amdgcn_permlane16_swap(o1, o3, false, false);
        st16<false>(out, (((mb0 + i) * 16 + fr) * D_ + t64 * 64 + wn * 32 + (g & 1) * 16 + (g >> 1) * 8) * 2, (u32x4){s0[0], s1[0], s0[1], s1[1]});
      }
    }
    STAMP_AT(37 );
  }
}

extern "C" void kernel_launch(void* const* d_in, const int* in_sizes, int n_in, void* d_out, int out_size, void* d_ws,
                              size_t ws_size, hipStream_t stream) {
  const u16* x = (const u16*)d_in[0];
  const u16* wqkv = (const u16*)d_in[1];
  const u16* wo = (const u16*)d_in[2];
  const u16* w1 = (const u16*)d_in[4];
  const u16* w3 = (const u16*)d_in[5];
  const u16* w2 = (const u16*)d_in[6];
  const u16* c = (const u16*)d_in[7];
  const u16* wmod = (const u16*)d_in[8];
  (void)hipFuncSetAttribute((const void*)k2_block, hipFuncAttributeMaxDynamicSharedMemorySize, MEGA_LDS);
  char* ws = (char*)d_ws;
  k2_block<<<256, 768, MEGA_LDS, stream>>>(x, wqkv, wo, w1, w3, w2, c, wmod, (u16*)d_out, ws);
}
```
